```python
import torch

def _softmax(x, axis=-1):
    return torch.softmax(x, dim=axis)
def _gelu(x):
    return torch.nn.functional.gelu(x, approximate="tanh")
def _silu(x):
    return torch.nn.functional.silu(x)
def _sigmoid(x):
    return torch.sigmoid(x)
def _layernorm(x, axis=-1, eps=1e-5):
    m = x.mean(dim=axis, keepdim=True)
    v = x.var(dim=axis, keepdim=True, unbiased=False)
    return (x - m) / torch.sqrt(v + eps)
def _scatter_add(tgt, idx, src, axis=0):
    return tgt.clone().index_add_(axis, idx, src)
def _segment_sum(src, seg, n):
    out = torch.zeros((n,) + src.shape[1:], dtype=src.dtype)
    return out.index_add_(0, seg, src)
def _pad(x, pw):
    flat = tuple(v for pair in reversed(pw) for v in pair)
    return torch.nn.functional.pad(x, flat)

kernel_name = 'sequential_preLN__sliding_block__dense_activation_gelu__prompt'

def setup_inputs():

    _w = (lambda a: (lambda t: t.to(torch.bfloat16) if t.is_floating_point() else t)(torch.as_tensor(a)))
    def _A_setup():
        B, S, D = 1, 2048, 576
        return {
            "x": _w((lambda *s: torch.randn(*s, dtype=torch.bfloat16) * 0.05)(B, S, D)),
            "wqkv": _w((lambda *s: torch.randn(*s, dtype=torch.bfloat16) * 0.05)(D, 3 * D)),
            "wo": _w((lambda *s: torch.randn(*s, dtype=torch.bfloat16) * 0.05)(D, D)),
        }
    _A_in = _A_setup()

    def _B_setup():
        B, S, D, F = 1, 2048, 576, 4608
        return {
            "x": _w((lambda *s: torch.randn(*s, dtype=torch.bfloat16) * 0.05)(B, S, D)),
            "w1": _w((lambda *s: torch.randn(*s, dtype=torch.bfloat16) * 0.8)(D, F) * (D ** -0.5)),
            "b1": _w((lambda *s: torch.randn(*s, dtype=torch.bfloat16) * 0.8)(F) * 0.01),
            "w2": _w((lambda *s: torch.randn(*s, dtype=torch.bfloat16) * 0.8)(F, D) * (F ** -0.5)),
            "b2": _w((lambda *s: torch.randn(*s, dtype=torch.bfloat16) * 0.8)(D) * 0.01),
        }
    _B_in = _B_setup()

    return {
        "A_x": _A_in["x"],
        "A_wqkv": _A_in["wqkv"],
        "A_wo": _A_in["wo"],
        "B_x": _B_in["x"],
        "B_w1": _B_in["w1"],
        "B_b1": _B_in["b1"],
        "B_w2": _B_in["w2"],
        "B_b2": _B_in["b2"],
    }

def reference(A_x, A_wqkv, A_wo, B_x, B_w1, B_b1, B_w2, B_b2):
    def _A_ref(x, wqkv, wo):
        B, S, D = x.shape
        H = 32; Dh = D // H; W = 256
        qkv = (lambda x, *d: x.permute(*d))((x @ wqkv).reshape(B, S, 3, H, Dh), 2, 0, 3, 1, 4)
        q, k, v = qkv[0], qkv[1], qkv[2]
        scores = torch.einsum("bhqd,bhkd->bhqk", q, k) * (Dh ** -0.5)
        idx = torch.arange(S)
        mask = (idx[:, None] - idx[None, :] >= 0) & (idx[:, None] - idx[None, :] < W)
        scores = torch.where(mask[None, None], scores, -1e9)
        p = _softmax(scores, axis=-1)
        o = torch.einsum("bhqk,bhkd->bhqd", p, v)
        return (lambda x, *d: x.permute(*d))(o, 0, 2, 1, 3).reshape(B, S, D) @ wo

    def _B_ref(x, w1, b1, w2, b2):
        h = torch.einsum("bsd,df->bsf", x, w1) + b1
        h = _gelu(h)
        return torch.einsum("bsf,fd->bsd", h, w2) + b2

    x = A_x
    h = x + _A_ref(_layernorm(x, axis=-1), A_wqkv, A_wo)
    h2 = h + _B_ref(_layernorm(h, axis=-1), B_w1, B_b1, B_w2, B_b2)
    return h2
```

```cpp
#include <hip/hip_runtime.h>
#include <stdint.h>

typedef unsigned short u16;
typedef __bf16 bf16x8 __attribute__((ext_vector_type(8)));
typedef __bf16 bf16x4 __attribute__((ext_vector_type(4)));
typedef __bf16 bf16x2 __attribute__((ext_vector_type(2)));
typedef float f32x2 __attribute__((ext_vector_type(2)));
typedef float f32x4 __attribute__((ext_vector_type(4)));
typedef float f32x16 __attribute__((ext_vector_type(16)));
typedef unsigned u32x2 __attribute__((ext_vector_type(2)));
typedef unsigned u32x4 __attribute__((ext_vector_type(4)));
#define LAS __attribute__((address_space(3)))
#define DI __device__ __forceinline__

constexpr int S = 2048, D = 576, H = 32, DH = 18, F = 4608, NQKV = 1728;
constexpr float LN_EPS = 1e-5f;

constexpr size_t WS_QKV = 0;
constexpr size_t WS_QKV_SZ = (size_t)3 * H * S * 24 * 2;
constexpr size_t WS_O = WS_QKV + WS_QKV_SZ;
constexpr size_t WS_H = WS_O + (size_t)S * D * 2;
constexpr size_t WS_H1 = WS_H + (size_t)S * D * 2;
constexpr size_t WS_PART = WS_H1 + (size_t)S * F * 2;
constexpr size_t WS_ST1 = WS_PART + (size_t)4 * S * D * 4;
constexpr size_t WS_ST2 = WS_ST1 + (size_t)S * 8;
constexpr size_t WS_CSQ = WS_ST2 + (size_t)S * 8 * 8;
constexpr size_t WS_CS1 = WS_CSQ + (size_t)NQKV * 4;
constexpr size_t WS_END = WS_CS1 + (size_t)F * 4;

DI unsigned rfl(unsigned x) { return __builtin_amdgcn_readfirstlane(x); }
DI float bf2f(u16 v) { return __uint_as_float((unsigned)v << 16); }
DI float bflo(unsigned w) { return __uint_as_float(w << 16); }
DI float bfhi(unsigned w) { return __uint_as_float(w & 0xffff0000u); }
DI unsigned pk2(float lo, float hi) { f32x2 v = {lo, hi}; bf16x2 b = __builtin_convertvector(v, bf16x2); return __builtin_bit_cast(unsigned, b); }

typedef __amdgpu_buffer_rsrc_t srd_t;
DI srd_t mk_srd(const void* p, unsigned bytes) { return __builtin_amdgcn_make_buffer_rsrc((void*)p, (short)0, (int)bytes, 0x00020000); }
DI void dma16(unsigned ldsaddr, unsigned voff, srd_t srd, unsigned soff) {
  asm volatile("s_mov_b32 m0, %0\n\ts_nop 0\n\tbuffer_load_dwordx4 %1, %2, %3 offen lds" :: "s"(ldsaddr), "v"(voff), "s"(srd), "s"(soff) : "memory");
}
template <int N> DI void wait_vm() { asm volatile("s_waitcnt vmcnt(%0)" :: "i"(N) : "memory"); }
DI void wait_lgkm0() { asm volatile("s_waitcnt lgkmcnt(0)" ::: "memory"); }
DI void bar() { asm volatile("" ::: "memory"); __builtin_amdgcn_s_barrier(); asm volatile("" ::: "memory"); }
DI bf16x8 lds_b128(unsigned addr) { return *(const LAS bf16x8*)(size_t)addr; }
DI bf16x4 lds_tr(unsigned addr) { return __builtin_amdgcn_ds_read_tr16_b64_v4bf16((LAS bf16x4*)(size_t)addr); }
DI bf16x8 cat8(bf16x4 a, bf16x4 b) { return __builtin_shufflevector(a, b, 0, 1, 2, 3, 4, 5, 6, 7); }

__global__ __launch_bounds__(256) void k0_stats(const u16* __restrict__ x, const u16* __restrict__ wqkv, const u16* __restrict__ w1,
                                                f32x2* __restrict__ st1, float* __restrict__ csq, float* __restrict__ cs1) {
  __shared__ float red[32][64];
  const int b = blockIdx.x, tid = threadIdx.x;
  if (b < 128) {
    const int w = tid >> 6, lane = tid & 63;
    for (int r = 0; r < 4; r++) {
      const int row = b * 16 + w * 4 + r;
      float v[9]; float s = 0;
      for (int i = 0; i < 9; i++) { v[i] = bf2f(x[row * D + lane + 64 * i]); s += v[i]; }
      for (int m = 32; m >= 1; m >>= 1) s += __shfl_xor(s, m);
      const float mean = s * (1.0f / D); float q = 0;
      for (int i = 0; i < 9; i++) { float d = v[i] - mean; q += d * d; }
      for (int m = 32; m >= 1; m >>= 1) q += __shfl_xor(q, m);
      if (lane == 0) st1[row] = f32x2{mean, rsqrtf(q * (1.0f / D) + LN_EPS)};
    }
  } else {
    const int cb = b - 128;
    const u16* W; int ld, c0; float* out;
    if (cb < 27) { W = wqkv; ld = NQKV; c0 = cb * 64; out = csq + c0; } else { W = w1; ld = F; c0 = (cb - 27) * 64; out = cs1 + c0; }
    const int ch = tid & 7, rg = tid >> 3;
    float a[8] = {0, 0, 0, 0, 0, 0, 0, 0};
    for (int i = 0; i < 18; i++) {
      const u32x4 v = *(const u32x4*)(W + (size_t)(rg * 18 + i) * ld + c0 + ch * 8);
      for (int j = 0; j < 4; j++) { a[2 * j] += bflo(v[j]); a[2 * j + 1] += bfhi(v[j]); }
    }
    for (int j = 0; j < 8; j++) red[rg][ch * 8 + j] = a[j];
    __syncthreads();
    if (tid < 64) { float s = 0; for (int r = 0; r < 32; r++) s += red[r][tid]; out[tid] = s; }
  }
}

template <int RT, int NSUB, int RING> struct GemmCfg {
  static constexpr int BM = 64 * RT;
  static constexpr int XBYTES = BM * 128;
  static constexpr int WBYTES = NSUB * 18432;
  static constexpr int SLOT = XBYTES + WBYTES;
  static constexpr int XP = BM / 8;
  static constexpr int WP = 18 * NSUB;
  static constexpr int LDS = SLOT * RING;
};

template <int RT, int NSUB, int RING, int NSLOT, int NT, int NT0>
DI void gemm_core(f32x4 (&acc)[RT][NT], unsigned lds0, int w, int lane, srd_t sx, unsigned xbase  , unsigned ldx2,
                  srd_t sw, unsigned wbase  , unsigned ldw2) {
  using C = GemmCfg<RT, NSUB, RING>;
  const int rg = w & 3, p = w >> 2;
  constexpr int XPW = C::XP / 8;
  constexpr int WPW = (C::WP + 7) / 8;
  const int xr = lane >> 3;
  const unsigned vx = (unsigned)xr * ldx2 + (unsigned)(((lane & 7) ^ ((4 * (w & 1) + (lane >> 4)) & 7)) * 16);
  unsigned vw[WPW];
#pragma unroll
  for (int i = 0; i < WPW; i++) {
    const int q = w + 8 * i, sub = q / 18, qq = q % 18, c = 64 * qq + lane, rho = c / 18, cc = c % 18, k = rho ^ (((rho >> 3) & 1) << 2);
    vw[i] = (unsigned)k * ldw2 + (unsigned)(sub * 144 + cc * 8) * 2;
  }
  const bool extra = (w + 8 * (WPW - 1)) < C::WP;
  auto issue = [&](int t) {
    const unsigned sb = lds0 + (unsigned)(t % RING) * C::SLOT;
#pragma unroll
    for (int i = 0; i < XPW; i++) dma16(sb + (unsigned)(w + 8 * i) * 1024, vx, sx, xbase + (unsigned)(8 * (w + 8 * i)) * ldx2 + (unsigned)t * 128);
#pragma unroll
    for (int i = 0; i < WPW; i++)
      if (i < WPW - 1 || extra) dma16(sb + C::XBYTES + (unsigned)(w + 8 * i) * 1024, vw[i], sw, wbase + (unsigned)t * 64 * ldw2);
  };
  const int tok = lane & 15, g = lane >> 4;
  const unsigned xo0 = (unsigned)(rg * 16 * RT + tok) * 128 + (unsigned)((g ^ (tok >> 1)) * 16);
  const unsigned xo1 = xo0 ^ 64u;
  const int q4 = (lane >> 2) & 3, pp = lane & 3;
  const unsigned wcol = C::XBYTES + (NSUB == 2 ? (unsigned)p * 18432u : 0u) + (unsigned)NT0 * 32u + (unsigned)pp * 8u;
  const unsigned wo0 = wcol + (unsigned)(8 * g + q4 + 4 * (g & 1)) * 288u;
  const unsigned wo1 = wcol + (unsigned)(8 * g + q4 + 4 * ((g & 1) ^ 1)) * 288u;

#pragma unroll
  for (int i = 0; i < RING - 1; i++) issue(i);
  constexpr int NP1 = XPW + WPW, NP0 = XPW + WPW - 1;
  for (int t = 0; t < NSLOT; t++) {
    const int ahead = (NSLOT - 1 - t) < (RING - 2) ? (NSLOT - 1 - t) : (RING - 2);
    if (extra) { if (ahead >= 2) wait_vm<2 * NP1>(); else if (ahead == 1) wait_vm<NP1>(); else wait_vm<0>(); }
    else       { if (ahead >= 2) wait_vm<2 * NP0>(); else if (ahead == 1) wait_vm<NP0>(); else wait_vm<0>(); }
    wait_lgkm0();
    bar();
    if (t + RING - 1 < NSLOT) issue(t + RING - 1);
    const unsigned sb = lds0 + (unsigned)(t % RING) * C::SLOT;
#pragma unroll
    for (int ks = 0; ks < 2; ks++) {
      bf16x8 xf[RT];
#pragma unroll
      for (int rt = 0; rt < RT; rt++) xf[rt] = lds_b128(sb + (ks ? xo1 : xo0) + rt * 2048);
#pragma unroll
      for (int nt = 0; nt < NT; nt++) {
        const bf16x8 wf = cat8(lds_tr(sb + wo0 + ks * 9216 + nt * 32), lds_tr(sb + wo1 + ks * 9216 + nt * 32));
#pragma unroll
        for (int rt = 0; rt < RT; rt++) acc[rt][nt] = __builtin_amdgcn_mfma_f32_16x16x32_bf16(wf, xf[rt], acc[rt][nt], 0, 0, 0);
      }
    }
  }
  wait_lgkm0();
  bar();
}

template <int NT, int NT0>
DI void k1_body(char* smem, const u16* x, const u16* wqkv, const f32x2* st1, const float* csq, u16* qkv, int rb, int ct, int w, int lane) {
  constexpr int RT = 2;
  const unsigned lds0 = (unsigned)(size_t)smem;
  f32x4 acc[RT][NT];
#pragma unroll
  for (int a = 0; a < RT; a++)
#pragma unroll
    for (int b = 0; b < NT; b++) acc[a][b] = f32x4{0, 0, 0, 0};
  srd_t sx = mk_srd(x, S * D * 2), sw = mk_srd(wqkv, D * NQKV * 2);
  gemm_core<RT, 1, 4, 9, NT, NT0>(acc, lds0, w, lane, sx, (unsigned)rb * 128 * D * 2, D * 2, sw, (unsigned)ct * 288, NQKV * 2);
  const int rg = w & 3, tok = lane & 15, g = lane >> 4;
  const int type = ct >> 2;
  const float qs = type == 0 ? 0.23570226039551584f * 1.4426950408889634f : 1.0f;
#pragma unroll
  for (int rt = 0; rt < RT; rt++) {
    const int tl = rg * 32 + rt * 16 + tok;
    const f32x2 st = st1[rb * 128 + tl];
    const float a = st[1] * qs, mean = st[0];
#pragma unroll
    for (int nt = 0; nt < NT; nt++) {
      const int n = 16 * (NT0 + nt) + 4 * g;
      const f32x4 cs = *(const f32x4*)(csq + ct * 144 + n);
      const f32x4 v = (acc[rt][nt] - mean * cs) * a;
      const int h0 = n / 18, d0 = n % 18, h1 = (n + 2) / 18, d1 = (n + 2) % 18;
      *(LAS unsigned*)(size_t)(lds0 + tl * 400 + h0 * 48 + d0 * 2) = pk2(v[0], v[1]);
      *(LAS unsigned*)(size_t)(lds0 + tl * 400 + h1 * 48 + d1 * 2) = pk2(v[2], v[3]);
    }
  }
  {
    const int tid = w * 64 + lane;
    const unsigned padv = type == 0 ? 0u : 0x00003f80u;
#pragma unroll
    for (int i = 0; i < 2; i++) {
      const int e = tid + 512 * i, tl = e >> 3, hh = e & 7;
      const unsigned a = lds0 + tl * 400 + hh * 48 + 36;
      *(LAS unsigned*)(size_t)a = padv; *(LAS unsigned*)(size_t)(a + 4) = 0; *(LAS unsigned*)(size_t)(a + 8) = 0;
    }
    wait_lgkm0(); bar();
    char* dst = (char*)qkv + ((size_t)(type * 32 + (ct & 3) * 8) * S + rb * 128) * 48;
#pragma unroll
    for (int i = 0; i < 6; i++) {
      const int c = tid + 512 * i, hh = c / 384, rem = c % 384, tl = rem / 3, part = rem % 3;
      const u32x4 v = *(const LAS u32x4*)(size_t)(lds0 + tl * 400 + hh * 48 + part * 16);
      *(u32x4*)(dst + (size_t)hh * S * 48 + rem * 16) = v;
    }
  }
}
__global__ __launch_bounds__(512, 2) void k1_qkv(const u16* __restrict__ x, const u16* __restrict__ wqkv, const f32x2* __restrict__ st1,
                                                 const float* __restrict__ csq, u16* __restrict__ qkv) {
  extern __shared__ __attribute__((aligned(16))) char smem[];
  const int b = blockIdx.x, xcd = b & 7, i = b >> 3;
  const int rb = (xcd >> 1) * 4 + (i & 3), ct = (xcd & 1) * 6 + (i >> 2);
  const int w = rfl(threadIdx.x >> 6), lane = threadIdx.x & 63;
  if (w < 4) k1_body<5, 0>(smem, x, wqkv, st1, csq, qkv, rb, ct, w, lane);
  else       k1_body<4, 5>(smem, x, wqkv, st1, csq, qkv, rb, ct, w, lane);
}

constexpr int K2_LDS = 2 * 24576 + 64;
__global__ __launch_bounds__(512, 2) void k2_attn(const u16* __restrict__ qkv, u16* __restrict__ o) {
  extern __shared__ __attribute__((aligned(16))) char smem[];
  const unsigned lds0 = (unsigned)(size_t)smem;
  const int b = blockIdx.x, xcd = b & 7, i = b >> 3;
  const int head = xcd * 4 + (i & 3), qb = i >> 2;
  const int w = rfl(threadIdx.x >> 6), lane = threadIdx.x & 63;
  const unsigned ldsK = lds0, ldsV = lds0 + 24576;
  srd_t sq = mk_srd(qkv, (unsigned)WS_QKV_SZ);
  const unsigned kbase = (unsigned)((32 + head) * S + 256 * (qb - 1)) * 48u, vbase = (unsigned)((64 + head) * S + 256 * (qb - 1)) * 48u;
  const unsigned vl = lane * 16;
#pragma unroll
  for (int hf = 0; hf < 2; hf++) {
    if (hf == 1 || qb > 0) {
#pragma unroll
      for (int j = 0; j < 3; j++) {
        const int pi = w * 3 + j;
        const unsigned pc = (unsigned)hf * 12288u + (unsigned)(pi % 12) * 1024;
        if (pi < 12) dma16(ldsK + pc, vl, sq, kbase + pc); else dma16(ldsV + pc, vl, sq, vbase + pc);
      }
    }
  }
  const int qq = lane & 31, h = lane >> 5;
  const int q0 = 256 * qb + 32 * w;
  const char* qrow = (const char*)qkv + ((size_t)head * S + q0 + qq) * 48;
  bf16x8 qf0 = *(const bf16x8*)(qrow + 16 * h);
  u32x4 qf1u = *(const u32x4*)(qrow + 32);
  if (h) qf1u = u32x4{0, 0, 0, 0};
  f32x16 cold, cdiag;
#pragma unroll
  for (int r = 0; r < 16; r++) {
    const int kk = 16 * (r >> 3) + 8 * h + (r & 7);
    cold[r] = kk > qq ? 0.f : -1e30f;
    cdiag[r] = kk <= qq ? 0.f : -1e30f;
  }
  const int ki = lane & 31, key = (ki & 19) | ((ki & 4) << 1) | ((ki & 8) >> 1);
  const unsigned ka0 = ldsK + (unsigned)(key + 32 * w) * 48 + 16 * h;
  const unsigned ka1 = ldsK + (unsigned)(key + 32 * w) * 48 + 32;
  const int blk = (lane >> 4) & 1, q4 = (lane >> 2) & 3, pp = lane & 3;
  const unsigned va = ldsV + (unsigned)(32 * w + 8 * h + q4) * 48 + 32 * blk + 8 * pp;
  wait_vm<0>(); bar();
  const int j0 = qb == 0 ? 8 - w : 0;
  const f32x16 zero = {};
  float m = -1e30f;
  {
    const bf16x8 qf1 = __builtin_bit_cast(bf16x8, qf1u);
    for (int j = j0; j < 9; j++) {
      const bf16x8 k0 = lds_b128(ka0 + j * 1536), k1 = lds_b128(ka1 + j * 1536);
      f32x16 s;
      if (j == 0) s = __builtin_amdgcn_mfma_f32_32x32x16_bf16(k0, qf0, cold, 0, 0, 0);
      else if (j == 8) s = __builtin_amdgcn_mfma_f32_32x32x16_bf16(k0, qf0, cdiag, 0, 0, 0);
      else s = __builtin_amdgcn_mfma_f32_32x32x16_bf16(k0, qf0, zero, 0, 0, 0);
      s = __builtin_amdgcn_mfma_f32_32x32x16_bf16(k1, qf1, s, 0, 0, 0);
#pragma unroll
      for (int r = 0; r < 16; r += 2) m = fmaxf(m, fmaxf(s[r], s[r + 1]));
    }
  }
  m = fmaxf(m, __shfl_xor(m, 32));
  {
    const unsigned mb = pk2(-m, 0.f);
    if (h == 0) qf1u[1] = mb;
  }
  const bf16x8 qf1 = __builtin_bit_cast(bf16x8, qf1u);
  f32x16 oacc = {};
  for (int j = j0; j < 9; j++) {
    const bf16x8 k0 = lds_b128(ka0 + j * 1536), k1 = lds_b128(ka1 + j * 1536);
    f32x16 s;
    if (j == 0) s = __builtin_amdgcn_mfma_f32_32x32x16_bf16(k0, qf0, cold, 0, 0, 0);
    else if (j == 8) s = __builtin_amdgcn_mfma_f32_32x32x16_bf16(k0, qf0, cdiag, 0, 0, 0);
    else s = __builtin_amdgcn_mfma_f32_32x32x16_bf16(k0, qf0, zero, 0, 0, 0);
    s = __builtin_amdgcn_mfma_f32_32x32x16_bf16(k1, qf1, s, 0, 0, 0);
    u32x4 p0, p1;
#pragma unroll
    for (int e = 0; e < 4; e++) {
      p0[e] = pk2(__builtin_amdgcn_exp2f(s[2 * e]), __builtin_amdgcn_exp2f(s[2 * e + 1]));
      p1[e] = pk2(__builtin_amdgcn_exp2f(s[8 + 2 * e]), __builtin_amdgcn_exp2f(s[8 + 2 * e + 1]));
    }
    const unsigned vb = va + j * 1536;
    const bf16x8 v0 = cat8(lds_tr(vb), lds_tr(vb + 4 * 48));
    const bf16x8 v1 = cat8(lds_tr(vb + 16 * 48), lds_tr(vb + 20 * 48));
    oacc = __builtin_amdgcn_mfma_f32_32x32x16_bf16(v0, __builtin_bit_cast(bf16x8, p0), oacc, 0, 0, 0);
    oacc = __builtin_amdgcn_mfma_f32_32x32x16_bf16(v1, __builtin_bit_cast(bf16x8, p1), oacc, 0, 0, 0);
  }
  const float l = __shfl(oacc[10], qq);
  const float inv = 1.0f / l;
  char* orow = (char*)o + (size_t)(q0 + qq) * (D * 2) + head * 36 + 8 * h;
  *(unsigned*)(orow) = pk2(oacc[0] * inv, oacc[1] * inv);
  *(unsigned*)(orow + 4) = pk2(oacc[2] * inv, oacc[3] * inv);
  *(unsigned*)(orow + 16) = pk2(oacc[4] * inv, oacc[5] * inv);
  *(unsigned*)(orow + 20) = pk2(oacc[6] * inv, oacc[7] * inv);
  if (h == 0) *(unsigned*)(orow + 32) = pk2(oacc[8] * inv, oacc[9] * inv);
}

template <int NT, int NT0>
DI void k3_body(char* smem, const u16* o, const u16* wo, const u16* x, u16* h, f32x2* st2, int rb, int ct, int w, int lane) {
  constexpr int RT = 1;
  const unsigned lds0 = (unsigned)(size_t)smem;
  f32x4 acc[RT][NT];
#pragma unroll
  for (int b = 0; b < NT; b++) acc[0][b] = f32x4{0, 0, 0, 0};
  srd_t sx = mk_srd(o, S * D * 2), sw = mk_srd(wo, D * D * 2);
  gemm_core<RT, 1, 4, 9, NT, NT0>(acc, lds0, w, lane, sx, (unsigned)rb * 64 * D * 2, D * 2, sw, (unsigned)ct * 288, D * 2);
  const int rg = w & 3, p = w >> 2, tok = lane & 15, g = lane >> 4;
  const int row = rb * 64 + rg * 16 + tok;
  float s = 0, q = 0;
#pragma unroll
  for (int nt = 0; nt < NT; nt++) {
    const int n = ct * 144 + 16 * (NT0 + nt) + 4 * g;
    const u32x2 xv = *(const u32x2*)(x + (size_t)row * D + n);
    u32x2 r;
    r[0] = pk2(bflo(xv[0]) + acc[0][nt][0], bfhi(xv[0]) + acc[0][nt][1]);
    r[1] = pk2(bflo(xv[1]) + acc[0][nt][2], bfhi(xv[1]) + acc[0][nt][3]);
    *(u32x2*)(h + (size_t)row * D + n) = r;
    const float a0 = bflo(r[0]), a1 = bfhi(r[0]), a2 = bflo(r[1]), a3 = bfhi(r[1]);
    s += (a0 + a1) + (a2 + a3); q += (a0 * a0 + a1 * a1) + (a2 * a2 + a3 * a3);
  }
  s += __shfl_xor(s, 16); s += __shfl_xor(s, 32); q += __shfl_xor(q, 16); q += __shfl_xor(q, 32);
  if (g == 0) st2[row * 8 + ct * 2 + p] = f32x2{s, q};
}
__global__ __launch_bounds__(512, 2) void k3_wo(const u16* __restrict__ o, const u16* __restrict__ wo, const u16* __restrict__ x,
                                                u16* __restrict__ h, f32x2* __restrict__ st2) {
  extern __shared__ __attribute__((aligned(16))) char smem[];
  const int b = blockIdx.x, xcd = b & 7, i = b >> 3;
  const int rb = xcd * 4 + (i & 3), ct = i >> 2;
  const int w = rfl(threadIdx.x >> 6), lane = threadIdx.x & 63;
  if (w < 4) k3_body<5, 0>(smem, o, wo, x, h, st2, rb, ct, w, lane);
  else       k3_body<4, 5>(smem, o, wo, x, h, st2, rb, ct, w, lane);
}

DI float gelu_tanh(float x) {
  constexpr float c0 = -2.0f * 0.7978845608028654f * 1.4426950408889634f, c1 = c0 * 0.044715f;
  const float t = x * (c0 + c1 * (x * x));
  return x * __builtin_amdgcn_rcpf(1.0f + __builtin_amdgcn_exp2f(t));
}
__global__ __launch_bounds__(512, 2) void k4_ffn1(const u16* __restrict__ h, const u16* __restrict__ w1, const u16* __restrict__ b1,
                                                  const f32x2* __restrict__ st2, const float* __restrict__ cs1, u16* __restrict__ h1) {
  extern __shared__ __attribute__((aligned(16))) char smem[];
  const unsigned lds0 = (unsigned)(size_t)smem;
  const int b = blockIdx.x, xcd = b & 7, i = b >> 3;
  const int rb = (xcd >> 2) * 8 + (i & 7), fc = (xcd & 3) * 4 + (i >> 3);
  const int w = rfl(threadIdx.x >> 6), lane = threadIdx.x & 63;
  constexpr int RT = 2, NT = 9;
  f32x4 acc[RT][NT];
#pragma unroll
  for (int a = 0; a < RT; a++)
#pragma unroll
    for (int c = 0; c < NT; c++) acc[a][c] = f32x4{0, 0, 0, 0};
  srd_t sx = mk_srd(h, S * D * 2), sw = mk_srd(w1, D * F * 2);
  gemm_core<RT, 2, 3, 9, NT, 0>(acc, lds0, w, lane, sx, (unsigned)rb * 128 * D * 2, D * 2, sw, (unsigned)fc * 576, F * 2);
  const int rg = w & 3, p = w >> 2, tok = lane & 15, g = lane >> 4;
#pragma unroll
  for (int rt = 0; rt < RT; rt++) {
    const int row = rb * 128 + rg * 32 + rt * 16 + tok;
    float s = 0, q = 0;
#pragma unroll
    for (int e = 0; e < 8; e++) { const f32x2 v = st2[row * 8 + e]; s += v[0]; q += v[1]; }
    const float mean = s * (1.0f / D), var = fmaxf(q * (1.0f / D) - mean * mean, 0.f), rstd = rsqrtf(var + LN_EPS);
#pragma unroll
    for (int nt = 0; nt < NT; nt++) {
      const int n = fc * 288 + p * 144 + 16 * nt + 4 * g;
      const f32x4 cs = *(const f32x4*)(cs1 + n);
      const u32x2 bv = *(const u32x2*)(b1 + n);
      const f32x4 v = (acc[rt][nt] - mean * cs) * rstd;
      u32x2 r;
      r[0] = pk2(gelu_tanh(v[0] + bflo(bv[0])), gelu_tanh(v[1] + bfhi(bv[0])));
      r[1] = pk2(gelu_tanh(v[2] + bflo(bv[1])), gelu_tanh(v[3] + bfhi(bv[1])));
      *(u32x2*)(h1 + (size_t)row * F + n) = r;
    }
  }
}

template <int NT, int NT0>
DI void k5_body(char* smem, const u16* h1, const u16* w2, float* part, int rb, int ct, int ks, int w, int lane) {
  constexpr int RT = 2;
  const unsigned lds0 = (unsigned)(size_t)smem;
  f32x4 acc[RT][NT];
#pragma unroll
  for (int a = 0; a < RT; a++)
#pragma unroll
    for (int b = 0; b < NT; b++) acc[a][b] = f32x4{0, 0, 0, 0};
  srd_t sx = mk_srd(h1, S * F * 2), sw = mk_srd(w2, F * D * 2);
  gemm_core<RT, 1, 4, 18, NT, NT0>(acc, lds0, w, lane, sx, (unsigned)rb * 128 * F * 2 + (unsigned)ks * 2304, F * 2,
                                   sw, (unsigned)ks * 1152 * D * 2 + (unsigned)ct * 288, D * 2);
  const int rg = w & 3, tok = lane & 15, g = lane >> 4;
#pragma unroll
  for (int rt = 0; rt < RT; rt++) {
    const int row = rb * 128 + rg * 32 + rt * 16 + tok;
#pragma unroll
    for (int nt = 0; nt < NT; nt++) {
      const int n = ct * 144 + 16 * (NT0 + nt) + 4 * g;
      *(f32x4*)(part + ((size_t)ks * S + row) * D + n) = acc[rt][nt];
    }
  }
}
__global__ __launch_bounds__(512, 2) void k5_ffn2(const u16* __restrict__ h1, const u16* __restrict__ w2, float* __restrict__ part) {
  extern __shared__ __attribute__((aligned(16))) char smem[];
  const int b = blockIdx.x, xcd = b & 7, i = b >> 3;
  const int rb = (xcd >> 2) * 8 + (i & 7), ct = i >> 3, ks = xcd & 3;
  const int w = rfl(threadIdx.x >> 6), lane = threadIdx.x & 63;
  if (w < 4) k5_body<5, 0>(smem, h1, w2, part, rb, ct, ks, w, lane);
  else       k5_body<4, 5>(smem, h1, w2, part, rb, ct, ks, w, lane);
}

__global__ __launch_bounds__(256) void k6_reduce(const float* __restrict__ part, const u16* __restrict__ h, const u16* __restrict__ b2, u16* __restrict__ out) {
  const int e = (blockIdx.x * 256 + threadIdx.x) * 4;
  const int n = e % D;
  f32x4 s = *(const f32x4*)(part + e);
#pragma unroll
  for (int k = 1; k < 4; k++) s += *(const f32x4*)(part + (size_t)k * S * D + e);
  const u32x2 hv = *(const u32x2*)(h + e), bv = *(const u32x2*)(b2 + n);
  u32x2 r;
  r[0] = pk2(bflo(hv[0]) + (s[0] + bflo(bv[0])), bfhi(hv[0]) + (s[1] + bfhi(bv[0])));
  r[1] = pk2(bflo(hv[1]) + (s[2] + bflo(bv[1])), bfhi(hv[1]) + (s[3] + bfhi(bv[1])));
  *(u32x2*)(out + e) = r;
}

extern "C" void kernel_launch(void* const* d_in, const int* in_sizes, int n_in, void* d_out, int out_size, void* d_ws, size_t ws_size,
                              hipStream_t stream) {
  const u16* x = (const u16*)d_in[0];
  const u16* wqkv = (const u16*)d_in[1];
  const u16* wo = (const u16*)d_in[2];
  const u16* w1 = (const u16*)d_in[4];
  const u16* b1 = (const u16*)d_in[5];
  const u16* w2 = (const u16*)d_in[6];
  const u16* b2 = (const u16*)d_in[7];
  char* ws = (char*)d_ws;
  u16* qkv = (u16*)(ws + WS_QKV); u16* o = (u16*)(ws + WS_O); u16* h = (u16*)(ws + WS_H); u16* h1 = (u16*)(ws + WS_H1);
  float* part = (float*)(ws + WS_PART); f32x2* st1 = (f32x2*)(ws + WS_ST1); f32x2* st2 = (f32x2*)(ws + WS_ST2);
  float* csq = (float*)(ws + WS_CSQ); float* cs1 = (float*)(ws + WS_CS1);
  constexpr int L1 = GemmCfg<2, 1, 4>::LDS, L3 = GemmCfg<1, 1, 4>::LDS, L4 = GemmCfg<2, 2, 3>::LDS;
  (void)hipFuncSetAttribute((const void*)k1_qkv, hipFuncAttributeMaxDynamicSharedMemorySize, L1);
  (void)hipFuncSetAttribute((const void*)k3_wo, hipFuncAttributeMaxDynamicSharedMemorySize, L3);
  (void)hipFuncSetAttribute((const void*)k4_ffn1, hipFuncAttributeMaxDynamicSharedMemorySize, L4);
  (void)hipFuncSetAttribute((const void*)k5_ffn2, hipFuncAttributeMaxDynamicSharedMemorySize, L1);
  k0_stats<<<128 + 99, 256, 0, stream>>>(x, wqkv, w1, st1, csq, cs1);
  k1_qkv<<<192, 512, L1, stream>>>(x, wqkv, st1, csq, qkv);
  k2_attn<<<256, 512, K2_LDS, stream>>>(qkv, o);
  k3_wo<<<128, 512, L3, stream>>>(o, wo, x, h, st2);
  k4_ffn1<<<256, 512, L4, stream>>>(h, w1, b1, st2, cs1, h1);
  k5_ffn2<<<256, 512, L1, stream>>>(h1, w2, part);
  k6_reduce<<<S * D / 4 / 256, 256, 0, stream>>>(part, h, b2, (u16*)d_out);
}
```

```cpp
#include <hip/hip_runtime.h>
#include <stdint.h>

typedef unsigned short u16;
typedef __bf16 bf16x8 __attribute__((ext_vector_type(8)));
typedef __bf16 bf16x4 __attribute__((ext_vector_type(4)));
typedef __bf16 bf16x2 __attribute__((ext_vector_type(2)));
typedef float f32x2 __attribute__((ext_vector_type(2)));
typedef float f32x4 __attribute__((ext_vector_type(4)));
typedef float f32x16 __attribute__((ext_vector_type(16)));
typedef unsigned u32x2 __attribute__((ext_vector_type(2)));
typedef unsigned u32x4 __attribute__((ext_vector_type(4)));
#define QA_DOT 1
#define QA_DMA 1
#define QA_WAIT 1
#define LAS __attribute__((address_space(3)))
#define DI __device__ __forceinline__
#define SB() __builtin_amdgcn_sched_barrier(0)
#define KATTR __launch_bounds__(512, 2) __attribute__((amdgpu_waves_per_eu(2, 2)))

constexpr int S = 2048, D = 576, H = 32, DH = 18, F = 4608, NQKV = 1728;
constexpr float LN_EPS = 1e-5f;

constexpr size_t WS_QKV = 0;
constexpr size_t WS_QKV_SZ = (size_t)3 * H * S * 24 * 2;
constexpr size_t WS_OP = WS_QKV + WS_QKV_SZ;
constexpr size_t WS_OL = WS_OP + (size_t)64 * 32 * 1024;
constexpr size_t WS_HP = WS_OL + (size_t)H * S * 4;
constexpr size_t WS_ST2 = WS_HP + (size_t)S * D * 2;
constexpr size_t WS_W1P = WS_ST2 + (size_t)S * 48;
constexpr size_t WS_W2P = WS_W1P + (size_t)D * F * 2;
constexpr size_t WS_H1P = WS_W2P + (size_t)D * F * 2;
constexpr size_t WS_PART = WS_H1P + (size_t)S * F * 2;
constexpr size_t WS_END = WS_PART + (size_t)4 * S * D * 4;

#define ST(kid, idx)
#define SARG
#define SHOST

DI unsigned rfl(unsigned x) { return __builtin_amdgcn_readfirstlane(x); }
DI float bflo(unsigned w) { return __uint_as_float(w << 16); }
DI float bfhi(unsigned w) { return __uint_as_float(w & 0xffff0000u); }
DI unsigned pk2(float lo, float hi) { f32x2 v = {lo, hi}; bf16x2 b = __builtin_convertvector(v, bf16x2); return __builtin_bit_cast(unsigned, b); }
DI float dot2(unsigned a, unsigned b, float c) { return __builtin_amdgcn_fdot2_f32_bf16(__builtin_bit_cast(bf16x2, a), __builtin_bit_cast(bf16x2, b), c, false); }

typedef __amdgpu_buffer_rsrc_t srd_t;
DI srd_t mk_srd(const void* p, unsigned bytes) { return __builtin_amdgcn_make_buffer_rsrc((void*)p, (short)0, (int)bytes, 0x00020000); }
DI void dma16(unsigned ldsaddr, unsigned voff, srd_t srd, unsigned soff) {
  asm volatile("s_mov_b32 m0, %0\n\ts_nop 0\n\tbuffer_load_dwordx4 %1, %2, %3 offen lds" :: "s"(ldsaddr), "v"(voff), "s"(srd), "s"(soff) : "memory");
}
DI void dma16h(unsigned ldsaddr, unsigned voff, srd_t srd, unsigned soff) {
  asm volatile("s_mov_b32 m0, %0\n\ts_mov_b32 exec_hi, 0\n\tbuffer_load_dwordx4 %1, %2, %3 offen lds\n\ts_mov_b32 exec_hi, -1" :: "s"(ldsaddr), "v"(voff), "s"(srd), "s"(soff) : "memory");
}
DI void dma16q(unsigned ldsaddr, unsigned voff, srd_t srd, unsigned soff) {
  asm volatile("s_mov_b32 m0, %0\n\ts_mov_b64 exec, 0xffff\n\tbuffer_load_dwordx4 %1, %2, %3 offen lds\n\ts_mov_b64 exec, -1" :: "s"(ldsaddr), "v"(voff), "s"(srd), "s"(soff) : "memory");
}
DI void touch128(unsigned ldsdump, unsigned voff, srd_t srd, unsigned soff) {
  asm volatile("s_mov_b32 m0, %0\n\ts_nop 0\n\tbuffer_load_dword %1, %2, %3 offen lds" :: "s"(ldsdump), "v"(voff), "s"(srd), "s"(soff) : "memory");
}
template <int N> DI void wait_vm() { asm volatile("s_waitcnt vmcnt(%0)" :: "i"(N) : "memory"); }
DI void wait_lgkm0() { asm volatile("s_waitcnt lgkmcnt(0)" ::: "memory"); }
DI void bar() { asm volatile("" ::: "memory"); __builtin_amdgcn_s_barrier(); asm volatile("" ::: "memory"); }
DI u32x4 lds_u128(unsigned addr) { return *(const LAS u32x4*)(size_t)addr; }
DI f32x4 lds_f128(unsigned addr) { return *(const LAS f32x4*)(size_t)addr; }
DI void lds_w128(unsigned addr, f32x4 v) { *(LAS f32x4*)(size_t)addr = v; }
DI bf16x8 asbf(u32x4 v) { return __builtin_bit_cast(bf16x8, v); }
DI u32x2 lds_tr(unsigned addr) { return __builtin_bit_cast(u32x2, __builtin_amdgcn_ds_read_tr16_b64_v4bf16((LAS bf16x4*)(size_t)addr)); }
DI u32x4 cat4(u32x2 a, u32x2 b) { return u32x4{a[0], a[1], b[0], b[1]}; }
DI f32x4 mfma16(u32x4 a, u32x4 b, f32x4 c) { return __builtin_amdgcn_mfma_f32_16x16x32_bf16(asbf(a), asbf(b), c, 0, 0, 0); }
DI f32x16 mfma32(u32x4 a, u32x4 b, f32x16 c) { return __builtin_amdgcn_mfma_f32_32x32x16_bf16(asbf(a), asbf(b), c, 0, 0, 0); }
DI int swap23(int i) { return (i & 19) | ((i & 4) << 1) | ((i & 8) >> 1); }

DI void relayout_w1(unsigned lds0, const u16* w1, u16* w1p, int fc, int part, int w, int lane) {
  const srd_t sw = mk_srd(w1, D * F * 2), so = mk_srd(w1p, D * F * 2);
  for (int pc = w; pc < 81; pc += 8) {
    const int c = pc * 64 + lane, row = c / 36, cc = c % 36;
    dma16(lds0 + (unsigned)pc * 1024u, (unsigned)row * (F * 2) + (unsigned)cc * 16u, sw, (unsigned)part * 144u * (F * 2) + (unsigned)fc * 576u);
  }
  wait_vm<0>(); bar();
  const int h = lane >> 5, blk = (lane >> 4) & 1, q = (lane >> 2) & 3, p = lane & 3;
  const unsigned ra = lds0 + (unsigned)(8 * h + q) * 576u + (unsigned)(16 * blk + 8 * (p & 1) + 4 * (p >> 1)) * 2u;
  for (int fr = w; fr < 81; fr += 8) {
    const int sl = fr / 9, tile = fr % 9, s = part * 9 + sl, kh = s / 18;
    const unsigned a = ra + (unsigned)sl * 16u * 576u + (unsigned)tile * 64u;
    const u32x4 v = cat4(lds_tr(a), lds_tr(a + 4 * 576));
    const int slot = tile == 8 ? 8 : (kh == 0 ? (tile ^ 1) : tile);
    __builtin_amdgcn_raw_buffer_store_b128(v, so, (unsigned)lane * 16u, ((unsigned)(fc * 9 + slot) * 36u + (unsigned)s) * 1024u, 0);
  }
}
DI void relayout_w2(unsigned ldsw, const u16* w2, u16* w2p, int kappa, int ct, int lane) {
  const srd_t sw = mk_srd(w2, D * F * 2), so = mk_srd(w2p, D * F * 2);
#pragma unroll
  for (int pc = 0; pc < 9; pc++) {
    const int c = pc * 64 + lane, row = c / 18, cc = c % 18;
    dma16(ldsw + (unsigned)pc * 1024u, (unsigned)row * (D * 2) + (unsigned)cc * 16u, sw, (unsigned)kappa * 32u * (D * 2) + (unsigned)ct * 288u);
  }
  wait_vm<0>();
  const int g = lane >> 4, q = (lane >> 2) & 3, p = lane & 3;
  const unsigned ra = ldsw + (unsigned)(8 * g + q) * 288u;
  const int ks = kappa / 36, kst = kappa % 36;
#pragma unroll
  for (int nt = 0; nt < 9; nt++) {
    const unsigned col = nt < 8 ? (unsigned)(32 * (nt >> 1) + 8 * p + 4 * (nt & 1)) * 2u : (unsigned)(128 + 4 * p) * 2u;
    const u32x4 v = cat4(lds_tr(ra + col), lds_tr(ra + col + 4 * 288));
    __builtin_amdgcn_raw_buffer_store_b128(v, so, (unsigned)lane * 16u, (((unsigned)(ks * 4 + ct) * 36u + (unsigned)kst) * 9u + (unsigned)nt) * 1024u, 0);
  }
}

constexpr int Q_SLOT = 34816, Q_RING = 4, Q_LDS = Q_SLOT * Q_RING + 8192;
DI void qkv_body(unsigned lds0, const u16* x, const u16* wqkv, u16* qkv, int rb, int ct, int w, int lane SARG) {
  const int rg = w & 3, kh = w >> 2;
  const srd_t sx = mk_srd(x, S * D * 2), sw = mk_srd(wqkv, D * NQKV * 2);
  const unsigned vx = (unsigned)(lane >> 3) * (D * 2) + (unsigned)(((lane & 7) ^ ((4 * (w & 1) + (lane >> 4)) & 7)) * 16);
  unsigned vw[3];
#pragma unroll
  for (int i = 0; i < 3; i++) {
    const int c = i < 2 ? 64 * (w + 8 * i) + lane : 64 * (16 + (w >> 2)) + 16 * (w & 3) + (lane & 15);
    const int rho = c / 18, cc = c % 18, k = rho ^ (((rho >> 3) & 1) << 2);
    vw[i] = (unsigned)k * (NQKV * 2) + (unsigned)cc * 16u;
  }
  const unsigned xg = (unsigned)rb * 128u * (D * 2) + (unsigned)w * 8u * (D * 2), wg = (unsigned)ct * 288u;
#define Q_ISSUE1(t, i, ring) do { \
    if ((i) < 2) dma16(lds0 + (ring) + (unsigned)(w + 8 * (i)) * 1024u, vx, sx, xg + (unsigned)(i) * 64u * (D * 2) + (unsigned)(t) * 128u); \
    else if ((i) < 4) dma16(lds0 + (ring) + 16384u + (unsigned)(w + 8 * ((i) - 2)) * 1024u, vw[(i) & 1], sw, wg + (unsigned)(t) * 64u * (NQKV * 2)); \
    else dma16q(lds0 + (ring) + 16384u + 16384u + (unsigned)w * 256u, vw[2], sw, wg + (unsigned)(t) * 64u * (NQKV * 2)); } while (0)
#pragma unroll
  for (int t = 0; t < 3; t++)
#pragma unroll
    for (int i = 0; i < 5; i++) Q_ISSUE1(t, i, (unsigned)t * Q_SLOT);
  const int tok = lane & 15, g = lane >> 4, q4 = (lane >> 2) & 3, pp = lane & 3;
  const int rot = rg == 3 ? 0 : 3 * rg;
  const unsigned xo = lds0 + (unsigned)(rg * 32 + tok) * 128u + (unsigned)(((4 * kh + g) ^ (tok >> 1)) * 16);
  const unsigned wrow = lds0 + 16384u + (unsigned)(32 * kh + 8 * g + q4) * 288u + (unsigned)pp * 8u;
  const unsigned wt0 = (unsigned)(4 * (g & 1)) * 288u, wt1 = (unsigned)(4 * ((g & 1) ^ 1)) * 288u;
  unsigned wg0[3], wg1[3];
#pragma unroll
  for (int j = 0; j < 3; j++) { const unsigned c = (unsigned)(((3 * j + rot) % 9) * 32 - 96 * j); wg0[j] = wrow + wt0 + c; wg1[j] = wrow + wt1 + c; }
  f32x4 acc[2][9];
#pragma unroll
  for (int a = 0; a < 2; a++)
#pragma unroll
    for (int n = 0; n < 9; n++) acc[a][n] = f32x4{0, 0, 0, 0};
  float sx0 = 0, sx1 = 0, sq0 = 0, sq1 = 0, cs[3] = {0, 0, 0};
  ST(1, 1);
  wait_vm<10>(); bar();
  ST(1, 2);
  constexpr int PF = 3;
  u32x4 wf[PF], xa, xb;
  xa = lds_u128(xo); xb = lds_u128(xo + 2048);
#pragma unroll
  for (int i = 0; i < PF; i++) wf[i] = cat4(lds_tr(wg0[0] + i * 32), lds_tr(wg1[0] + i * 32));
  unsigned r0 = 0, r1 = Q_SLOT, r2 = 2 * Q_SLOT, r3 = 3 * Q_SLOT;
#define Q_PERIOD(t, ISSUE, VMW) do { \
    if (QA_WAIT) wait_vm<VMW>(); bar(); \
    unsigned a1_ = wg0[1] + r0, b1_ = wg1[1] + r0, a2_ = wg0[2] + r0, b2_ = wg1[2] + r0, an_ = wg0[0] + r1, bn_ = wg1[0] + r1, xn = xo + r1; \
    asm volatile("" : "+v"(a1_), "+v"(b1_), "+v"(a2_), "+v"(b2_), "+v"(an_), "+v"(bn_), "+v"(xn)); \
    u32x4 xa2, xb2; \
    _Pragma("unroll") for (int s = 0; s < 9; s++) { \
      const u32x4 cur = wf[s % PF]; \
      if (s < 3) wf[s % PF] = cat4(lds_tr(a1_ + (s + 3) * 32), lds_tr(b1_ + (s + 3) * 32)); \
      else if (s < 6) wf[s % PF] = cat4(lds_tr(a2_ + (s + 3) * 32), lds_tr(b2_ + (s + 3) * 32)); \
      else wf[s % PF] = cat4(lds_tr(an_ + (s - 6) * 32), lds_tr(bn_ + (s - 6) * 32)); \
      if (s == 5) { xa2 = lds_u128(xn); xb2 = lds_u128(xn + 2048); } \
      acc[0][s] = mfma16(cur, xa, acc[0][s]); acc[1][s] = mfma16(cur, xb, acc[1][s]); \
      if (QA_DMA && (ISSUE) && s < 5) Q_ISSUE1((t) + 3, s, r3); \
      if (QA_DOT && s < 3) { cs[s] = dot2(cur[0], 0x3f803f80u, cs[s]); cs[s] = dot2(cur[1], 0x3f803f80u, cs[s]); cs[s] = dot2(cur[2], 0x3f803f80u, cs[s]); cs[s] = dot2(cur[3], 0x3f803f80u, cs[s]); } \
      if (QA_DOT && s >= 3 && s < 7) { sx0 = dot2(xa[s - 3], 0x3f803f80u, sx0); sq0 = dot2(xa[s - 3], xa[s - 3], sq0); sx1 = dot2(xb[s - 3], 0x3f803f80u, sx1); sq1 = dot2(xb[s - 3], xb[s - 3], sq1); } \
      SB(); \
    } \
    xa = xa2; xb = xb2; \
    const unsigned rr_ = r0; r0 = r1; r1 = r2; r2 = r3; r3 = rr_; \
  } while (0)
#pragma unroll 1
  for (int t = 0; t < 6; t++) Q_PERIOD(t, true, 5);
  Q_PERIOD(6, false, 5);
  Q_PERIOD(7, false, 0);
  Q_PERIOD(8, false, 0);
  ST(1, 3);
  wait_lgkm0(); bar();
  const unsigned xch = lds0 + (unsigned)rg * 18432u + (unsigned)lane * 16u;
#pragma unroll
  for (int n = 0; n < 9; n++) lds_w128(xch + (unsigned)(kh ^ 1) * 9216u + n * 1024, kh ? acc[0][n] : acc[1][n]);
  const unsigned stb = lds0 + 73728u;
  sx0 += __shfl_xor(sx0, 16); sx0 += __shfl_xor(sx0, 32); sq0 += __shfl_xor(sq0, 16); sq0 += __shfl_xor(sq0, 32);
  sx1 += __shfl_xor(sx1, 16); sx1 += __shfl_xor(sx1, 32); sq1 += __shfl_xor(sq1, 16); sq1 += __shfl_xor(sq1, 32);
  if (g == 0) {
    *(LAS f32x2*)(size_t)(stb + (unsigned)(rg * 32 + tok) * 16u + kh * 8u) = f32x2{sx0, sq0};
    *(LAS f32x2*)(size_t)(stb + (unsigned)(rg * 32 + 16 + tok) * 16u + kh * 8u) = f32x2{sx1, sq1};
  }
  const unsigned csb = stb + 2048u;
#pragma unroll
  for (int n = 0; n < 3; n++) {
    float v = cs[n]; v += __shfl_xor(v, 16); v += __shfl_xor(v, 32);
    if (g == 0 && rg < 3) *(LAS float*)(size_t)(csb + (unsigned)(16 * (rot + n) + tok) * 8u + kh * 4u) = v;
  }
  wait_lgkm0(); bar();
  ST(1, 5);
  f32x4 o[9];
#pragma unroll
  for (int n = 0; n < 9; n++) o[n] = lds_f128(xch + (unsigned)kh * 9216u + n * 1024) + (kh ? acc[1][n] : acc[0][n]);
  const int tl = rg * 32 + kh * 16 + tok;
  const f32x4 sv = lds_f128(stb + (unsigned)tl * 16u);
  const float mean = (sv[0] + sv[2]) * (1.0f / D), var = fmaxf((sv[1] + sv[3]) * (1.0f / D) - mean * mean, 0.f);
  const int type = ct >> 2;
  const float ra = rsqrtf(var + LN_EPS) * (type == 0 ? 0.23570226039551584f * 1.4426950408889634f : 1.0f);
  wait_lgkm0(); bar();
  {
    const int tid = w * 64 + lane;
    const unsigned padv = type == 0 ? 0u : 0x00003f80u;
#pragma unroll
    for (int i = 0; i < 2; i++) {
      const int e = tid + 512 * i, t2 = e >> 3, hh = e & 7;
      const unsigned a = lds0 + t2 * 400 + hh * 48 + 36;
      *(LAS unsigned*)(size_t)a = padv; *(LAS unsigned*)(size_t)(a + 4) = 0; *(LAS unsigned*)(size_t)(a + 8) = 0;
    }
  }
  wait_lgkm0(); bar();
  ST(1, 6);
#pragma unroll
  for (int n = 0; n < 9; n++) {
    const int nt = n + rot >= 9 ? n + rot - 9 : n + rot;
    const int c0 = 16 * nt + 4 * g;
    const f32x4 ca = lds_f128(csb + c0 * 8u), cb = lds_f128(csb + c0 * 8u + 16);
    const f32x4 csv = {ca[0] + ca[1], ca[2] + ca[3], cb[0] + cb[1], cb[2] + cb[3]};
    const f32x4 v = (o[n] - mean * csv) * ra;
    const int h0 = (c0 * 3641) >> 16, d0 = c0 - 18 * h0, h1 = ((c0 + 2) * 3641) >> 16, d1 = c0 + 2 - 18 * h1;
    *(LAS unsigned*)(size_t)(lds0 + tl * 400 + h0 * 48 + d0 * 2) = pk2(v[0], v[1]);
    *(LAS unsigned*)(size_t)(lds0 + tl * 400 + h1 * 48 + d1 * 2) = pk2(v[2], v[3]);
  }
  wait_lgkm0(); bar();
  ST(1, 7);
  {
    const int tid = w * 64 + lane;
    char* dst = (char*)qkv + ((size_t)(type * 32 + (ct & 3) * 8) * S + rb * 128) * 48;
#pragma unroll
    for (int i = 0; i < 6; i++) {
      const int c = tid + 512 * i, hh = c / 384, rem = c % 384, t2 = rem / 3, part = rem % 3;
      const u32x4 v = lds_u128(lds0 + t2 * 400 + hh * 48 + part * 16);
      *(u32x4*)(dst + (size_t)hh * S * 48 + rem * 16) = v;
    }
  }
}
__global__ KATTR void ka_qkv(const u16* __restrict__ x, const u16* __restrict__ wqkv, const u16* __restrict__ w1, u16* __restrict__ qkv, u16* __restrict__ w1p SARG) {
  ST(1, 0);
  extern __shared__ __attribute__((aligned(16))) char smem[];
  const unsigned lds0 = (unsigned)(size_t)smem;
  const int b = blockIdx.x, xcd = b & 7, i = b >> 3;
  const int w = rfl(threadIdx.x >> 6), lane = threadIdx.x & 63;
  if (i < 24) {
    const int rb = (xcd >> 1) * 4 + (i & 3), ct = (xcd & 1) * 6 + (i >> 2);
    qkv_body(lds0, x, wqkv, qkv, rb, ct, w, lane);
  } else {
    const int j = i - 24;
    relayout_w1(lds0, w1, w1p, (xcd & 3) * 4 + (j >> 1), (xcd >> 2) * 2 + (j & 1), w, lane);
  }
  ST(1, 4);
}

constexpr int B_LDS = 2 * 24576 + 64 + 8 * 9216;
__global__ KATTR void kb_attn(const u16* __restrict__ qkv, const u16* __restrict__ w2, u16* __restrict__ op, u16* __restrict__ ol, u16* __restrict__ w2p SARG) {
  ST(2, 0);
  extern __shared__ __attribute__((aligned(16))) char smem[];
  const unsigned lds0 = (unsigned)(size_t)smem;
  const int b = blockIdx.x, xcd = b & 7, i = b >> 3;
  const int head = xcd * 4 + (i & 3), qb = i >> 2;
  const int w = rfl(threadIdx.x >> 6), lane = threadIdx.x & 63;
  const unsigned ldsK = lds0, ldsV = lds0 + 24576;
  const srd_t sq = mk_srd(qkv, (unsigned)WS_QKV_SZ);
  const unsigned kbase = (unsigned)((32 + head) * S + 256 * (qb - 1)) * 48u, vbase = (unsigned)((64 + head) * S + 256 * (qb - 1)) * 48u;
  const unsigned vl = lane * 16;
#pragma unroll
  for (int hf = 0; hf < 2; hf++) {
    if (hf == 1 || qb > 0) {
#pragma unroll
      for (int j = 0; j < 3; j++) {
        const int pi = w * 3 + j;
        const unsigned pc = (unsigned)hf * 12288u + (unsigned)(pi % 12) * 1024;
        if (pi < 12) dma16(ldsK + pc, vl, sq, kbase + pc); else dma16(ldsV + pc, vl, sq, vbase + pc);
      }
    }
  }
  const int qq = lane & 31, h = lane >> 5;
  const int q0 = 256 * qb + 32 * w;
  const char* qrow = (const char*)qkv + ((size_t)head * S + q0 + qq) * 48;
  const u32x4 qf0 = *(const u32x4*)(qrow + 16 * h);
  u32x4 qf1 = *(const u32x4*)(qrow + 32);
  if (h) qf1 = u32x4{0, 0, 0, 0};
  f32x16 cold, cdiag;
#pragma unroll
  for (int r = 0; r < 16; r++) {
    const int kk = 16 * (r >> 3) + 8 * h + (r & 7);
    cold[r] = kk > qq ? 0.f : -1e30f;
    cdiag[r] = kk <= qq ? 0.f : -1e30f;
  }
  const int key = swap23(lane & 31);
  const unsigned ka0 = ldsK + (unsigned)(key + 32 * w) * 48 + 16 * h;
  const unsigned ka1 = ldsK + (unsigned)(key + 32 * w) * 48 + 32;
  const int blk = (lane >> 4) & 1, q4 = (lane >> 2) & 3, pp = lane & 3;
  const unsigned va = ldsV + (unsigned)(32 * w + 8 * h + q4) * 48 + 32 * blk + 8 * pp;
  wait_vm<0>(); bar();
  ST(2, 2);
  const int j0 = qb == 0 ? 8 - w : 0;
  const f32x16 zero = {};
  float m = -1e30f;
  for (int j = j0; j < 9; j++) {
    const u32x4 k0 = lds_u128(ka0 + j * 1536), k1 = lds_u128(ka1 + j * 1536);
    f32x16 s;
    if (j == 0) s = mfma32(k0, qf0, cold); else if (j == 8) s = mfma32(k0, qf0, cdiag); else s = mfma32(k0, qf0, zero);
    s = mfma32(k1, qf1, s);
#pragma unroll
    for (int r = 0; r < 16; r += 2) m = fmaxf(m, fmaxf(s[r], s[r + 1]));
  }
  m = fmaxf(m, __shfl_xor(m, 32));
  if (h == 0) qf1[1] = pk2(-m, 0.f);
  ST(2, 3);
  f32x16 oacc = {};
  for (int j = j0; j < 9; j++) {
    const u32x4 k0 = lds_u128(ka0 + j * 1536), k1 = lds_u128(ka1 + j * 1536);
    f32x16 s;
    if (j == 0) s = mfma32(k0, qf0, cold); else if (j == 8) s = mfma32(k0, qf0, cdiag); else s = mfma32(k0, qf0, zero);
    s = mfma32(k1, qf1, s);
    u32x4 p0, p1;
#pragma unroll
    for (int e = 0; e < 4; e++) {
      p0[e] = pk2(__builtin_amdgcn_exp2f(s[2 * e]), __builtin_amdgcn_exp2f(s[2 * e + 1]));
      p1[e] = pk2(__builtin_amdgcn_exp2f(s[8 + 2 * e]), __builtin_amdgcn_exp2f(s[8 + 2 * e + 1]));
    }
    const unsigned vb = va + j * 1536;
    const u32x4 v0 = cat4(lds_tr(vb), lds_tr(vb + 4 * 48)), v1 = cat4(lds_tr(vb + 16 * 48), lds_tr(vb + 20 * 48));
    oacc = mfma32(v0, p0, oacc);
    oacc = mfma32(v1, p1, oacc);
  }
  const float l = __shfl(oacc[10], qq);
  const float inv = 1.0f / l;
  const u32x4 om = {pk2(oacc[0] * inv, oacc[1] * inv), pk2(oacc[2] * inv, oacc[3] * inv), pk2(oacc[4] * inv, oacc[5] * inv), pk2(oacc[6] * inv, oacc[7] * inv)};
  *(u32x4*)((char*)op + ((size_t)(q0 >> 5) * 32 + head) * 1024 + lane * 16) = om;
  if (h == 0) *(unsigned*)((char*)ol + ((size_t)head * S + q0 + qq) * 4) = pk2(oacc[8] * inv, oacc[9] * inv);
  ST(2, 4);
  if (i < 24 && w < 3) {
    const int u = i * 3 + w, ks = xcd & 3, a = xcd >> 2;
    relayout_w2(lds0 + 49216u + (unsigned)w * 9216u, w2, w2p, ks * 36 + a * 18 + (u >> 2), u & 3, lane);
  }
}

constexpr int C_W = 110592, C_LDS = C_W + 2048;
__global__ KATTR void kc_wo(const u16* __restrict__ op, const u16* __restrict__ ol, const u16* __restrict__ wo, const u16* __restrict__ x,
                            u16* __restrict__ hp, f32x2* __restrict__ st2 SARG) {
  ST(3, 0);
  extern __shared__ __attribute__((aligned(16))) char smem[];
  const unsigned lds0 = (unsigned)(size_t)smem;
  const int b = blockIdx.x, xcd = b & 7, bi = b >> 3;
  const int rb = xcd * 4 + (bi & 3), ct = bi >> 2;
  const int w = rfl(threadIdx.x >> 6), lane = threadIdx.x & 63, rg = w & 1, kq = w >> 1;
  const srd_t sw = mk_srd(wo, D * D * 2);
  {
    unsigned vw[3];
#pragma unroll
    for (int i = 0; i < 3; i++) { const int c = 64 * i + lane; vw[i] = (unsigned)(c / 12) * (D * 2) + (unsigned)(c % 12) * 16u; }
#pragma unroll
    for (int gi = 0; gi < 5; gi++) {
      const int grp = w + 8 * gi;
      if (grp < 36) {
#pragma unroll
        for (int i = 0; i < 3; i++) dma16(lds0 + (unsigned)(grp * 3 + i) * 1024u, vw[i], sw, (unsigned)grp * 16u * (D * 2) + (unsigned)ct * 192u);
      }
    }
  }
  const int qt = rb * 2 + rg, tok = lane & 31, h = lane >> 5;
  u32x4 of[9];
  {
    const u32x4* src = (const u32x4*)((const char*)op + ((size_t)qt * 32 + kq * 9) * 1024) + lane;
#pragma unroll
    for (int s = 0; s < 5; s++) of[s] = src[s * 64];
    if (kq < 3) {
#pragma unroll
      for (int s = 5; s < 9; s++) of[s] = src[s * 64];
    } else {
#pragma unroll
      for (int G = 0; G < 4; G++)
#pragma unroll
        for (int i = 0; i < 4; i++) of[5 + G][i] = *(const unsigned*)((const char*)ol + ((size_t)(8 * G + 4 * h + i) * S + qt * 32 + tok) * 4);
    }
  }
  const int row = qt * 32 + tok;
  const u32x4 xr0 = *(const u32x4*)(x + (size_t)row * D + ct * 96 + 16 * kq + 8 * h);
  const u32x4 xr1 = *(const u32x4*)(x + (size_t)row * D + ct * 96 + 16 * (kq < 2 ? kq + 4 : kq) + 8 * h);
  ST(3, 1);
  wait_vm<0>(); bar();
  ST(3, 2);
  const int blk = (lane >> 4) & 1, q4 = (lane >> 2) & 3, pp = lane & 3;
  const unsigned colb = (unsigned)(16 * blk + 8 * (pp & 1) + 4 * (pp >> 1)) * 2u;
  f32x16 acc[3] = {};
#pragma unroll
  for (int s = 0; s < 9; s++) {
    unsigned r0, r1;
    if (s < 5 || kq < 3) { const int hd = kq * 9 + s; r0 = (unsigned)(18 * hd + 4 * h + q4); r1 = r0 + 8; }
    else { const int G = s - 5; r0 = (unsigned)(18 * (8 * G + 4 * h + (q4 >> 1)) + 16 + (q4 & 1)); r1 = r0 + 36; }
    const unsigned a0 = lds0 + r0 * 192u + colb, a1 = lds0 + r1 * 192u + colb;
#pragma unroll
    for (int n = 0; n < 3; n++) acc[n] = mfma32(cat4(lds_tr(a0 + n * 64), lds_tr(a1 + n * 64)), of[s], acc[n]);
  }
  ST(3, 3);
  wait_lgkm0(); bar();
  const unsigned xb = lds0 + (unsigned)rg * 49152u + (unsigned)lane * 16u;
#pragma unroll
  for (int f = 0; f < 6; f++) {
    const int r8 = 8 * (f & 1);
    lds_w128(xb + (unsigned)(f * 4 + kq) * 2048u, f32x4{acc[f >> 1][r8], acc[f >> 1][r8 + 1], acc[f >> 1][r8 + 2], acc[f >> 1][r8 + 3]});
    lds_w128(xb + (unsigned)(f * 4 + kq) * 2048u + 1024u, f32x4{acc[f >> 1][r8 + 4], acc[f >> 1][r8 + 5], acc[f >> 1][r8 + 6], acc[f >> 1][r8 + 7]});
  }
  wait_lgkm0(); bar();
  float ssum = 0, ssq = 0;
  const int nf = kq < 2 ? 2 : 1;
  for (int i = 0; i < nf; i++) {
    const int f = kq + 4 * i;
    f32x4 a = {0, 0, 0, 0}, c = {0, 0, 0, 0};
#pragma unroll
    for (int k = 0; k < 4; k++) { a += lds_f128(xb + (unsigned)(f * 4 + k) * 2048u); c += lds_f128(xb + (unsigned)(f * 4 + k) * 2048u + 1024u); }
    const u32x4 xr = i ? xr1 : xr0;
    u32x4 o;
    o[0] = pk2(bflo(xr[0]) + a[0], bfhi(xr[0]) + a[1]); o[1] = pk2(bflo(xr[1]) + a[2], bfhi(xr[1]) + a[3]);
    o[2] = pk2(bflo(xr[2]) + c[0], bfhi(xr[2]) + c[1]); o[3] = pk2(bflo(xr[3]) + c[2], bfhi(xr[3]) + c[3]);
    *(u32x4*)((char*)hp + ((size_t)qt * 36 + ct * 6 + f) * 1024 + lane * 16) = o;
#pragma unroll
    for (int e = 0; e < 4; e++) { ssum = dot2(o[e], 0x3f803f80u, ssum); ssq = dot2(o[e], o[e], ssq); }
  }
  ssum += __shfl_xor(ssum, 32); ssq += __shfl_xor(ssq, 32);
  const unsigned stb = lds0 + C_W;
  if (h == 0) *(LAS f32x2*)(size_t)(stb + (unsigned)(rg * 32 + tok) * 32u + kq * 8u) = f32x2{ssum, ssq};
  wait_lgkm0(); bar();
  if (threadIdx.x < 64) {
    const f32x4 a = lds_f128(stb + threadIdx.x * 32u), c = lds_f128(stb + threadIdx.x * 32u + 16);
    st2[(size_t)(rb * 64 + threadIdx.x) * 6 + ct] = f32x2{(a[0] + a[2]) + (c[0] + c[2]), (a[1] + a[3]) + (c[1] + c[3])};
  }
  ST(3, 4);
}

constexpr float GC0 = -2.0f * 0.7978845608028654f * 1.4426950408889634f, GC1 = GC0 * 0.044715f;
DI f32x2 gelu2(f32x2 x) {
  const f32x2 t = x * (GC1 * (x * x) + GC0);
  f32x2 e; e[0] = __builtin_amdgcn_exp2f(t[0]); e[1] = __builtin_amdgcn_exp2f(t[1]);
  const f32x2 d = e + 1.0f;
  f32x2 r; r[0] = __builtin_amdgcn_rcpf(d[0]); r[1] = __builtin_amdgcn_rcpf(d[1]);
  return x * r;
}
struct G2 { float x0, x1, t0, t1; };
DI void g2_s1(G2& g, float a0, float a1, float p0, float p1) {
  asm volatile("v_add_f32_e32 %0, %4, %6\n\tv_add_f32_e32 %1, %5, %7\n\tv_mul_f32_e32 %2, %0, %0\n\tv_mul_f32_e32 %3, %1, %1"
               : "=&v"(g.x0), "=&v"(g.x1), "=&v"(g.t0), "=&v"(g.t1) : "v"(a0), "v"(a1), "v"(p0), "v"(p1));
}
DI void g2_s2(G2& g, float vc1) {
  asm volatile("v_fmaak_f32 %0, %0, %4, 0xc0135761\n\tv_fmaak_f32 %1, %1, %4, 0xc0135761\n\tv_mul_f32_e32 %0, %2, %0\n\tv_mul_f32_e32 %1, %3, %1"
               : "+v"(g.t0), "+v"(g.t1) : "v"(g.x0), "v"(g.x1), "v"(vc1));
}
DI void g2_s3(G2& g) {
  asm volatile("v_exp_f32_e32 %0, %0\n\tv_exp_f32_e32 %1, %1\n\tv_add_f32_e32 %0, 1.0, %0\n\tv_add_f32_e32 %1, 1.0, %1" : "+v"(g.t0), "+v"(g.t1));
}
DI unsigned g2_s4(G2& g) {
  unsigned o;
  asm volatile("v_rcp_f32_e32 %1, %1\n\tv_rcp_f32_e32 %2, %2\n\tv_mul_f32_e32 %1, %3, %1\n\tv_mul_f32_e32 %2, %4, %2\n\tv_cvt_pk_bf16_f32 %0, %1, %2"
               : "=v"(o), "+v"(g.t0), "+v"(g.t1) : "v"(g.x0), "v"(g.x1));
  return o;
}
constexpr int F1_SLOT = 36864, F1_XCH = 3 * F1_SLOT, F1_UV = F1_XCH + 32768, F1_DUMP = F1_UV + 1152, F1_LDS = F1_DUMP + 256;
__global__ KATTR void kd_ffn1(const u16* __restrict__ hp, const u16* __restrict__ w1p, const u16* __restrict__ b1,
                              const f32x2* __restrict__ st2, u16* __restrict__ h1p SARG) {
  ST(4, 0);
  extern __shared__ __attribute__((aligned(16))) char smem[];
  const unsigned lds0 = (unsigned)(size_t)smem;
  const int b = blockIdx.x, xcd = b & 7, bi = b >> 3;
  const int rb = (xcd >> 2) * 8 + (bi & 7), fc = (xcd & 3) * 4 + (bi >> 3);
  const int w = rfl(threadIdx.x >> 6), lane = threadIdx.x & 63, rg = w & 3, kh = w >> 2;
  const srd_t sw1 = mk_srd(w1p, D * F * 2), sh1 = mk_srd(h1p, S * F * 2), shp = mk_srd(hp, S * D * 2);
  const unsigned vl = lane * 16;
  const unsigned wbase = (unsigned)fc * 9 * F1_SLOT + (unsigned)w * 1024u;
  const unsigned whalf = (unsigned)fc * 9 * F1_SLOT + 32768u + (unsigned)w * 512u;
  const int tok = lane & 31, h = lane >> 5;
  u32x4 stv[3]; unsigned bv16;
  {
    const srd_t sst = mk_srd(st2, S * 48), sb1 = mk_srd(b1, F * 2);
    const unsigned so = (unsigned)(rb * 128 + rg * 32) * 48u, vo = (unsigned)tok * 48u;
#pragma unroll
    for (int e = 0; e < 3; e++)
      asm volatile("buffer_load_dwordx4 %0, %1, %2, %3 offen offset:%4" : "=&v"(stv[e]) : "v"(vo), "s"(sst), "s"(so), "i"(e * 16) : "memory");
    const unsigned bo = threadIdx.x < 288 ? threadIdx.x * 2u : 0u;
    asm volatile("buffer_load_ushort %0, %1, %2, %3 offen" : "=&v"(bv16) : "v"(bo), "s"(sb1), "s"((unsigned)fc * 576u) : "memory");
  }
  u32x4 xf[18];
  {
    const unsigned so = ((unsigned)(rb * 4 + rg) * 36u + (unsigned)kh * 18u) * 1024u;
#pragma unroll
    for (int s = 0; s < 18; s++)
      asm volatile("buffer_load_dwordx4 %0, %1, %2, %3 offen offset:%4" : "=&v"(xf[s]) : "v"(vl), "s"(shp), "s"(so + (s >> 2) * 4096u), "i"((s & 3) * 1024) : "memory");
  }
#define F1_ISSUE1(t, i, ring) do { if ((i) < 4) dma16(lds0 + (ring) + (unsigned)w * 1024u + (i) * 8192u, vl, sw1, (t) + wbase + (i) * 8192u); \
                                   else dma16h(lds0 + (ring) + 32768u + (unsigned)w * 512u, vl, sw1, (t) + whalf); } while (0)
#pragma unroll
  for (int i = 0; i < 5; i++) F1_ISSUE1(0u, i, 0u);
#pragma unroll
  for (int i = 0; i < 5; i++) F1_ISSUE1((unsigned)F1_SLOT, i, (unsigned)F1_SLOT);
  {
    const unsigned ln = (unsigned)w * 32u + (unsigned)(lane & 31);
    touch128(lds0 + F1_DUMP, (lane < 32 && ln < 252u) ? ln * 128u : 0x7ff00000u, sw1, (unsigned)fc * 9 * F1_SLOT + 2u * F1_SLOT + (unsigned)(bi & 7) * 32256u);
  }
  float ra, rc;
  {
    asm volatile("s_waitcnt vmcnt(29)" : "+v"(stv[0]), "+v"(stv[1]), "+v"(stv[2]), "+v"(bv16) :: "memory");
    float s = 0, q = 0;
#pragma unroll
    for (int e = 0; e < 3; e++) { s += __uint_as_float(stv[e][0]) + __uint_as_float(stv[e][2]); q += __uint_as_float(stv[e][1]) + __uint_as_float(stv[e][3]); }
    const float mean = s * (1.0f / D), var = fmaxf(q * (1.0f / D) - mean * mean, 0.f), rstd = rsqrtf(var + LN_EPS);
    ra = rstd; rc = -mean * rstd;
    if (threadIdx.x < 288) *(LAS float*)(size_t)(lds0 + F1_UV + threadIdx.x * 4) = __uint_as_float(bv16 << 16);
  }
  ST(4, 1);
#pragma unroll
  for (int s = 0; s < 18; s++) {
    asm volatile("s_waitcnt vmcnt(%1)" : "+v"(xf[s]) : "i"(28 - s) : "memory");
#pragma unroll
    for (int e = 0; e < 4; e++) {
      const f32x2 v = f32x2{bflo(xf[s][e]), bfhi(xf[s][e])} * ra + rc;
      xf[s][e] = pk2(v[0], v[1]);
    }
    asm volatile("" : "+v"(xf[s]));
    SB();
  }
  const unsigned rd = lds0 + (unsigned)kh * 18432u + vl;
  const unsigned hvo = (unsigned)(tok >> 4) * 1024u + (unsigned)h * 256u + (unsigned)(tok & 15) * 16u;
  const unsigned hbase = ((unsigned)(rb * 4 + rg) * 144u + (unsigned)fc * 9u) * 2048u;
  const unsigned bia = lds0 + F1_UV + (unsigned)h * 32u;
  wait_vm<6>();
  wait_lgkm0(); bar();
  ST(4, 2);
  constexpr int PF = 3;
  u32x4 wf[PF];
#pragma unroll
  for (int i = 0; i < PF; i++) wf[i] = lds_u128(rd + i * 1024);
  const f32x16 zero = {};
  f32x16 prevO = zero, binit;
#define F1_BIAS(ba_) do { const f32x4 b0_ = lds_f128(ba_), b1_ = lds_f128(ba_ + 16), b2_ = lds_f128(ba_ + 64), b3_ = lds_f128(ba_ + 80); \
    binit = f32x16{b0_[0], b0_[1], b0_[2], b0_[3], b1_[0], b1_[1], b1_[2], b1_[3], b2_[0], b2_[1], b2_[2], b2_[3], b3_[0], b3_[1], b3_[2], b3_[3]}; } while (0)
  F1_BIAS(bia + (unsigned)(1 - kh) * 128u);
  float vc1 = GC1; asm volatile("" : "+v"(vc1));
  unsigned r0 = 0, r1 = F1_SLOT, r2 = 2 * F1_SLOT;
  const unsigned xwr = lds0 + F1_XCH + (unsigned)(kh ^ 1) * 16384u + (unsigned)rg * 4096u + vl;
  const unsigned xrd = lds0 + F1_XCH + (unsigned)kh * 16384u + (unsigned)rg * 4096u + vl;
  f32x4 pp[4] = {};
#define F1_STAGE(q, G, SRC, R0, O) do { const int p_ = (q) >> 2, st_ = (q) & 3, r_ = (R0) + 2 * p_; \
      if (st_ == 0) g2_s1(G, SRC[r_], SRC[r_ + 1], pp[r_ >> 2][r_ & 3], pp[r_ >> 2][(r_ & 3) + 1]); \
      if (st_ == 1) g2_s2(G, vc1); if (st_ == 2) g2_s3(G); if (st_ == 3) O[p_] = g2_s4(G); } while (0)
#define F1_PERIOD(t, ACC, GELU, HALF, VMW, ISSUE) do { \
    if ((VMW) == 2) { if ((t) == 2) wait_vm<0>(); else wait_vm<1>(); } else wait_vm<(VMW) & 1>(); \
    bar(); \
    const unsigned dg_ = (ISSUE) ? (unsigned)((t) + 2) * F1_SLOT : 0u; \
    unsigned sb = rd + r0, sn = rd + r1; \
    asm volatile("" : "+v"(sb), "+v"(sn)); \
    u32x4 o; G2 g; \
    const int gt_ = ((t) - 2 - (HALF)) + kh;      \
    if ((GELU) && (HALF) == 0) { _Pragma("unroll") for (int e = 0; e < 4; e++) pp[e] = lds_f128(xrd + e * 1024); } \
    SB(); \
    _Pragma("unroll") for (int s = 0; s < 18; s++) { \
      const u32x4 cur = wf[s % PF]; \
      wf[s % PF] = lds_u128(s + PF < 18 ? sb + (s + PF) * 1024 : sn + (s + PF - 18) * 1024); \
      ACC = mfma32(cur, xf[s], s == 0 ? ((HALF) == 0 ? binit : zero) : ACC); \
      if ((ISSUE) && s % 2 == 0 && s / 2 < 5) F1_ISSUE1(dg_, s / 2, r2); \
      if ((HALF) == 1 && s % 4 == 1 && s / 4 < 4) { const int e = s / 4; lds_w128(xwr + e * 1024, f32x4{accG[4 * e], accG[4 * e + 1], accG[4 * e + 2], accG[4 * e + 3]}); } \
      if (GELU) { \
        if (s >= 2 && s <= 17) F1_STAGE(s - 2, g, prevO, 8 * (HALF), o); \
        if (s == 17) __builtin_amdgcn_raw_buffer_store_b128(o, sh1, hvo + (HALF) * 512u, hbase + (unsigned)gt_ * 2048u, 0); \
      } \
      if ((HALF) == 1 && s == 12) {     \
        const int nt_ = (t) + 1 == 8 ? 8 : (t) + 1 + (1 - kh); \
        unsigned ba_ = bia + (unsigned)nt_ * 128u; asm volatile("" : "+v"(ba_)); \
        F1_BIAS(ba_); \
      } \
      SB(); \
    } \
    const unsigned rr_ = r0; r0 = r1; r1 = r2; r2 = rr_; \
  } while (0)
  {
    f32x16 accG, accO;
    F1_PERIOD(0, accG, false, 0, 0, true);
    F1_PERIOD(1, accO, false, 1, 0, true);
    prevO = accO;
#pragma unroll 1
    for (int t = 2; t < 6; t += 2) {
      F1_PERIOD(t, accG, true, 0, 2, true);
      F1_PERIOD(t + 1, accO, true, 1, 1, true);
      prevO = accO;
    }
    F1_PERIOD(6, accG, true, 0, 1, true);
    F1_PERIOD(7, accO, true, 1, 1, false);
    prevO = accO;
    {
      wait_vm<1>(); bar();
      unsigned sb = rd + r0;
      asm volatile("" : "+v"(sb));
      u32x4 o, o2; G2 g, g2;
      const int gt_ = 6 + kh;
#pragma unroll
      for (int e = 0; e < 4; e++) pp[e] = lds_f128(xrd + e * 1024);
      if (kh == 0) binit = zero;
      SB();
#pragma unroll
      for (int s = 0; s < 18; s++) {
        const u32x4 cur = wf[s % PF];
        if (s + PF < 18) wf[s % PF] = lds_u128(sb + (s + PF) * 1024);
        accG = mfma32(cur, xf[s], s == 0 ? binit : accG);
        if (s >= 2 && s <= 17) { F1_STAGE(s - 2, g, prevO, 0, o); F1_STAGE(s - 2, g2, prevO, 8, o2); }
        if (s == 17) { __builtin_amdgcn_raw_buffer_store_b128(o, sh1, hvo, hbase + (unsigned)gt_ * 2048u, 0);
                       __builtin_amdgcn_raw_buffer_store_b128(o2, sh1, hvo + 512u, hbase + (unsigned)gt_ * 2048u, 0); }
        SB();
      }
      ST(4, 3);
      bar();
      lds_w128(xwr, kh ? f32x4{accG[0], accG[1], accG[2], accG[3]} : f32x4{accG[8], accG[9], accG[10], accG[11]});
      lds_w128(xwr + 1024, kh ? f32x4{accG[4], accG[5], accG[6], accG[7]} : f32x4{accG[12], accG[13], accG[14], accG[15]});
      wait_lgkm0(); bar();
      const f32x4 q0 = lds_f128(xrd), q1 = lds_f128(xrd + 1024);
      const f32x4 k0 = (kh ? f32x4{accG[8], accG[9], accG[10], accG[11]} : f32x4{accG[0], accG[1], accG[2], accG[3]}) + q0;
      const f32x4 k1 = (kh ? f32x4{accG[12], accG[13], accG[14], accG[15]} : f32x4{accG[4], accG[5], accG[6], accG[7]}) + q1;
      const f32x2 z0 = gelu2(f32x2{k0[0], k0[1]}), z1 = gelu2(f32x2{k0[2], k0[3]}), z2 = gelu2(f32x2{k1[0], k1[1]}), z3 = gelu2(f32x2{k1[2], k1[3]});
      o = u32x4{pk2(z0[0], z0[1]), pk2(z1[0], z1[1]), pk2(z2[0], z2[1]), pk2(z3[0], z3[1])};
      __builtin_amdgcn_raw_buffer_store_b128(o, sh1, hvo + (unsigned)kh * 512u, hbase + 8u * 2048u, 0);
    }
  }
  ST(4, 4);
}

constexpr int F2_SLOT = 36864, F2_DUMP = 3 * F2_SLOT, F2_LDS = 3 * F2_SLOT + 256;
__global__ KATTR void ke_ffn2(const u16* __restrict__ h1p, const u16* __restrict__ w2p, float* __restrict__ part SARG) {
  ST(5, 0);
  extern __shared__ __attribute__((aligned(16))) char smem[];
  const unsigned lds0 = (unsigned)(size_t)smem;
  const int b = blockIdx.x, xcd = b & 7, bi = b >> 3;
  const int rb = (xcd >> 2) * 8 + (bi & 7), ct = bi >> 3, ks = xcd & 3;
  const int w = rfl(threadIdx.x >> 6), lane = threadIdx.x & 63, rg = w & 3, kh = w >> 2;
  const srd_t sw = mk_srd(w2p, D * F * 2), sh = mk_srd(h1p, S * F * 2);
  const unsigned wbase = (unsigned)(ks * 4 + ct) * 36u * 9216u, vl = lane * 16;
#define F2_ISSUE1(t, i, ring) do { if ((i) < 4) dma16(lds0 + (ring) + (unsigned)w * 1024u + (i) * 8192u, vl, sw, wbase + (unsigned)(t) * F2_SLOT + (unsigned)w * 1024u + (i) * 8192u); \
                                   else dma16h(lds0 + (ring) + 32768u + (unsigned)w * 512u, vl, sw, wbase + (unsigned)(t) * F2_SLOT + 32768u + (unsigned)w * 512u); } while (0)
  const unsigned hoff0 = ((unsigned)(rb * 4 + rg) * 144u + (unsigned)ks * 36u + 2u * kh) * 2048u;
  u32x4 hA[4], hB[4], hC[4];
#define HLOAD1(dst, t, i) do { const unsigned so_ = hoff0 + (unsigned)(t) * 8192u; \
    asm volatile("buffer_load_dwordx4 %0, %1, %2, %3 offen offset:" #i "*1024" : "=&v"(dst[i]) : "v"(vl), "s"(sh), "s"(so_) : "memory"); } while (0)
#define HWAIT(N, dst) asm volatile("s_waitcnt vmcnt(" #N ")" : "+v"(dst[0]), "+v"(dst[1]), "+v"(dst[2]), "+v"(dst[3]) :: "memory")
#pragma unroll
  for (int i = 0; i < 5; i++) F2_ISSUE1(0, i, 0u);
  HLOAD1(hA, 0, 0); HLOAD1(hA, 0, 1); HLOAD1(hA, 0, 2); HLOAD1(hA, 0, 3);
#pragma unroll
  for (int i = 0; i < 5; i++) F2_ISSUE1(1, i, (unsigned)F2_SLOT);
  HLOAD1(hB, 1, 0); HLOAD1(hB, 1, 1); HLOAD1(hB, 1, 2); HLOAD1(hB, 1, 3);
  {
    const unsigned ln = (unsigned)w * 32u + (unsigned)(lane & 31);
    touch128(lds0 + F2_DUMP, (lane < 32 && ln < 252u) ? ln * 128u : 0x7ff00000u, sw, wbase + 2u * F2_SLOT + (unsigned)(bi & 7) * 32256u);
  }
  f32x4 acc[2][9];
#pragma unroll
  for (int a = 0; a < 2; a++)
#pragma unroll
    for (int n = 0; n < 9; n++) acc[a][n] = f32x4{0, 0, 0, 0};
  const unsigned rd = lds0 + (unsigned)kh * 18432u + vl;
  ST(5, 1);
  wait_vm<14>();
  bar();
  ST(5, 2);
  constexpr int PF = 3;
  u32x4 wf[PF];
#pragma unroll
  for (int i = 0; i < PF; i++) wf[i] = lds_u128(rd + i * 1024);
  unsigned r0 = 0, r1 = F2_SLOT, r2 = 2 * F2_SLOT;
#define F2_PERIOD(t, HC, HN, VMW, ISSUE) do { \
    HWAIT(VMW, HC); \
    bar(); \
    unsigned sb = rd + r0, sn = rd + r1; \
    asm volatile("" : "+v"(sb), "+v"(sn)); \
    _Pragma("unroll") for (int s = 0; s < 18; s++) { \
      const u32x4 cur = wf[s % PF]; \
      wf[s % PF] = lds_u128(s + PF < 18 ? sb + (s + PF) * 1024 : sn + (s + PF - 18) * 1024); \
      const int k = s / 9, nt = s % 9; \
      acc[0][nt] = mfma16(cur, HC[2 * k], acc[0][nt]); \
      acc[1][nt] = mfma16(cur, HC[2 * k + 1], acc[1][nt]); \
      if (ISSUE) { \
        if (s % 2 == 0 && s / 2 < 5) F2_ISSUE1((t) + 2, s / 2, r2); \
        if (s == 10) HLOAD1(HN, (t) + 2, 0); if (s == 12) HLOAD1(HN, (t) + 2, 1); if (s == 14) HLOAD1(HN, (t) + 2, 2); if (s == 16) HLOAD1(HN, (t) + 2, 3); \
      } \
      SB(); \
    } \
    const unsigned rr_ = r0; r0 = r1; r1 = r2; r2 = rr_; \
  } while (0)
  F2_PERIOD(0, hA, hC, 5, true);
#pragma unroll 1
  for (int t = 1; t < 7; t += 3) {
    F2_PERIOD(t, hB, hA, 4, true);
    F2_PERIOD(t + 1, hC, hB, 4, true);
    F2_PERIOD(t + 2, hA, hC, 4, true);
  }
  F2_PERIOD(7, hB, hA, 4, false);
  F2_PERIOD(8, hC, hB, 0, false);
  ST(5, 3);
  wait_lgkm0(); bar();
  const unsigned xb = lds0 + (unsigned)rg * 18432u + vl;
#pragma unroll
  for (int n = 0; n < 9; n++) lds_w128(xb + (unsigned)(kh ^ 1) * 9216u + n * 1024, kh ? acc[0][n] : acc[1][n]);
  wait_lgkm0(); bar();
  const int tok = lane & 15, g = lane >> 4;
  const int row = rb * 128 + rg * 32 + kh * 16 + tok;
  float* prow = part + ((size_t)ks * S + row) * D + ct * 144;
#pragma unroll
  for (int n = 0; n < 9; n++) {
    const f32x4 o = lds_f128(xb + kh * 9216u + n * 1024) + (kh ? acc[1][n] : acc[0][n]);
    const int col = n < 8 ? 32 * (n >> 1) + 8 * g + 4 * (n & 1) : 128 + 4 * g;
    *(f32x4*)(prow + col) = o;
  }
  ST(5, 4);
}

__global__ __launch_bounds__(256) void kf_reduce(const float* __restrict__ part, const u16* __restrict__ hp, const u16* __restrict__ b2, u16* __restrict__ out) {
  const int idx = blockIdx.x * 256 + threadIdx.x;
  const int row = idx / 72, c8 = idx % 72, n = c8 * 8, e = row * D + n;
  f32x4 s0 = *(const f32x4*)(part + e), s1 = *(const f32x4*)(part + e + 4);
#pragma unroll
  for (int k = 1; k < 4; k++) { s0 += *(const f32x4*)(part + (size_t)k * S * D + e); s1 += *(const f32x4*)(part + (size_t)k * S * D + e + 4); }
  const u32x4 hv = *(const u32x4*)((const char*)hp + ((size_t)(row >> 5) * 36 + (c8 >> 1)) * 1024 + ((c8 & 1) * 32 + (row & 31)) * 16);
  const u32x4 bv = *(const u32x4*)(b2 + n);
  u32x4 r;
  r[0] = pk2(bflo(hv[0]) + (s0[0] + bflo(bv[0])), bfhi(hv[0]) + (s0[1] + bfhi(bv[0])));
  r[1] = pk2(bflo(hv[1]) + (s0[2] + bflo(bv[1])), bfhi(hv[1]) + (s0[3] + bfhi(bv[1])));
  r[2] = pk2(bflo(hv[2]) + (s1[0] + bflo(bv[2])), bfhi(hv[2]) + (s1[1] + bfhi(bv[2])));
  r[3] = pk2(bflo(hv[3]) + (s1[2] + bflo(bv[3])), bfhi(hv[3]) + (s1[3] + bfhi(bv[3])));
  *(u32x4*)(out + e) = r;
}

extern "C" void kernel_launch(void* const* d_in, const int* in_sizes, int n_in, void* d_out, int out_size, void* d_ws, size_t ws_size,
                              hipStream_t stream) {
  const u16* x = (const u16*)d_in[0];
  const u16* wqkv = (const u16*)d_in[1];
  const u16* wo = (const u16*)d_in[2];
  const u16* w1 = (const u16*)d_in[4];
  const u16* b1 = (const u16*)d_in[5];
  const u16* w2 = (const u16*)d_in[6];
  const u16* b2 = (const u16*)d_in[7];
  char* ws = (char*)d_ws;
  u16* qkv = (u16*)(ws + WS_QKV); u16* op = (u16*)(ws + WS_OP); u16* ol = (u16*)(ws + WS_OL); u16* hp = (u16*)(ws + WS_HP);
  f32x2* st2 = (f32x2*)(ws + WS_ST2); u16* w1p = (u16*)(ws + WS_W1P); u16* w2p = (u16*)(ws + WS_W2P); u16* h1p = (u16*)(ws + WS_H1P);
  float* part = (float*)(ws + WS_PART);
  (void)hipFuncSetAttribute((const void*)ka_qkv, hipFuncAttributeMaxDynamicSharedMemorySize, Q_LDS);
  (void)hipFuncSetAttribute((const void*)kb_attn, hipFuncAttributeMaxDynamicSharedMemorySize, B_LDS);
  (void)hipFuncSetAttribute((const void*)kc_wo, hipFuncAttributeMaxDynamicSharedMemorySize, C_LDS);
  (void)hipFuncSetAttribute((const void*)kd_ffn1, hipFuncAttributeMaxDynamicSharedMemorySize, F1_LDS);
  (void)hipFuncSetAttribute((const void*)ke_ffn2, hipFuncAttributeMaxDynamicSharedMemorySize, F2_LDS);
  ka_qkv<<<256, 512, Q_LDS, stream>>>(x, wqkv, w1, qkv, w1p SHOST);
  kb_attn<<<256, 512, B_LDS, stream>>>(qkv, w2, op, ol, w2p SHOST);
  kc_wo<<<192, 512, C_LDS, stream>>>(op, ol, wo, x, hp, st2 SHOST);
  kd_ffn1<<<256, 512, F1_LDS, stream>>>(hp, w1p, b1, st2, h1p SHOST);
  ke_ffn2<<<256, 512, F2_LDS, stream>>>(h1p, w2p, part SHOST);
  kf_reduce<<<S * D / 8 / 256, 256, 0, stream>>>(part, hp, b2, (u16*)d_out);
}
```

```cpp
#include <hip/hip_runtime.h>
#include <stdint.h>

typedef unsigned short u16;
typedef __bf16 bf16x8 __attribute__((ext_vector_type(8)));
typedef __bf16 bf16x4 __attribute__((ext_vector_type(4)));
typedef __bf16 bf16x2 __attribute__((ext_vector_type(2)));
typedef float f32x2 __attribute__((ext_vector_type(2)));
typedef float f32x4 __attribute__((ext_vector_type(4)));
typedef float f32x16 __attribute__((ext_vector_type(16)));
typedef unsigned u32x2 __attribute__((ext_vector_type(2)));
typedef unsigned u32x4 __attribute__((ext_vector_type(4)));
#define QA_DOT 1
#define QA_DMA 1
#define QA_WAIT 1
#define LAS __attribute__((address_space(3)))
#define DI __device__ __forceinline__
#define SB() __builtin_amdgcn_sched_barrier(0)
#define KATTR __launch_bounds__(512, 2) __attribute__((amdgpu_waves_per_eu(2, 2)))

constexpr int S = 2048, D = 576, H = 32, DH = 18, F = 4608, NQKV = 1728;
constexpr float LN_EPS = 1e-5f;

constexpr size_t WS_FLG = 0;
constexpr int FL_N = 0, FL_D = 1, FL_Q = FL_D + 256, FL_W1 = FL_Q + 192, FL_A = FL_W1 + 64, FL_W2 = FL_A + 256, FL_H = FL_W2 + 64, FL_H1 = FL_H + 192,
              FL_P = FL_H1 + 256, FL_END = FL_P + 256;
constexpr size_t WS_QKV = 262144;
static_assert(FL_END * 128 <= WS_QKV, "flag area");
constexpr size_t WS_QKV_SZ = (size_t)3 * H * S * 24 * 2;
constexpr size_t WS_OP = WS_QKV + WS_QKV_SZ;
constexpr size_t WS_OL = WS_OP + (size_t)64 * 32 * 1024;
constexpr size_t WS_HP = WS_OL + (size_t)H * S * 4;
constexpr size_t WS_ST2 = WS_HP + (size_t)S * D * 2;
constexpr size_t WS_W1P = WS_ST2 + (size_t)S * 48;
constexpr size_t WS_W2P = WS_W1P + (size_t)D * F * 2;
constexpr size_t WS_H1P = WS_W2P + (size_t)D * F * 2;
constexpr size_t WS_XCH = WS_H1P + (size_t)S * F * 2;
constexpr size_t WS_END = WS_XCH + (size_t)16 * 4 * 4 * 4 * 2 * 4608;
constexpr unsigned WS_BYTES = (unsigned)WS_END;

#define ST(kid, idx) asm volatile("; MARK " #kid " " #idx)

DI unsigned rfl(unsigned x) { return __builtin_amdgcn_readfirstlane(x); }
DI float bflo(unsigned w) { return __uint_as_float(w << 16); }
DI float bfhi(unsigned w) { return __uint_as_float(w & 0xffff0000u); }
DI unsigned pk2(float lo, float hi) { f32x2 v = {lo, hi}; bf16x2 b = __builtin_convertvector(v, bf16x2); return __builtin_bit_cast(unsigned, b); }
DI float dot2(unsigned a, unsigned b, float c) { return __builtin_amdgcn_fdot2_f32_bf16(__builtin_bit_cast(bf16x2, a), __builtin_bit_cast(bf16x2, b), c, false); }

typedef __amdgpu_buffer_rsrc_t srd_t;
DI srd_t mk_srd(const void* p, unsigned bytes) { return __builtin_amdgcn_make_buffer_rsrc((void*)p, (short)0, (int)bytes, 0x00020000); }
DI void dma16(unsigned ldsaddr, unsigned voff, srd_t srd, unsigned soff) {
  asm volatile("s_mov_b32 m0, %0\n\ts_nop 0\n\tbuffer_load_dwordx4 %1, %2, %3 offen lds" :: "s"(ldsaddr), "v"(voff), "s"(srd), "s"(soff) : "memory");
}
DI void dma16h(unsigned ldsaddr, unsigned voff, srd_t srd, unsigned soff) {
  asm volatile("s_mov_b32 m0, %0\n\ts_mov_b32 exec_hi, 0\n\tbuffer_load_dwordx4 %1, %2, %3 offen lds\n\ts_mov_b32 exec_hi, -1" :: "s"(ldsaddr), "v"(voff), "s"(srd), "s"(soff) : "memory");
}
DI void dma16q(unsigned ldsaddr, unsigned voff, srd_t srd, unsigned soff) {
  asm volatile("s_mov_b32 m0, %0\n\ts_mov_b64 exec, 0xffff\n\tbuffer_load_dwordx4 %1, %2, %3 offen lds\n\ts_mov_b64 exec, -1" :: "s"(ldsaddr), "v"(voff), "s"(srd), "s"(soff) : "memory");
}
DI void touch128(unsigned ldsdump, unsigned voff, srd_t srd, unsigned soff) {
  asm volatile("s_mov_b32 m0, %0\n\ts_nop 0\n\tbuffer_load_dword %1, %2, %3 offen lds" :: "s"(ldsdump), "v"(voff), "s"(srd), "s"(soff) : "memory");
}
template <int N> DI void wait_vm() { asm volatile("s_waitcnt vmcnt(%0)" :: "i"(N) : "memory"); }
DI void wait_lgkm0() { asm volatile("s_waitcnt lgkmcnt(0)" ::: "memory"); }
DI void bar() { asm volatile("" ::: "memory"); __builtin_amdgcn_s_barrier(); asm volatile("" ::: "memory"); }
DI u32x4 lds_u128(unsigned addr) { return *(const LAS u32x4*)(size_t)addr; }
DI f32x4 lds_f128(unsigned addr) { return *(const LAS f32x4*)(size_t)addr; }
DI void lds_w128(unsigned addr, f32x4 v) { *(LAS f32x4*)(size_t)addr = v; }
DI bf16x8 asbf(u32x4 v) { return __builtin_bit_cast(bf16x8, v); }
DI u32x2 lds_tr(unsigned addr) { return __builtin_bit_cast(u32x2, __builtin_amdgcn_ds_read_tr16_b64_v4bf16((LAS bf16x4*)(size_t)addr)); }
DI u32x4 cat4(u32x2 a, u32x2 b) { return u32x4{a[0], a[1], b[0], b[1]}; }
DI f32x4 mfma16(u32x4 a, u32x4 b, f32x4 c) { return __builtin_amdgcn_mfma_f32_16x16x32_bf16(asbf(a), asbf(b), c, 0, 0, 0); }
DI f32x16 mfma32(u32x4 a, u32x4 b, f32x16 c) { return __builtin_amdgcn_mfma_f32_32x32x16_bf16(asbf(a), asbf(b), c, 0, 0, 0); }
DI int swap23(int i) { return (i & 19) | ((i & 4) << 1) | ((i & 8) >> 1); }

constexpr int SC1 = 16;
DI void dma16s(unsigned ldsaddr, unsigned voff, srd_t srd, unsigned soff) {
  asm volatile("s_mov_b32 m0, %0\n\ts_nop 0\n\tbuffer_load_dwordx4 %1, %2, %3 offen sc1 lds" :: "s"(ldsaddr), "v"(voff), "s"(srd), "s"(soff) : "memory");
}
DI void dma16hs(unsigned ldsaddr, unsigned voff, srd_t srd, unsigned soff) {
  asm volatile("s_mov_b32 m0, %0\n\ts_mov_b32 exec_hi, 0\n\tbuffer_load_dwordx4 %1, %2, %3 offen sc1 lds\n\ts_mov_b32 exec_hi, -1" :: "s"(ldsaddr), "v"(voff), "s"(srd), "s"(soff) : "memory");
}
DI void st128s(u32x4 v, srd_t srd, unsigned voff, unsigned soff) {
  asm volatile("buffer_store_dwordx4 %0, %1, %2, %3 offen sc1\n\ts_nop 1" :: "v"(v), "v"(voff), "s"(srd), "s"(soff) : "memory");
}
DI u32x4 ld128s(srd_t srd, unsigned voff, unsigned soff) { return __builtin_amdgcn_raw_buffer_load_b128(srd, voff, soff, SC1); }

struct Ctx {
  const u16 *x, *wqkv, *wo, *w1, *b1, *w2, *b2;
  u16* out;
  char* ws;
  srd_t sws;
  unsigned lds0;
  unsigned long long nonce;
  int b, xcd, bi, w, lane;
};
DI void publish(const Ctx& c, int flag) {
  wait_vm<0>();
  bar();
  if (threadIdx.x == 0) __hip_atomic_store((unsigned long long*)(c.ws + (size_t)flag * 128), c.nonce, __ATOMIC_RELAXED, __HIP_MEMORY_SCOPE_AGENT);
}
DI void poll(const Ctx& c, int flag, bool active) {
  const unsigned long long* p = (const unsigned long long*)(c.ws + (size_t)flag * 128);
  const unsigned long long t0 = __builtin_amdgcn_s_memrealtime();
  for (;;) {
    const unsigned long long v = active ? __hip_atomic_load(p, __ATOMIC_RELAXED, __HIP_MEMORY_SCOPE_AGENT) : c.nonce;
    if (__all(v == c.nonce)) break;
    if (__builtin_amdgcn_s_memrealtime() - t0 > 2000000ull) break;
    __builtin_amdgcn_s_sleep(1);
  }
}

DI void relayout_w1(const Ctx& c, int fc, int part) {
  const int w = c.w, lane = c.lane; const unsigned lds0 = c.lds0;
  const srd_t sw = mk_srd(c.w1, D * F * 2);
  for (int pc = w; pc < 81; pc += 8) {
    const int ch = pc * 64 + lane, row = ch / 36, cc = ch % 36;
    dma16(lds0 + (unsigned)pc * 1024u, (unsigned)row * (F * 2) + (unsigned)cc * 16u, sw, (unsigned)part * 144u * (F * 2) + (unsigned)fc * 576u);
  }
  wait_vm<0>(); bar();
  const int h = lane >> 5, blk = (lane >> 4) & 1, q = (lane >> 2) & 3, p = lane & 3;
  const unsigned ra = lds0 + (unsigned)(8 * h + q) * 576u + (unsigned)(16 * blk + 8 * (p & 1) + 4 * (p >> 1)) * 2u;
  for (int fr = w; fr < 81; fr += 8) {
    const int sl = fr / 9, tile = fr % 9, s = part * 9 + sl, kh = s / 18;
    const unsigned a = ra + (unsigned)sl * 16u * 576u + (unsigned)tile * 64u;
    const u32x4 v = cat4(lds_tr(a), lds_tr(a + 4 * 576));
    const int slot = tile == 8 ? 8 : (kh == 0 ? (tile ^ 1) : tile);
    st128s(v, c.sws, (unsigned)lane * 16u, (unsigned)WS_W1P + ((unsigned)(fc * 9 + slot) * 36u + (unsigned)s) * 1024u);
  }
}
DI void relayout_w2(const Ctx& c, unsigned ldsw, int kappa, int ct) {
  const int lane = c.lane;
  const srd_t sw = mk_srd(c.w2, D * F * 2);
#pragma unroll
  for (int pc = 0; pc < 9; pc++) {
    const int ch = pc * 64 + lane, row = ch / 18, cc = ch % 18;
    dma16(ldsw + (unsigned)pc * 1024u, (unsigned)row * (D * 2) + (unsigned)cc * 16u, sw, (unsigned)kappa * 32u * (D * 2) + (unsigned)ct * 288u);
  }
  wait_vm<0>();
  const int g = lane >> 4, q = (lane >> 2) & 3, p = lane & 3;
  const unsigned ra = ldsw + (unsigned)(8 * g + q) * 288u;
  const int ks = kappa / 36, kst = kappa % 36;
#pragma unroll
  for (int nt = 0; nt < 9; nt++) {
    const unsigned col = nt < 8 ? (unsigned)(32 * (nt >> 1) + 8 * p + 4 * (nt & 1)) * 2u : (unsigned)(128 + 4 * p) * 2u;
    const u32x4 v = cat4(lds_tr(ra + col), lds_tr(ra + col + 4 * 288));
    st128s(v, c.sws, (unsigned)lane * 16u, (unsigned)WS_W2P + (((unsigned)(ks * 4 + ct) * 36u + (unsigned)kst) * 9u + (unsigned)nt) * 1024u);
  }
  wait_lgkm0();
}

constexpr int Q_SLOT = 36864;
DI void p1_qkv(const Ctx& c, int rb, int ct) {
  const int w = c.w, lane = c.lane, rg = w & 3, kh = w >> 2; const unsigned lds0 = c.lds0;
  const srd_t sx = mk_srd(c.x, S * D * 2), sw = mk_srd(c.wqkv, D * NQKV * 2);
  const unsigned vx = (unsigned)(lane >> 3) * (D * 2) + (unsigned)(((lane & 7) ^ ((4 * (w & 1) + (lane >> 4)) & 7)) * 16);
  unsigned vw[3];
#pragma unroll
  for (int i = 0; i < 3; i++) {
    const int ch = i < 2 ? 64 * (w + 8 * i) + lane : 64 * (16 + (w >> 1)) + 32 * (w & 1) + (lane & 31);
    const int row = ch / 20, cc = ch % 20;
    vw[i] = (unsigned)row * (NQKV * 2) + (unsigned)cc * 16u;
  }
  const unsigned xg = (unsigned)rb * 128u * (D * 2) + (unsigned)w * 8u * (D * 2), wg = (unsigned)ct * 288u;
#define Q_ISSUE1(t, i, ring) do { \
    if ((i) < 2) dma16(lds0 + (ring) + (unsigned)(w + 8 * (i)) * 1024u, vx, sx, xg + (unsigned)(i) * 64u * (D * 2) + (unsigned)(t) * 128u); \
    else if ((i) < 4) dma16(lds0 + (ring) + 16384u + (unsigned)(w + 8 * ((i) - 2)) * 1024u, vw[(i) & 1], sw, wg + (unsigned)(t) * 64u * (NQKV * 2)); \
    else dma16h(lds0 + (ring) + 16384u + 16384u + (unsigned)w * 512u, vw[2], sw, wg + (unsigned)(t) * 64u * (NQKV * 2)); } while (0)
#pragma unroll
  for (int t = 0; t < 3; t++)
#pragma unroll
    for (int i = 0; i < 5; i++) Q_ISSUE1(t, i, (unsigned)t * Q_SLOT);
  const int tok = lane & 31, h = lane >> 5, blk = (lane >> 4) & 1, q4 = (lane >> 2) & 3, pp = lane & 3;
  const unsigned xo = lds0 + (unsigned)(rg * 32 + tok) * 128u;
  const unsigned xc0 = (unsigned)(((4 * kh + h) ^ ((tok >> 1) & 7)) * 16), xc1 = (unsigned)(((4 * kh + 2 + h) ^ ((tok >> 1) & 7)) * 16);
  const unsigned wrow = lds0 + 16384u + (unsigned)(32 * kh + 8 * h + q4) * 320u + (unsigned)(16 * blk + 8 * (pp & 1) + 4 * (pp >> 1)) * 2u;
  unsigned wa[5];
#pragma unroll
  for (int j = 0; j < 5; j++) wa[j] = wrow + (unsigned)((j + rg) % 5) * 64u;
  f32x16 acc[5];
#pragma unroll
  for (int n = 0; n < 5; n++) acc[n] = f32x16{};
  float sx0 = 0, sq0 = 0, cs0 = 0, cs4 = 0;
  ST(1, 1);
  wait_vm<10>(); bar();
  ST(1, 2);
  constexpr int PF = 5;
  u32x4 wf[PF], xa, xb;
  xa = lds_u128(xo + xc0); xb = lds_u128(xo + xc1);
#pragma unroll
  for (int i = 0; i < PF; i++) wf[i] = cat4(lds_tr(wa[i]), lds_tr(wa[i] + 4 * 320));
  unsigned r0 = 0, r1 = Q_SLOT, r2 = 2 * Q_SLOT, r3 = 3 * Q_SLOT;
#define Q_PERIOD(t, ISSUE, VMW) do { \
    wait_vm<VMW>(); bar(); \
    unsigned ac_[5], an_[5]; \
    _Pragma("unroll") for (int j = 0; j < 5; j++) { ac_[j] = wa[j] + r0 + 16u * 320u; an_[j] = wa[j] + r1; asm volatile("" : "+v"(ac_[j]), "+v"(an_[j])); } \
    unsigned xn = xo + r1; asm volatile("" : "+v"(xn)); \
    u32x4 xa2, xb2; \
    _Pragma("unroll") for (int s = 0; s < 10; s++) { \
      const u32x4 cur = wf[s % 5]; \
      if (s < 5) wf[s] = cat4(lds_tr(ac_[s]), lds_tr(ac_[s] + 4 * 320)); else wf[s - 5] = cat4(lds_tr(an_[s - 5]), lds_tr(an_[s - 5] + 4 * 320)); \
      if (s == 6) { xa2 = lds_u128(xn + xc0); xb2 = lds_u128(xn + xc1); } \
      acc[s % 5] = mfma32(cur, s < 5 ? xa : xb, acc[s % 5]); \
      if ((ISSUE) && (s & 1) == 0) Q_ISSUE1((t) + 3, s >> 1, r3); \
      if (s % 5 == 0) { cs0 = dot2(cur[0], 0x3f803f80u, cs0); cs0 = dot2(cur[1], 0x3f803f80u, cs0); cs0 = dot2(cur[2], 0x3f803f80u, cs0); cs0 = dot2(cur[3], 0x3f803f80u, cs0); } \
      if (s % 5 == 4) { cs4 = dot2(cur[0], 0x3f803f80u, cs4); cs4 = dot2(cur[1], 0x3f803f80u, cs4); cs4 = dot2(cur[2], 0x3f803f80u, cs4); cs4 = dot2(cur[3], 0x3f803f80u, cs4); } \
      if (s == 1 || s == 2) { const int e = 2 * (s - 1); sx0 = dot2(xa[e], 0x3f803f80u, sx0); sq0 = dot2(xa[e], xa[e], sq0); sx0 = dot2(xa[e + 1], 0x3f803f80u, sx0); sq0 = dot2(xa[e + 1], xa[e + 1], sq0); } \
      if (s == 7 || s == 8) { const int e = 2 * (s - 7); sx0 = dot2(xb[e], 0x3f803f80u, sx0); sq0 = dot2(xb[e], xb[e], sq0); sx0 = dot2(xb[e + 1], 0x3f803f80u, sx0); sq0 = dot2(xb[e + 1], xb[e + 1], sq0); } \
      SB(); \
    } \
    xa = xa2; xb = xb2; \
    const unsigned rr_ = r0; r0 = r1; r1 = r2; r2 = r3; r3 = rr_; \
  } while (0)
#pragma unroll 1
  for (int t = 0; t < 6; t++) Q_PERIOD(t, true, 5);
  Q_PERIOD(6, false, 5);
  Q_PERIOD(7, false, 0);
  Q_PERIOD(8, false, 0);
  ST(1, 3);
  wait_lgkm0(); bar();
  const unsigned xch = lds0 + (unsigned)(rg * 2) * 10240u + (unsigned)lane * 16u;
#pragma unroll
  for (int n = 0; n < 5; n++) {
    const int o8 = 8;
    lds_w128(xch + (unsigned)(kh ^ 1) * 10240u + n * 2048, kh ? f32x4{acc[n][0], acc[n][1], acc[n][2], acc[n][3]} : f32x4{acc[n][o8], acc[n][o8 + 1], acc[n][o8 + 2], acc[n][o8 + 3]});
    lds_w128(xch + (unsigned)(kh ^ 1) * 10240u + n * 2048 + 1024, kh ? f32x4{acc[n][4], acc[n][5], acc[n][6], acc[n][7]} : f32x4{acc[n][o8 + 4], acc[n][o8 + 5], acc[n][o8 + 6], acc[n][o8 + 7]});
  }
  const unsigned stb = lds0 + 81920u;
  *(LAS f32x2*)(size_t)(stb + (unsigned)(rg * 32 + tok) * 32u + (unsigned)(kh * 2 + h) * 8u) = f32x2{sx0, sq0};
  const unsigned csb = stb + 4096u;
  {
    const int col = swap23(tok);
    *(LAS float*)(size_t)(csb + (unsigned)(32 * rg + col) * 16u + (unsigned)(kh * 2 + h) * 4u) = cs0;
    if (rg == 0) *(LAS float*)(size_t)(csb + (unsigned)(128 + col) * 16u + (unsigned)(kh * 2 + h) * 4u) = cs4;
  }
  wait_lgkm0(); bar();
  float o[5][8];
#pragma unroll
  for (int n = 0; n < 5; n++) {
    const f32x4 a = lds_f128(xch + (unsigned)kh * 10240u + n * 2048), b2 = lds_f128(xch + (unsigned)kh * 10240u + n * 2048 + 1024);
#pragma unroll
    for (int e = 0; e < 4; e++) { o[n][e] = a[e] + (kh ? acc[n][8 + e] : acc[n][e]); o[n][4 + e] = b2[e] + (kh ? acc[n][12 + e] : acc[n][4 + e]); }
  }
  const int tl = rg * 32 + tok;
  const f32x4 s0 = lds_f128(stb + (unsigned)tl * 32u), s1 = lds_f128(stb + (unsigned)tl * 32u + 16);
  const float mean = ((s0[0] + s0[2]) + (s1[0] + s1[2])) * (1.0f / D), var = fmaxf(((s0[1] + s0[3]) + (s1[1] + s1[3])) * (1.0f / D) - mean * mean, 0.f);
  const int type = ct >> 2;
  const float ra = rsqrtf(var + LN_EPS) * (type == 0 ? 0.23570226039551584f * 1.4426950408889634f : 1.0f);
  const unsigned cst = csb + 2560u;
  if (w * 64 + lane < 160) { const f32x4 v = lds_f128(csb + (unsigned)(w * 64 + lane) * 16u); *(LAS float*)(size_t)(cst + (unsigned)(w * 64 + lane) * 4u) = (v[0] + v[1]) + (v[2] + v[3]); }
  wait_lgkm0(); bar();
  {
    const int tid = w * 64 + lane;
    const unsigned padv = type == 0 ? 0x3f800000u : 0x00003f80u;
#pragma unroll
    for (int i = 0; i < 2; i++) {
      const int e = tid + 512 * i, t2 = e >> 3, hh = e & 7;
      const unsigned a = lds0 + t2 * 400 + hh * 48 + 36;
      *(LAS unsigned*)(size_t)a = padv; *(LAS unsigned*)(size_t)(a + 4) = 0; *(LAS unsigned*)(size_t)(a + 8) = 0;
    }
  }
#pragma unroll
  for (int n = 0; n < 5; n++) {
    const int tile = (n + rg) % 5, c0 = 32 * tile + 16 * kh + 8 * h;
    if (c0 < 144) {
      const f32x4 ca = lds_f128(cst + (unsigned)c0 * 4u), cb = lds_f128(cst + (unsigned)c0 * 4u + 16);
#pragma unroll
      for (int e = 0; e < 4; e++) {
        const int cc = c0 + 2 * e, hh = (cc * 3641) >> 16, d = cc - 18 * hh;
        const float c0v = e < 2 ? ca[2 * e] : cb[2 * e - 4], c1v = e < 2 ? ca[2 * e + 1] : cb[2 * e - 3];
        const float v0 = (o[n][2 * e] - mean * c0v) * ra, v1 = (o[n][2 * e + 1] - mean * c1v) * ra;
        *(LAS unsigned*)(size_t)(lds0 + tl * 400 + hh * 48 + d * 2) = pk2(v0, v1);
      }
    }
  }
  wait_lgkm0(); bar();
  {
    const int tid = w * 64 + lane;
    const unsigned dst = (unsigned)WS_QKV + ((unsigned)(type * 32 + (ct & 3) * 8) * S + (unsigned)rb * 128u) * 48u;
    u32x4 v[6];
#pragma unroll
    for (int i = 0; i < 6; i++) { const int ch = tid + 512 * i, hh = ch / 384, rem = ch % 384, t2 = rem / 3, part = rem % 3; v[i] = lds_u128(lds0 + t2 * 400 + hh * 48 + part * 16); }
#pragma unroll
    for (int i = 0; i < 6; i++) { const int ch = tid + 512 * i, hh = ch / 384, rem = ch % 384; st128s(v[i], c.sws, (unsigned)hh * (S * 48) + (unsigned)rem * 16u, dst); }
  }
  wait_lgkm0();
  publish(c, FL_Q + rb * 12 + ct);
}

constexpr int L_K = 0, L_V = 24576, L_WO = 49216;
DI void p2_attn(const Ctx& c, int head, int qb) {
  const int w = c.w, lane = c.lane; const unsigned lds0 = c.lds0;
  const unsigned ldsK = lds0 + L_K, ldsV = lds0 + L_V;
  if (w == 0) {
    const int hg = head >> 3, l = lane;
    const int ty = l < 2 ? 0 : (l < 6 ? 1 : 2), rbl = l < 2 ? 2 * qb + l : 2 * qb - 2 + ((l - 2) & 3);
    poll(c, FL_Q + rbl * 12 + ty * 4 + hg, l < 10 && rbl >= 0);
  }
  bar();
  ST(2, 1);
  const unsigned kbase = (unsigned)WS_QKV + (unsigned)((32 + head) * S + 256 * (qb - 1)) * 48u, vbase = (unsigned)WS_QKV + (unsigned)((64 + head) * S + 256 * (qb - 1)) * 48u;
  const unsigned vl = lane * 16;
#pragma unroll
  for (int hf = 0; hf < 2; hf++) {
    if (hf == 1 || qb > 0) {
#pragma unroll
      for (int j = 0; j < 3; j++) {
        const int pi = w * 3 + j;
        const unsigned pc = (unsigned)hf * 12288u + (unsigned)(pi % 12) * 1024;
        if (pi < 12) dma16s(ldsK + pc, vl, c.sws, kbase + pc); else dma16s(ldsV + pc, vl, c.sws, vbase + pc);
      }
    }
  }
  if (qb == 0) {
    const int tid = w * 64 + lane;
    if (tid < 256) {
      const u32x4 z = {0, 0, 0, 0};
      *(LAS u32x4*)(size_t)(ldsK + tid * 48) = z; *(LAS u32x4*)(size_t)(ldsK + tid * 48 + 16) = z; *(LAS u32x4*)(size_t)(ldsK + tid * 48 + 32) = u32x4{0, 0xf1493f80u, 0, 0};
      *(LAS u32x4*)(size_t)(ldsV + tid * 48) = z; *(LAS u32x4*)(size_t)(ldsV + tid * 48 + 16) = z; *(LAS u32x4*)(size_t)(ldsV + tid * 48 + 32) = z;
    }
  }
  const int qq = lane & 31, h = lane >> 5;
  const int q0 = 256 * qb + 32 * w;
  const unsigned qo = (unsigned)WS_QKV + (unsigned)(head * S + q0) * 48u;
  const u32x4 qf0 = ld128s(c.sws, (unsigned)qq * 48u + 16u * h, qo);
  u32x4 qf1 = ld128s(c.sws, (unsigned)qq * 48u + 32u, qo);
  if (h) qf1 = u32x4{0, 0, 0, 0};
  f32x16 cold, cdiag;
#pragma unroll
  for (int r = 0; r < 16; r++) {
    const int kk = 16 * (r >> 3) + 8 * h + (r & 7);
    cold[r] = kk > qq ? 0.f : -1e30f;
    cdiag[r] = kk <= qq ? 0.f : -1e30f;
  }
  const int key = swap23(lane & 31);
  unsigned ka0 = ldsK + (unsigned)(key + 32 * w) * 48 + 16 * h;
  unsigned ka1 = ldsK + (unsigned)(key + 32 * w) * 48 + 32;
  const int blk = (lane >> 4) & 1, q4 = (lane >> 2) & 3, pp = lane & 3;
  unsigned va = ldsV + (unsigned)(32 * w + 8 * h + q4) * 48 + 32 * blk + 8 * pp;
  asm volatile("" : "+v"(ka0), "+v"(ka1), "+v"(va));
  wait_vm<0>(); wait_lgkm0(); bar();
  ST(2, 2);
  const f32x16 zero = {};
  u32x4 k0[9], k1[9];
#pragma unroll
  for (int j = 0; j < 9; j++) { k0[j] = lds_u128(ka0 + j * 1536); k1[j] = lds_u128(ka1 + j * 1536); }
  float m = -1e30f;
  {
    f32x16 sA = mfma32(k1[0], qf1, mfma32(k0[0], qf0, cold)), sB;
#pragma unroll
    for (int j = 0; j < 9; j++) {
      f32x16& cur = (j & 1) ? sB : sA; f32x16& nxt = (j & 1) ? sA : sB;
      if (j + 1 < 9) nxt = mfma32(k1[j + 1], qf1, mfma32(k0[j + 1], qf0, j + 1 == 8 ? cdiag : zero));
#pragma unroll
      for (int r = 0; r < 16; r += 2) m = fmaxf(m, fmaxf(cur[r], cur[r + 1]));
      SB();
    }
  }
  { const auto pr = __builtin_amdgcn_permlane32_swap(__float_as_uint(m), __float_as_uint(m), false, false); m = fmaxf(__uint_as_float(pr[0]), __uint_as_float(pr[1])); }
  if (h == 0) qf1[1] = (qf1[1] & 0xffff0000u) | (pk2(-m, 0.f) & 0xffffu);
  ST(2, 3);
  f32x16 oacc = {};
  {
    f32x16 sA = mfma32(k1[0], qf1, mfma32(k0[0], qf0, cold)), sB;
#pragma unroll
    for (int j = 0; j < 9; j++) {
      f32x16& cur = (j & 1) ? sB : sA; f32x16& nxt = (j & 1) ? sA : sB;
      const unsigned vb = va + j * 1536;
      const u32x4 v0 = cat4(lds_tr(vb), lds_tr(vb + 4 * 48)), v1 = cat4(lds_tr(vb + 16 * 48), lds_tr(vb + 20 * 48));
      if (j + 1 < 9) nxt = mfma32(k1[j + 1], qf1, mfma32(k0[j + 1], qf0, j + 1 == 8 ? cdiag : zero));
      u32x4 p0, p1;
#pragma unroll
      for (int e = 0; e < 4; e++) p0[e] = pk2(__builtin_amdgcn_exp2f(cur[2 * e]), __builtin_amdgcn_exp2f(cur[2 * e + 1]));
      oacc = mfma32(v0, p0, oacc);
#pragma unroll
      for (int e = 0; e < 4; e++) p1[e] = pk2(__builtin_amdgcn_exp2f(cur[8 + 2 * e]), __builtin_amdgcn_exp2f(cur[8 + 2 * e + 1]));
      oacc = mfma32(v1, p1, oacc);
      SB();
    }
  }
  const float l = __shfl(oacc[10], qq);
  const float inv = 1.0f / l;
  const u32x4 om = {pk2(oacc[0] * inv, oacc[1] * inv), pk2(oacc[2] * inv, oacc[3] * inv), pk2(oacc[4] * inv, oacc[5] * inv), pk2(oacc[6] * inv, oacc[7] * inv)};
  st128s(om, c.sws, (unsigned)lane * 16u, (unsigned)WS_OP + (unsigned)((q0 >> 5) * 32 + head) * 1024u);
  __builtin_amdgcn_raw_buffer_store_b32(pk2(oacc[8] * inv, oacc[9] * inv), c.sws, h == 0 ? (unsigned)qq * 4u : 0x7ff00000u, (unsigned)WS_OL + (unsigned)(head * S + q0) * 4u, SC1);
  publish(c, FL_A + qb * 32 + head);
}

DI void p3_prefetch(const Ctx& c, int ct) {
  const int w = c.w, lane = c.lane;
  const srd_t sw = mk_srd(c.wo, D * D * 2);
  unsigned vw[3];
#pragma unroll
  for (int i = 0; i < 3; i++) { const int ch = 64 * i + lane; vw[i] = (unsigned)(ch / 12) * (D * 2) + (unsigned)(ch % 12) * 16u; }
#pragma unroll
  for (int gi = 0; gi < 5; gi++) {
    const int grp = w + 8 * gi;
    if (grp < 36) {
#pragma unroll
      for (int i = 0; i < 3; i++) dma16(c.lds0 + L_WO + (unsigned)(grp * 3 + i) * 1024u, vw[i], sw, (unsigned)grp * 16u * (D * 2) + (unsigned)ct * 192u);
    }
  }
}
DI void p3_wo(const Ctx& c, int rb, int ct) {
  const int w = c.w, lane = c.lane, rg = w & 1, kq = w >> 1; const unsigned lds0 = c.lds0;
  const int qt = rb * 2 + rg, tok = lane & 31, h = lane >> 5;
  const int row = qt * 32 + tok;
  const u32x4 xr0 = *(const u32x4*)(c.x + (size_t)row * D + ct * 96 + 16 * kq + 8 * h);
  const u32x4 xr1 = *(const u32x4*)(c.x + (size_t)row * D + ct * 96 + 16 * (kq < 2 ? kq + 4 : kq) + 8 * h);
  if (w == 0) poll(c, FL_A + (rb >> 2) * 32 + (lane & 31), lane < 32);
  bar();
  ST(3, 1);
  u32x4 of[9];
  {
    const unsigned so = (unsigned)WS_OP + (unsigned)(qt * 32 + kq * 9) * 1024u;
#pragma unroll
    for (int s = 0; s < 5; s++) of[s] = ld128s(c.sws, (unsigned)lane * 16u + s * 1024u, so);
    if (kq < 3) {
#pragma unroll
      for (int s = 5; s < 9; s++) of[s] = ld128s(c.sws, (unsigned)lane * 16u + s * 1024u, so);
    } else {
#pragma unroll
      for (int G = 0; G < 4; G++)
#pragma unroll
        for (int i = 0; i < 4; i++)
          of[5 + G][i] = __builtin_amdgcn_raw_buffer_load_b32(c.sws, (unsigned)((4 * h + i) * S + tok) * 4u, (unsigned)WS_OL + (unsigned)(8 * G * S + qt * 32) * 4u, SC1);
    }
  }
  wait_vm<0>(); bar();
  ST(3, 2);
  const int blk = (lane >> 4) & 1, q4 = (lane >> 2) & 3, pp = lane & 3;
  const unsigned colb = lds0 + L_WO + (unsigned)(16 * blk + 8 * (pp & 1) + 4 * (pp >> 1)) * 2u;
  f32x16 acc[3] = {};
#pragma unroll
  for (int s = 0; s < 9; s++) {
    unsigned r0, r1;
    if (s < 5 || kq < 3) { const int hd = kq * 9 + s; r0 = (unsigned)(18 * hd + 4 * h + q4); r1 = r0 + 8; }
    else { const int G = s - 5; r0 = (unsigned)(18 * (8 * G + 4 * h + (q4 >> 1)) + 16 + (q4 & 1)); r1 = r0 + 36; }
    const unsigned a0 = colb + r0 * 192u, a1 = colb + r1 * 192u;
#pragma unroll
    for (int n = 0; n < 3; n++) acc[n] = mfma32(cat4(lds_tr(a0 + n * 64), lds_tr(a1 + n * 64)), of[s], acc[n]);
  }
  ST(3, 3);
  wait_lgkm0(); bar();
  const unsigned xb = lds0 + L_WO + (unsigned)rg * 49152u + (unsigned)lane * 16u;
#pragma unroll
  for (int f = 0; f < 6; f++) {
    const int r8 = 8 * (f & 1);
    lds_w128(xb + (unsigned)(f * 4 + kq) * 2048u, f32x4{acc[f >> 1][r8], acc[f >> 1][r8 + 1], acc[f >> 1][r8 + 2], acc[f >> 1][r8 + 3]});
    lds_w128(xb + (unsigned)(f * 4 + kq) * 2048u + 1024u, f32x4{acc[f >> 1][r8 + 4], acc[f >> 1][r8 + 5], acc[f >> 1][r8 + 6], acc[f >> 1][r8 + 7]});
  }
  wait_lgkm0(); bar();
  float ssum = 0, ssq = 0;
  const int nf = kq < 2 ? 2 : 1;
  for (int i = 0; i < nf; i++) {
    const int f = kq + 4 * i;
    f32x4 a = {0, 0, 0, 0}, cc = {0, 0, 0, 0};
#pragma unroll
    for (int k = 0; k < 4; k++) { a += lds_f128(xb + (unsigned)(f * 4 + k) * 2048u); cc += lds_f128(xb + (unsigned)(f * 4 + k) * 2048u + 1024u); }
    const u32x4 xr = i ? xr1 : xr0;
    u32x4 o;
    o[0] = pk2(bflo(xr[0]) + a[0], bfhi(xr[0]) + a[1]); o[1] = pk2(bflo(xr[1]) + a[2], bfhi(xr[1]) + a[3]);
    o[2] = pk2(bflo(xr[2]) + cc[0], bfhi(xr[2]) + cc[1]); o[3] = pk2(bflo(xr[3]) + cc[2], bfhi(xr[3]) + cc[3]);
    st128s(o, c.sws, (unsigned)lane * 16u, (unsigned)WS_HP + (unsigned)(qt * 36 + ct * 6 + f) * 1024u);
#pragma unroll
    for (int e = 0; e < 4; e++) { ssum = dot2(o[e], 0x3f803f80u, ssum); ssq = dot2(o[e], o[e], ssq); }
  }
  { const auto a = __builtin_amdgcn_permlane32_swap(__float_as_uint(ssum), __float_as_uint(ssum), false, false); ssum = __uint_as_float(a[0]) + __uint_as_float(a[1]);
    const auto b2 = __builtin_amdgcn_permlane32_swap(__float_as_uint(ssq), __float_as_uint(ssq), false, false); ssq = __uint_as_float(b2[0]) + __uint_as_float(b2[1]); }
  const unsigned stb = lds0 + L_WO + 98304u;
  if (h == 0) *(LAS f32x2*)(size_t)(stb + (unsigned)(rg * 32 + tok) * 32u + kq * 8u) = f32x2{ssum, ssq};
  wait_lgkm0(); bar();
  if (w == 0) {
    const f32x4 a = lds_f128(stb + lane * 32u), cc = lds_f128(stb + lane * 32u + 16);
    const u32x2 v = {__float_as_uint((a[0] + a[2]) + (cc[0] + cc[2])), __float_as_uint((a[1] + a[3]) + (cc[1] + cc[3]))};
    __builtin_amdgcn_raw_buffer_store_b64(v, c.sws, (unsigned)lane * 8u, (unsigned)WS_ST2 + (unsigned)(rb * 6 + ct) * 512u, SC1);
  }
  publish(c, FL_H + rb * 6 + ct);
}

constexpr float GC0 = -2.0f * 0.7978845608028654f * 1.4426950408889634f, GC1 = GC0 * 0.044715f;
DI f32x2 gelu2(f32x2 x) {
  const f32x2 t = x * (GC1 * (x * x) + GC0);
  f32x2 e; e[0] = __builtin_amdgcn_exp2f(t[0]); e[1] = __builtin_amdgcn_exp2f(t[1]);
  const f32x2 d = e + 1.0f;
  f32x2 r; r[0] = __builtin_amdgcn_rcpf(d[0]); r[1] = __builtin_amdgcn_rcpf(d[1]);
  return x * r;
}
struct G2 { float x0, x1, t0, t1; };
DI void g2_s1(G2& g, float a0, float a1, float p0, float p1) {
  asm volatile("v_add_f32_e32 %0, %4, %6\n\tv_add_f32_e32 %1, %5, %7\n\tv_mul_f32_e32 %2, %0, %0\n\tv_mul_f32_e32 %3, %1, %1"
               : "=&v"(g.x0), "=&v"(g.x1), "=&v"(g.t0), "=&v"(g.t1) : "v"(a0), "v"(a1), "v"(p0), "v"(p1));
}
DI void g2_s2(G2& g, float vc1) {
  asm volatile("v_fmaak_f32 %0, %0, %4, 0xc0135761\n\tv_fmaak_f32 %1, %1, %4, 0xc0135761\n\tv_mul_f32_e32 %0, %2, %0\n\tv_mul_f32_e32 %1, %3, %1"
               : "+v"(g.t0), "+v"(g.t1) : "v"(g.x0), "v"(g.x1), "v"(vc1));
}
DI void g2_s3(G2& g) {
  asm volatile("v_exp_f32_e32 %0, %0\n\tv_exp_f32_e32 %1, %1\n\tv_add_f32_e32 %0, 1.0, %0\n\tv_add_f32_e32 %1, 1.0, %1" : "+v"(g.t0), "+v"(g.t1));
}
DI unsigned g2_s4(G2& g) {
  unsigned o;
  asm volatile("v_rcp_f32_e32 %1, %1\n\tv_rcp_f32_e32 %2, %2\n\tv_mul_f32_e32 %1, %3, %1\n\tv_mul_f32_e32 %2, %4, %2\n\tv_cvt_pk_bf16_f32 %0, %1, %2"
               : "=v"(o), "+v"(g.t0), "+v"(g.t1) : "v"(g.x0), "v"(g.x1));
  return o;
}

constexpr int F_SLOT = 36864, F1_XCH = 3 * F_SLOT, F1_UV = F1_XCH + 32768, F1_DUMP = F1_UV + 1152, F_LDS = F1_DUMP + 256;
#define F_ISSUE1(gbase, i, ring) do { if ((i) < 4) dma16s(lds0 + (ring) + (unsigned)w * 1024u + (i) * 8192u, vl, c.sws, (gbase) + (unsigned)w * 1024u + (i) * 8192u); \
                                      else dma16hs(lds0 + (ring) + 32768u + (unsigned)w * 512u, vl, c.sws, (gbase) + 32768u + (unsigned)w * 512u); } while (0)
DI void p4_prefetch(const Ctx& c, int fc, int slot) {
  const int w = c.w; const unsigned lds0 = c.lds0, vl = c.lane * 16;
#pragma unroll
  for (int i = 0; i < 5; i++) F_ISSUE1((unsigned)WS_W1P + (unsigned)(fc * 9 + slot) * F_SLOT, i, (unsigned)slot * F_SLOT);
}
DI void p45_ffn(const Ctx& c, int rb, int fc) {
  const int w = c.w, lane = c.lane, rg = w & 3, kh = w >> 2; const unsigned lds0 = c.lds0;
  const unsigned vl = lane * 16;
  const int ks = fc >> 2, ct = fc & 3;
  const unsigned w1g = (unsigned)WS_W1P + (unsigned)fc * 9 * F_SLOT, w2g = (unsigned)WS_W2P + (unsigned)(ks * 4 + ct) * 9 * F_SLOT;
  const int tok = lane & 31, h = lane >> 5;
  if (threadIdx.x < 288) *(LAS float*)(size_t)(lds0 + F1_UV + threadIdx.x * 4) = __uint_as_float((unsigned)c.b1[fc * 288 + threadIdx.x] << 16);
  if (w == 0) poll(c, lane < 12 ? FL_H + (2 * rb + lane / 6) * 6 + lane % 6 : FL_W2 + (ks * 4 + ct) * 4 + (lane & 3), lane < 16);
  wait_lgkm0(); bar();
  ST(4, 1);
  u32x2 stv[6];
#pragma unroll
  for (int e = 0; e < 6; e++)
    asm volatile("buffer_load_dwordx2 %0, %1, %2, %3 offen offset:%4 sc1" : "=&v"(stv[e]) : "v"((unsigned)tok * 8u), "s"(c.sws),
                 "s"((unsigned)WS_ST2 + (unsigned)((2 * rb + (rg >> 1)) * 6) * 512u + (unsigned)(rg & 1) * 256u), "i"(e * 512) : "memory");
  u32x4 xf[18];
  {
    const unsigned so = (unsigned)WS_HP + ((unsigned)(rb * 4 + rg) * 36u + (unsigned)kh * 18u) * 1024u;
#pragma unroll
    for (int s = 0; s < 18; s++)
      asm volatile("buffer_load_dwordx4 %0, %1, %2, %3 offen offset:%4 sc1" : "=&v"(xf[s]) : "v"(vl), "s"(c.sws), "s"(so + (s >> 2) * 4096u), "i"((s & 3) * 1024) : "memory");
  }
  float ra, rc;
  {
    asm volatile("s_waitcnt vmcnt(18)" : "+v"(stv[0]), "+v"(stv[1]), "+v"(stv[2]), "+v"(stv[3]), "+v"(stv[4]), "+v"(stv[5]) :: "memory");
    float s = 0, q = 0;
#pragma unroll
    for (int e = 0; e < 6; e++) { s += __uint_as_float(stv[e][0]); q += __uint_as_float(stv[e][1]); }
    const float mean = s * (1.0f / D), var = fmaxf(q * (1.0f / D) - mean * mean, 0.f), rstd = rsqrtf(var + LN_EPS);
    ra = rstd; rc = -mean * rstd;
  }
#pragma unroll
  for (int s = 0; s < 18; s++) {
    asm volatile("s_waitcnt vmcnt(%1)" : "+v"(xf[s]) : "i"(17 - s) : "memory");
#pragma unroll
    for (int e = 0; e < 4; e++) {
      const f32x2 v = f32x2{bflo(xf[s][e]), bfhi(xf[s][e])} * ra + rc;
      xf[s][e] = pk2(v[0], v[1]);
    }
    asm volatile("" : "+v"(xf[s]));
    SB();
  }
  const unsigned rd = lds0 + (unsigned)kh * 18432u + vl;
  const unsigned hvo = (unsigned)(tok >> 4) * 1024u + (unsigned)h * 256u + (unsigned)(tok & 15) * 16u;
  const unsigned hbase = (unsigned)WS_H1P + ((unsigned)(rb * 4 + rg) * 144u + (unsigned)fc * 9u) * 2048u;
  const unsigned bia = lds0 + F1_UV + (unsigned)h * 32u;
  bar();
  ST(4, 2);
  constexpr int PF = 3;
  u32x4 wf[PF];
#pragma unroll
  for (int i = 0; i < PF; i++) wf[i] = lds_u128(rd + i * 1024);
  const f32x16 zero = {};
  f32x16 prevO = zero, binit;
#define F1_BIAS(ba_) do { const f32x4 b0_ = lds_f128(ba_), b1_ = lds_f128(ba_ + 16), b2_ = lds_f128(ba_ + 64), b3_ = lds_f128(ba_ + 80); \
    binit = f32x16{b0_[0], b0_[1], b0_[2], b0_[3], b1_[0], b1_[1], b1_[2], b1_[3], b2_[0], b2_[1], b2_[2], b2_[3], b3_[0], b3_[1], b3_[2], b3_[3]}; } while (0)
  F1_BIAS(bia + (unsigned)(1 - kh) * 128u);
  float vc1 = GC1; asm volatile("" : "+v"(vc1));
  unsigned r0 = 0, r1 = F_SLOT, r2 = 2 * F_SLOT;
  const unsigned xwr = lds0 + F1_XCH + (unsigned)(kh ^ 1) * 16384u + (unsigned)rg * 4096u + vl;
  const unsigned xrd = lds0 + F1_XCH + (unsigned)kh * 16384u + (unsigned)rg * 4096u + vl;
  f32x4 pp[4] = {};
#define F1_STAGE(q, G, SRC, R0, O) do { const int p_ = (q) >> 2, st_ = (q) & 3, r_ = (R0) + 2 * p_; \
      if (st_ == 0) g2_s1(G, SRC[r_], SRC[r_ + 1], pp[r_ >> 2][r_ & 3], pp[r_ >> 2][(r_ & 3) + 1]); \
      if (st_ == 1) g2_s2(G, vc1); if (st_ == 2) g2_s3(G); if (st_ == 3) O[p_] = g2_s4(G); } while (0)
#define F1_PERIOD(t, ACC, GELU, HALF, VMW, DG) do { \
    if ((VMW) == 2) { if ((t) == 2) wait_vm<0>(); else wait_vm<1>(); } else wait_vm<(VMW) & 1>(); \
    bar(); \
    const unsigned dg_ = (DG); \
    unsigned sb = rd + r0, sn = rd + r1; \
    asm volatile("" : "+v"(sb), "+v"(sn)); \
    u32x4 o; G2 g; \
    const int gt_ = ((t) - 2 - (HALF)) + kh;      \
    if ((GELU) && (HALF) == 0) { _Pragma("unroll") for (int e = 0; e < 4; e++) pp[e] = lds_f128(xrd + e * 1024); } \
    SB(); \
    _Pragma("unroll") for (int s = 0; s < 18; s++) { \
      const u32x4 cur = wf[s % PF]; \
      wf[s % PF] = lds_u128(s + PF < 18 ? sb + (s + PF) * 1024 : sn + (s + PF - 18) * 1024); \
      ACC = mfma32(cur, xf[s], s == 0 ? ((HALF) == 0 ? binit : zero) : ACC); \
      if (s % 2 == 0 && s / 2 < 5) F_ISSUE1(dg_, s / 2, r2); \
      if ((HALF) == 1 && s % 4 == 1 && s / 4 < 4) { const int e = s / 4; lds_w128(xwr + e * 1024, f32x4{accG[4 * e], accG[4 * e + 1], accG[4 * e + 2], accG[4 * e + 3]}); } \
      if (GELU) { \
        if (s >= 2 && s <= 17) F1_STAGE(s - 2, g, prevO, 8 * (HALF), o); \
        if (s == 17) st128s(o, c.sws, hvo + (HALF) * 512u, hbase + (unsigned)gt_ * 2048u); \
      } \
      if ((HALF) == 1 && s == 12) {     \
        const int nt_ = (t) + 1 == 8 ? 8 : (t) + 1 + (1 - kh); \
        unsigned ba_ = bia + (unsigned)nt_ * 128u; asm volatile("" : "+v"(ba_)); \
        F1_BIAS(ba_); \
      } \
      SB(); \
    } \
    const unsigned rr_ = r0; r0 = r1; r1 = r2; r2 = rr_; \
  } while (0)
  {
    f32x16 accG, accO;
    F1_PERIOD(0, accG, false, 0, 0, w1g + 2u * F_SLOT);
    F1_PERIOD(1, accO, false, 1, 0, w1g + 3u * F_SLOT);
    prevO = accO;
#pragma unroll 1
    for (int t = 2; t < 6; t += 2) {
      F1_PERIOD(t, accG, true, 0, 2, w1g + (unsigned)(t + 2) * F_SLOT);
      F1_PERIOD(t + 1, accO, true, 1, 1, w1g + (unsigned)(t + 3) * F_SLOT);
      prevO = accO;
    }
    F1_PERIOD(6, accG, true, 0, 1, w1g + 8u * F_SLOT);
    F1_PERIOD(7, accO, true, 1, 1, w2g);
    prevO = accO;
    {
      wait_vm<1>(); bar();
      unsigned sb = rd + r0, sn = rd + r1;
      asm volatile("" : "+v"(sb), "+v"(sn));
      u32x4 o, o2; G2 g, g2;
      const int gt_ = 6 + kh;
#pragma unroll
      for (int e = 0; e < 4; e++) pp[e] = lds_f128(xrd + e * 1024);
      if (kh == 0) binit = zero;
      SB();
#pragma unroll
      for (int s = 0; s < 18; s++) {
        const u32x4 cur = wf[s % PF];
        wf[s % PF] = lds_u128(s + PF < 18 ? sb + (s + PF) * 1024 : sn + (s + PF - 18) * 1024);
        accG = mfma32(cur, xf[s], s == 0 ? binit : accG);
        if (s % 2 == 0 && s / 2 < 5) F_ISSUE1(w2g + F_SLOT, s / 2, r2);
        if (s >= 2 && s <= 17) { F1_STAGE(s - 2, g, prevO, 0, o); F1_STAGE(s - 2, g2, prevO, 8, o2); }
        if (s == 17) { st128s(o, c.sws, hvo, hbase + (unsigned)gt_ * 2048u); st128s(o2, c.sws, hvo + 512u, hbase + (unsigned)gt_ * 2048u); }
        SB();
      }
      const unsigned rr_ = r0; r0 = r1; r1 = r2; r2 = rr_;
      ST(4, 3);
      bar();
      lds_w128(xwr, kh ? f32x4{accG[0], accG[1], accG[2], accG[3]} : f32x4{accG[8], accG[9], accG[10], accG[11]});
      lds_w128(xwr + 1024, kh ? f32x4{accG[4], accG[5], accG[6], accG[7]} : f32x4{accG[12], accG[13], accG[14], accG[15]});
      wait_lgkm0(); bar();
      const f32x4 q0 = lds_f128(xrd), q1 = lds_f128(xrd + 1024);
      const f32x4 k0 = (kh ? f32x4{accG[8], accG[9], accG[10], accG[11]} : f32x4{accG[0], accG[1], accG[2], accG[3]}) + q0;
      const f32x4 k1 = (kh ? f32x4{accG[12], accG[13], accG[14], accG[15]} : f32x4{accG[4], accG[5], accG[6], accG[7]}) + q1;
      const f32x2 z0 = gelu2(f32x2{k0[0], k0[1]}), z1 = gelu2(f32x2{k0[2], k0[3]}), z2 = gelu2(f32x2{k1[0], k1[1]}), z3 = gelu2(f32x2{k1[2], k1[3]});
      o = u32x4{pk2(z0[0], z0[1]), pk2(z1[0], z1[1]), pk2(z2[0], z2[1]), pk2(z3[0], z3[1])};
      st128s(o, c.sws, hvo + (unsigned)kh * 512u, hbase + 8u * 2048u);
    }
  }
  publish(c, FL_H1 + rb * 16 + fc);
  ST(4, 4);
  if (w == 0) poll(c, FL_H1 + rb * 16 + ks * 4 + (lane & 3), lane < 4);
  bar();
  ST(5, 1);
  const unsigned hoff0 = (unsigned)WS_H1P + ((unsigned)(rb * 4 + rg) * 144u + (unsigned)ks * 36u + 2u * kh) * 2048u;
  u32x4 hA[4], hB[4], hC[4];
#define HLOAD1(dst, t, i) do { const unsigned so_ = hoff0 + (unsigned)(t) * 8192u; \
    asm volatile("buffer_load_dwordx4 %0, %1, %2, %3 offen offset:" #i "*1024 sc1" : "=&v"(dst[i]) : "v"(vl), "s"(c.sws), "s"(so_) : "memory"); } while (0)
#define HWAIT(N, dst) asm volatile("s_waitcnt vmcnt(" #N ")" : "+v"(dst[0]), "+v"(dst[1]), "+v"(dst[2]), "+v"(dst[3]) :: "memory")
  HLOAD1(hA, 0, 0); HLOAD1(hA, 0, 1); HLOAD1(hA, 0, 2); HLOAD1(hA, 0, 3);
  HLOAD1(hB, 1, 0); HLOAD1(hB, 1, 1); HLOAD1(hB, 1, 2); HLOAD1(hB, 1, 3);
  f32x4 acc[2][9];
#pragma unroll
  for (int a = 0; a < 2; a++)
#pragma unroll
    for (int n = 0; n < 9; n++) acc[a][n] = f32x4{0, 0, 0, 0};
#define F2_PERIOD(t, HC, HN, VMW, ISSUE) do { \
    HWAIT(VMW, HC); \
    bar(); \
    unsigned sb = rd + r0, sn = rd + r1; \
    asm volatile("" : "+v"(sb), "+v"(sn)); \
    _Pragma("unroll") for (int s = 0; s < 18; s++) { \
      const u32x4 cur = wf[s % PF]; \
      wf[s % PF] = lds_u128(s + PF < 18 ? sb + (s + PF) * 1024 : sn + (s + PF - 18) * 1024); \
      const int k = s / 9, nt = s % 9; \
      acc[0][nt] = mfma16(cur, HC[2 * k], acc[0][nt]); \
      acc[1][nt] = mfma16(cur, HC[2 * k + 1], acc[1][nt]); \
      if (ISSUE) { \
        if (s % 2 == 0 && s / 2 < 5) F_ISSUE1(w2g + (unsigned)((t) + 2) * F_SLOT, s / 2, r2); \
        if (s == 10) HLOAD1(HN, (t) + 2, 0); if (s == 12) HLOAD1(HN, (t) + 2, 1); if (s == 14) HLOAD1(HN, (t) + 2, 2); if (s == 16) HLOAD1(HN, (t) + 2, 3); \
      } \
      SB(); \
    } \
    const unsigned rr_ = r0; r0 = r1; r1 = r2; r2 = rr_; \
  } while (0)
  F2_PERIOD(0, hA, hC, 4, true);
  ST(5, 2);
#pragma unroll 1
  for (int t = 1; t < 7; t += 3) {
    F2_PERIOD(t, hB, hA, 4, true);
    F2_PERIOD(t + 1, hC, hB, 4, true);
    F2_PERIOD(t + 2, hA, hC, 4, true);
  }
  F2_PERIOD(7, hB, hA, 4, false);
  F2_PERIOD(8, hC, hB, 0, false);
  ST(5, 3);
  wait_lgkm0(); bar();
  const unsigned xb = lds0 + (unsigned)rg * 18432u + vl;
#pragma unroll
  for (int n = 0; n < 9; n++) lds_w128(xb + (unsigned)(kh ^ 1) * 9216u + n * 1024, kh ? acc[0][n] : acc[1][n]);
  wait_lgkm0(); bar();
  f32x4 o[9];
#pragma unroll
  for (int n = 0; n < 9; n++) o[n] = lds_f128(xb + kh * 9216u + n * 1024) + (kh ? acc[1][n] : acc[0][n]);
  const unsigned xg = (unsigned)WS_XCH + (unsigned)((rb * 4 + ct) * 16) * 9216u + (unsigned)kh * 4608u;
  if (rg != ks) {
    const unsigned d = xg + (unsigned)(rg * 4 + ks) * 9216u;
#pragma unroll
    for (int T = 0; T < 4; T++)
      st128s(u32x4{pk2(o[2 * T][0], o[2 * T][1]), pk2(o[2 * T][2], o[2 * T][3]), pk2(o[2 * T + 1][0], o[2 * T + 1][1]), pk2(o[2 * T + 1][2], o[2 * T + 1][3])}, c.sws, vl + T * 1024u, d);
    __builtin_amdgcn_raw_buffer_store_b64(u32x2{pk2(o[8][0], o[8][1]), pk2(o[8][2], o[8][3])}, c.sws, 4096u + lane * 8u, d, SC1);
  }
  publish(c, FL_P + (rb * 4 + ct) * 4 + ks);
  ST(5, 4);
  if (rg == ks) {
    const int tk = lane & 15, g = lane >> 4;
    const int row = rb * 128 + rg * 32 + kh * 16 + tk;
    u32x4 hv[4], bv[4]; u32x2 hv8, bv8;
#pragma unroll
    for (int T = 0; T < 4; T++) {
      const int col = ct * 144 + 32 * T + 8 * g;
      hv[T] = ld128s(c.sws, (unsigned)(((col >> 3) & 1) * 32 + kh * 16 + tk) * 16u + (unsigned)(col >> 4) * 1024u, (unsigned)WS_HP + (unsigned)(rb * 4 + rg) * 36u * 1024u);
      bv[T] = *(const u32x4*)(c.b2 + col);
    }
    {
      const int col = ct * 144 + 128 + 4 * g;
      hv8 = __builtin_amdgcn_raw_buffer_load_b64(c.sws, (unsigned)(((col >> 3) & 1) * 32 + kh * 16 + tk) * 16u + (unsigned)(col >> 4) * 1024u + (unsigned)(col & 7) * 2u,
                                                 (unsigned)WS_HP + (unsigned)(rb * 4 + rg) * 36u * 1024u, SC1);
      bv8 = *(const u32x2*)(c.b2 + col);
    }
    poll(c, FL_P + (rb * 4 + ct) * 4 + (lane & 3), lane < 4);
    asm volatile("" ::: "memory");
#pragma unroll
    for (int src = 0; src < 4; src++) {
      if (src != ks) {
        const unsigned d = xg + (unsigned)(rg * 4 + src) * 9216u;
        u32x4 pv[4];
#pragma unroll
        for (int T = 0; T < 4; T++) pv[T] = ld128s(c.sws, vl + T * 1024u, d);
        const u32x2 p8 = __builtin_amdgcn_raw_buffer_load_b64(c.sws, 4096u + lane * 8u, d, SC1);
#pragma unroll
        for (int T = 0; T < 4; T++) {
          o[2 * T] += f32x4{bflo(pv[T][0]), bfhi(pv[T][0]), bflo(pv[T][1]), bfhi(pv[T][1])};
          o[2 * T + 1] += f32x4{bflo(pv[T][2]), bfhi(pv[T][2]), bflo(pv[T][3]), bfhi(pv[T][3])};
        }
        o[8] += f32x4{bflo(p8[0]), bfhi(p8[0]), bflo(p8[1]), bfhi(p8[1])};
      }
    }
    u16* orow = c.out + (size_t)row * D + ct * 144;
#pragma unroll
    for (int T = 0; T < 4; T++) {
      u32x4 r;
      r[0] = pk2(bflo(hv[T][0]) + (o[2 * T][0] + bflo(bv[T][0])), bfhi(hv[T][0]) + (o[2 * T][1] + bfhi(bv[T][0])));
      r[1] = pk2(bflo(hv[T][1]) + (o[2 * T][2] + bflo(bv[T][1])), bfhi(hv[T][1]) + (o[2 * T][3] + bfhi(bv[T][1])));
      r[2] = pk2(bflo(hv[T][2]) + (o[2 * T + 1][0] + bflo(bv[T][2])), bfhi(hv[T][2]) + (o[2 * T + 1][1] + bfhi(bv[T][2])));
      r[3] = pk2(bflo(hv[T][3]) + (o[2 * T + 1][2] + bflo(bv[T][3])), bfhi(hv[T][3]) + (o[2 * T + 1][3] + bfhi(bv[T][3])));
      *(u32x4*)(orow + 32 * T + 8 * g) = r;
    }
    u32x2 r8;
    r8[0] = pk2(bflo(hv8[0]) + (o[8][0] + bflo(bv8[0])), bfhi(hv8[0]) + (o[8][1] + bfhi(bv8[0])));
    r8[1] = pk2(bflo(hv8[1]) + (o[8][2] + bflo(bv8[1])), bfhi(hv8[1]) + (o[8][3] + bfhi(bv8[1])));
    *(u32x2*)(orow + 128 + 4 * g) = r8;
  }
  ST(5, 5);
}

constexpr int M_LDS = 163840;
static_assert(L_WO + 110592 <= M_LDS && 4 * Q_SLOT <= M_LDS && F_LDS <= M_LDS, "LDS map");
__global__ KATTR void fused_block(const u16* __restrict__ x, const u16* __restrict__ wqkv, const u16* __restrict__ wo, const u16* __restrict__ w1,
                                  const u16* __restrict__ b1, const u16* __restrict__ w2, const u16* __restrict__ b2, u16* __restrict__ out, char* __restrict__ ws) {
  extern __shared__ __attribute__((aligned(16))) char smem[];
  Ctx c;
  c.x = x; c.wqkv = wqkv; c.wo = wo; c.w1 = w1; c.b1 = b1; c.w2 = w2; c.b2 = b2; c.out = out; c.ws = ws;
  c.sws = mk_srd(ws, WS_BYTES);
  c.lds0 = (unsigned)(size_t)smem;
  c.b = blockIdx.x; c.xcd = c.b & 7; c.bi = c.b >> 3;
  c.w = rfl(threadIdx.x >> 6); c.lane = threadIdx.x & 63;
  ST(1, 0);
  const unsigned long long n0 = __hip_atomic_load((const unsigned long long*)(ws + FL_N * 128), __ATOMIC_RELAXED, __HIP_MEMORY_SCOPE_AGENT);
  c.nonce = (((unsigned long long)rfl((unsigned)(n0 >> 32)) << 32) | rfl((unsigned)n0)) + 1ull;
  if (threadIdx.x == 0) __hip_atomic_store((unsigned long long*)(ws + (size_t)(FL_D + c.b) * 128), c.nonce, __ATOMIC_RELAXED, __HIP_MEMORY_SCOPE_AGENT);
  const int xcd = c.xcd, bi = c.bi;
  if (bi < 24) {
    p1_qkv(c, (xcd >> 1) * 4 + (bi & 3), (xcd & 1) * 6 + (bi >> 2));
  } else {
    const int j = bi - 24, fc = (xcd & 3) * 4 + (j >> 1), part = (xcd >> 2) * 2 + (j & 1);
    relayout_w1(c, fc, part);
    publish(c, FL_W1 + fc * 4 + part);
  }
  ST(1, 4);
  if (bi < 24) p3_prefetch(c, bi >> 2);
  p2_attn(c, xcd * 4 + (bi & 3), bi >> 2);
  ST(2, 4);
  const int rb4 = (xcd >> 2) * 8 + (bi & 7), fc4 = (xcd & 3) * 4 + (bi >> 3);
  if (c.w == 1) poll(c, FL_W1 + fc4 * 4 + (c.lane & 3), c.lane < 4);
  if (c.b == 0 && c.w == 2) {
#pragma unroll
    for (int i = 0; i < 4; i++) poll(c, FL_D + i * 64 + c.lane, true);
    if (c.lane == 0) __hip_atomic_store((unsigned long long*)(ws + FL_N * 128), c.nonce, __ATOMIC_RELAXED, __HIP_MEMORY_SCOPE_AGENT);
  }
  bar();
  p4_prefetch(c, fc4, 0);
  if (bi < 24) {
    p3_wo(c, xcd * 4 + (bi & 3), bi >> 2);
  } else {
    const int j = bi - 24, ks = xcd & 3, ct = j >> 1, part = (xcd >> 2) * 2 + (j & 1);
    relayout_w2(c, c.lds0 + L_WO + (unsigned)c.w * 9216u, ks * 36 + part * 9 + c.w, ct);
    if (c.w == 0) relayout_w2(c, c.lds0 + L_WO, ks * 36 + part * 9 + 8, ct);
    publish(c, FL_W2 + (ks * 4 + ct) * 4 + part);
  }
  ST(3, 4);
  p4_prefetch(c, fc4, 1);
  p45_ffn(c, rb4, fc4);
}

extern "C" void kernel_launch(void* const* d_in, const int* in_sizes, int n_in, void* d_out, int out_size, void* d_ws, size_t ws_size,
                              hipStream_t stream) {
  (void)hipFuncSetAttribute((const void*)fused_block, hipFuncAttributeMaxDynamicSharedMemorySize, M_LDS);
  fused_block<<<256, 512, M_LDS, stream>>>((const u16*)d_in[0], (const u16*)d_in[1], (const u16*)d_in[2], (const u16*)d_in[4], (const u16*)d_in[5],
                                           (const u16*)d_in[6], (const u16*)d_in[7], (u16*)d_out, (char*)d_ws);
}
```

```cpp
#include <hip/hip_runtime.h>
#include <stdint.h>

typedef unsigned short u16;
typedef __bf16 bf16x8 __attribute__((ext_vector_type(8)));
typedef __bf16 bf16x4 __attribute__((ext_vector_type(4)));
typedef __bf16 bf16x2 __attribute__((ext_vector_type(2)));
typedef float f32x2 __attribute__((ext_vector_type(2)));
typedef float f32x4 __attribute__((ext_vector_type(4)));
typedef float f32x16 __attribute__((ext_vector_type(16)));
typedef unsigned u32x2 __attribute__((ext_vector_type(2)));
typedef unsigned u32x4 __attribute__((ext_vector_type(4)));
#define QA_DOT 1
#define QA_DMA 1
#define QA_WAIT 1
#define LAS __attribute__((address_space(3)))
#define DI __device__ __forceinline__
#define SB() __builtin_amdgcn_sched_barrier(0)
#define KATTR __launch_bounds__(512, 2) __attribute__((amdgpu_waves_per_eu(2, 2)))

constexpr int S = 2048, D = 576, H = 32, DH = 18, F = 4608, NQKV = 1728;
constexpr float LN_EPS = 1e-5f;

constexpr size_t WS_FLG = 0;
constexpr int FL_N = 0, FL_D = 1, FL_Q = FL_D + 256, FL_W1 = FL_Q + 192, FL_A = FL_W1 + 64, FL_W2 = FL_A + 256, FL_H = FL_W2 + 64, FL_H1 = FL_H + 192,
              FL_P = FL_H1 + 256, FL_S1 = FL_P + 256, FL_END = FL_S1 + 64;
constexpr size_t WS_QKV = 262144;
static_assert(FL_END * 128 <= WS_QKV, "flag area");
constexpr size_t WS_QKV_SZ = (size_t)3 * H * S * 24 * 2;
constexpr size_t WS_OP = WS_QKV + WS_QKV_SZ;
constexpr size_t WS_OL = WS_OP + (size_t)64 * 32 * 1024;
constexpr size_t WS_HP = WS_OL + (size_t)H * S * 4;
constexpr size_t WS_ST2 = WS_HP + (size_t)S * D * 2;
constexpr size_t WS_W1P = WS_ST2 + (size_t)S * 48;
constexpr size_t WS_W2P = WS_W1P + (size_t)D * F * 2;
constexpr size_t WS_H1P = WS_W2P + (size_t)D * F * 2;
constexpr size_t WS_XCH = WS_H1P + (size_t)S * F * 2;
constexpr size_t WS_ST1 = WS_XCH + (size_t)16 * 4 * 4 * 4 * 2 * 4608;
constexpr size_t WS_CSQ = WS_ST1 + (size_t)S * 8;
constexpr size_t WS_END = WS_CSQ + (size_t)(NQKV + 64) * 4;
constexpr unsigned WS_BYTES = (unsigned)WS_END;

#define ST(kid, idx) asm volatile("; MARK " #kid " " #idx)

DI unsigned rfl(unsigned x) { return __builtin_amdgcn_readfirstlane(x); }
DI float bflo(unsigned w) { return __uint_as_float(w << 16); }
DI float bfhi(unsigned w) { return __uint_as_float(w & 0xffff0000u); }
DI unsigned pk2(float lo, float hi) { f32x2 v = {lo, hi}; bf16x2 b = __builtin_convertvector(v, bf16x2); return __builtin_bit_cast(unsigned, b); }
DI float dot2(unsigned a, unsigned b, float c) { return __builtin_amdgcn_fdot2_f32_bf16(__builtin_bit_cast(bf16x2, a), __builtin_bit_cast(bf16x2, b), c, false); }

typedef __amdgpu_buffer_rsrc_t srd_t;
DI srd_t mk_srd(const void* p, unsigned bytes) { return __builtin_amdgcn_make_buffer_rsrc((void*)p, (short)0, (int)bytes, 0x00020000); }
DI void dma16(unsigned ldsaddr, unsigned voff, srd_t srd, unsigned soff) {
  asm volatile("s_mov_b32 m0, %0\n\ts_nop 0\n\tbuffer_load_dwordx4 %1, %2, %3 offen lds" :: "s"(ldsaddr), "v"(voff), "s"(srd), "s"(soff) : "memory");
}
DI void dma16h(unsigned ldsaddr, unsigned voff, srd_t srd, unsigned soff) {
  asm volatile("s_mov_b32 m0, %0\n\ts_mov_b32 exec_hi, 0\n\tbuffer_load_dwordx4 %1, %2, %3 offen lds\n\ts_mov_b32 exec_hi, -1" :: "s"(ldsaddr), "v"(voff), "s"(srd), "s"(soff) : "memory");
}
DI void dma16q(unsigned ldsaddr, unsigned voff, srd_t srd, unsigned soff) {
  asm volatile("s_mov_b32 m0, %0\n\ts_mov_b64 exec, 0xffff\n\tbuffer_load_dwordx4 %1, %2, %3 offen lds\n\ts_mov_b64 exec, -1" :: "s"(ldsaddr), "v"(voff), "s"(srd), "s"(soff) : "memory");
}
DI void touch128(unsigned ldsdump, unsigned voff, srd_t srd, unsigned soff) {
  asm volatile("s_mov_b32 m0, %0\n\ts_nop 0\n\tbuffer_load_dword %1, %2, %3 offen lds" :: "s"(ldsdump), "v"(voff), "s"(srd), "s"(soff) : "memory");
}
template <int N> DI void wait_vm() { asm volatile("s_waitcnt vmcnt(%0)" :: "i"(N) : "memory"); }
DI void wait_lgkm0() { asm volatile("s_waitcnt lgkmcnt(0)" ::: "memory"); }
DI void bar() { asm volatile("" ::: "memory"); __builtin_amdgcn_s_barrier(); asm volatile("" ::: "memory"); }
DI u32x4 lds_u128(unsigned addr) { return *(const LAS u32x4*)(size_t)addr; }
DI f32x4 lds_f128(unsigned addr) { return *(const LAS f32x4*)(size_t)addr; }
DI void lds_w128(unsigned addr, f32x4 v) { *(LAS f32x4*)(size_t)addr = v; }
DI bf16x8 asbf(u32x4 v) { return __builtin_bit_cast(bf16x8, v); }
DI u32x2 lds_tr(unsigned addr) { return __builtin_bit_cast(u32x2, __builtin_amdgcn_ds_read_tr16_b64_v4bf16((LAS bf16x4*)(size_t)addr)); }
DI u32x4 cat4(u32x2 a, u32x2 b) { return u32x4{a[0], a[1], b[0], b[1]}; }
DI f32x4 mfma16(u32x4 a, u32x4 b, f32x4 c) { return __builtin_amdgcn_mfma_f32_16x16x32_bf16(asbf(a), asbf(b), c, 0, 0, 0); }
DI f32x16 mfma32(u32x4 a, u32x4 b, f32x16 c) { return __builtin_amdgcn_mfma_f32_32x32x16_bf16(asbf(a), asbf(b), c, 0, 0, 0); }
DI int swap23(int i) { return (i & 19) | ((i & 4) << 1) | ((i & 8) >> 1); }

constexpr int SC1 = 16;
DI void dma16s(unsigned ldsaddr, unsigned voff, srd_t srd, unsigned soff) {
  asm volatile("s_mov_b32 m0, %0\n\ts_nop 0\n\tbuffer_load_dwordx4 %1, %2, %3 offen sc1 lds" :: "s"(ldsaddr), "v"(voff), "s"(srd), "s"(soff) : "memory");
}
DI void dma16hs(unsigned ldsaddr, unsigned voff, srd_t srd, unsigned soff) {
  asm volatile("s_mov_b32 m0, %0\n\ts_mov_b32 exec_hi, 0\n\tbuffer_load_dwordx4 %1, %2, %3 offen sc1 lds\n\ts_mov_b32 exec_hi, -1" :: "s"(ldsaddr), "v"(voff), "s"(srd), "s"(soff) : "memory");
}
DI void st128s(u32x4 v, srd_t srd, unsigned voff, unsigned soff) {
  asm volatile("buffer_store_dwordx4 %0, %1, %2, %3 offen sc1\n\ts_nop 1" :: "v"(v), "v"(voff), "s"(srd), "s"(soff) : "memory");
}
DI u32x4 ld128s(srd_t srd, unsigned voff, unsigned soff) { return __builtin_amdgcn_raw_buffer_load_b128(srd, voff, soff, SC1); }

struct Ctx {
  const u16 *x, *wqkv, *wo, *w1, *b1, *w2, *b2;
  u16* out;
  char* ws;
  srd_t sws;
  unsigned lds0;
  unsigned long long nonce;
  int b, xcd, bi, w, lane;
};
DI void publish(const Ctx& c, int flag) {
  wait_vm<0>();
  bar();
  if (threadIdx.x == 0) __hip_atomic_store((unsigned long long*)(c.ws + (size_t)flag * 128), c.nonce, __ATOMIC_RELAXED, __HIP_MEMORY_SCOPE_AGENT);
}
DI void poll(const Ctx& c, int flag, bool active) {
  const unsigned long long* p = (const unsigned long long*)(c.ws + (size_t)flag * 128);
  const unsigned long long t0 = __builtin_amdgcn_s_memrealtime();
  for (;;) {
    const unsigned long long v = active ? __hip_atomic_load(p, __ATOMIC_RELAXED, __HIP_MEMORY_SCOPE_AGENT) : c.nonce;
    if (__all(v == c.nonce)) break;
    if (__builtin_amdgcn_s_memrealtime() - t0 > 2000000ull) break;
    __builtin_amdgcn_s_sleep(1);
  }
}

constexpr int L_DUMP = 163840 - 256;
DI u32x2 entry_load(const Ctx& c) {
  u32x2 nv;
  asm volatile("buffer_load_dwordx2 %0, %1, %2, 0 offen sc1" : "=&v"(nv) : "v"((unsigned)(FL_N * 128)), "s"(c.sws) : "memory");
  return nv;
}
DI void entry_prefetch(const Ctx& c) {
  unsigned long long pc; unsigned len;
  asm volatile("s_getpc_b64 %0\n.Lcp%=:\n\ts_mov_b32 %1, .Lfused_code_end-.Lcp%=" : "=s"(pc), "=s"(len));
  const srd_t sc = mk_srd((const void*)pc, len);
  touch128(c.lds0 + L_DUMP, (unsigned)(c.w * 64 + c.lane) * 128u, sc, 0u);
  const srd_t st = c.w == 1 ? mk_srd(c.out, S * D * 2) : c.sws;
  touch128(c.lds0 + L_DUMP, c.w == 0 ? (c.lane < 24 ? ((unsigned)c.bi * 24u + (unsigned)c.lane) * 65536u : 0x7ff00000u) : (c.w == 1 ? (unsigned)c.lane * 65536u : 0x7ff00000u), st, 0u);
}
DI void entry_nonce(Ctx& c, u32x2 nv) {
  asm volatile("" : "+v"(nv));
  c.nonce = (((unsigned long long)rfl(nv[1]) << 32) | rfl(nv[0])) + 1ull;
  if (threadIdx.x == 0) __hip_atomic_store((unsigned long long*)(c.ws + (size_t)(FL_D + c.b) * 128), c.nonce, __ATOMIC_RELAXED, __HIP_MEMORY_SCOPE_AGENT);
}

DI void helper_stats(Ctx& c, int hb) {
  const int w = c.w, lane = c.lane;
  const srd_t sx = mk_srd(c.x, S * D * 2), sw = mk_srd(c.wqkv, hb < 27 ? D * NQKV * 2 : 0);
  const int tid = w * 64 + lane, ch = tid & 7, rg = tid >> 3;
  u32x4 a[4], b2[4], v9[9];
#pragma unroll
  for (int r = 0; r < 4; r++) {
    const unsigned so = (unsigned)(hb * 32 + w * 4 + r) * (D * 2);
    a[r] = __builtin_amdgcn_raw_buffer_load_b128(sx, (unsigned)lane * 16u, so, 0);
    b2[r] = __builtin_amdgcn_raw_buffer_load_b128(sx, lane < 8 ? 1024u + (unsigned)lane * 16u : 0x7ff00000u, so, 0);
  }
#pragma unroll
  for (int i = 0; i < 9; i++) v9[i] = __builtin_amdgcn_raw_buffer_load_b128(sw, (unsigned)(rg * 9 + i) * (NQKV * 2) + (unsigned)ch * 16u, (unsigned)hb * 128u, 0);
  const u32x2 nv = entry_load(c);
  entry_prefetch(c);
  float sm[4], sq[4];
#pragma unroll
  for (int r = 0; r < 4; r++) {
    sm[r] = 0; sq[r] = 0;
#pragma unroll
    for (int e = 0; e < 4; e++) { sm[r] = dot2(a[r][e], 0x3f803f80u, sm[r]); sq[r] = dot2(a[r][e], a[r][e], sq[r]); sm[r] = dot2(b2[r][e], 0x3f803f80u, sm[r]); sq[r] = dot2(b2[r][e], b2[r][e], sq[r]); }
  }
#pragma unroll
  for (int m = 32; m >= 1; m >>= 1)
#pragma unroll
    for (int r = 0; r < 4; r++) { sm[r] += __shfl_xor(sm[r], m); sq[r] += __shfl_xor(sq[r], m); }
  const int r = lane & 3;
  const float s1 = r == 0 ? sm[0] : r == 1 ? sm[1] : r == 2 ? sm[2] : sm[3], s2 = r == 0 ? sq[0] : r == 1 ? sq[1] : r == 2 ? sq[2] : sq[3];
  const float mean = s1 * (1.0f / D), var = fmaxf(s2 * (1.0f / D) - mean * mean, 0.f);
  const u32x2 v = {__float_as_uint(mean), __float_as_uint(rsqrtf(var + LN_EPS))};
  __builtin_amdgcn_raw_buffer_store_b64(v, c.sws, lane < 4 ? (unsigned)lane * 8u : 0x7ff00000u, (unsigned)WS_ST1 + (unsigned)(hb * 32 + w * 4) * 8u, SC1);
  float s8[8] = {0, 0, 0, 0, 0, 0, 0, 0};
#pragma unroll
  for (int i = 0; i < 9; i++)
#pragma unroll
    for (int e = 0; e < 4; e++) { s8[2 * e] += bflo(v9[i][e]); s8[2 * e + 1] += bfhi(v9[i][e]); }
#pragma unroll
  for (int m = 8; m <= 32; m <<= 1)
#pragma unroll
    for (int e = 0; e < 8; e++) s8[e] += __shfl_xor(s8[e], m);
  if (lane < 8) { lds_w128(c.lds0 + (unsigned)(w * 64 + lane * 8) * 4u, f32x4{s8[0], s8[1], s8[2], s8[3]}); lds_w128(c.lds0 + (unsigned)(w * 64 + lane * 8) * 4u + 16, f32x4{s8[4], s8[5], s8[6], s8[7]}); }
  wait_lgkm0(); bar();
  if (w == 0) {
    float t = 0;
#pragma unroll
    for (int k = 0; k < 8; k++) t += *(const LAS float*)(size_t)(c.lds0 + (unsigned)(k * 64 + lane) * 4u);
    __builtin_amdgcn_raw_buffer_store_b32(__float_as_uint(t), c.sws, hb < 27 ? (unsigned)lane * 4u : 0x7ff00000u, (unsigned)WS_CSQ + (unsigned)hb * 256u, SC1);
  }
  wait_lgkm0();
  wait_vm<2>();
  entry_nonce(c, nv);
  publish(c, FL_S1 + hb);
}

DI void relayout_w1(const Ctx& c, int fc, int part) {
  const int w = c.w, lane = c.lane; const unsigned lds0 = c.lds0;
  const srd_t sw = mk_srd(c.w1, D * F * 2);
  for (int pc = w; pc < 81; pc += 8) {
    const int ch = pc * 64 + lane, row = ch / 36, cc = ch % 36;
    dma16(lds0 + (unsigned)pc * 1024u, (unsigned)row * (F * 2) + (unsigned)cc * 16u, sw, (unsigned)part * 144u * (F * 2) + (unsigned)fc * 576u);
  }
  wait_vm<0>(); bar();
  const int h = lane >> 5, blk = (lane >> 4) & 1, q = (lane >> 2) & 3, p = lane & 3;
  const unsigned ra = lds0 + (unsigned)(8 * h + q) * 576u + (unsigned)(16 * blk + 8 * (p & 1) + 4 * (p >> 1)) * 2u;
  for (int fr = w; fr < 81; fr += 8) {
    const int sl = fr / 9, tile = fr % 9, s = part * 9 + sl, kh = s / 18;
    const unsigned a = ra + (unsigned)sl * 16u * 576u + (unsigned)tile * 64u;
    const u32x4 v = cat4(lds_tr(a), lds_tr(a + 4 * 576));
    const int slot = tile == 8 ? 8 : (kh == 0 ? (tile ^ 1) : tile);
    st128s(v, c.sws, (unsigned)lane * 16u, (unsigned)WS_W1P + ((unsigned)(fc * 9 + slot) * 36u + (unsigned)s) * 1024u);
  }
}
DI void relayout_w2(const Ctx& c, unsigned ldsw, int kappa, int ct) {
  const int lane = c.lane;
  const srd_t sw = mk_srd(c.w2, D * F * 2);
#pragma unroll
  for (int pc = 0; pc < 9; pc++) {
    const int ch = pc * 64 + lane, row = ch / 18, cc = ch % 18;
    dma16(ldsw + (unsigned)pc * 1024u, (unsigned)row * (D * 2) + (unsigned)cc * 16u, sw, (unsigned)kappa * 32u * (D * 2) + (unsigned)ct * 288u);
  }
  wait_vm<0>();
  const int g = lane >> 4, q = (lane >> 2) & 3, p = lane & 3;
  const unsigned ra = ldsw + (unsigned)(8 * g + q) * 288u;
  const int ks = kappa / 36, kst = (kappa % 36 + 36 - 9 * ct) % 36;
#pragma unroll
  for (int nt = 0; nt < 9; nt++) {
    const unsigned col = nt < 8 ? (unsigned)(32 * (nt >> 1) + 8 * p + 4 * (nt & 1)) * 2u : (unsigned)(128 + 4 * p) * 2u;
    const u32x4 v = cat4(lds_tr(ra + col), lds_tr(ra + col + 4 * 288));
    st128s(v, c.sws, (unsigned)lane * 16u, (unsigned)WS_W2P + (((unsigned)(ks * 4 + ct) * 36u + (unsigned)kst) * 9u + (unsigned)nt) * 1024u);
  }
  wait_lgkm0();
}

constexpr int Q_SLOT = 36864;
constexpr int Q_CT = 4 * Q_SLOT;
template <int NT, int T0>
DI void p1_body(Ctx& c, int rb, int ct) {
  const int w = c.w, lane = c.lane, rg = w & 3; const unsigned lds0 = c.lds0;
  const srd_t sx = mk_srd(c.x, S * D * 2), sw = mk_srd(c.wqkv, D * NQKV * 2);
  const unsigned vx = (unsigned)(lane >> 3) * (D * 2) + (unsigned)(((lane & 7) ^ ((4 * (w & 1) + (lane >> 4)) & 7)) * 16);
  unsigned vw[3];
#pragma unroll
  for (int i = 0; i < 3; i++) {
    const int ch = i < 2 ? 64 * (w + 8 * i) + lane : 64 * (16 + (w >> 1)) + 32 * (w & 1) + (lane & 31);
    const int row = ch / 20, cc = ch % 20;
    vw[i] = (unsigned)row * (NQKV * 2) + (unsigned)cc * 16u;
  }
  const unsigned xg = (unsigned)rb * 128u * (D * 2) + (unsigned)w * 8u * (D * 2), wg = (unsigned)ct * 288u;
#define Q_ISSUE1(t, i, ring) do { \
    if ((i) < 2) dma16(lds0 + (ring) + (unsigned)(w + 8 * (i)) * 1024u, vx, sx, xg + (unsigned)(i) * 64u * (D * 2) + (unsigned)(t) * 128u); \
    else if ((i) < 4) dma16(lds0 + (ring) + 16384u + (unsigned)(w + 8 * ((i) - 2)) * 1024u, vw[(i) & 1], sw, wg + (unsigned)(t) * 64u * (NQKV * 2)); \
    else dma16h(lds0 + (ring) + 16384u + 16384u + (unsigned)w * 512u, vw[2], sw, wg + (unsigned)(t) * 64u * (NQKV * 2)); } while (0)
  const u32x2 nv = entry_load(c);
#pragma unroll
  for (int t = 0; t < 3; t++)
#pragma unroll
    for (int i = 0; i < 5; i++) Q_ISSUE1(t, i, (unsigned)t * Q_SLOT);
  const int tok = lane & 31, h = lane >> 5, blk = (lane >> 4) & 1, q4 = (lane >> 2) & 3, pp = lane & 3;
  unsigned xj[4], wj[4];
#pragma unroll
  for (int j = 0; j < 4; j++) {
    const int ks = (j + rg) & 3;
    xj[j] = lds0 + (unsigned)(rg * 32 + tok) * 128u + (unsigned)(((2 * ks + h) ^ ((tok >> 1) & 7)) * 16);
    wj[j] = lds0 + 16384u + (unsigned)(16 * ks + 8 * h + q4) * 320u + (unsigned)(32 * T0 + 16 * blk + 8 * (pp & 1) + 4 * (pp >> 1)) * 2u;
  }
  f32x16 acc[NT];
#pragma unroll
  for (int n = 0; n < NT; n++) acc[n] = f32x16{};
  ST(1, 1);
  entry_prefetch(c);
  wait_vm<12>(); bar();
  entry_nonce(c, nv);
  ST(1, 2);
  u32x4 wf[4][NT], xf[4];
#pragma unroll
  for (int j = 0; j < 2; j++) {
    xf[j] = lds_u128(xj[j]);
#pragma unroll
    for (int n = 0; n < NT; n++) wf[j][n] = cat4(lds_tr(wj[j] + n * 64), lds_tr(wj[j] + n * 64 + 4 * 320));
  }
  unsigned r0 = 0, r1 = Q_SLOT, r2 = 2 * Q_SLOT, r3 = 3 * Q_SLOT;
#define Q_PERIOD(t, ISSUE, VMW, EXTRA) do { \
    wait_vm<VMW>(); bar(); \
    EXTRA; \
    unsigned wa_[4], xa_[4]; \
    _Pragma("unroll") for (int j = 0; j < 4; j++) { wa_[j] = wj[(j + 2) & 3] + (j < 2 ? r0 : r1); xa_[j] = xj[(j + 2) & 3] + (j < 2 ? r0 : r1); asm volatile("" : "+v"(wa_[j]), "+v"(xa_[j])); } \
    _Pragma("unroll") for (int j = 0; j < 4; j++) { \
      const u32x4 xc = xf[j]; \
      xf[(j + 2) & 3] = lds_u128(xa_[j]); \
      _Pragma("unroll") for (int n = 0; n < NT; n++) { \
        const u32x4 cur = wf[j][n]; \
        wf[(j + 2) & 3][n] = cat4(lds_tr(wa_[j] + n * 64), lds_tr(wa_[j] + n * 64 + 4 * 320)); \
        acc[n] = mfma32(cur, xc, acc[n]); \
        const int s_ = j * NT + n; \
        if (QA_DMA && (ISSUE) && (NT == 3 ? (s_ % 2 == 0 && s_ < 10) : (s_ != 2 && s_ != 4 && s_ < 7))) Q_ISSUE1((t) + 3, NT == 3 ? s_ / 2 : (s_ < 2 ? s_ : (s_ == 3 ? 2 : s_ - 2)), r3); \
        SB(); \
      } \
    } \
    const unsigned rr_ = r0; r0 = r1; r1 = r2; r2 = r3; r3 = rr_; \
  } while (0)
#pragma unroll 1
  for (int t = 0; t < 6; t++) Q_PERIOD(t, true, 5, (void)0);
  if (w == 7) { const int g0 = (144 * ct) >> 6; poll(c, FL_S1 + (lane < 4 ? 4 * rb + lane : min(g0 + (lane & 3), 26)), lane < 8); }
  const int tl = rg * 32 + tok, type = ct >> 2;
  u32x2 stv; u32x4 csv;
  Q_PERIOD(6, false, 5, (
    { asm volatile("buffer_load_dwordx2 %0, %1, %2, %3 offen sc1" : "=&v"(stv) : "v"((unsigned)tl * 8u), "s"(c.sws), "s"((unsigned)WS_ST1 + (unsigned)rb * 1024u) : "memory");
      asm volatile("buffer_load_dwordx4 %0, %1, %2, %3 offen sc1" : "=&v"(csv) : "v"((unsigned)min(lane, 39) * 16u), "s"(c.sws), "s"((unsigned)WS_CSQ + (unsigned)ct * 576u) : "memory"); }));
  Q_PERIOD(7, false, 0, (
    { asm volatile("" : "+v"(stv), "+v"(csv));
      if (w == 0 && lane < 40) *(LAS u32x4*)(size_t)(lds0 + Q_CT + (unsigned)lane * 16u) = csv; }));
  Q_PERIOD(8, false, 0, (void)0);
  ST(1, 3);
  wait_lgkm0(); bar();
  ST(1, 5);
  f32x2 st;
  st[0] = __uint_as_float(stv[1]) * (type == 0 ? 0.23570226039551584f * 1.4426950408889634f : 1.0f);
  st[1] = -__uint_as_float(stv[0]) * st[0];
  {
#pragma unroll
    for (int n = 0; n < NT; n++)
#pragma unroll
      for (int hf = 0; hf < 2; hf++) {
        if (T0 + n == 4 && hf == 1) continue;
        const int c0 = 32 * (T0 + n) + 16 * hf;
        const f32x4 ca = lds_f128(lds0 + Q_CT + (unsigned)(c0 + 8 * h) * 4u), cb = lds_f128(lds0 + Q_CT + (unsigned)(c0 + 8 * h) * 4u + 16);
        u32x4 o;
#pragma unroll
        for (int e = 0; e < 4; e++) {
          const float c0v = e < 2 ? ca[2 * e] : cb[2 * e - 4], c1v = e < 2 ? ca[2 * e + 1] : cb[2 * e - 3];
          o[e] = pk2(__builtin_fmaf(acc[n][8 * hf + 2 * e], st[0], c0v * st[1]), __builtin_fmaf(acc[n][8 * hf + 2 * e + 1], st[0], c1v * st[1]));
        }
        *(LAS u32x4*)(size_t)(lds0 + (unsigned)tl * 304u + (unsigned)(c0 + 8 * h) * 2u) = o;
      }
  }
  wait_lgkm0(); bar();
  ST(1, 6);
  if (w < 6) {
    const int t = w * 64 + lane, tk = t / 3, part = t - 3 * tk;
    const unsigned src = lds0 + (unsigned)tk * 304u + (unsigned)part * 16u;
    const unsigned keep = part < 2 ? 0xffffffffu : 0u, padv = part < 2 ? 0u : (type == 0 ? 0x3f800000u : 0x00003f80u);
    const unsigned dst = (unsigned)WS_QKV + ((unsigned)(type * 32 + (ct & 3) * 8) * S + (unsigned)rb * 128u) * 48u;
    u32x4 v[8];
#pragma unroll
    for (int hh = 0; hh < 8; hh++)
#pragma unroll
      for (int e = 0; e < 4; e++) v[hh][e] = *(const LAS unsigned*)(size_t)(src + hh * 36 + e * 4);
#pragma unroll
    for (int hh = 0; hh < 8; hh++) {
      v[hh][1] = (v[hh][1] & keep) | padv; v[hh][2] &= keep; v[hh][3] &= keep;
      st128s(v[hh], c.sws, (unsigned)t * 16u, dst + (unsigned)hh * (S * 48));
    }
  }
  wait_lgkm0();
  ST(1, 7);
}
DI void p1_qkv(Ctx& c, int rb, int ct) {
  if (c.w < 4) p1_body<3, 0>(c, rb, ct); else p1_body<2, 3>(c, rb, ct);
  publish(c, FL_Q + rb * 12 + ct);
}

constexpr int L_K = 0, L_V = 24576, L_WO = 49216;
DI void p2_attn(const Ctx& c, int head, int qb) {
  const int w = c.w, lane = c.lane; const unsigned lds0 = c.lds0;
  const unsigned ldsK = lds0 + L_K, ldsV = lds0 + L_V;
  if (w == 0) {
    const int hg = head >> 3, l = lane;
    const int ty = l < 2 ? 0 : (l < 6 ? 1 : 2), rbl = l < 2 ? 2 * qb + l : 2 * qb - 2 + ((l - 2) & 3);
    poll(c, FL_Q + rbl * 12 + ty * 4 + hg, l < 10 && rbl >= 0);
  }
  bar();
  ST(2, 1);
  const unsigned kbase = (unsigned)WS_QKV + (unsigned)((32 + head) * S + 256 * (qb - 1)) * 48u, vbase = (unsigned)WS_QKV + (unsigned)((64 + head) * S + 256 * (qb - 1)) * 48u;
  const unsigned vl = lane * 16;
#pragma unroll
  for (int hf = 0; hf < 2; hf++) {
    if (hf == 1 || qb > 0) {
#pragma unroll
      for (int j = 0; j < 3; j++) {
        const int pi = w * 3 + j;
        const unsigned pc = (unsigned)hf * 12288u + (unsigned)(pi % 12) * 1024;
        if (pi < 12) dma16s(ldsK + pc, vl, c.sws, kbase + pc); else dma16s(ldsV + pc, vl, c.sws, vbase + pc);
      }
    }
  }
  if (qb == 0) {
    const int tid = w * 64 + lane;
    if (tid < 256) {
      const u32x4 z = {0, 0, 0, 0};
      *(LAS u32x4*)(size_t)(ldsK + tid * 48) = z; *(LAS u32x4*)(size_t)(ldsK + tid * 48 + 16) = z; *(LAS u32x4*)(size_t)(ldsK + tid * 48 + 32) = u32x4{0, 0xf1493f80u, 0, 0};
      *(LAS u32x4*)(size_t)(ldsV + tid * 48) = z; *(LAS u32x4*)(size_t)(ldsV + tid * 48 + 16) = z; *(LAS u32x4*)(size_t)(ldsV + tid * 48 + 32) = z;
    }
  }
  const int qq = lane & 31, h = lane >> 5;
  const int q0 = 256 * qb + 32 * w;
  const unsigned qo = (unsigned)WS_QKV + (unsigned)(head * S + q0) * 48u;
  const u32x4 qf0 = ld128s(c.sws, (unsigned)qq * 48u + 16u * h, qo);
  u32x4 qf1 = ld128s(c.sws, (unsigned)qq * 48u + 32u, qo);
  if (h) qf1 = u32x4{0, 0, 0, 0};
  f32x16 cold, cdiag;
#pragma unroll
  for (int r = 0; r < 16; r++) {
    const int kk = 16 * (r >> 3) + 8 * h + (r & 7);
    cold[r] = kk > qq ? 0.f : -1e30f;
    cdiag[r] = kk <= qq ? 0.f : -1e30f;
  }
  const int key = swap23(lane & 31);
  unsigned ka0 = ldsK + (unsigned)(key + 32 * w) * 48 + 16 * h;
  unsigned ka1 = ldsK + (unsigned)(key + 32 * w) * 48 + 32;
  const int blk = (lane >> 4) & 1, q4 = (lane >> 2) & 3, pp = lane & 3;
  unsigned va = ldsV + (unsigned)(32 * w + 8 * h + q4) * 48 + 32 * blk + 8 * pp;
  asm volatile("" : "+v"(ka0), "+v"(ka1), "+v"(va));
  wait_vm<0>(); wait_lgkm0(); bar();
  ST(2, 2);
  const f32x16 zero = {};
  u32x4 k0[9], k1[9];
#pragma unroll
  for (int j = 0; j < 9; j++) { k0[j] = lds_u128(ka0 + j * 1536); k1[j] = lds_u128(ka1 + j * 1536); }
  float m = -1e30f;
  {
    f32x16 sA = mfma32(k1[0], qf1, mfma32(k0[0], qf0, cold)), sB;
#pragma unroll
    for (int j = 0; j < 9; j++) {
      f32x16& cur = (j & 1) ? sB : sA; f32x16& nxt = (j & 1) ? sA : sB;
      if (j + 1 < 9) nxt = mfma32(k1[j + 1], qf1, mfma32(k0[j + 1], qf0, j + 1 == 8 ? cdiag : zero));
#pragma unroll
      for (int r = 0; r < 16; r += 2) m = fmaxf(m, fmaxf(cur[r], cur[r + 1]));
      SB();
    }
  }
  { const auto pr = __builtin_amdgcn_permlane32_swap(__float_as_uint(m), __float_as_uint(m), false, false); m = fmaxf(__uint_as_float(pr[0]), __uint_as_float(pr[1])); }
  if (h == 0) qf1[1] = (qf1[1] & 0xffff0000u) | (pk2(-m, 0.f) & 0xffffu);
  ST(2, 3);
  f32x16 oacc = {};
  {
    f32x16 sA = mfma32(k1[0], qf1, mfma32(k0[0], qf0, cold)), sB;
#pragma unroll
    for (int j = 0; j < 9; j++) {
      f32x16& cur = (j & 1) ? sB : sA; f32x16& nxt = (j & 1) ? sA : sB;
      const unsigned vb = va + j * 1536;
      const u32x4 v0 = cat4(lds_tr(vb), lds_tr(vb + 4 * 48)), v1 = cat4(lds_tr(vb + 16 * 48), lds_tr(vb + 20 * 48));
      if (j + 1 < 9) nxt = mfma32(k1[j + 1], qf1, mfma32(k0[j + 1], qf0, j + 1 == 8 ? cdiag : zero));
      u32x4 p0, p1;
#pragma unroll
      for (int e = 0; e < 4; e++) p0[e] = pk2(__builtin_amdgcn_exp2f(cur[2 * e]), __builtin_amdgcn_exp2f(cur[2 * e + 1]));
      oacc = mfma32(v0, p0, oacc);
#pragma unroll
      for (int e = 0; e < 4; e++) p1[e] = pk2(__builtin_amdgcn_exp2f(cur[8 + 2 * e]), __builtin_amdgcn_exp2f(cur[8 + 2 * e + 1]));
      oacc = mfma32(v1, p1, oacc);
      SB();
    }
  }
  const float l = __shfl(oacc[10], qq);
  const float inv = 1.0f / l;
  const u32x4 om = {pk2(oacc[0] * inv, oacc[1] * inv), pk2(oacc[2] * inv, oacc[3] * inv), pk2(oacc[4] * inv, oacc[5] * inv), pk2(oacc[6] * inv, oacc[7] * inv)};
  st128s(om, c.sws, (unsigned)lane * 16u, (unsigned)WS_OP + (unsigned)((q0 >> 5) * 32 + head) * 1024u);
  __builtin_amdgcn_raw_buffer_store_b32(pk2(oacc[8] * inv, oacc[9] * inv), c.sws, h == 0 ? (unsigned)qq * 4u : 0x7ff00000u, (unsigned)WS_OL + (unsigned)(head * S + q0) * 4u, SC1);
  publish(c, FL_A + qb * 32 + head);
}

DI void p3_prefetch(const Ctx& c, int ct) {
  const int w = c.w, lane = c.lane;
  const srd_t sw = mk_srd(c.wo, D * D * 2);
  unsigned vw[3];
#pragma unroll
  for (int i = 0; i < 3; i++) { const int ch = 64 * i + lane; vw[i] = (unsigned)(ch / 12) * (D * 2) + (unsigned)(ch % 12) * 16u; }
#pragma unroll
  for (int gi = 0; gi < 5; gi++) {
    const int grp = w + 8 * gi;
    if (grp < 36) {
#pragma unroll
      for (int i = 0; i < 3; i++) dma16(c.lds0 + L_WO + (unsigned)(grp * 3 + i) * 1024u, vw[i], sw, (unsigned)grp * 16u * (D * 2) + (unsigned)ct * 192u);
    }
  }
}
DI void p3_wo(const Ctx& c, int rb, int ct) {
  const int w = c.w, lane = c.lane, rg = w & 1, kq = w >> 1; const unsigned lds0 = c.lds0;
  const int qt = rb * 2 + rg, tok = lane & 31, h = lane >> 5;
  const int row = qt * 32 + tok;
  const u32x4 xr0 = *(const u32x4*)(c.x + (size_t)row * D + ct * 96 + 16 * kq + 8 * h);
  const u32x4 xr1 = *(const u32x4*)(c.x + (size_t)row * D + ct * 96 + 16 * (kq < 2 ? kq + 4 : kq) + 8 * h);
  if (w == 0) poll(c, FL_A + (rb >> 2) * 32 + (lane & 31), lane < 32);
  bar();
  ST(3, 1);
  u32x4 of[9];
  {
    const unsigned so = (unsigned)WS_OP + (unsigned)(qt * 32 + kq * 9) * 1024u;
#pragma unroll
    for (int s = 0; s < 5; s++) of[s] = ld128s(c.sws, (unsigned)lane * 16u + s * 1024u, so);
    if (kq < 3) {
#pragma unroll
      for (int s = 5; s < 9; s++) of[s] = ld128s(c.sws, (unsigned)lane * 16u + s * 1024u, so);
    } else {
#pragma unroll
      for (int G = 0; G < 4; G++)
#pragma unroll
        for (int i = 0; i < 4; i++)
          of[5 + G][i] = __builtin_amdgcn_raw_buffer_load_b32(c.sws, (unsigned)((4 * h + i) * S + tok) * 4u, (unsigned)WS_OL + (unsigned)(8 * G * S + qt * 32) * 4u, SC1);
    }
  }
  wait_vm<0>(); bar();
  ST(3, 2);
  const int blk = (lane >> 4) & 1, q4 = (lane >> 2) & 3, pp = lane & 3;
  const unsigned colb = lds0 + L_WO + (unsigned)(16 * blk + 8 * (pp & 1) + 4 * (pp >> 1)) * 2u;
  f32x16 acc[3] = {};
#pragma unroll
  for (int s = 0; s < 9; s++) {
    unsigned r0, r1;
    if (s < 5 || kq < 3) { const int hd = kq * 9 + s; r0 = (unsigned)(18 * hd + 4 * h + q4); r1 = r0 + 8; }
    else { const int G = s - 5; r0 = (unsigned)(18 * (8 * G + 4 * h + (q4 >> 1)) + 16 + (q4 & 1)); r1 = r0 + 36; }
    const unsigned a0 = colb + r0 * 192u, a1 = colb + r1 * 192u;
#pragma unroll
    for (int n = 0; n < 3; n++) acc[n] = mfma32(cat4(lds_tr(a0 + n * 64), lds_tr(a1 + n * 64)), of[s], acc[n]);
  }
  ST(3, 3);
  wait_lgkm0(); bar();
  const unsigned xb = lds0 + L_WO + (unsigned)rg * 49152u + (unsigned)lane * 16u;
#pragma unroll
  for (int f = 0; f < 6; f++) {
    const int r8 = 8 * (f & 1);
    lds_w128(xb + (unsigned)(f * 4 + kq) * 2048u, f32x4{acc[f >> 1][r8], acc[f >> 1][r8 + 1], acc[f >> 1][r8 + 2], acc[f >> 1][r8 + 3]});
    lds_w128(xb + (unsigned)(f * 4 + kq) * 2048u + 1024u, f32x4{acc[f >> 1][r8 + 4], acc[f >> 1][r8 + 5], acc[f >> 1][r8 + 6], acc[f >> 1][r8 + 7]});
  }
  wait_lgkm0(); bar();
  float ssum = 0, ssq = 0;
  const int nf = kq < 2 ? 2 : 1;
  for (int i = 0; i < nf; i++) {
    const int f = kq + 4 * i;
    f32x4 a = {0, 0, 0, 0}, cc = {0, 0, 0, 0};
#pragma unroll
    for (int k = 0; k < 4; k++) { a += lds_f128(xb + (unsigned)(f * 4 + k) * 2048u); cc += lds_f128(xb + (unsigned)(f * 4 + k) * 2048u + 1024u); }
    const u32x4 xr = i ? xr1 : xr0;
    u32x4 o;
    o[0] = pk2(bflo(xr[0]) + a[0], bfhi(xr[0]) + a[1]); o[1] = pk2(bflo(xr[1]) + a[2], bfhi(xr[1]) + a[3]);
    o[2] = pk2(bflo(xr[2]) + cc[0], bfhi(xr[2]) + cc[1]); o[3] = pk2(bflo(xr[3]) + cc[2], bfhi(xr[3]) + cc[3]);
    st128s(o, c.sws, (unsigned)lane * 16u, (unsigned)WS_HP + (unsigned)(qt * 36 + ct * 6 + f) * 1024u);
#pragma unroll
    for (int e = 0; e < 4; e++) { ssum = dot2(o[e], 0x3f803f80u, ssum); ssq = dot2(o[e], o[e], ssq); }
  }
  { const auto a = __builtin_amdgcn_permlane32_swap(__float_as_uint(ssum), __float_as_uint(ssum), false, false); ssum = __uint_as_float(a[0]) + __uint_as_float(a[1]);
    const auto b2 = __builtin_amdgcn_permlane32_swap(__float_as_uint(ssq), __float_as_uint(ssq), false, false); ssq = __uint_as_float(b2[0]) + __uint_as_float(b2[1]); }
  const unsigned stb = lds0 + L_WO + 98304u;
  if (h == 0) *(LAS f32x2*)(size_t)(stb + (unsigned)(rg * 32 + tok) * 32u + kq * 8u) = f32x2{ssum, ssq};
  wait_lgkm0(); bar();
  if (w == 0) {
    const f32x4 a = lds_f128(stb + lane * 32u), cc = lds_f128(stb + lane * 32u + 16);
    const u32x2 v = {__float_as_uint((a[0] + a[2]) + (cc[0] + cc[2])), __float_as_uint((a[1] + a[3]) + (cc[1] + cc[3]))};
    __builtin_amdgcn_raw_buffer_store_b64(v, c.sws, (unsigned)lane * 8u, (unsigned)WS_ST2 + (unsigned)(rb * 6 + ct) * 512u, SC1);
  }
  publish(c, FL_H + rb * 6 + ct);
}

constexpr float GC0 = -2.0f * 0.7978845608028654f * 1.4426950408889634f, GC1 = GC0 * 0.044715f;
DI f32x2 gelu2(f32x2 x) {
  const f32x2 t = x * (GC1 * (x * x) + GC0);
  f32x2 e; e[0] = __builtin_amdgcn_exp2f(t[0]); e[1] = __builtin_amdgcn_exp2f(t[1]);
  const f32x2 d = e + 1.0f;
  f32x2 r; r[0] = __builtin_amdgcn_rcpf(d[0]); r[1] = __builtin_amdgcn_rcpf(d[1]);
  return x * r;
}
struct G2 { float x0, x1, t0, t1; };
DI void g2_s1(G2& g, float a0, float a1, float p0, float p1) {
  asm volatile("v_add_f32_e32 %0, %4, %6\n\tv_add_f32_e32 %1, %5, %7\n\tv_mul_f32_e32 %2, %0, %0\n\tv_mul_f32_e32 %3, %1, %1"
               : "=&v"(g.x0), "=&v"(g.x1), "=&v"(g.t0), "=&v"(g.t1) : "v"(a0), "v"(a1), "v"(p0), "v"(p1));
}
DI void g2_s2(G2& g, float vc1) {
  asm volatile("v_fmaak_f32 %0, %0, %4, 0xc0135761\n\tv_fmaak_f32 %1, %1, %4, 0xc0135761\n\tv_mul_f32_e32 %0, %2, %0\n\tv_mul_f32_e32 %1, %3, %1"
               : "+v"(g.t0), "+v"(g.t1) : "v"(g.x0), "v"(g.x1), "v"(vc1));
}
DI void g2_s3(G2& g) {
  asm volatile("v_exp_f32_e32 %0, %0\n\tv_exp_f32_e32 %1, %1\n\tv_add_f32_e32 %0, 1.0, %0\n\tv_add_f32_e32 %1, 1.0, %1" : "+v"(g.t0), "+v"(g.t1));
}
DI unsigned g2_s4(G2& g) {
  unsigned o;
  asm volatile("v_rcp_f32_e32 %1, %1\n\tv_rcp_f32_e32 %2, %2\n\tv_mul_f32_e32 %1, %3, %1\n\tv_mul_f32_e32 %2, %4, %2\n\tv_cvt_pk_bf16_f32 %0, %1, %2"
               : "=v"(o), "+v"(g.t0), "+v"(g.t1) : "v"(g.x0), "v"(g.x1));
  return o;
}

constexpr int F_SLOT = 36864, F1_XCH = 3 * F_SLOT, F1_UV = F1_XCH + 32768, F1_DUMP = F1_UV + 1152, F_LDS = F1_DUMP + 256;
#define FDMA dma16s
#define FDMAH dma16hs
#define ABL_F1ST 1
#define ABL_F1WAIT 1
#define ABL_W2T 1
#define F_ISSUE1(gbase, i, ring) do { if ((i) < 4) FDMA(lds0 + (ring) + (unsigned)w * 1024u + (i) * 8192u, vl, c.sws, (gbase) + (unsigned)w * 1024u + (i) * 8192u); \
                                      else FDMAH(lds0 + (ring) + 32768u + (unsigned)w * 512u, vl, c.sws, (gbase) + 32768u + (unsigned)w * 512u); } while (0)
DI void p4_prefetch(const Ctx& c, int fc, int slot) {
  const int w = c.w; const unsigned lds0 = c.lds0, vl = c.lane * 16;
#pragma unroll
  for (int i = 0; i < 5; i++) F_ISSUE1((unsigned)WS_W1P + (unsigned)(fc * 9 + slot) * F_SLOT, i, (unsigned)slot * F_SLOT);
}
DI void p45_ffn(const Ctx& c, int rb, int fc) {
  const int w = c.w, lane = c.lane, rg = w & 3, kh = w >> 2; const unsigned lds0 = c.lds0;
  const unsigned vl = lane * 16;
  const int ks = fc >> 2, ct = fc & 3;
  const unsigned w1g = (unsigned)WS_W1P + (unsigned)fc * 9 * F_SLOT, w2g = (unsigned)WS_W2P + (unsigned)(ks * 4 + ct) * 9 * F_SLOT;
  const int tok = lane & 31, h = lane >> 5;
  if (threadIdx.x < 288) *(LAS float*)(size_t)(lds0 + F1_UV + threadIdx.x * 4) = __uint_as_float((unsigned)c.b1[fc * 288 + threadIdx.x] << 16);
  if (w == 0) poll(c, lane < 12 ? FL_H + (2 * rb + lane / 6) * 6 + lane % 6 : FL_W2 + (ks * 4 + ct) * 4 + (lane & 3), lane < 16);
  wait_lgkm0(); bar();
  ST(4, 1);
  {
    const unsigned l = (unsigned)(w * 64 + lane);
    touch128(lds0 + L_DUMP, ABL_W2T && l < 324u ? l * 128u : 0x7ff00000u, c.sws, w2g + (unsigned)(c.bi & 7) * 41472u);
  }
  u32x2 stv[6];
#pragma unroll
  for (int e = 0; e < 6; e++)
    asm volatile("buffer_load_dwordx2 %0, %1, %2, %3 offen offset:%4 sc1" : "=&v"(stv[e]) : "v"((unsigned)tok * 8u), "s"(c.sws),
                 "s"((unsigned)WS_ST2 + (unsigned)((2 * rb + (rg >> 1)) * 6) * 512u + (unsigned)(rg & 1) * 256u), "i"(e * 512) : "memory");
  u32x4 xf[18];
  {
    const unsigned so = (unsigned)WS_HP + ((unsigned)(rb * 4 + rg) * 36u + (unsigned)kh * 18u) * 1024u;
#pragma unroll
    for (int s = 0; s < 18; s++)
      asm volatile("buffer_load_dwordx4 %0, %1, %2, %3 offen offset:%4 sc1" : "=&v"(xf[s]) : "v"(vl), "s"(c.sws), "s"(so + (s >> 2) * 4096u), "i"((s & 3) * 1024) : "memory");
  }
  float ra, rc;
  {
    asm volatile("s_waitcnt vmcnt(18)" : "+v"(stv[0]), "+v"(stv[1]), "+v"(stv[2]), "+v"(stv[3]), "+v"(stv[4]), "+v"(stv[5]) :: "memory");
    float s = 0, q = 0;
#pragma unroll
    for (int e = 0; e < 6; e++) { s += __uint_as_float(stv[e][0]); q += __uint_as_float(stv[e][1]); }
    const float mean = s * (1.0f / D), var = fmaxf(q * (1.0f / D) - mean * mean, 0.f), rstd = rsqrtf(var + LN_EPS);
    ra = rstd; rc = -mean * rstd;
  }
#pragma unroll
  for (int s = 0; s < 18; s++) {
    asm volatile("s_waitcnt vmcnt(%1)" : "+v"(xf[s]) : "i"(17 - s) : "memory");
#pragma unroll
    for (int e = 0; e < 4; e++) {
      const f32x2 v = f32x2{bflo(xf[s][e]), bfhi(xf[s][e])} * ra + rc;
      xf[s][e] = pk2(v[0], v[1]);
    }
    asm volatile("" : "+v"(xf[s]));
    SB();
  }
  const unsigned rd = lds0 + (unsigned)kh * 18432u + vl;
  const unsigned hvo = (unsigned)(tok >> 4) * 1024u + (unsigned)h * 256u + (unsigned)(tok & 15) * 16u;
  const unsigned hbase = (unsigned)WS_H1P + ((unsigned)(rb * 4 + rg) * 144u + (unsigned)fc * 9u) * 2048u;
  const unsigned bia = lds0 + F1_UV + (unsigned)h * 32u;
  bar();
  ST(4, 2);
  constexpr int PF = 3;
  u32x4 wf[PF];
#pragma unroll
  for (int i = 0; i < PF; i++) wf[i] = lds_u128(rd + i * 1024);
  const f32x16 zero = {};
  f32x16 binit;
#define F1_BIAS(ba_) do { const f32x4 b0_ = lds_f128(ba_), b1_ = lds_f128(ba_ + 16), b2_ = lds_f128(ba_ + 64), b3_ = lds_f128(ba_ + 80); \
    binit = f32x16{b0_[0], b0_[1], b0_[2], b0_[3], b1_[0], b1_[1], b1_[2], b1_[3], b2_[0], b2_[1], b2_[2], b2_[3], b3_[0], b3_[1], b3_[2], b3_[3]}; } while (0)
  F1_BIAS(bia + (unsigned)(1 - kh) * 128u);
  float vc1 = GC1; asm volatile("" : "+v"(vc1));
  unsigned r0 = 0, r1 = F_SLOT, r2 = 2 * F_SLOT;
  const unsigned xwr = lds0 + F1_XCH + (unsigned)(kh ^ 1) * 16384u + (unsigned)rg * 4096u + vl;
  const unsigned xrd = lds0 + F1_XCH + (unsigned)kh * 16384u + (unsigned)rg * 4096u + vl;
  f32x4 pp[4] = {};
#define F1_STAGE(q, G, SRC, R0, O) do { const int p_ = (q) >> 2, st_ = (q) & 3, r_ = (R0) + 2 * p_; \
      if (st_ == 0) g2_s1(G, SRC[r_], SRC[r_ + 1], pp[r_ >> 2][r_ & 3], pp[r_ >> 2][(r_ & 3) + 1]); \
      if (st_ == 1) g2_s2(G, vc1); if (st_ == 2) g2_s3(G); if (st_ == 3) O[p_] = g2_s4(G); } while (0)
#define F1_PERIOD(t, ACC, PREV, GELU, HALF, VMW, DG) do { \
    if (ABL_F1WAIT) wait_vm<VMW>(); \
    bar(); \
    const unsigned dg_ = (DG); \
    unsigned sb = rd + r0, sn = rd + r1; \
    asm volatile("" : "+v"(sb), "+v"(sn)); \
    u32x4 o; G2 g; \
    const int gt_ = ((t) - 2 - (HALF)) + kh;      \
    if ((GELU) && (HALF) == 0) { _Pragma("unroll") for (int e = 0; e < 4; e++) pp[e] = lds_f128(xrd + e * 1024); } \
    SB(); \
    _Pragma("unroll") for (int s = 0; s < 18; s++) { \
      const u32x4 cur = wf[s % PF]; \
      wf[s % PF] = lds_u128(s + PF < 18 ? sb + (s + PF) * 1024 : sn + (s + PF - 18) * 1024); \
      ACC = mfma32(cur, xf[s], s == 0 ? ((HALF) == 0 ? binit : zero) : ACC); \
      if (s % 2 == 0 && s / 2 < 5) F_ISSUE1(dg_, s / 2, r2); \
      if ((HALF) == 1 && s % 4 == 1 && s / 4 < 4) { const int e = s / 4; lds_w128(xwr + e * 1024, f32x4{accG[4 * e], accG[4 * e + 1], accG[4 * e + 2], accG[4 * e + 3]}); } \
      if (GELU) { \
        if (s >= 2 && s <= 17) F1_STAGE(s - 2, g, PREV, 8 * (HALF), o); \
        if (ABL_F1ST && s == 17) st128s(o, c.sws, hvo + (HALF) * 512u, hbase + (unsigned)gt_ * 2048u); \
      } \
      if ((HALF) == 1 && s == 12) {     \
        const int nt_ = (t) + 1 == 8 ? 8 : (t) + 1 + (1 - kh); \
        unsigned ba_ = bia + (unsigned)nt_ * 128u; asm volatile("" : "+v"(ba_)); \
        F1_BIAS(ba_); \
      } \
      SB(); \
    } \
    const unsigned rr_ = r0; r0 = r1; r1 = r2; r2 = rr_; \
  } while (0)
  {
    f32x16 accG, accP, accQ;
    F1_PERIOD(0, accG, accQ, false, 0, 0, w1g + 2u * F_SLOT);
    F1_PERIOD(1, accP, accQ, false, 1, 0, w1g + 3u * F_SLOT);
    F1_PERIOD(2, accG, accP, true, 0, 0, w1g + 4u * F_SLOT);
    F1_PERIOD(3, accQ, accP, true, 1, 1, w1g + 5u * F_SLOT);
    F1_PERIOD(4, accG, accQ, true, 0, 1, w1g + 6u * F_SLOT);
    F1_PERIOD(5, accP, accQ, true, 1, 1, w1g + 7u * F_SLOT);
    F1_PERIOD(6, accG, accP, true, 0, 1, w1g + 8u * F_SLOT);
    F1_PERIOD(7, accQ, accP, true, 1, 1, w2g);
    {
      wait_vm<1>(); bar();
      unsigned sb = rd + r0, sn = rd + r1;
      asm volatile("" : "+v"(sb), "+v"(sn));
      u32x4 o, o2; G2 g, g2;
      const int gt_ = 6 + kh;
#pragma unroll
      for (int e = 0; e < 4; e++) pp[e] = lds_f128(xrd + e * 1024);
      if (kh == 0) binit = zero;
      SB();
#pragma unroll
      for (int s = 0; s < 18; s++) {
        const u32x4 cur = wf[s % PF];
        wf[s % PF] = lds_u128(s + PF < 18 ? sb + (s + PF) * 1024 : sn + (s + PF - 18) * 1024);
        accG = mfma32(cur, xf[s], s == 0 ? binit : accG);
        if (s % 2 == 0 && s / 2 < 5) F_ISSUE1(w2g + F_SLOT, s / 2, r2);
        if (s >= 2 && s <= 17) { F1_STAGE(s - 2, g, accQ, 0, o); F1_STAGE(s - 2, g2, accQ, 8, o2); }
        if (s == 17) { st128s(o, c.sws, hvo, hbase + (unsigned)gt_ * 2048u); st128s(o2, c.sws, hvo + 512u, hbase + (unsigned)gt_ * 2048u); }
        SB();
      }
      const unsigned rr_ = r0; r0 = r1; r1 = r2; r2 = rr_;
      ST(4, 3);
      bar();
      lds_w128(xwr, kh ? f32x4{accG[0], accG[1], accG[2], accG[3]} : f32x4{accG[8], accG[9], accG[10], accG[11]});
      lds_w128(xwr + 1024, kh ? f32x4{accG[4], accG[5], accG[6], accG[7]} : f32x4{accG[12], accG[13], accG[14], accG[15]});
      wait_lgkm0(); bar();
      const f32x4 q0 = lds_f128(xrd), q1 = lds_f128(xrd + 1024);
      const f32x4 k0 = (kh ? f32x4{accG[8], accG[9], accG[10], accG[11]} : f32x4{accG[0], accG[1], accG[2], accG[3]}) + q0;
      const f32x4 k1 = (kh ? f32x4{accG[12], accG[13], accG[14], accG[15]} : f32x4{accG[4], accG[5], accG[6], accG[7]}) + q1;
      const f32x2 z0 = gelu2(f32x2{k0[0], k0[1]}), z1 = gelu2(f32x2{k0[2], k0[3]}), z2 = gelu2(f32x2{k1[0], k1[1]}), z3 = gelu2(f32x2{k1[2], k1[3]});
      o = u32x4{pk2(z0[0], z0[1]), pk2(z1[0], z1[1]), pk2(z2[0], z2[1]), pk2(z3[0], z3[1])};
      st128s(o, c.sws, hvo + (unsigned)kh * 512u, hbase + 8u * 2048u);
    }
  }
  publish(c, FL_H1 + rb * 16 + fc);
  ST(4, 4);
  ST(5, 1);
  const unsigned hoff0 = (unsigned)WS_H1P + ((unsigned)(rb * 4 + rg) * 144u + (unsigned)ks * 36u) * 2048u;
  u32x4 hA[4], hB[4], hC[4];
#define HLOAD1(dst, t, i) do { const int p_ = 4 * (t) + 2 * kh + ((i) >> 1) + 9 * ct; const unsigned so_ = hoff0 + (unsigned)(p_ >= 36 ? p_ - 36 : p_) * 2048u; \
    if (((i) & 1) == 0) asm volatile("buffer_load_dwordx4 %0, %1, %2, %3 offen sc1" : "=&v"(dst[i]) : "v"(vl), "s"(c.sws), "s"(so_) : "memory"); \
    else asm volatile("buffer_load_dwordx4 %0, %1, %2, %3 offen offset:1024 sc1" : "=&v"(dst[i]) : "v"(vl), "s"(c.sws), "s"(so_) : "memory"); } while (0)
#define HWAIT(N, dst) asm volatile("s_waitcnt vmcnt(" #N ")" : "+v"(dst[0]), "+v"(dst[1]), "+v"(dst[2]), "+v"(dst[3]) :: "memory")
  HLOAD1(hA, 0, 0); HLOAD1(hA, 0, 1); HLOAD1(hA, 0, 2); HLOAD1(hA, 0, 3);
  HLOAD1(hB, 1, 0); HLOAD1(hB, 1, 1); HLOAD1(hB, 1, 2); HLOAD1(hB, 1, 3);
  f32x4 acc[2][9];
#pragma unroll
  for (int a = 0; a < 2; a++)
#pragma unroll
    for (int n = 0; n < 9; n++) acc[a][n] = f32x4{0, 0, 0, 0};
#define F2_PERIOD(t, HC, HN, HM, VMW, MODE) do { \
    HWAIT(VMW, HC); \
    bar(); \
    unsigned sb = rd + r0, sn = rd + r1; \
    asm volatile("" : "+v"(sb), "+v"(sn)); \
    _Pragma("unroll") for (int s = 0; s < 18; s++) { \
      const u32x4 cur = wf[s % PF]; \
      wf[s % PF] = lds_u128(s + PF < 18 ? sb + (s + PF) * 1024 : sn + (s + PF - 18) * 1024); \
      const int k = s / 9, nt = s % 9; \
      acc[0][nt] = mfma16(cur, HC[2 * k], acc[0][nt]); \
      acc[1][nt] = mfma16(cur, HC[2 * k + 1], acc[1][nt]); \
      if ((MODE) == 3) { if (s == 0) { HLOAD1(HM, (t) + 1, 0); HLOAD1(HM, (t) + 1, 1); } if (s == 1) { HLOAD1(HM, (t) + 1, 2); HLOAD1(HM, (t) + 1, 3); } } \
      if ((MODE) != 1 && s % 2 == 0 && s / 2 < 5) F_ISSUE1(w2g + (unsigned)((t) + 2) * F_SLOT, s / 2, r2); \
      if ((MODE) == 0 || (MODE) == 3) { if (s == 10) HLOAD1(HN, (t) + 2, 0); if (s == 12) HLOAD1(HN, (t) + 2, 1); if (s == 14) HLOAD1(HN, (t) + 2, 2); if (s == 16) HLOAD1(HN, (t) + 2, 3); } \
      SB(); \
    } \
    const unsigned rr_ = r0; r0 = r1; r1 = r2; r2 = rr_; \
  } while (0)
  F2_PERIOD(0, hA, hC, hC, 4, 2);
  ST(5, 2);
  if (w == 0) poll(c, FL_H1 + rb * 16 + ks * 4 + (lane & 3), lane < 4);
  F2_PERIOD(1, hB, hA, hC, 0, 3);
  F2_PERIOD(2, hC, hB, hB, 4, 0);
  F2_PERIOD(3, hA, hC, hC, 4, 0);
#pragma unroll 1
  for (int t = 4; t < 7; t += 3) {
    F2_PERIOD(t, hB, hA, hA, 4, 0);
    F2_PERIOD(t + 1, hC, hB, hB, 4, 0);
    F2_PERIOD(t + 2, hA, hC, hC, 4, 0);
  }
  F2_PERIOD(7, hB, hA, hA, 4, 1);
  F2_PERIOD(8, hC, hB, hB, 0, 1);
  ST(5, 3);
  wait_lgkm0(); bar();
  const unsigned xb = lds0 + (unsigned)rg * 18432u + vl;
#pragma unroll
  for (int n = 0; n < 9; n++) lds_w128(xb + (unsigned)(kh ^ 1) * 9216u + n * 1024, kh ? acc[0][n] : acc[1][n]);
  wait_lgkm0(); bar();
  f32x4 o[9];
#pragma unroll
  for (int n = 0; n < 9; n++) o[n] = lds_f128(xb + kh * 9216u + n * 1024) + (kh ? acc[1][n] : acc[0][n]);
  const unsigned xg = (unsigned)WS_XCH + (unsigned)((rb * 4 + ct) * 16) * 9216u + (unsigned)kh * 4608u;
  if (rg != ks) {
    const unsigned d = xg + (unsigned)(rg * 4 + ks) * 9216u;
#pragma unroll
    for (int T = 0; T < 4; T++)
      st128s(u32x4{pk2(o[2 * T][0], o[2 * T][1]), pk2(o[2 * T][2], o[2 * T][3]), pk2(o[2 * T + 1][0], o[2 * T + 1][1]), pk2(o[2 * T + 1][2], o[2 * T + 1][3])}, c.sws, vl + T * 1024u, d);
    __builtin_amdgcn_raw_buffer_store_b64(u32x2{pk2(o[8][0], o[8][1]), pk2(o[8][2], o[8][3])}, c.sws, 4096u + lane * 8u, d, SC1);
  }
  publish(c, FL_P + (rb * 4 + ct) * 4 + ks);
  ST(5, 4);
  if (rg == ks) {
    const int tk = lane & 15, g = lane >> 4;
    const int row = rb * 128 + rg * 32 + kh * 16 + tk;
    u32x4 hv[4], bv[4]; u32x2 hv8, bv8;
#pragma unroll
    for (int T = 0; T < 4; T++) {
      const int col = ct * 144 + 32 * T + 8 * g;
      hv[T] = ld128s(c.sws, (unsigned)(((col >> 3) & 1) * 32 + kh * 16 + tk) * 16u + (unsigned)(col >> 4) * 1024u, (unsigned)WS_HP + (unsigned)(rb * 4 + rg) * 36u * 1024u);
      bv[T] = *(const u32x4*)(c.b2 + col);
    }
    {
      const int col = ct * 144 + 128 + 4 * g;
      hv8 = __builtin_amdgcn_raw_buffer_load_b64(c.sws, (unsigned)(((col >> 3) & 1) * 32 + kh * 16 + tk) * 16u + (unsigned)(col >> 4) * 1024u + (unsigned)(col & 7) * 2u,
                                                 (unsigned)WS_HP + (unsigned)(rb * 4 + rg) * 36u * 1024u, SC1);
      bv8 = *(const u32x2*)(c.b2 + col);
    }
    poll(c, FL_P + (rb * 4 + ct) * 4 + (lane & 3), lane < 4);
    asm volatile("" ::: "memory");
#pragma unroll
    for (int src = 0; src < 4; src++) {
      if (src != ks) {
        const unsigned d = xg + (unsigned)(rg * 4 + src) * 9216u;
        u32x4 pv[4];
#pragma unroll
        for (int T = 0; T < 4; T++) pv[T] = ld128s(c.sws, vl + T * 1024u, d);
        const u32x2 p8 = __builtin_amdgcn_raw_buffer_load_b64(c.sws, 4096u + lane * 8u, d, SC1);
#pragma unroll
        for (int T = 0; T < 4; T++) {
          o[2 * T] += f32x4{bflo(pv[T][0]), bfhi(pv[T][0]), bflo(pv[T][1]), bfhi(pv[T][1])};
          o[2 * T + 1] += f32x4{bflo(pv[T][2]), bfhi(pv[T][2]), bflo(pv[T][3]), bfhi(pv[T][3])};
        }
        o[8] += f32x4{bflo(p8[0]), bfhi(p8[0]), bflo(p8[1]), bfhi(p8[1])};
      }
    }
    u16* orow = c.out + (size_t)row * D + ct * 144;
#pragma unroll
    for (int T = 0; T < 4; T++) {
      u32x4 r;
      r[0] = pk2(bflo(hv[T][0]) + (o[2 * T][0] + bflo(bv[T][0])), bfhi(hv[T][0]) + (o[2 * T][1] + bfhi(bv[T][0])));
      r[1] = pk2(bflo(hv[T][1]) + (o[2 * T][2] + bflo(bv[T][1])), bfhi(hv[T][1]) + (o[2 * T][3] + bfhi(bv[T][1])));
      r[2] = pk2(bflo(hv[T][2]) + (o[2 * T + 1][0] + bflo(bv[T][2])), bfhi(hv[T][2]) + (o[2 * T + 1][1] + bfhi(bv[T][2])));
      r[3] = pk2(bflo(hv[T][3]) + (o[2 * T + 1][2] + bflo(bv[T][3])), bfhi(hv[T][3]) + (o[2 * T + 1][3] + bfhi(bv[T][3])));
      *(u32x4*)(orow + 32 * T + 8 * g) = r;
    }
    u32x2 r8;
    r8[0] = pk2(bflo(hv8[0]) + (o[8][0] + bflo(bv8[0])), bfhi(hv8[0]) + (o[8][1] + bfhi(bv8[0])));
    r8[1] = pk2(bflo(hv8[1]) + (o[8][2] + bflo(bv8[1])), bfhi(hv8[1]) + (o[8][3] + bfhi(bv8[1])));
    *(u32x2*)(orow + 128 + 4 * g) = r8;
  }
  ST(5, 5);
}

constexpr int M_LDS = 163840;
static_assert(L_WO + 110592 <= M_LDS && 4 * Q_SLOT <= M_LDS && F_LDS <= M_LDS, "LDS map");
__global__ KATTR void fused_block(const u16* __restrict__ x, const u16* __restrict__ wqkv, const u16* __restrict__ wo, const u16* __restrict__ w1,
                                  const u16* __restrict__ b1, const u16* __restrict__ w2, const u16* __restrict__ b2, u16* __restrict__ out, char* __restrict__ ws) {
  extern __shared__ __attribute__((aligned(16))) char smem[];
  Ctx c;
  c.x = x; c.wqkv = wqkv; c.wo = wo; c.w1 = w1; c.b1 = b1; c.w2 = w2; c.b2 = b2; c.out = out; c.ws = ws;
  c.sws = mk_srd(ws, WS_BYTES);
  c.lds0 = (unsigned)(size_t)smem;
  c.b = blockIdx.x; c.xcd = c.b & 7; c.bi = c.b >> 3;
  c.w = rfl(threadIdx.x >> 6); c.lane = threadIdx.x & 63;
  ST(1, 0);
  const int xcd = c.xcd, bi = c.bi;
  if (bi < 24) {
    p1_qkv(c, (xcd >> 1) * 4 + (bi & 3), (xcd & 1) * 6 + (bi >> 2));
  } else {
    const int j = bi - 24, fc = (xcd & 3) * 4 + (j >> 1), part = (xcd >> 2) * 2 + (j & 1);
    helper_stats(c, xcd * 8 + j);
    relayout_w1(c, fc, part);
    publish(c, FL_W1 + fc * 4 + part);
    {
      const srd_t sw2 = mk_srd(w2, D * F * 2);
      const int ks = xcd & 3, ct2 = j >> 1, row0 = (ks * 36 + part * 9) * 32;
#pragma unroll
      for (int i = 0; i < 2; i++) {
        const int l = (c.w * 2 + i) * 64 + c.lane, row = l / 3;
        touch128(c.lds0 + L_DUMP, l < 864 ? (unsigned)row * (D * 2) + (unsigned)(l - 3 * row) * 128u : 0x7ff00000u, sw2, (unsigned)row0 * (D * 2) + (((unsigned)ct2 * 288u) & ~127u));
      }
    }
  }
  ST(1, 4);
  if (bi < 24) p3_prefetch(c, bi >> 2);
  p2_attn(c, xcd * 4 + (bi & 3), bi >> 2);
  ST(2, 4);
  const int rb4 = (xcd >> 2) * 8 + (bi & 7), fc4 = (xcd & 3) * 4 + (bi >> 3);
  if (c.w == 1) poll(c, FL_W1 + fc4 * 4 + (c.lane & 3), c.lane < 4);
  if (c.b == 0 && c.w == 2) {
#pragma unroll
    for (int i = 0; i < 4; i++) poll(c, FL_D + i * 64 + c.lane, true);
    if (c.lane == 0) __hip_atomic_store((unsigned long long*)(ws + FL_N * 128), c.nonce, __ATOMIC_RELAXED, __HIP_MEMORY_SCOPE_AGENT);
  }
  bar();
  p4_prefetch(c, fc4, 0);
  {
    const unsigned l = (unsigned)(c.w * 64 + c.lane);
    touch128(c.lds0 + L_DUMP, l < 288u ? l * 128u : 0x7ff00000u, c.sws, (unsigned)WS_W1P + (unsigned)(fc4 * 9 + 1) * F_SLOT + (unsigned)(bi & 7) * 36864u);
  }
  if (bi < 24) {
    p3_wo(c, xcd * 4 + (bi & 3), bi >> 2);
  } else {
    const int j = bi - 24, ks = xcd & 3, ct = j >> 1, part = (xcd >> 2) * 2 + (j & 1);
    relayout_w2(c, c.lds0 + L_WO + (unsigned)c.w * 9216u, ks * 36 + part * 9 + c.w, ct);
    if (c.w == 0) relayout_w2(c, c.lds0 + L_WO, ks * 36 + part * 9 + 8, ct);
    publish(c, FL_W2 + (ks * 4 + ct) * 4 + part);
  }
  ST(3, 4);
  p4_prefetch(c, fc4, 1);
  p45_ffn(c, rb4, fc4);
  asm volatile(".Lfused_code_end:");
}

extern "C" void kernel_launch(void* const* d_in, const int* in_sizes, int n_in, void* d_out, int out_size, void* d_ws, size_t ws_size,
                              hipStream_t stream) {
  (void)hipFuncSetAttribute((const void*)fused_block, hipFuncAttributeMaxDynamicSharedMemorySize, M_LDS);
  fused_block<<<256, 512, M_LDS, stream>>>((const u16*)d_in[0], (const u16*)d_in[1], (const u16*)d_in[2], (const u16*)d_in[4], (const u16*)d_in[5],
                                           (const u16*)d_in[6], (const u16*)d_in[7], (u16*)d_out, (char*)d_ws);
}
```

```cpp
#include <hip/hip_runtime.h>
#include <stdint.h>

typedef unsigned short u16;
typedef __bf16 bf16x8 __attribute__((ext_vector_type(8)));
typedef __bf16 bf16x4 __attribute__((ext_vector_type(4)));
typedef __bf16 bf16x2 __attribute__((ext_vector_type(2)));
typedef float f32x2 __attribute__((ext_vector_type(2)));
typedef float f32x4 __attribute__((ext_vector_type(4)));
typedef float f32x16 __attribute__((ext_vector_type(16)));
typedef unsigned u32x2 __attribute__((ext_vector_type(2)));
typedef unsigned u32x4 __attribute__((ext_vector_type(4)));
#define QA_DOT 1
#define QA_DMA 1
#define QA_WAIT 1
#define LAS __attribute__((address_space(3)))
#define DI __device__ __forceinline__
#define SB() __builtin_amdgcn_sched_barrier(0)
#define KATTR __launch_bounds__(512, 2) __attribute__((amdgpu_waves_per_eu(2, 2)))

constexpr int S = 2048, D = 576, H = 32, DH = 18, F = 4608, NQKV = 1728;
constexpr float LN_EPS = 1e-5f;

constexpr size_t WS_FLG = 0;
constexpr int FL_N = 0, FL_D = 1, FL_Q = FL_D + 256, FL_W1 = FL_Q + 192, FL_A = FL_W1 + 64, FL_W2 = FL_A + 256, FL_H = FL_W2 + 64, FL_H1 = FL_H + 192,
              FL_P = FL_H1 + 256, FL_S1 = FL_P + 256, FL_END = FL_S1 + 64;
constexpr size_t WS_QKV = 262144;
static_assert(FL_END * 128 <= WS_QKV, "flag area");
constexpr size_t WS_QKV_SZ = (size_t)3 * H * S * 24 * 2;
constexpr size_t WS_OP = WS_QKV + WS_QKV_SZ;
constexpr size_t WS_OL = WS_OP + (size_t)64 * 32 * 1024;
constexpr size_t WS_HP = WS_OL + (size_t)H * S * 4;
constexpr size_t WS_ST2 = WS_HP + (size_t)S * D * 2;
constexpr size_t WS_W1P = WS_ST2 + (size_t)S * 48;
constexpr size_t WS_W2P = WS_W1P + (size_t)D * F * 2;
constexpr size_t WS_H1P = WS_W2P + (size_t)D * F * 2;
constexpr size_t WS_XCH = WS_H1P + (size_t)S * F * 2;
constexpr size_t WS_ST1 = WS_XCH + (size_t)16 * 4 * 4 * 4 * 2 * 4608;
constexpr size_t WS_CSQ = WS_ST1 + (size_t)S * 8;
constexpr size_t WS_END = WS_CSQ + (size_t)(NQKV + 64) * 4;
constexpr unsigned WS_BYTES = (unsigned)WS_END;

#define ST(kid, idx) asm volatile("; MARK " #kid " " #idx)

DI unsigned rfl(unsigned x) { return __builtin_amdgcn_readfirstlane(x); }
DI float bflo(unsigned w) { return __uint_as_float(w << 16); }
DI float bfhi(unsigned w) { return __uint_as_float(w & 0xffff0000u); }
DI unsigned pk2(float lo, float hi) { f32x2 v = {lo, hi}; bf16x2 b = __builtin_convertvector(v, bf16x2); return __builtin_bit_cast(unsigned, b); }
DI float dot2(unsigned a, unsigned b, float c) { return __builtin_amdgcn_fdot2_f32_bf16(__builtin_bit_cast(bf16x2, a), __builtin_bit_cast(bf16x2, b), c, false); }

typedef __amdgpu_buffer_rsrc_t srd_t;
DI srd_t mk_srd(const void* p, unsigned bytes) { return __builtin_amdgcn_make_buffer_rsrc((void*)p, (short)0, (int)bytes, 0x00020000); }
DI void dma16(unsigned ldsaddr, unsigned voff, srd_t srd, unsigned soff) {
  asm volatile("s_mov_b32 m0, %0\n\ts_nop 0\n\tbuffer_load_dwordx4 %1, %2, %3 offen lds" :: "s"(ldsaddr), "v"(voff), "s"(srd), "s"(soff) : "memory");
}
DI void dma16h(unsigned ldsaddr, unsigned voff, srd_t srd, unsigned soff) {
  asm volatile("s_mov_b32 m0, %0\n\ts_mov_b32 exec_hi, 0\n\tbuffer_load_dwordx4 %1, %2, %3 offen lds\n\ts_mov_b32 exec_hi, -1" :: "s"(ldsaddr), "v"(voff), "s"(srd), "s"(soff) : "memory");
}
DI void dma16q(unsigned ldsaddr, unsigned voff, srd_t srd, unsigned soff) {
  asm volatile("s_mov_b32 m0, %0\n\ts_mov_b64 exec, 0xffff\n\tbuffer_load_dwordx4 %1, %2, %3 offen lds\n\ts_mov_b64 exec, -1" :: "s"(ldsaddr), "v"(voff), "s"(srd), "s"(soff) : "memory");
}
DI void touch128(unsigned ldsdump, unsigned voff, srd_t srd, unsigned soff) {
  asm volatile("s_mov_b32 m0, %0\n\ts_nop 0\n\tbuffer_load_dword %1, %2, %3 offen lds" :: "s"(ldsdump), "v"(voff), "s"(srd), "s"(soff) : "memory");
}
template <int N> DI void wait_vm() { asm volatile("s_waitcnt vmcnt(%0)" :: "i"(N) : "memory"); }
DI void wait_lgkm0() { asm volatile("s_waitcnt lgkmcnt(0)" ::: "memory"); }
DI void bar() { asm volatile("" ::: "memory"); __builtin_amdgcn_s_barrier(); asm volatile("" ::: "memory"); }
DI u32x4 lds_u128(unsigned addr) { return *(const LAS u32x4*)(size_t)addr; }
DI f32x4 lds_f128(unsigned addr) { return *(const LAS f32x4*)(size_t)addr; }
DI void lds_w128(unsigned addr, f32x4 v) { *(LAS f32x4*)(size_t)addr = v; }
DI bf16x8 asbf(u32x4 v) { return __builtin_bit_cast(bf16x8, v); }
DI u32x2 lds_tr(unsigned addr) { return __builtin_bit_cast(u32x2, __builtin_amdgcn_ds_read_tr16_b64_v4bf16((LAS bf16x4*)(size_t)addr)); }
DI u32x4 cat4(u32x2 a, u32x2 b) { return u32x4{a[0], a[1], b[0], b[1]}; }
DI f32x4 mfma16(u32x4 a, u32x4 b, f32x4 c) { return __builtin_amdgcn_mfma_f32_16x16x32_bf16(asbf(a), asbf(b), c, 0, 0, 0); }
DI f32x16 mfma32(u32x4 a, u32x4 b, f32x16 c) { return __builtin_amdgcn_mfma_f32_32x32x16_bf16(asbf(a), asbf(b), c, 0, 0, 0); }
DI int swap23(int i) { return (i & 19) | ((i & 4) << 1) | ((i & 8) >> 1); }

constexpr int SC1 = 16;
DI void dma16s(unsigned ldsaddr, unsigned voff, srd_t srd, unsigned soff) {
  asm volatile("s_mov_b32 m0, %0\n\ts_nop 0\n\tbuffer_load_dwordx4 %1, %2, %3 offen sc1 lds" :: "s"(ldsaddr), "v"(voff), "s"(srd), "s"(soff) : "memory");
}
DI void dma16hs(unsigned ldsaddr, unsigned voff, srd_t srd, unsigned soff) {
  asm volatile("s_mov_b32 m0, %0\n\ts_mov_b32 exec_hi, 0\n\tbuffer_load_dwordx4 %1, %2, %3 offen sc1 lds\n\ts_mov_b32 exec_hi, -1" :: "s"(ldsaddr), "v"(voff), "s"(srd), "s"(soff) : "memory");
}
DI void st128s(u32x4 v, srd_t srd, unsigned voff, unsigned soff) {
  asm volatile("buffer_store_dwordx4 %0, %1, %2, %3 offen sc1\n\ts_nop 1" :: "v"(v), "v"(voff), "s"(srd), "s"(soff) : "memory");
}
DI u32x4 ld128s(srd_t srd, unsigned voff, unsigned soff) { return __builtin_amdgcn_raw_buffer_load_b128(srd, voff, soff, SC1); }

struct Ctx {
  const u16 *x, *wqkv, *wo, *w1, *b1, *w2, *b2;
  u16* out;
  char* ws;
  srd_t sws;
  unsigned lds0;
  unsigned long long nonce;
  int b, xcd, bi, w, lane;
};
DI void publish(const Ctx& c, int flag) {
  wait_vm<0>();
  bar();
  if (threadIdx.x == 0) __hip_atomic_store((unsigned long long*)(c.ws + (size_t)flag * 128), c.nonce, __ATOMIC_RELAXED, __HIP_MEMORY_SCOPE_AGENT);
}
DI void poll(const Ctx& c, int flag, bool active) {
  const unsigned long long* p = (const unsigned long long*)(c.ws + (size_t)flag * 128);
  const unsigned long long t0 = __builtin_amdgcn_s_memrealtime();
  for (;;) {
    const unsigned long long v = active ? __hip_atomic_load(p, __ATOMIC_RELAXED, __HIP_MEMORY_SCOPE_AGENT) : c.nonce;
    if (__all(v == c.nonce)) break;
    if (__builtin_amdgcn_s_memrealtime() - t0 > 2000000ull) break;
    __builtin_amdgcn_s_sleep(1);
  }
}

#define ENT_TLB 0
#define ENT_CODE 1
constexpr int L_DUMP = 163840 - 256;
DI u32x2 entry_load(const Ctx& c) {
  u32x2 nv;
  asm volatile("buffer_load_dwordx2 %0, %1, %2, 0 offen sc1" : "=&v"(nv) : "v"((unsigned)(FL_N * 128)), "s"(c.sws) : "memory");
  return nv;
}
DI void entry_prefetch(const Ctx& c) {
  unsigned long long pc; unsigned len;
  asm volatile("s_getpc_b64 %0\n.Lcp%=:\n\ts_mov_b32 %1, .Lfused_code_end-.Lcp%=" : "=s"(pc), "=s"(len));
  const srd_t sc = mk_srd((const void*)pc, len);
  touch128(c.lds0 + L_DUMP, ENT_CODE ? (unsigned)(c.w * 64 + c.lane) * 128u : 0x7ff00000u, sc, 0u);
  touch128(c.lds0 + L_DUMP, ENT_CODE ? (unsigned)(c.w * 64 + c.lane) * 128u : 0x7ff00000u, sc, 65536u);
  const srd_t st = c.w == 1 ? mk_srd(c.out, S * D * 2) : c.sws;
  touch128(c.lds0 + L_DUMP, !ENT_TLB ? 0x7ff00000u : c.w == 0 ? (c.lane < 24 ? ((unsigned)c.bi * 24u + (unsigned)c.lane) * 65536u : 0x7ff00000u) : (c.w == 1 ? (unsigned)c.lane * 65536u : 0x7ff00000u), st, 0u);
}
DI void entry_nonce(Ctx& c, u32x2 nv) {
  asm volatile("" : "+v"(nv));
  c.nonce = (((unsigned long long)rfl(nv[1]) << 32) | rfl(nv[0])) + 1ull;
  if (threadIdx.x == 0) __hip_atomic_store((unsigned long long*)(c.ws + (size_t)(FL_D + c.b) * 128), c.nonce, __ATOMIC_RELAXED, __HIP_MEMORY_SCOPE_AGENT);
}

DI void helper_stats(Ctx& c, int hb) {
  const int w = c.w, lane = c.lane;
  const srd_t sx = mk_srd(c.x, S * D * 2), sw = mk_srd(c.wqkv, hb < 27 ? D * NQKV * 2 : 0);
  const int tid = w * 64 + lane, ch = tid & 7, rg = tid >> 3;
  u32x4 a[4], b2[4], v9[9];
#pragma unroll
  for (int r = 0; r < 4; r++) {
    const unsigned so = (unsigned)(hb * 32 + w * 4 + r) * (D * 2);
    a[r] = __builtin_amdgcn_raw_buffer_load_b128(sx, (unsigned)lane * 16u, so, 0);
    b2[r] = __builtin_amdgcn_raw_buffer_load_b128(sx, lane < 8 ? 1024u + (unsigned)lane * 16u : 0x7ff00000u, so, 0);
  }
#pragma unroll
  for (int i = 0; i < 9; i++) v9[i] = __builtin_amdgcn_raw_buffer_load_b128(sw, (unsigned)(rg * 9 + i) * (NQKV * 2) + (unsigned)ch * 16u, (unsigned)hb * 128u, 0);
  const u32x2 nv = entry_load(c);
  entry_prefetch(c);
  float sm[4], sq[4];
#pragma unroll
  for (int r = 0; r < 4; r++) {
    sm[r] = 0; sq[r] = 0;
#pragma unroll
    for (int e = 0; e < 4; e++) { sm[r] = dot2(a[r][e], 0x3f803f80u, sm[r]); sq[r] = dot2(a[r][e], a[r][e], sq[r]); sm[r] = dot2(b2[r][e], 0x3f803f80u, sm[r]); sq[r] = dot2(b2[r][e], b2[r][e], sq[r]); }
  }
#pragma unroll
  for (int m = 32; m >= 1; m >>= 1)
#pragma unroll
    for (int r = 0; r < 4; r++) { sm[r] += __shfl_xor(sm[r], m); sq[r] += __shfl_xor(sq[r], m); }
  const int r = lane & 3;
  const float s1 = r == 0 ? sm[0] : r == 1 ? sm[1] : r == 2 ? sm[2] : sm[3], s2 = r == 0 ? sq[0] : r == 1 ? sq[1] : r == 2 ? sq[2] : sq[3];
  const float mean = s1 * (1.0f / D), var = fmaxf(s2 * (1.0f / D) - mean * mean, 0.f);
  const u32x2 v = {__float_as_uint(mean), __float_as_uint(rsqrtf(var + LN_EPS))};
  __builtin_amdgcn_raw_buffer_store_b64(v, c.sws, lane < 4 ? (unsigned)lane * 8u : 0x7ff00000u, (unsigned)WS_ST1 + (unsigned)(hb * 32 + w * 4) * 8u, SC1);
  float s8[8] = {0, 0, 0, 0, 0, 0, 0, 0};
#pragma unroll
  for (int i = 0; i < 9; i++)
#pragma unroll
    for (int e = 0; e < 4; e++) { s8[2 * e] += bflo(v9[i][e]); s8[2 * e + 1] += bfhi(v9[i][e]); }
#pragma unroll
  for (int m = 8; m <= 32; m <<= 1)
#pragma unroll
    for (int e = 0; e < 8; e++) s8[e] += __shfl_xor(s8[e], m);
  if (lane < 8) { lds_w128(c.lds0 + (unsigned)(w * 64 + lane * 8) * 4u, f32x4{s8[0], s8[1], s8[2], s8[3]}); lds_w128(c.lds0 + (unsigned)(w * 64 + lane * 8) * 4u + 16, f32x4{s8[4], s8[5], s8[6], s8[7]}); }
  wait_lgkm0(); bar();
  if (w == 0) {
    float t = 0;
#pragma unroll
    for (int k = 0; k < 8; k++) t += *(const LAS float*)(size_t)(c.lds0 + (unsigned)(k * 64 + lane) * 4u);
    __builtin_amdgcn_raw_buffer_store_b32(__float_as_uint(t), c.sws, hb < 27 ? (unsigned)lane * 4u : 0x7ff00000u, (unsigned)WS_CSQ + (unsigned)hb * 256u, SC1);
  }
  wait_lgkm0();
  wait_vm<3>();
  entry_nonce(c, nv);
  publish(c, FL_S1 + hb);
}

DI void relayout_w1(const Ctx& c, int fc, int part) {
  const int w = c.w, lane = c.lane; const unsigned lds0 = c.lds0;
  const srd_t sw = mk_srd(c.w1, D * F * 2);
  for (int pc = w; pc < 81; pc += 8) {
    const int ch = pc * 64 + lane, row = ch / 36, cc = ch % 36;
    dma16(lds0 + (unsigned)pc * 1024u, (unsigned)row * (F * 2) + (unsigned)cc * 16u, sw, (unsigned)part * 144u * (F * 2) + (unsigned)fc * 576u);
  }
  wait_vm<0>(); bar();
  const int h = lane >> 5, blk = (lane >> 4) & 1, q = (lane >> 2) & 3, p = lane & 3;
  const unsigned ra = lds0 + (unsigned)(8 * h + q) * 576u + (unsigned)(16 * blk + 8 * (p & 1) + 4 * (p >> 1)) * 2u;
  for (int fr = w; fr < 81; fr += 8) {
    const int sl = fr / 9, tile = fr % 9, s = part * 9 + sl, kh = s / 18;
    const unsigned a = ra + (unsigned)sl * 16u * 576u + (unsigned)tile * 64u;
    const u32x4 v = cat4(lds_tr(a), lds_tr(a + 4 * 576));
    const int slot = tile == 8 ? 8 : (kh == 0 ? (tile ^ 1) : tile);
    st128s(v, c.sws, (unsigned)lane * 16u, (unsigned)WS_W1P + ((unsigned)(fc * 9 + slot) * 36u + (unsigned)s) * 1024u);
  }
}
DI void relayout_w2(const Ctx& c, unsigned ldsw, int kappa, int ct) {
  const int lane = c.lane;
  const srd_t sw = mk_srd(c.w2, D * F * 2);
#pragma unroll
  for (int pc = 0; pc < 9; pc++) {
    const int ch = pc * 64 + lane, row = ch / 18, cc = ch % 18;
    dma16(ldsw + (unsigned)pc * 1024u, (unsigned)row * (D * 2) + (unsigned)cc * 16u, sw, (unsigned)kappa * 32u * (D * 2) + (unsigned)ct * 288u);
  }
  wait_vm<0>();
  const int g = lane >> 4, q = (lane >> 2) & 3, p = lane & 3;
  const unsigned ra = ldsw + (unsigned)(8 * g + q) * 288u;
  const int ks = kappa / 36, kst = (kappa % 36 + 36 - 9 * ct) % 36;
#pragma unroll
  for (int nt = 0; nt < 9; nt++) {
    const unsigned col = nt < 8 ? (unsigned)(32 * (nt >> 1) + 8 * p + 4 * (nt & 1)) * 2u : (unsigned)(128 + 4 * p) * 2u;
    const u32x4 v = cat4(lds_tr(ra + col), lds_tr(ra + col + 4 * 288));
    st128s(v, c.sws, (unsigned)lane * 16u, (unsigned)WS_W2P + (((unsigned)(ks * 4 + ct) * 36u + (unsigned)kst) * 9u + (unsigned)nt) * 1024u);
  }
  wait_lgkm0();
}

constexpr int Q_SLOT = 36864;
constexpr int Q_CT = 4 * Q_SLOT;
template <int NT, int T0>
DI void p1_body(Ctx& c, int rb, int ct) {
  const int w = c.w, lane = c.lane, rg = w & 3; const unsigned lds0 = c.lds0;
  const srd_t sx = mk_srd(c.x, S * D * 2), sw = mk_srd(c.wqkv, D * NQKV * 2);
  const unsigned vx = (unsigned)(lane >> 3) * (D * 2) + (unsigned)(((lane & 7) ^ ((4 * (w & 1) + (lane >> 4)) & 7)) * 16);
  unsigned vw[3];
#pragma unroll
  for (int i = 0; i < 3; i++) {
    const int ch = i < 2 ? 64 * (w + 8 * i) + lane : 64 * (16 + (w >> 1)) + 32 * (w & 1) + (lane & 31);
    const int row = ch / 20, cc = ch % 20;
    vw[i] = (unsigned)row * (NQKV * 2) + (unsigned)cc * 16u;
  }
  const unsigned xg = (unsigned)rb * 128u * (D * 2) + (unsigned)w * 8u * (D * 2), wg = (unsigned)ct * 288u;
#define Q_ISSUE1(t, i, ring) do { \
    if ((i) < 2) dma16(lds0 + (ring) + (unsigned)(w + 8 * (i)) * 1024u, vx, sx, xg + (unsigned)(i) * 64u * (D * 2) + (unsigned)(t) * 128u); \
    else if ((i) < 4) dma16(lds0 + (ring) + 16384u + (unsigned)(w + 8 * ((i) - 2)) * 1024u, vw[(i) & 1], sw, wg + (unsigned)(t) * 64u * (NQKV * 2)); \
    else dma16h(lds0 + (ring) + 16384u + 16384u + (unsigned)w * 512u, vw[2], sw, wg + (unsigned)(t) * 64u * (NQKV * 2)); } while (0)
  const u32x2 nv = entry_load(c);
#pragma unroll
  for (int t = 0; t < 3; t++)
#pragma unroll
    for (int i = 0; i < 5; i++) Q_ISSUE1(t, i, (unsigned)t * Q_SLOT);
  const int tok = lane & 31, h = lane >> 5, blk = (lane >> 4) & 1, q4 = (lane >> 2) & 3, pp = lane & 3;
  unsigned xj[4], wj[4];
#pragma unroll
  for (int j = 0; j < 4; j++) {
    const int ks = (j + rg) & 3;
    xj[j] = lds0 + (unsigned)(rg * 32 + tok) * 128u + (unsigned)(((2 * ks + h) ^ ((tok >> 1) & 7)) * 16);
    wj[j] = lds0 + 16384u + (unsigned)(16 * ks + 8 * h + q4) * 320u + (unsigned)(32 * T0 + 16 * blk + 8 * (pp & 1) + 4 * (pp >> 1)) * 2u;
  }
  f32x16 acc[NT];
#pragma unroll
  for (int n = 0; n < NT; n++) acc[n] = f32x16{};
  ST(1, 1);
  entry_prefetch(c);
  wait_vm<13>(); bar();
  entry_nonce(c, nv);
  ST(1, 2);
  u32x4 wf[4][NT], xf[4];
#pragma unroll
  for (int j = 0; j < 2; j++) {
    xf[j] = lds_u128(xj[j]);
#pragma unroll
    for (int n = 0; n < NT; n++) wf[j][n] = cat4(lds_tr(wj[j] + n * 64), lds_tr(wj[j] + n * 64 + 4 * 320));
  }
  unsigned r0 = 0, r1 = Q_SLOT, r2 = 2 * Q_SLOT, r3 = 3 * Q_SLOT;
#define Q_PERIOD(t, ISSUE, VMW, EXTRA) do { \
    wait_vm<VMW>(); bar(); \
    EXTRA; \
    unsigned wa_[4], xa_[4]; \
    _Pragma("unroll") for (int j = 0; j < 4; j++) { wa_[j] = wj[(j + 2) & 3] + (j < 2 ? r0 : r1); xa_[j] = xj[(j + 2) & 3] + (j < 2 ? r0 : r1); asm volatile("" : "+v"(wa_[j]), "+v"(xa_[j])); } \
    _Pragma("unroll") for (int j = 0; j < 4; j++) { \
      const u32x4 xc = xf[j]; \
      xf[(j + 2) & 3] = lds_u128(xa_[j]); \
      _Pragma("unroll") for (int n = 0; n < NT; n++) { \
        const u32x4 cur = wf[j][n]; \
        wf[(j + 2) & 3][n] = cat4(lds_tr(wa_[j] + n * 64), lds_tr(wa_[j] + n * 64 + 4 * 320)); \
        acc[n] = mfma32(cur, xc, acc[n]); \
        const int s_ = j * NT + n; \
        if (QA_DMA && (ISSUE) && (NT == 3 ? (s_ % 2 == 0 && s_ < 10) : (s_ != 2 && s_ != 4 && s_ < 7))) Q_ISSUE1((t) + 3, NT == 3 ? s_ / 2 : (s_ < 2 ? s_ : (s_ == 3 ? 2 : s_ - 2)), r3); \
        SB(); \
      } \
    } \
    const unsigned rr_ = r0; r0 = r1; r1 = r2; r2 = r3; r3 = rr_; \
  } while (0)
#pragma unroll 1
  for (int t = 0; t < 6; t++) Q_PERIOD(t, true, 5, (void)0);
  if (w == 7) { const int g0 = (144 * ct) >> 6; poll(c, FL_S1 + (lane < 4 ? 4 * rb + lane : min(g0 + (lane & 3), 26)), lane < 8); }
  const int tl = rg * 32 + tok, type = ct >> 2;
  u32x2 stv; u32x4 csv;
  Q_PERIOD(6, false, 5, (
    { asm volatile("buffer_load_dwordx2 %0, %1, %2, %3 offen sc1" : "=&v"(stv) : "v"((unsigned)tl * 8u), "s"(c.sws), "s"((unsigned)WS_ST1 + (unsigned)rb * 1024u) : "memory");
      asm volatile("buffer_load_dwordx4 %0, %1, %2, %3 offen sc1" : "=&v"(csv) : "v"((unsigned)min(lane, 39) * 16u), "s"(c.sws), "s"((unsigned)WS_CSQ + (unsigned)ct * 576u) : "memory"); }));
  Q_PERIOD(7, false, 0, (
    { asm volatile("" : "+v"(stv), "+v"(csv));
      if (w == 0 && lane < 40) *(LAS u32x4*)(size_t)(lds0 + Q_CT + (unsigned)lane * 16u) = csv; }));
  Q_PERIOD(8, false, 0, (void)0);
  ST(1, 3);
  wait_lgkm0(); bar();
  ST(1, 5);
  f32x2 st;
  st[0] = __uint_as_float(stv[1]) * (type == 0 ? 0.23570226039551584f * 1.4426950408889634f : 1.0f);
  st[1] = -__uint_as_float(stv[0]) * st[0];
  {
#pragma unroll
    for (int n = 0; n < NT; n++)
#pragma unroll
      for (int hf = 0; hf < 2; hf++) {
        if (T0 + n == 4 && hf == 1) continue;
        const int c0 = 32 * (T0 + n) + 16 * hf;
        const f32x4 ca = lds_f128(lds0 + Q_CT + (unsigned)(c0 + 8 * h) * 4u), cb = lds_f128(lds0 + Q_CT + (unsigned)(c0 + 8 * h) * 4u + 16);
        u32x4 o;
#pragma unroll
        for (int e = 0; e < 4; e++) {
          const float c0v = e < 2 ? ca[2 * e] : cb[2 * e - 4], c1v = e < 2 ? ca[2 * e + 1] : cb[2 * e - 3];
          o[e] = pk2(__builtin_fmaf(acc[n][8 * hf + 2 * e], st[0], c0v * st[1]), __builtin_fmaf(acc[n][8 * hf + 2 * e + 1], st[0], c1v * st[1]));
        }
        *(LAS u32x4*)(size_t)(lds0 + (unsigned)tl * 304u + (unsigned)(c0 + 8 * h) * 2u) = o;
      }
  }
  wait_lgkm0(); bar();
  ST(1, 6);
  if (w < 6) {
    const int t = w * 64 + lane, tk = t / 3, part = t - 3 * tk;
    const unsigned src = lds0 + (unsigned)tk * 304u + (unsigned)part * 16u;
    const unsigned keep = part < 2 ? 0xffffffffu : 0u, padv = part < 2 ? 0u : (type == 0 ? 0x3f800000u : 0x00003f80u);
    const unsigned dst = (unsigned)WS_QKV + ((unsigned)(type * 32 + (ct & 3) * 8) * S + (unsigned)rb * 128u) * 48u;
    u32x4 v[8];
#pragma unroll
    for (int hh = 0; hh < 8; hh++)
#pragma unroll
      for (int e = 0; e < 4; e++) v[hh][e] = *(const LAS unsigned*)(size_t)(src + hh * 36 + e * 4);
#pragma unroll
    for (int hh = 0; hh < 8; hh++) {
      v[hh][1] = (v[hh][1] & keep) | padv; v[hh][2] &= keep; v[hh][3] &= keep;
      st128s(v[hh], c.sws, (unsigned)t * 16u, dst + (unsigned)hh * (S * 48));
    }
  }
  wait_lgkm0();
  ST(1, 7);
}
DI void p1_qkv(Ctx& c, int rb, int ct) {
  if (c.w < 4) p1_body<3, 0>(c, rb, ct); else p1_body<2, 3>(c, rb, ct);
  publish(c, FL_Q + rb * 12 + ct);
}

constexpr int L_K = 0, L_V = 24576, L_WO = 49216;
DI void p2_attn(const Ctx& c, int head, int qb) {
  const int w = c.w, lane = c.lane; const unsigned lds0 = c.lds0;
  const unsigned ldsK = lds0 + L_K, ldsV = lds0 + L_V;
  if (w == 0) {
    const int hg = head >> 3, l = lane;
    const int ty = l < 2 ? 0 : (l < 6 ? 1 : 2), rbl = l < 2 ? 2 * qb + l : 2 * qb - 2 + ((l - 2) & 3);
    poll(c, FL_Q + rbl * 12 + ty * 4 + hg, l < 10 && rbl >= 0);
  }
  bar();
  ST(2, 1);
  const unsigned kbase = (unsigned)WS_QKV + (unsigned)((32 + head) * S + 256 * (qb - 1)) * 48u, vbase = (unsigned)WS_QKV + (unsigned)((64 + head) * S + 256 * (qb - 1)) * 48u;
  const unsigned vl = lane * 16;
#pragma unroll
  for (int hf = 0; hf < 2; hf++) {
    if (hf == 1 || qb > 0) {
#pragma unroll
      for (int j = 0; j < 3; j++) {
        const int pi = w * 3 + j;
        const unsigned pc = (unsigned)hf * 12288u + (unsigned)(pi % 12) * 1024;
        if (pi < 12) dma16s(ldsK + pc, vl, c.sws, kbase + pc); else dma16s(ldsV + pc, vl, c.sws, vbase + pc);
      }
    }
  }
  if (qb == 0) {
    const int tid = w * 64 + lane;
    if (tid < 256) {
      const u32x4 z = {0, 0, 0, 0};
      *(LAS u32x4*)(size_t)(ldsK + tid * 48) = z; *(LAS u32x4*)(size_t)(ldsK + tid * 48 + 16) = z; *(LAS u32x4*)(size_t)(ldsK + tid * 48 + 32) = u32x4{0, 0xf1493f80u, 0, 0};
      *(LAS u32x4*)(size_t)(ldsV + tid * 48) = z; *(LAS u32x4*)(size_t)(ldsV + tid * 48 + 16) = z; *(LAS u32x4*)(size_t)(ldsV + tid * 48 + 32) = z;
    }
  }
  const int qq = lane & 31, h = lane >> 5;
  const int q0 = 256 * qb + 32 * w;
  const unsigned qo = (unsigned)WS_QKV + (unsigned)(head * S + q0) * 48u;
  const u32x4 qf0 = ld128s(c.sws, (unsigned)qq * 48u + 16u * h, qo);
  u32x4 qf1 = ld128s(c.sws, (unsigned)qq * 48u + 32u, qo);
  if (h) qf1 = u32x4{0, 0, 0, 0};
  f32x16 cold, cdiag;
#pragma unroll
  for (int r = 0; r < 16; r++) {
    const int kk = 16 * (r >> 3) + 8 * h + (r & 7);
    cold[r] = kk > qq ? 0.f : -1e30f;
    cdiag[r] = kk <= qq ? 0.f : -1e30f;
  }
  const int key = swap23(lane & 31);
  unsigned ka0 = ldsK + (unsigned)(key + 32 * w) * 48 + 16 * h;
  unsigned ka1 = ldsK + (unsigned)(key + 32 * w) * 48 + 32;
  const int blk = (lane >> 4) & 1, q4 = (lane >> 2) & 3, pp = lane & 3;
  unsigned va = ldsV + (unsigned)(32 * w + 8 * h + q4) * 48 + 32 * blk + 8 * pp;
  asm volatile("" : "+v"(ka0), "+v"(ka1), "+v"(va));
  wait_vm<0>(); wait_lgkm0(); bar();
  ST(2, 2);
  const f32x16 zero = {};
  u32x4 k0[9], k1[9];
#pragma unroll
  for (int j = 0; j < 9; j++) { k0[j] = lds_u128(ka0 + j * 1536); k1[j] = lds_u128(ka1 + j * 1536); }
  float m = -1e30f;
  {
    f32x16 sA = mfma32(k1[0], qf1, mfma32(k0[0], qf0, cold)), sB;
#pragma unroll
    for (int j = 0; j < 9; j++) {
      f32x16& cur = (j & 1) ? sB : sA; f32x16& nxt = (j & 1) ? sA : sB;
      if (j + 1 < 9) nxt = mfma32(k1[j + 1], qf1, mfma32(k0[j + 1], qf0, j + 1 == 8 ? cdiag : zero));
#pragma unroll
      for (int r = 0; r < 16; r += 2) m = fmaxf(m, fmaxf(cur[r], cur[r + 1]));
      SB();
    }
  }
  { const auto pr = __builtin_amdgcn_permlane32_swap(__float_as_uint(m), __float_as_uint(m), false, false); m = fmaxf(__uint_as_float(pr[0]), __uint_as_float(pr[1])); }
  if (h == 0) qf1[1] = (qf1[1] & 0xffff0000u) | (pk2(-m, 0.f) & 0xffffu);
  ST(2, 3);
  f32x16 oacc = {};
  {
    f32x16 sA = mfma32(k1[0], qf1, mfma32(k0[0], qf0, cold)), sB;
#pragma unroll
    for (int j = 0; j < 9; j++) {
      f32x16& cur = (j & 1) ? sB : sA; f32x16& nxt = (j & 1) ? sA : sB;
      const unsigned vb = va + j * 1536;
      const u32x4 v0 = cat4(lds_tr(vb), lds_tr(vb + 4 * 48)), v1 = cat4(lds_tr(vb + 16 * 48), lds_tr(vb + 20 * 48));
      if (j + 1 < 9) nxt = mfma32(k1[j + 1], qf1, mfma32(k0[j + 1], qf0, j + 1 == 8 ? cdiag : zero));
      u32x4 p0, p1;
#pragma unroll
      for (int e = 0; e < 4; e++) p0[e] = pk2(__builtin_amdgcn_exp2f(cur[2 * e]), __builtin_amdgcn_exp2f(cur[2 * e + 1]));
      oacc = mfma32(v0, p0, oacc);
#pragma unroll
      for (int e = 0; e < 4; e++) p1[e] = pk2(__builtin_amdgcn_exp2f(cur[8 + 2 * e]), __builtin_amdgcn_exp2f(cur[8 + 2 * e + 1]));
      oacc = mfma32(v1, p1, oacc);
      SB();
    }
  }
  const float l = __shfl(oacc[10], qq);
  const float inv = 1.0f / l;
  const u32x4 om = {pk2(oacc[0] * inv, oacc[1] * inv), pk2(oacc[2] * inv, oacc[3] * inv), pk2(oacc[4] * inv, oacc[5] * inv), pk2(oacc[6] * inv, oacc[7] * inv)};
  st128s(om, c.sws, (unsigned)lane * 16u, (unsigned)WS_OP + (unsigned)((q0 >> 5) * 32 + head) * 1024u);
  __builtin_amdgcn_raw_buffer_store_b32(pk2(oacc[8] * inv, oacc[9] * inv), c.sws, h == 0 ? (unsigned)qq * 4u : 0x7ff00000u, (unsigned)WS_OL + (unsigned)(head * S + q0) * 4u, SC1);
  publish(c, FL_A + qb * 32 + head);
}

DI void p3_prefetch(const Ctx& c, int ct) {
  const int w = c.w, lane = c.lane;
  const srd_t sw = mk_srd(c.wo, D * D * 2);
  unsigned vw[3];
#pragma unroll
  for (int i = 0; i < 3; i++) { const int ch = 64 * i + lane; vw[i] = (unsigned)(ch / 12) * (D * 2) + (unsigned)(ch % 12) * 16u; }
#pragma unroll
  for (int gi = 0; gi < 5; gi++) {
    const int grp = w + 8 * gi;
    if (grp < 36) {
#pragma unroll
      for (int i = 0; i < 3; i++) dma16(c.lds0 + L_WO + (unsigned)(grp * 3 + i) * 1024u, vw[i], sw, (unsigned)grp * 16u * (D * 2) + (unsigned)ct * 192u);
    }
  }
}
DI void p3_wo(const Ctx& c, int rb, int ct) {
  const int w = c.w, lane = c.lane, rg = w & 1, kq = w >> 1; const unsigned lds0 = c.lds0;
  const int qt = rb * 2 + rg, tok = lane & 31, h = lane >> 5;
  const int row = qt * 32 + tok;
  const u32x4 xr0 = *(const u32x4*)(c.x + (size_t)row * D + ct * 96 + 16 * kq + 8 * h);
  const u32x4 xr1 = *(const u32x4*)(c.x + (size_t)row * D + ct * 96 + 16 * (kq < 2 ? kq + 4 : kq) + 8 * h);
  if (w == 0) poll(c, FL_A + (rb >> 2) * 32 + (lane & 31), lane < 32);
  bar();
  ST(3, 1);
  u32x4 of[9];
  {
    const unsigned so = (unsigned)WS_OP + (unsigned)(qt * 32 + kq * 9) * 1024u;
#pragma unroll
    for (int s = 0; s < 5; s++) of[s] = ld128s(c.sws, (unsigned)lane * 16u + s * 1024u, so);
    if (kq < 3) {
#pragma unroll
      for (int s = 5; s < 9; s++) of[s] = ld128s(c.sws, (unsigned)lane * 16u + s * 1024u, so);
    } else {
#pragma unroll
      for (int G = 0; G < 4; G++)
#pragma unroll
        for (int i = 0; i < 4; i++)
          of[5 + G][i] = __builtin_amdgcn_raw_buffer_load_b32(c.sws, (unsigned)((4 * h + i) * S + tok) * 4u, (unsigned)WS_OL + (unsigned)(8 * G * S + qt * 32) * 4u, SC1);
    }
  }
  wait_vm<0>(); bar();
  ST(3, 2);
  const int blk = (lane >> 4) & 1, q4 = (lane >> 2) & 3, pp = lane & 3;
  const unsigned colb = lds0 + L_WO + (unsigned)(16 * blk + 8 * (pp & 1) + 4 * (pp >> 1)) * 2u;
  f32x16 acc[3] = {};
#pragma unroll
  for (int s = 0; s < 9; s++) {
    unsigned r0, r1;
    if (s < 5 || kq < 3) { const int hd = kq * 9 + s; r0 = (unsigned)(18 * hd + 4 * h + q4); r1 = r0 + 8; }
    else { const int G = s - 5; r0 = (unsigned)(18 * (8 * G + 4 * h + (q4 >> 1)) + 16 + (q4 & 1)); r1 = r0 + 36; }
    const unsigned a0 = colb + r0 * 192u, a1 = colb + r1 * 192u;
#pragma unroll
    for (int n = 0; n < 3; n++) acc[n] = mfma32(cat4(lds_tr(a0 + n * 64), lds_tr(a1 + n * 64)), of[s], acc[n]);
  }
  ST(3, 3);
  wait_lgkm0(); bar();
  const unsigned xb = lds0 + L_WO + (unsigned)rg * 49152u + (unsigned)lane * 16u;
#pragma unroll
  for (int f = 0; f < 6; f++) {
    const int r8 = 8 * (f & 1);
    lds_w128(xb + (unsigned)(f * 4 + kq) * 2048u, f32x4{acc[f >> 1][r8], acc[f >> 1][r8 + 1], acc[f >> 1][r8 + 2], acc[f >> 1][r8 + 3]});
    lds_w128(xb + (unsigned)(f * 4 + kq) * 2048u + 1024u, f32x4{acc[f >> 1][r8 + 4], acc[f >> 1][r8 + 5], acc[f >> 1][r8 + 6], acc[f >> 1][r8 + 7]});
  }
  wait_lgkm0(); bar();
  float ssum = 0, ssq = 0;
  const int nf = kq < 2 ? 2 : 1;
  for (int i = 0; i < nf; i++) {
    const int f = kq + 4 * i;
    f32x4 a = {0, 0, 0, 0}, cc = {0, 0, 0, 0};
#pragma unroll
    for (int k = 0; k < 4; k++) { a += lds_f128(xb + (unsigned)(f * 4 + k) * 2048u); cc += lds_f128(xb + (unsigned)(f * 4 + k) * 2048u + 1024u); }
    const u32x4 xr = i ? xr1 : xr0;
    u32x4 o;
    o[0] = pk2(bflo(xr[0]) + a[0], bfhi(xr[0]) + a[1]); o[1] = pk2(bflo(xr[1]) + a[2], bfhi(xr[1]) + a[3]);
    o[2] = pk2(bflo(xr[2]) + cc[0], bfhi(xr[2]) + cc[1]); o[3] = pk2(bflo(xr[3]) + cc[2], bfhi(xr[3]) + cc[3]);
    st128s(o, c.sws, (unsigned)lane * 16u, (unsigned)WS_HP + (unsigned)(qt * 36 + ct * 6 + f) * 1024u);
#pragma unroll
    for (int e = 0; e < 4; e++) { ssum = dot2(o[e], 0x3f803f80u, ssum); ssq = dot2(o[e], o[e], ssq); }
  }
  { const auto a = __builtin_amdgcn_permlane32_swap(__float_as_uint(ssum), __float_as_uint(ssum), false, false); ssum = __uint_as_float(a[0]) + __uint_as_float(a[1]);
    const auto b2 = __builtin_amdgcn_permlane32_swap(__float_as_uint(ssq), __float_as_uint(ssq), false, false); ssq = __uint_as_float(b2[0]) + __uint_as_float(b2[1]); }
  const unsigned stb = lds0 + L_WO + 98304u;
  if (h == 0) *(LAS f32x2*)(size_t)(stb + (unsigned)(rg * 32 + tok) * 32u + kq * 8u) = f32x2{ssum, ssq};
  wait_lgkm0(); bar();
  if (w == 0) {
    const f32x4 a = lds_f128(stb + lane * 32u), cc = lds_f128(stb + lane * 32u + 16);
    const u32x2 v = {__float_as_uint((a[0] + a[2]) + (cc[0] + cc[2])), __float_as_uint((a[1] + a[3]) + (cc[1] + cc[3]))};
    __builtin_amdgcn_raw_buffer_store_b64(v, c.sws, (unsigned)lane * 8u, (unsigned)WS_ST2 + (unsigned)(rb * 6 + ct) * 512u, SC1);
  }
  publish(c, FL_H + rb * 6 + ct);
}

constexpr float GC0 = -2.0f * 0.7978845608028654f * 1.4426950408889634f, GC1 = GC0 * 0.044715f;
DI f32x2 gelu2(f32x2 x) {
  const f32x2 t = x * (GC1 * (x * x) + GC0);
  f32x2 e; e[0] = __builtin_amdgcn_exp2f(t[0]); e[1] = __builtin_amdgcn_exp2f(t[1]);
  const f32x2 d = e + 1.0f;
  f32x2 r; r[0] = __builtin_amdgcn_rcpf(d[0]); r[1] = __builtin_amdgcn_rcpf(d[1]);
  return x * r;
}
struct G2 { float x0, x1, t0, t1; };
DI void g2_s1(G2& g, float a0, float a1, float p0, float p1) {
  asm volatile("v_add_f32_e32 %0, %4, %6\n\tv_add_f32_e32 %1, %5, %7\n\tv_mul_f32_e32 %2, %0, %0\n\tv_mul_f32_e32 %3, %1, %1"
               : "=&v"(g.x0), "=&v"(g.x1), "=&v"(g.t0), "=&v"(g.t1) : "v"(a0), "v"(a1), "v"(p0), "v"(p1));
}
DI void g2_s2(G2& g, float vc1) {
  asm volatile("v_fmaak_f32 %0, %0, %4, 0xc0135761\n\tv_fmaak_f32 %1, %1, %4, 0xc0135761\n\tv_mul_f32_e32 %0, %2, %0\n\tv_mul_f32_e32 %1, %3, %1"
               : "+v"(g.t0), "+v"(g.t1) : "v"(g.x0), "v"(g.x1), "v"(vc1));
}
DI void g2_s3(G2& g) {
  asm volatile("v_exp_f32_e32 %0, %0\n\tv_exp_f32_e32 %1, %1\n\tv_add_f32_e32 %0, 1.0, %0\n\tv_add_f32_e32 %1, 1.0, %1" : "+v"(g.t0), "+v"(g.t1));
}
DI unsigned g2_s4(G2& g) {
  unsigned o;
  asm volatile("v_rcp_f32_e32 %1, %1\n\tv_rcp_f32_e32 %2, %2\n\tv_mul_f32_e32 %1, %3, %1\n\tv_mul_f32_e32 %2, %4, %2\n\tv_cvt_pk_bf16_f32 %0, %1, %2"
               : "=v"(o), "+v"(g.t0), "+v"(g.t1) : "v"(g.x0), "v"(g.x1));
  return o;
}

constexpr int F_SLOT = 36864, F1_XCH = 3 * F_SLOT, F1_UV = F1_XCH + 32768, F1_DUMP = F1_UV + 1152, F_LDS = F1_DUMP + 256;
#define FDMA dma16s
#define FDMAH dma16hs
#define ABL_F1DMA 1
#define ABL_F1XW 1
#define ABL_F1XR 1
#define ABL_F1BI 1
#define ABL_F1GELU 1
#define F1STAG 1
#define F1GOFF 0
#define F1GO (KH * F1GOFF)
#define F2STAG 1
#define ABL_HALF 1
#define ABL_F1ST 1
#define ABL_F1WAIT 1
#define ABL_W2T 1
#define F_ISSUE1(gbase, i, ring) do { if ((i) < 4) FDMA(lds0 + (ring) + (unsigned)w * 1024u + (i) * 8192u, vl, c.sws, (gbase) + (unsigned)w * 1024u + (i) * 8192u); \
                                      else if (ABL_HALF == 1) FDMAH(lds0 + (ring) + 32768u + (unsigned)w * 512u, vl, c.sws, (gbase) + 32768u + (unsigned)w * 512u); \
                                      else if (ABL_HALF == 2) FDMA(lds0 + (ring) + 32768u + (unsigned)(w & 3) * 1024u, vl, c.sws, (gbase) + 32768u + (unsigned)(w & 3) * 1024u); } while (0)
DI void p4_prefetch(const Ctx& c, int fc, int slot) {
  const int w = c.w; const unsigned lds0 = c.lds0, vl = c.lane * 16;
#pragma unroll
  for (int i = 0; i < 5; i++) F_ISSUE1((unsigned)WS_W1P + (unsigned)(fc * 9 + slot) * F_SLOT, i, (unsigned)slot * F_SLOT);
}
template <int KH>
DI void p45_ffn(const Ctx& c, int rb, int fc) {
  constexpr int kh = KH;
  const int w = c.w, lane = c.lane, rg = w & 3; const unsigned lds0 = c.lds0;
  const unsigned vl = lane * 16;
  const int ks = fc >> 2, ct = fc & 3;
  const unsigned w1g = (unsigned)WS_W1P + (unsigned)fc * 9 * F_SLOT, w2g = (unsigned)WS_W2P + (unsigned)(ks * 4 + ct) * 9 * F_SLOT;
  const int tok = lane & 31, h = lane >> 5;
  if (threadIdx.x < 288) *(LAS float*)(size_t)(lds0 + F1_UV + threadIdx.x * 4) = __uint_as_float((unsigned)c.b1[fc * 288 + threadIdx.x] << 16);
  if (w == 0) poll(c, lane < 12 ? FL_H + (2 * rb + lane / 6) * 6 + lane % 6 : FL_W2 + (ks * 4 + ct) * 4 + (lane & 3), lane < 16);
  wait_lgkm0(); bar();
  ST(4, 1);
  u32x2 stv[6];
#pragma unroll
  for (int e = 0; e < 6; e++)
    asm volatile("buffer_load_dwordx2 %0, %1, %2, %3 offen offset:%4 sc1" : "=&v"(stv[e]) : "v"((unsigned)tok * 8u), "s"(c.sws),
                 "s"((unsigned)WS_ST2 + (unsigned)((2 * rb + (rg >> 1)) * 6) * 512u + (unsigned)(rg & 1) * 256u), "i"(e * 512) : "memory");
  u32x4 xf[18];
  {
    const unsigned so = (unsigned)WS_HP + ((unsigned)(rb * 4 + rg) * 36u + (unsigned)kh * 18u) * 1024u;
#pragma unroll
    for (int s = 0; s < 18; s++)
      asm volatile("buffer_load_dwordx4 %0, %1, %2, %3 offen offset:%4 sc1" : "=&v"(xf[s]) : "v"(vl), "s"(c.sws), "s"(so + (s >> 2) * 4096u), "i"((s & 3) * 1024) : "memory");
  }
  float ra, rc;
  {
    asm volatile("s_waitcnt vmcnt(18)" : "+v"(stv[0]), "+v"(stv[1]), "+v"(stv[2]), "+v"(stv[3]), "+v"(stv[4]), "+v"(stv[5]) :: "memory");
    float s = 0, q = 0;
#pragma unroll
    for (int e = 0; e < 6; e++) { s += __uint_as_float(stv[e][0]); q += __uint_as_float(stv[e][1]); }
    const float mean = s * (1.0f / D), var = fmaxf(q * (1.0f / D) - mean * mean, 0.f), rstd = rsqrtf(var + LN_EPS);
    ra = rstd; rc = -mean * rstd;
  }
#pragma unroll
  for (int s = 0; s < 18; s++) {
    asm volatile("s_waitcnt vmcnt(%1)" : "+v"(xf[s]) : "i"(17 - s) : "memory");
#pragma unroll
    for (int e = 0; e < 4; e++) {
      const f32x2 v = f32x2{bflo(xf[s][e]), bfhi(xf[s][e])} * ra + rc;
      xf[s][e] = pk2(v[0], v[1]);
    }
    asm volatile("" : "+v"(xf[s]));
    SB();
  }
  const unsigned rd = lds0 + (unsigned)kh * 18432u + vl;
  const unsigned hvo = (unsigned)(tok >> 4) * 1024u + (unsigned)h * 256u + (unsigned)(tok & 15) * 16u;
  const unsigned hbase = (unsigned)WS_H1P + ((unsigned)(rb * 4 + rg) * 144u + (unsigned)fc * 9u) * 2048u;
  const unsigned bia = lds0 + F1_UV + (unsigned)h * 32u;
  bar();
  ST(4, 2);
  {
    const unsigned l = (unsigned)(w * 64 + lane);
    touch128(lds0 + L_DUMP, ABL_W2T && l < 324u ? l * 128u : 0x7ff00000u, c.sws, w2g + (unsigned)(c.bi & 7) * 41472u);
  }

  constexpr int PF = 3;
  u32x4 wf[PF];
#pragma unroll
  for (int i = 0; i < PF; i++) wf[i] = lds_u128(rd + i * 1024);
  const f32x16 zero = {};
  f32x16 binit;
#define F1_BIAS(ba_) do { const f32x4 b0_ = lds_f128(ba_), b1_ = lds_f128(ba_ + 16), b2_ = lds_f128(ba_ + 64), b3_ = lds_f128(ba_ + 80); \
    binit = f32x16{b0_[0], b0_[1], b0_[2], b0_[3], b1_[0], b1_[1], b1_[2], b1_[3], b2_[0], b2_[1], b2_[2], b2_[3], b3_[0], b3_[1], b3_[2], b3_[3]}; } while (0)
  F1_BIAS(bia + (unsigned)(1 - kh) * 128u);
  float vc1 = GC1; asm volatile("" : "+v"(vc1));
  unsigned r0 = 0, r1 = F_SLOT, r2 = 2 * F_SLOT;
  const unsigned xwr = lds0 + F1_XCH + (unsigned)(kh ^ 1) * 16384u + (unsigned)rg * 4096u + vl;
  const unsigned xrd = lds0 + F1_XCH + (unsigned)kh * 16384u + (unsigned)rg * 4096u + vl;
  f32x4 pp[4] = {};
#define F1_STAGE(q, G, SRC, R0, O) do { const int p_ = (q) >> 2, st_ = (q) & 3, r_ = (R0) + 2 * p_; \
      if (st_ == 0) g2_s1(G, SRC[r_], SRC[r_ + 1], pp[r_ >> 2][r_ & 3], pp[r_ >> 2][(r_ & 3) + 1]); \
      if (st_ == 1) g2_s2(G, vc1); if (st_ == 2) g2_s3(G); if (st_ == 3) O[p_] = g2_s4(G); } while (0)
#define F1_PERIOD(t, ACC, PREV, GELU, HALF, VMW, DG) do { \
    if (ABL_F1WAIT) wait_vm<VMW>(); \
    bar(); \
    const unsigned dg_ = (DG); \
    unsigned sb = rd + r0, sn = rd + r1; \
    asm volatile("" : "+v"(sb), "+v"(sn)); \
    u32x4 o; G2 g; \
    const int gt_ = ((t) - 2 - (HALF)) + kh;      \
    if (ABL_F1XR && (GELU) && (HALF) == 0) { _Pragma("unroll") for (int e = 0; e < 4; e++) pp[e] = lds_f128(xrd + e * 1024); } \
    SB(); \
    _Pragma("unroll") for (int s = 0; s < 18; s++) { \
      const u32x4 cur = wf[s % PF]; \
      wf[s % PF] = lds_u128(s + PF < 18 ? sb + (s + PF) * 1024 : sn + (s + PF - 18) * 1024); \
      ACC = mfma32(cur, xf[s], s == 0 ? ((HALF) == 0 ? binit : zero) : ACC); \
      if (ABL_F1DMA && s % 2 == (KH & F1STAG) && s / 2 < 5) F_ISSUE1(dg_, s / 2, r2); \
      if (ABL_F1XW && (HALF) == 1 && s % 4 == 1 && s / 4 < 4) { const int e = s / 4; lds_w128(xwr + e * 1024, f32x4{accG[4 * e], accG[4 * e + 1], accG[4 * e + 2], accG[4 * e + 3]}); } \
      if (ABL_F1GELU && (GELU)) { \
        if (s >= 2 - F1GO && s <= 17 - F1GO) F1_STAGE(s - 2 + F1GO, g, PREV, 8 * (HALF), o); \
        if (ABL_F1ST && s == 17) st128s(o, c.sws, hvo + (HALF) * 512u, hbase + (unsigned)gt_ * 2048u); \
      } \
      if (ABL_F1BI && (HALF) == 1 && s == 12) {     \
        const int nt_ = (t) + 1 == 8 ? 8 : (t) + 1 + (1 - kh); \
        unsigned ba_ = bia + (unsigned)nt_ * 128u; asm volatile("" : "+v"(ba_)); \
        F1_BIAS(ba_); \
      } \
      SB(); \
    } \
    asm volatile("" :: "v"(ACC)); \
    const unsigned rr_ = r0; r0 = r1; r1 = r2; r2 = rr_; \
  } while (0)
  const unsigned hoff0 = (unsigned)WS_H1P + ((unsigned)(rb * 4 + rg) * 144u + (unsigned)ks * 36u) * 2048u;
  u32x4 hA[4], hB[4], hC[4];
#define HLOAD1(dst, t, i) do { const int p_ = 4 * (t) + 2 * kh + ((i) >> 1) + 9 * ct; const unsigned so_ = hoff0 + (unsigned)(p_ >= 36 ? p_ - 36 : p_) * 2048u; \
    if (((i) & 1) == 0) asm volatile("buffer_load_dwordx4 %0, %1, %2, %3 offen sc1" : "=&v"(dst[i]) : "v"(vl), "s"(c.sws), "s"(so_) : "memory"); \
    else asm volatile("buffer_load_dwordx4 %0, %1, %2, %3 offen offset:1024 sc1" : "=&v"(dst[i]) : "v"(vl), "s"(c.sws), "s"(so_) : "memory"); } while (0)
#define HWAIT(N, dst) asm volatile("s_waitcnt vmcnt(" #N ")" : "+v"(dst[0]), "+v"(dst[1]), "+v"(dst[2]), "+v"(dst[3]) :: "memory")
  {
    f32x16 accG, accP, accQ;
    F1_PERIOD(0, accG, accQ, false, 0, 1, w1g + 2u * F_SLOT);
    F1_PERIOD(1, accP, accQ, false, 1, 0, w1g + 3u * F_SLOT);
    F1_PERIOD(2, accG, accP, true, 0, 0, w1g + 4u * F_SLOT);
    F1_PERIOD(3, accQ, accP, true, 1, 1, w1g + 5u * F_SLOT);
    F1_PERIOD(4, accG, accQ, true, 0, 1, w1g + 6u * F_SLOT);
    F1_PERIOD(5, accP, accQ, true, 1, 1, w1g + 7u * F_SLOT);
    F1_PERIOD(6, accG, accP, true, 0, 1, w1g + 8u * F_SLOT);
    F1_PERIOD(7, accQ, accP, true, 1, 1, w2g);
    {
      wait_vm<1>(); bar();
      unsigned sb = rd + r0, sn = rd + r1;
      asm volatile("" : "+v"(sb), "+v"(sn));
      u32x4 o, o2; G2 g, g2;
      const int gt_ = 6 + kh;
#pragma unroll
      for (int e = 0; e < 4; e++) pp[e] = lds_f128(xrd + e * 1024);
      if (kh == 0) binit = zero;
      SB();
#pragma unroll
      for (int s = 0; s < 18; s++) {
        const u32x4 cur = wf[s % PF];
        wf[s % PF] = lds_u128(s + PF < 18 ? sb + (s + PF) * 1024 : sn + (s + PF - 18) * 1024);
        accG = mfma32(cur, xf[s], s == 0 ? binit : accG);
        if (s % 2 == (KH & F1STAG) && s / 2 < 5) F_ISSUE1(w2g + F_SLOT, s / 2, r2);
        if (s == 10 + KH) HLOAD1(hA, 0, 0); if (s == 12 + KH) HLOAD1(hA, 0, 1); if (s == 14 + KH) HLOAD1(hA, 0, 2); if (s == 16 + KH) HLOAD1(hA, 0, 3);
        if (s >= 2 && s <= 17) { F1_STAGE(s - 2, g, accQ, 0, o); F1_STAGE(s - 2, g2, accQ, 8, o2); }
        if (s == 17) { st128s(o, c.sws, hvo, hbase + (unsigned)gt_ * 2048u); st128s(o2, c.sws, hvo + 512u, hbase + (unsigned)gt_ * 2048u); }
        SB();
      }
      const unsigned rr_ = r0; r0 = r1; r1 = r2; r2 = rr_;
      ST(4, 3);
      bar();
      lds_w128(xwr, kh ? f32x4{accG[0], accG[1], accG[2], accG[3]} : f32x4{accG[8], accG[9], accG[10], accG[11]});
      lds_w128(xwr + 1024, kh ? f32x4{accG[4], accG[5], accG[6], accG[7]} : f32x4{accG[12], accG[13], accG[14], accG[15]});
      wait_lgkm0(); bar();
      const f32x4 q0 = lds_f128(xrd), q1 = lds_f128(xrd + 1024);
      const f32x4 k0 = (kh ? f32x4{accG[8], accG[9], accG[10], accG[11]} : f32x4{accG[0], accG[1], accG[2], accG[3]}) + q0;
      const f32x4 k1 = (kh ? f32x4{accG[12], accG[13], accG[14], accG[15]} : f32x4{accG[4], accG[5], accG[6], accG[7]}) + q1;
      const f32x2 z0 = gelu2(f32x2{k0[0], k0[1]}), z1 = gelu2(f32x2{k0[2], k0[3]}), z2 = gelu2(f32x2{k1[0], k1[1]}), z3 = gelu2(f32x2{k1[2], k1[3]});
      o = u32x4{pk2(z0[0], z0[1]), pk2(z1[0], z1[1]), pk2(z2[0], z2[1]), pk2(z3[0], z3[1])};
      st128s(o, c.sws, hvo + (unsigned)kh * 512u, hbase + 8u * 2048u);
    }
  }
  publish(c, FL_H1 + rb * 16 + fc);
  ST(4, 4);
  ST(5, 1);
  HLOAD1(hB, 1, 0); HLOAD1(hB, 1, 1); HLOAD1(hB, 1, 2); HLOAD1(hB, 1, 3);
  f32x4 acc[2][9];
#pragma unroll
  for (int a = 0; a < 2; a++)
#pragma unroll
    for (int n = 0; n < 9; n++) acc[a][n] = f32x4{0, 0, 0, 0};
#define F2_PERIOD(t, HC, HN, HM, VMW, MODE) do { \
    HWAIT(VMW, HC); \
    bar(); \
    unsigned sb = rd + r0, sn = rd + r1; \
    asm volatile("" : "+v"(sb), "+v"(sn)); \
    _Pragma("unroll") for (int s = 0; s < 18; s++) { \
      const u32x4 cur = wf[s % PF]; \
      wf[s % PF] = lds_u128(s + PF < 18 ? sb + (s + PF) * 1024 : sn + (s + PF - 18) * 1024); \
      const int k = s / 9, nt = s % 9; \
      acc[0][nt] = mfma16(cur, HC[2 * k], acc[0][nt]); \
      acc[1][nt] = mfma16(cur, HC[2 * k + 1], acc[1][nt]); \
      if ((MODE) == 3) { if (s == 0) { HLOAD1(HM, (t) + 1, 0); HLOAD1(HM, (t) + 1, 1); } if (s == 1) { HLOAD1(HM, (t) + 1, 2); HLOAD1(HM, (t) + 1, 3); } } \
      if ((MODE) != 1 && s % 2 == (KH & F2STAG) && s / 2 < 5) F_ISSUE1(w2g + (unsigned)((t) + 2) * F_SLOT, s / 2, r2); \
      if ((MODE) == 0 || (MODE) == 3) { constexpr int o_ = KH & F2STAG; if (s == 10 + o_) HLOAD1(HN, (t) + 2, 0); if (s == 12 + o_) HLOAD1(HN, (t) + 2, 1); if (s == 14 + o_) HLOAD1(HN, (t) + 2, 2); if (s == 16 + o_) HLOAD1(HN, (t) + 2, 3); } \
      SB(); \
    } \
    const unsigned rr_ = r0; r0 = r1; r1 = r2; r2 = rr_; \
  } while (0)
  F2_PERIOD(0, hA, hC, hC, 4, 2);
  ST(5, 2);
  if (w == 0) poll(c, FL_H1 + rb * 16 + ks * 4 + (lane & 3), lane < 4);
  F2_PERIOD(1, hB, hA, hC, 0, 3);
  F2_PERIOD(2, hC, hB, hB, 4, 0);
  F2_PERIOD(3, hA, hC, hC, 4, 0);
#pragma unroll 1
  for (int t = 4; t < 7; t += 3) {
    F2_PERIOD(t, hB, hA, hA, 4, 0);
    F2_PERIOD(t + 1, hC, hB, hB, 4, 0);
    F2_PERIOD(t + 2, hA, hC, hC, 4, 0);
  }
  F2_PERIOD(7, hB, hA, hA, 4, 1);
  F2_PERIOD(8, hC, hB, hB, 0, 1);
  ST(5, 3);
  wait_lgkm0(); bar();
  const unsigned xb = lds0 + (unsigned)rg * 18432u + vl;
#pragma unroll
  for (int n = 0; n < 9; n++) lds_w128(xb + (unsigned)(kh ^ 1) * 9216u + n * 1024, kh ? acc[0][n] : acc[1][n]);
  wait_lgkm0(); bar();
  f32x4 o[9];
#pragma unroll
  for (int n = 0; n < 9; n++) o[n] = lds_f128(xb + kh * 9216u + n * 1024) + (kh ? acc[1][n] : acc[0][n]);
  const unsigned xg = (unsigned)WS_XCH + (unsigned)((rb * 4 + ct) * 16) * 9216u + (unsigned)kh * 4608u;
  if (rg != ks) {
    const unsigned d = xg + (unsigned)(rg * 4 + ks) * 9216u;
#pragma unroll
    for (int T = 0; T < 4; T++)
      st128s(u32x4{pk2(o[2 * T][0], o[2 * T][1]), pk2(o[2 * T][2], o[2 * T][3]), pk2(o[2 * T + 1][0], o[2 * T + 1][1]), pk2(o[2 * T + 1][2], o[2 * T + 1][3])}, c.sws, vl + T * 1024u, d);
    __builtin_amdgcn_raw_buffer_store_b64(u32x2{pk2(o[8][0], o[8][1]), pk2(o[8][2], o[8][3])}, c.sws, 4096u + lane * 8u, d, SC1);
  }
  publish(c, FL_P + (rb * 4 + ct) * 4 + ks);
  ST(5, 4);
  if (rg == ks) {
    const int tk = lane & 15, g = lane >> 4;
    const int row = rb * 128 + rg * 32 + kh * 16 + tk;
    u32x4 hv[4], bv[4]; u32x2 hv8, bv8;
#pragma unroll
    for (int T = 0; T < 4; T++) {
      const int col = ct * 144 + 32 * T + 8 * g;
      hv[T] = ld128s(c.sws, (unsigned)(((col >> 3) & 1) * 32 + kh * 16 + tk) * 16u + (unsigned)(col >> 4) * 1024u, (unsigned)WS_HP + (unsigned)(rb * 4 + rg) * 36u * 1024u);
      bv[T] = *(const u32x4*)(c.b2 + col);
    }
    {
      const int col = ct * 144 + 128 + 4 * g;
      hv8 = __builtin_amdgcn_raw_buffer_load_b64(c.sws, (unsigned)(((col >> 3) & 1) * 32 + kh * 16 + tk) * 16u + (unsigned)(col >> 4) * 1024u + (unsigned)(col & 7) * 2u,
                                                 (unsigned)WS_HP + (unsigned)(rb * 4 + rg) * 36u * 1024u, SC1);
      bv8 = *(const u32x2*)(c.b2 + col);
    }
    poll(c, FL_P + (rb * 4 + ct) * 4 + (lane & 3), lane < 4);
    asm volatile("" ::: "memory");
#pragma unroll
    for (int src = 0; src < 4; src++) {
      if (src != ks) {
        const unsigned d = xg + (unsigned)(rg * 4 + src) * 9216u;
        u32x4 pv[4];
#pragma unroll
        for (int T = 0; T < 4; T++) pv[T] = ld128s(c.sws, vl + T * 1024u, d);
        const u32x2 p8 = __builtin_amdgcn_raw_buffer_load_b64(c.sws, 4096u + lane * 8u, d, SC1);
#pragma unroll
        for (int T = 0; T < 4; T++) {
          o[2 * T] += f32x4{bflo(pv[T][0]), bfhi(pv[T][0]), bflo(pv[T][1]), bfhi(pv[T][1])};
          o[2 * T + 1] += f32x4{bflo(pv[T][2]), bfhi(pv[T][2]), bflo(pv[T][3]), bfhi(pv[T][3])};
        }
        o[8] += f32x4{bflo(p8[0]), bfhi(p8[0]), bflo(p8[1]), bfhi(p8[1])};
      }
    }
    u16* orow = c.out + (size_t)row * D + ct * 144;
#pragma unroll
    for (int T = 0; T < 4; T++) {
      u32x4 r;
      r[0] = pk2(bflo(hv[T][0]) + (o[2 * T][0] + bflo(bv[T][0])), bfhi(hv[T][0]) + (o[2 * T][1] + bfhi(bv[T][0])));
      r[1] = pk2(bflo(hv[T][1]) + (o[2 * T][2] + bflo(bv[T][1])), bfhi(hv[T][1]) + (o[2 * T][3] + bfhi(bv[T][1])));
      r[2] = pk2(bflo(hv[T][2]) + (o[2 * T + 1][0] + bflo(bv[T][2])), bfhi(hv[T][2]) + (o[2 * T + 1][1] + bfhi(bv[T][2])));
      r[3] = pk2(bflo(hv[T][3]) + (o[2 * T + 1][2] + bflo(bv[T][3])), bfhi(hv[T][3]) + (o[2 * T + 1][3] + bfhi(bv[T][3])));
      *(u32x4*)(orow + 32 * T + 8 * g) = r;
    }
    u32x2 r8;
    r8[0] = pk2(bflo(hv8[0]) + (o[8][0] + bflo(bv8[0])), bfhi(hv8[0]) + (o[8][1] + bfhi(bv8[0])));
    r8[1] = pk2(bflo(hv8[1]) + (o[8][2] + bflo(bv8[1])), bfhi(hv8[1]) + (o[8][3] + bfhi(bv8[1])));
    *(u32x2*)(orow + 128 + 4 * g) = r8;
  }
  ST(5, 5);
}

constexpr int M_LDS = 163840;
static_assert(L_WO + 110592 <= M_LDS && 4 * Q_SLOT <= M_LDS && F_LDS <= M_LDS, "LDS map");
__global__ KATTR void fused_block(const u16* __restrict__ x, const u16* __restrict__ wqkv, const u16* __restrict__ wo, const u16* __restrict__ w1,
                                  const u16* __restrict__ b1, const u16* __restrict__ w2, const u16* __restrict__ b2, u16* __restrict__ out, char* __restrict__ ws) {
  extern __shared__ __attribute__((aligned(16))) char smem[];
  Ctx c;
  c.x = x; c.wqkv = wqkv; c.wo = wo; c.w1 = w1; c.b1 = b1; c.w2 = w2; c.b2 = b2; c.out = out; c.ws = ws;
  c.sws = mk_srd(ws, WS_BYTES);
  c.lds0 = (unsigned)(size_t)smem;
  c.b = blockIdx.x; c.xcd = c.b & 7; c.bi = c.b >> 3;
  c.w = rfl(threadIdx.x >> 6); c.lane = threadIdx.x & 63;
  ST(1, 0);
  const int xcd = c.xcd, bi = c.bi;
  if (bi < 24) {
    p1_qkv(c, (xcd >> 1) * 4 + (bi & 3), (xcd & 1) * 6 + (bi >> 2));
  } else {
    const int j = bi - 24, fc = (xcd & 3) * 4 + (j >> 1), part = (xcd >> 2) * 2 + (j & 1);
    helper_stats(c, xcd * 8 + j);
    relayout_w1(c, fc, part);
    publish(c, FL_W1 + fc * 4 + part);
    {
      const srd_t sw2 = mk_srd(w2, D * F * 2);
      const int ks = xcd & 3, ct2 = j >> 1, row0 = (ks * 36 + part * 9) * 32;
#pragma unroll
      for (int i = 0; i < 2; i++) {
        const int l = (c.w * 2 + i) * 64 + c.lane, row = l / 3;
        touch128(c.lds0 + L_DUMP, l < 864 ? (unsigned)row * (D * 2) + (unsigned)(l - 3 * row) * 128u : 0x7ff00000u, sw2, (unsigned)row0 * (D * 2) + (((unsigned)ct2 * 288u) & ~127u));
      }
    }
  }
  ST(1, 4);
  if (bi < 24) p3_prefetch(c, bi >> 2);
  p2_attn(c, xcd * 4 + (bi & 3), bi >> 2);
  ST(2, 4);
  const int rb4 = (xcd >> 2) * 8 + (bi & 7), fc4 = (xcd & 3) * 4 + (bi >> 3);
  if (c.w == 1) poll(c, FL_W1 + fc4 * 4 + (c.lane & 3), c.lane < 4);
  if (c.b == 0 && c.w == 2) {
#pragma unroll
    for (int i = 0; i < 4; i++) poll(c, FL_D + i * 64 + c.lane, true);
    if (c.lane == 0) __hip_atomic_store((unsigned long long*)(ws + FL_N * 128), c.nonce, __ATOMIC_RELAXED, __HIP_MEMORY_SCOPE_AGENT);
  }
  bar();
  p4_prefetch(c, fc4, 0);
  {
    const unsigned l = (unsigned)(c.w * 64 + c.lane);
    touch128(c.lds0 + L_DUMP, l < 288u ? l * 128u : 0x7ff00000u, c.sws, (unsigned)WS_W1P + (unsigned)(fc4 * 9 + 1) * F_SLOT + (unsigned)(bi & 7) * 36864u);
  }
  if (bi < 24) {
    p3_wo(c, xcd * 4 + (bi & 3), bi >> 2);
  } else {
    const int j = bi - 24, ks = xcd & 3, ct = j >> 1, part = (xcd >> 2) * 2 + (j & 1);
    relayout_w2(c, c.lds0 + L_WO + (unsigned)c.w * 9216u, ks * 36 + part * 9 + c.w, ct);
    if (c.w == 0) relayout_w2(c, c.lds0 + L_WO, ks * 36 + part * 9 + 8, ct);
    publish(c, FL_W2 + (ks * 4 + ct) * 4 + part);
  }
  ST(3, 4);
  p4_prefetch(c, fc4, 1);
  if (c.w < 4) p45_ffn<0>(c, rb4, fc4); else p45_ffn<1>(c, rb4, fc4);
  asm volatile(".Lfused_code_end:");
}

extern "C" void kernel_launch(void* const* d_in, const int* in_sizes, int n_in, void* d_out, int out_size, void* d_ws, size_t ws_size,
                              hipStream_t stream) {
  (void)hipFuncSetAttribute((const void*)fused_block, hipFuncAttributeMaxDynamicSharedMemorySize, M_LDS);
  fused_block<<<256, 512, M_LDS, stream>>>((const u16*)d_in[0], (const u16*)d_in[1], (const u16*)d_in[2], (const u16*)d_in[4], (const u16*)d_in[5],
                                           (const u16*)d_in[6], (const u16*)d_in[7], (u16*)d_out, (char*)d_ws);
}
```

```cpp
#include <hip/hip_runtime.h>
#include <stdint.h>

typedef unsigned short u16;
typedef __bf16 bf16x8 __attribute__((ext_vector_type(8)));
typedef __bf16 bf16x4 __attribute__((ext_vector_type(4)));
typedef __bf16 bf16x2 __attribute__((ext_vector_type(2)));
typedef float f32x2 __attribute__((ext_vector_type(2)));
typedef float f32x4 __attribute__((ext_vector_type(4)));
typedef float f32x16 __attribute__((ext_vector_type(16)));
typedef unsigned u32x2 __attribute__((ext_vector_type(2)));
typedef unsigned u32x4 __attribute__((ext_vector_type(4)));
#define LAS __attribute__((address_space(3)))
#define DI __device__ __forceinline__
#define SB() __builtin_amdgcn_sched_barrier(0)
#define KATTR __launch_bounds__(768, 3) __attribute__((amdgpu_waves_per_eu(3, 3)))

constexpr int S = 2048, D = 576, H = 32, DH = 18, F = 4608, NQKV = 1728;
constexpr float LN_EPS = 1e-5f;

constexpr size_t WS_FLG = 0;
constexpr int FL_N = 0, FL_D = 1, FL_Q = FL_D + 256, FL_W1 = FL_Q + 192, FL_A = FL_W1 + 64, FL_W2 = FL_A + 256, FL_H = FL_W2 + 64, FL_H1 = FL_H + 192,
              FL_P = FL_H1 + 256, FL_S1 = FL_P + 256, FL_END = FL_S1 + 64;
constexpr size_t WS_QKV = 262144;
static_assert(FL_END * 128 <= WS_QKV, "flag area");
constexpr size_t WS_QKV_SZ = (size_t)3 * H * S * 24 * 2;
constexpr size_t WS_OP = WS_QKV + WS_QKV_SZ;
constexpr size_t WS_OL = WS_OP + (size_t)64 * 32 * 1024;
constexpr size_t WS_HP = WS_OL + (size_t)H * S * 4;
constexpr size_t WS_ST2 = WS_HP + (size_t)S * D * 2;
constexpr size_t WS_W1P = WS_ST2 + (size_t)S * 48;
constexpr size_t WS_W2P = WS_W1P + (size_t)D * F * 2;
constexpr size_t WS_H1P = WS_W2P + (size_t)D * F * 2;
constexpr size_t WS_XCH = WS_H1P + (size_t)S * F * 2;
constexpr size_t WS_ST1 = WS_XCH + (size_t)16 * 4 * 4 * 4 * 2 * 4608;
constexpr size_t WS_CSQ = WS_ST1 + (size_t)S * 8;
constexpr size_t WS_END = WS_CSQ + (size_t)(NQKV + 64) * 4;
constexpr unsigned WS_BYTES = (unsigned)WS_END;

#define ST(kid, idx) asm volatile("; MARK " #kid " " #idx)

DI unsigned rfl(unsigned x) { return __builtin_amdgcn_readfirstlane(x); }
DI float bflo(unsigned w) { return __uint_as_float(w << 16); }
DI float bfhi(unsigned w) { return __uint_as_float(w & 0xffff0000u); }
DI unsigned pk2(float lo, float hi) { f32x2 v = {lo, hi}; bf16x2 b = __builtin_convertvector(v, bf16x2); return __builtin_bit_cast(unsigned, b); }
DI float dot2(unsigned a, unsigned b, float c) { return __builtin_amdgcn_fdot2_f32_bf16(__builtin_bit_cast(bf16x2, a), __builtin_bit_cast(bf16x2, b), c, false); }

typedef __amdgpu_buffer_rsrc_t srd_t;
DI srd_t mk_srd(const void* p, unsigned bytes) { return __builtin_amdgcn_make_buffer_rsrc((void*)p, (short)0, (int)bytes, 0x00020000); }
DI void dma16(unsigned ldsaddr, unsigned voff, srd_t srd, unsigned soff) {
  asm volatile("s_mov_b32 m0, %0\n\ts_nop 0\n\tbuffer_load_dwordx4 %1, %2, %3 offen lds" :: "s"(ldsaddr), "v"(voff), "s"(srd), "s"(soff) : "memory");
}
DI void dma16h(unsigned ldsaddr, unsigned voff, srd_t srd, unsigned soff) {
  asm volatile("s_mov_b32 m0, %0\n\ts_mov_b32 exec_hi, 0\n\tbuffer_load_dwordx4 %1, %2, %3 offen lds\n\ts_mov_b32 exec_hi, -1" :: "s"(ldsaddr), "v"(voff), "s"(srd), "s"(soff) : "memory");
}
DI void dma16q(unsigned ldsaddr, unsigned voff, srd_t srd, unsigned soff) {
  asm volatile("s_mov_b32 m0, %0\n\ts_mov_b64 exec, 0xffff\n\tbuffer_load_dwordx4 %1, %2, %3 offen lds\n\ts_mov_b64 exec, -1" :: "s"(ldsaddr), "v"(voff), "s"(srd), "s"(soff) : "memory");
}
DI void touch128(unsigned ldsdump, unsigned voff, srd_t srd, unsigned soff) {
  asm volatile("s_mov_b32 m0, %0\n\ts_nop 0\n\tbuffer_load_dword %1, %2, %3 offen lds" :: "s"(ldsdump), "v"(voff), "s"(srd), "s"(soff) : "memory");
}
template <int N> DI void wait_vm() { asm volatile("s_waitcnt vmcnt(%0)" :: "i"(N) : "memory"); }
DI void wait_lgkm0() { asm volatile("s_waitcnt lgkmcnt(0)" ::: "memory"); }
DI void bar() { asm volatile("" ::: "memory"); __builtin_amdgcn_s_barrier(); asm volatile("" ::: "memory"); }
DI u32x4 lds_u128(unsigned addr) { return *(const LAS u32x4*)(size_t)addr; }
DI f32x4 lds_f128(unsigned addr) { return *(const LAS f32x4*)(size_t)addr; }
DI void lds_w128(unsigned addr, f32x4 v) { *(LAS f32x4*)(size_t)addr = v; }
DI bf16x8 asbf(u32x4 v) { return __builtin_bit_cast(bf16x8, v); }
DI u32x2 lds_tr(unsigned addr) { return __builtin_bit_cast(u32x2, __builtin_amdgcn_ds_read_tr16_b64_v4bf16((LAS bf16x4*)(size_t)addr)); }
DI u32x4 cat4(u32x2 a, u32x2 b) { return u32x4{a[0], a[1], b[0], b[1]}; }
DI f32x4 mfma16(u32x4 a, u32x4 b, f32x4 c) { return __builtin_amdgcn_mfma_f32_16x16x32_bf16(asbf(a), asbf(b), c, 0, 0, 0); }
DI f32x16 mfma32(u32x4 a, u32x4 b, f32x16 c) { return __builtin_amdgcn_mfma_f32_32x32x16_bf16(asbf(a), asbf(b), c, 0, 0, 0); }
template <int NOP> DI float max16(float m, const f32x16& t) {
  float u;
  asm volatile("s_nop %18\n\tv_max3_f32 %1, %2, %3, %4\n\tv_max3_f32 %0, %0, %5, %6\n\tv_max3_f32 %1, %1, %7, %8\n\tv_max3_f32 %0, %0, %9, %10\n\t"
               "v_max3_f32 %1, %1, %11, %12\n\tv_max3_f32 %0, %0, %13, %14\n\tv_max3_f32 %1, %1, %15, %16\n\tv_max3_f32 %0, %0, %1, %17"
               : "+v"(m), "=&v"(u)
               : "v"(t[0]), "v"(t[1]), "v"(t[2]), "v"(t[3]), "v"(t[4]), "v"(t[5]), "v"(t[6]), "v"(t[7]), "v"(t[8]), "v"(t[9]), "v"(t[10]), "v"(t[11]), "v"(t[12]),
                 "v"(t[13]), "v"(t[14]), "v"(t[15]), "i"(NOP));
  return m;
}
DI int swap23(int i) { return (i & 19) | ((i & 4) << 1) | ((i & 8) >> 1); }

constexpr int SC1 = 16;
DI void dma16s(unsigned ldsaddr, unsigned voff, srd_t srd, unsigned soff) {
  asm volatile("s_mov_b32 m0, %0\n\ts_nop 0\n\tbuffer_load_dwordx4 %1, %2, %3 offen sc1 lds" :: "s"(ldsaddr), "v"(voff), "s"(srd), "s"(soff) : "memory");
}
DI void dma16hs(unsigned ldsaddr, unsigned voff, srd_t srd, unsigned soff) {
  asm volatile("s_mov_b32 m0, %0\n\ts_mov_b32 exec_hi, 0\n\tbuffer_load_dwordx4 %1, %2, %3 offen sc1 lds\n\ts_mov_b32 exec_hi, -1" :: "s"(ldsaddr), "v"(voff), "s"(srd), "s"(soff) : "memory");
}
DI void st128s(u32x4 v, srd_t srd, unsigned voff, unsigned soff) {
  asm volatile("buffer_store_dwordx4 %0, %1, %2, %3 offen sc1\n\ts_nop 1" :: "v"(v), "v"(voff), "s"(srd), "s"(soff) : "memory");
}
DI u32x4 ld128s(srd_t srd, unsigned voff, unsigned soff) { return __builtin_amdgcn_raw_buffer_load_b128(srd, voff, soff, SC1); }

struct Ctx {
  const u16 *x, *wqkv, *wo, *w1, *b1, *w2, *b2;
  u16* out;
  char* ws;
  srd_t sws;
  unsigned lds0;
  unsigned long long nonce;
  int b, xcd, bi, w, lane;
};
DI void publish(const Ctx& c, int flag, bool stored) {
  if (stored) wait_vm<0>();
  bar();
  if (threadIdx.x == 0) __hip_atomic_store((unsigned long long*)(c.ws + (size_t)flag * 128), c.nonce, __ATOMIC_RELAXED, __HIP_MEMORY_SCOPE_AGENT);
}
DI void poll(const Ctx& c, int flag, bool active) {
  const unsigned long long* p = (const unsigned long long*)(c.ws + (size_t)flag * 128);
  const unsigned long long t0 = __builtin_amdgcn_s_memrealtime();
  for (;;) {
    const unsigned long long v = active ? __hip_atomic_load(p, __ATOMIC_RELAXED, __HIP_MEMORY_SCOPE_AGENT) : c.nonce;
    if (__all(v == c.nonce)) break;
    if (__builtin_amdgcn_s_memrealtime() - t0 > 2000000ull) break;
  }
}

#define ENT_CODE 1
constexpr int L_DUMP = 163840 - 256;
DI u32x2 entry_load(const Ctx& c) {
  u32x2 nv;
  asm volatile("buffer_load_dwordx2 %0, %1, %2, 0 offen sc1" : "=&v"(nv) : "v"((unsigned)(FL_N * 128)), "s"(c.sws) : "memory");
  return nv;
}
DI void entry_code(unsigned lds0, int w, int lane) {
  unsigned long long pc; unsigned len;
  asm volatile("s_getpc_b64 %0\n.Lcp%=:\n\ts_mov_b32 %1, .Lfused_code_end-.Lcp%=" : "=s"(pc), "=s"(len));
  touch128(lds0 + L_DUMP, (unsigned)(w * 64 + lane) * 128u, mk_srd((const void*)pc, len), 0u);
}
DI void entry_prefetch(const Ctx& c) {
  const int l = c.w - 8;
  const srd_t st = l == 1 ? mk_srd(c.out, S * D * 2) : c.sws;
  touch128(c.lds0 + L_DUMP, l == 0 ? (c.lane < 24 ? ((unsigned)c.bi * 24u + (unsigned)c.lane) * 65536u : 0x7ff00000u) : (l == 1 ? (unsigned)c.lane * 65536u : 0x7ff00000u), st, 0u);
}
DI void entry_nonce(Ctx& c, u32x2 nv) {
  asm volatile("" : "+v"(nv));
  c.nonce = (((unsigned long long)rfl(nv[1]) << 32) | rfl(nv[0])) + 1ull;
  if (threadIdx.x == 0) __hip_atomic_store((unsigned long long*)(c.ws + (size_t)(FL_D + c.b) * 128), c.nonce, __ATOMIC_RELAXED, __HIP_MEMORY_SCOPE_AGENT);
}

DI void helper_stats(Ctx& c, int hb) {
  const int w = c.w, lane = c.lane;
  const srd_t sx = mk_srd(c.x, S * D * 2), sw = mk_srd(c.wqkv, hb < 27 ? D * NQKV * 2 : 0);
  if (w >= 8) {
    const u32x2 nv = entry_load(c);
    entry_prefetch(c);
    wait_vm<1>();
    entry_nonce(c, nv);
    bar();
    publish(c, FL_S1 + hb, false);
    return;
  }
  const int tid = w * 64 + lane, ch = tid & 7, rg = tid >> 3;
  u32x4 a[4], b2[4], v9[9];
#pragma unroll
  for (int r = 0; r < 4; r++) {
    const unsigned so = (unsigned)(hb * 32 + w * 4 + r) * (D * 2);
    a[r] = __builtin_amdgcn_raw_buffer_load_b128(sx, (unsigned)lane * 16u, so, 0);
    b2[r] = __builtin_amdgcn_raw_buffer_load_b128(sx, lane < 8 ? 1024u + (unsigned)lane * 16u : 0x7ff00000u, so, 0);
  }
#pragma unroll
  for (int i = 0; i < 9; i++) v9[i] = __builtin_amdgcn_raw_buffer_load_b128(sw, (unsigned)(rg * 9 + i) * (NQKV * 2) + (unsigned)ch * 16u, (unsigned)hb * 128u, 0);
  const u32x2 nv = entry_load(c);
  float sm[4], sq[4];
#pragma unroll
  for (int r = 0; r < 4; r++) {
    sm[r] = 0; sq[r] = 0;
#pragma unroll
    for (int e = 0; e < 4; e++) { sm[r] = dot2(a[r][e], 0x3f803f80u, sm[r]); sq[r] = dot2(a[r][e], a[r][e], sq[r]); sm[r] = dot2(b2[r][e], 0x3f803f80u, sm[r]); sq[r] = dot2(b2[r][e], b2[r][e], sq[r]); }
  }
#pragma unroll
  for (int m = 32; m >= 1; m >>= 1)
#pragma unroll
    for (int r = 0; r < 4; r++) { sm[r] += __shfl_xor(sm[r], m); sq[r] += __shfl_xor(sq[r], m); }
  const int r = lane & 3;
  const float s1 = r == 0 ? sm[0] : r == 1 ? sm[1] : r == 2 ? sm[2] : sm[3], s2 = r == 0 ? sq[0] : r == 1 ? sq[1] : r == 2 ? sq[2] : sq[3];
  const float mean = s1 * (1.0f / D), var = fmaxf(s2 * (1.0f / D) - mean * mean, 0.f);
  const u32x2 v = {__float_as_uint(mean), __float_as_uint(rsqrtf(var + LN_EPS))};
  __builtin_amdgcn_raw_buffer_store_b64(v, c.sws, lane < 4 ? (unsigned)lane * 8u : 0x7ff00000u, (unsigned)WS_ST1 + (unsigned)(hb * 32 + w * 4) * 8u, SC1);
  float s8[8] = {0, 0, 0, 0, 0, 0, 0, 0};
#pragma unroll
  for (int i = 0; i < 9; i++)
#pragma unroll
    for (int e = 0; e < 4; e++) { s8[2 * e] += bflo(v9[i][e]); s8[2 * e + 1] += bfhi(v9[i][e]); }
#pragma unroll
  for (int m = 8; m <= 32; m <<= 1)
#pragma unroll
    for (int e = 0; e < 8; e++) s8[e] += __shfl_xor(s8[e], m);
  if (lane < 8) { lds_w128(c.lds0 + (unsigned)(w * 64 + lane * 8) * 4u, f32x4{s8[0], s8[1], s8[2], s8[3]}); lds_w128(c.lds0 + (unsigned)(w * 64 + lane * 8) * 4u + 16, f32x4{s8[4], s8[5], s8[6], s8[7]}); }
  wait_lgkm0(); bar();
  if (w == 0) {
    float t = 0;
#pragma unroll
    for (int k = 0; k < 8; k++) t += *(const LAS float*)(size_t)(c.lds0 + (unsigned)(k * 64 + lane) * 4u);
    __builtin_amdgcn_raw_buffer_store_b32(__float_as_uint(t), c.sws, hb < 27 ? (unsigned)lane * 4u : 0x7ff00000u, (unsigned)WS_CSQ + (unsigned)hb * 256u, SC1);
  }
  wait_lgkm0();
  wait_vm<0>();
  entry_nonce(c, nv);
  publish(c, FL_S1 + hb, true);
}

DI void relayout_w1(const Ctx& c, int fc, int part) {
  const int w = c.w, lane = c.lane; const unsigned lds0 = c.lds0;
  const srd_t sw = mk_srd(c.w1, D * F * 2);
  for (int pc = w; pc < 81; pc += 12) {
    const int ch = pc * 64 + lane, row = ch / 36, cc = ch % 36;
    dma16(lds0 + (unsigned)pc * 1024u, (unsigned)row * (F * 2) + (unsigned)cc * 16u, sw, (unsigned)part * 144u * (F * 2) + (unsigned)fc * 576u);
  }
  wait_vm<0>(); bar();
  const int h = lane >> 5, blk = (lane >> 4) & 1, q = (lane >> 2) & 3, p = lane & 3;
  const unsigned ra = lds0 + (unsigned)(8 * h + q) * 576u + (unsigned)(16 * blk + 8 * (p & 1) + 4 * (p >> 1)) * 2u;
  for (int fr = w; fr < 81; fr += 12) {
    const int sl = fr / 9, tile = fr % 9, s = part * 9 + sl, kh = s / 18;
    const unsigned a = ra + (unsigned)sl * 16u * 576u + (unsigned)tile * 64u;
    const u32x4 v = cat4(lds_tr(a), lds_tr(a + 4 * 576));
    const int slot = tile == 8 ? 8 : (kh == 0 ? (tile ^ 1) : tile);
    st128s(v, c.sws, (unsigned)lane * 16u, (unsigned)WS_W1P + ((unsigned)(fc * 9 + slot) * 36u + (unsigned)s) * 1024u);
  }
}
DI void relayout_w2(const Ctx& c, unsigned ldsw, int kappa, int ct) {
  const int lane = c.lane;
  const srd_t sw = mk_srd(c.w2, D * F * 2);
#pragma unroll
  for (int pc = 0; pc < 9; pc++) {
    const int ch = pc * 64 + lane, row = ch / 18, cc = ch % 18;
    dma16(ldsw + (unsigned)pc * 1024u, (unsigned)row * (D * 2) + (unsigned)cc * 16u, sw, (unsigned)kappa * 32u * (D * 2) + (unsigned)ct * 288u);
  }
  wait_vm<0>();
  const int g = lane >> 4, q = (lane >> 2) & 3, p = lane & 3;
  const unsigned ra = ldsw + (unsigned)(8 * g + q) * 288u;
  const int ks = kappa / 36, kst = (kappa % 36 + 36 - 9 * ct) % 36;
#pragma unroll
  for (int nt = 0; nt < 9; nt++) {
    const unsigned col = nt < 8 ? (unsigned)(32 * (nt >> 1) + 8 * p + 4 * (nt & 1)) * 2u : (unsigned)(128 + 4 * p) * 2u;
    const u32x4 v = cat4(lds_tr(ra + col), lds_tr(ra + col + 4 * 288));
    st128s(v, c.sws, (unsigned)lane * 16u, (unsigned)WS_W2P + (((unsigned)(ks * 4 + ct) * 36u + (unsigned)kst) * 9u + (unsigned)nt) * 1024u);
  }
  wait_lgkm0();
}

#define ABL_QW 1
#define ABL_QX 1
constexpr int Q_SLOT = 36864;
constexpr int Q_CT = 4 * Q_SLOT;
DI unsigned q_voff(int p, int lane) {
  if (p < 16) return (unsigned)(lane >> 3) * (D * 2) + (unsigned)(((lane & 7) ^ ((4 * (p & 1) + (lane >> 4)) & 7)) * 16);
  const int ch = 64 * (p - 16) + lane, row = ch / 20, cc = ch % 20;
  return (unsigned)row * (NQKV * 2) + (unsigned)cc * 16u;
}
DI void q_issue(const Ctx& c, srd_t sx, srd_t sw, int rb, int ct, int p, unsigned voff, int t, unsigned ring) {
  if (p < 16) dma16(c.lds0 + ring + (unsigned)p * 1024u, voff, sx, ((unsigned)rb * 128u + (unsigned)p * 8u) * (D * 2) + (unsigned)t * 128u);
  else dma16(c.lds0 + ring + (unsigned)p * 1024u, voff, sw, (unsigned)ct * 288u + (unsigned)t * 64u * (NQKV * 2));
}
DI void p1_loader(Ctx& c, int rb, int ct) {
  const int w = c.w, lane = c.lane, l = w - 8;
  const srd_t sx = mk_srd(c.x, S * D * 2), sw = mk_srd(c.wqkv, D * NQKV * 2);
  const u32x2 nv = entry_load(c);
#pragma unroll
  for (int t = 0; t < 2; t++)
#pragma unroll
    for (int i = 0; i < 3; i++) q_issue(c, sx, sw, rb, ct, w + 12 * i, q_voff(w + 12 * i, lane), t, (unsigned)t * Q_SLOT);
  unsigned vw[5];
#pragma unroll
  for (int i = 0; i < 5; i++) vw[i] = q_voff(16 + l + 4 * i, lane);
#define Q_W_ISSUE(t, ring) do { if (ABL_QW || (t) < 3) \
    _Pragma("unroll") for (int i = 0; i < 5; i++) dma16(c.lds0 + (ring) + (unsigned)(16 + l + 4 * i) * 1024u, vw[i], sw, (unsigned)ct * 288u + (unsigned)(t) * 64u * (NQKV * 2)); } while (0)
  Q_W_ISSUE(2, 2u * Q_SLOT);
  entry_prefetch(c);
  wait_vm<6>();
  entry_nonce(c, nv);
  bar();
  Q_W_ISSUE(3, 3u * Q_SLOT);
#pragma unroll 1
  for (int t = 1; t < 6; t++) {
    wait_vm<5>(); bar();
    Q_W_ISSUE(t + 3, (unsigned)((t + 3) & 3) * Q_SLOT);
  }
  if (l == 0) { const int g0 = (144 * ct) >> 6; poll(c, FL_S1 + (lane < 4 ? 4 * rb + lane : min(g0 + (lane & 3), 26)), lane < 8); }
  wait_vm<5>(); bar();
  wait_vm<0>(); bar();
  bar();
  bar();
  bar();
}
template <int NT, int T0>
DI void p1_body(Ctx& c, int rb, int ct) {
  const int w = c.w, lane = c.lane, rg = w & 3; const unsigned lds0 = c.lds0;
  constexpr bool XI = NT == 2;
  const srd_t sx = mk_srd(c.x, S * D * 2);
  const unsigned vx = q_voff(rg, lane);
#define Q_X_ISSUE1(t, ring, i) dma16(lds0 + (ring) + (unsigned)(rg + 4 * (i)) * 1024u, vx, sx, ((unsigned)rb * 128u + (unsigned)(rg + 4 * (i)) * 8u) * (D * 2) + (unsigned)(t) * 128u)
  {
    const srd_t sw = mk_srd(c.wqkv, D * NQKV * 2);
    const u32x2 nv = entry_load(c);
#pragma unroll
    for (int t = 0; t < 2; t++)
#pragma unroll
      for (int i = 0; i < 3; i++) q_issue(c, sx, sw, rb, ct, w + 12 * i, q_voff(w + 12 * i, lane), t, (unsigned)t * Q_SLOT);
    if (XI) { Q_X_ISSUE1(2, 2u * Q_SLOT, 0); Q_X_ISSUE1(2, 2u * Q_SLOT, 1); Q_X_ISSUE1(2, 2u * Q_SLOT, 2); Q_X_ISSUE1(2, 2u * Q_SLOT, 3); wait_vm<4>(); } else wait_vm<0>();
    entry_nonce(c, nv);
  }
  const int tok = lane & 31, h = lane >> 5, blk = (lane >> 4) & 1, q4 = (lane >> 2) & 3, pp = lane & 3;
  const unsigned xrow = lds0 + (unsigned)(rg * 32 + tok) * 128u;
  unsigned xj[4];
#pragma unroll
  for (int j = 0; j < 4; j++) xj[j] = xrow + (unsigned)(((2 * j + h) ^ ((tok >> 1) & 7)) * 16);
  const unsigned wj = lds0 + 16384u + (unsigned)(8 * h + q4) * 320u + (unsigned)(32 * T0 + 16 * blk + 8 * (pp & 1) + 4 * (pp >> 1)) * 2u;
  f32x16 acc[NT];
#pragma unroll
  for (int n = 0; n < NT; n++) acc[n] = f32x16{};
  ST(1, 1);
  bar();
  ST(1, 2);
  u32x4 wf[4][NT], xf[4];
#pragma unroll
  for (int j = 0; j < 2; j++) {
    xf[j] = lds_u128(xj[j]);
#pragma unroll
    for (int n = 0; n < NT; n++) wf[j][n] = cat4(lds_tr(wj + j * 5120 + n * 64), lds_tr(wj + j * 5120 + n * 64 + 4 * 320));
  }
  unsigned r0 = 0, r1 = Q_SLOT, r3 = 3 * Q_SLOT;
#define Q_PERIOD(t, SYNC, ISSUE, EXTRA) do { \
    if (XI && (SYNC) == 2) wait_vm<4>(); \
    if (XI && (SYNC) == 3) wait_vm<0>(); \
    if (SYNC) bar(); \
    EXTRA; \
    unsigned wa_ = wj + r0, wn_ = wj + r1, xa_[4]; \
    _Pragma("unroll") for (int j = 0; j < 4; j++) { xa_[j] = xj[(j + 2) & 3] + (j < 2 ? r0 : r1); asm volatile("" : "+v"(xa_[j])); } \
    asm volatile("" : "+v"(wa_), "+v"(wn_)); \
    _Pragma("unroll") for (int j = 0; j < 4; j++) { \
      const u32x4 xc = xf[j]; \
      xf[(j + 2) & 3] = lds_u128(xa_[j]); \
      _Pragma("unroll") for (int n = 0; n < NT; n++) { \
        const u32x4 cur = wf[j][n]; \
        const unsigned a_ = (j < 2 ? wa_ : wn_) + ((j + 2) & 3) * 5120 + n * 64; \
        wf[(j + 2) & 3][n] = cat4(lds_tr(a_), lds_tr(a_ + 4 * 320)); \
        acc[n] = mfma32(cur, xc, acc[n]); \
        if (ABL_QX && XI && (ISSUE) && n == 0) Q_X_ISSUE1((t) + 3, r3, j); \
        SB(); \
      } \
    } \
    r0 = r1; r1 = r1 == 3u * Q_SLOT ? 0u : r1 + Q_SLOT; r3 = r3 == 3u * Q_SLOT ? 0u : r3 + Q_SLOT; \
  } while (0)
  Q_PERIOD(0, 0, true, (void)0);
#pragma unroll 1
  for (int t = 1; t < 6; t++) Q_PERIOD(t, 2, true, (void)0);
  const int tl = rg * 32 + tok, type = ct >> 2;
  u32x2 stv; u32x4 csv;
  Q_PERIOD(6, 2, false, (
    { asm volatile("buffer_load_dwordx2 %0, %1, %2, %3 offen sc1" : "=&v"(stv) : "v"((unsigned)tl * 8u), "s"(c.sws), "s"((unsigned)WS_ST1 + (unsigned)rb * 1024u) : "memory");
      asm volatile("buffer_load_dwordx4 %0, %1, %2, %3 offen sc1" : "=&v"(csv) : "v"((unsigned)min(lane, 39) * 16u), "s"(c.sws), "s"((unsigned)WS_CSQ + (unsigned)ct * 576u) : "memory"); }));
  asm volatile("s_waitcnt vmcnt(0)" : "+v"(stv), "+v"(csv) :: "memory");
  Q_PERIOD(7, 1, false, { if (w == 0 && lane < 40) *(LAS u32x4*)(size_t)(lds0 + Q_CT + (unsigned)lane * 16u) = csv; });
  Q_PERIOD(8, 1, false, (void)0);
  ST(1, 3);
  wait_lgkm0(); bar();
  ST(1, 5);
  f32x2 st;
  st[0] = __uint_as_float(stv[1]) * (type == 0 ? 0.23570226039551584f * 1.4426950408889634f : 1.0f);
  st[1] = -__uint_as_float(stv[0]) * st[0];
#pragma unroll
  for (int n = 0; n < NT; n++)
#pragma unroll
    for (int hf = 0; hf < 2; hf++) {
      if (T0 + n == 4 && hf == 1) continue;
      const int c0 = 32 * (T0 + n) + 16 * hf;
      const f32x4 ca = lds_f128(lds0 + Q_CT + (unsigned)(c0 + 8 * h) * 4u), cb = lds_f128(lds0 + Q_CT + (unsigned)(c0 + 8 * h) * 4u + 16);
      u32x4 o;
#pragma unroll
      for (int e = 0; e < 4; e++) {
        const float c0v = e < 2 ? ca[2 * e] : cb[2 * e - 4], c1v = e < 2 ? ca[2 * e + 1] : cb[2 * e - 3];
        o[e] = pk2(__builtin_fmaf(acc[n][8 * hf + 2 * e], st[0], c0v * st[1]), __builtin_fmaf(acc[n][8 * hf + 2 * e + 1], st[0], c1v * st[1]));
      }
      *(LAS u32x4*)(size_t)(lds0 + (unsigned)tl * 304u + (unsigned)(c0 + 8 * h) * 2u) = o;
    }
  wait_lgkm0(); bar();
  ST(1, 6);
}
DI void p1_qkv(Ctx& c, int rb, int ct) {
  if (c.w >= 8) p1_loader(c, rb, ct); else if (c.w < 4) p1_body<3, 0>(c, rb, ct); else p1_body<2, 3>(c, rb, ct);
  {
    const int tid = c.w * 64 + c.lane, tg = tid >= 384, t = tid - 384 * tg, tk = t / 3, part = t - 3 * tk, type = ct >> 2;
    const unsigned src = c.lds0 + (unsigned)tk * 304u + (unsigned)part * 16u + (unsigned)tg * 144u;
    const unsigned keep = part < 2 ? 0xffffffffu : 0u, padv = part < 2 ? 0u : (type == 0 ? 0x3f800000u : 0x00003f80u);
    const unsigned dst = (unsigned)WS_QKV + ((unsigned)(type * 32 + (ct & 3) * 8) * S + (unsigned)rb * 128u) * 48u;
    u32x4 v[4];
#pragma unroll
    for (int hh = 0; hh < 4; hh++)
#pragma unroll
      for (int e = 0; e < 4; e++) v[hh][e] = *(const LAS unsigned*)(size_t)(src + hh * 36 + e * 4);
#pragma unroll
    for (int hh = 0; hh < 4; hh++) {
      v[hh][1] = (v[hh][1] & keep) | padv; v[hh][2] &= keep; v[hh][3] &= keep;
      st128s(v[hh], c.sws, (unsigned)t * 16u + (unsigned)(4 * tg + hh) * (S * 48), dst);
    }
  }
  wait_lgkm0();
  ST(1, 7);
  publish(c, FL_Q + rb * 12 + ct, true);
}

constexpr int L_K = 0, L_V = 24576, L_WO = 49216;
DI void p2_attn(const Ctx& c, int head, int qb) {
  const int w = c.w, lane = c.lane; const unsigned lds0 = c.lds0;
  const unsigned ldsK = lds0 + L_K, ldsV = lds0 + L_V;
  if (w == 8) {
    const int hg = head >> 3, l = lane;
    const int ty = l < 2 ? 0 : (l < 6 ? 1 : 2), rbl = l < 2 ? 2 * qb + l : 2 * qb - 2 + ((l - 2) & 3);
    poll(c, FL_Q + rbl * 12 + ty * 4 + hg, l < 10 && rbl >= 0);
  }
  bar();
  ST(2, 1);
  if (w >= 8) { bar(); publish(c, FL_A + qb * 32 + head, false); return; }
  const unsigned kbase = (unsigned)WS_QKV + (unsigned)((32 + head) * S + 256 * (qb - 1)) * 48u, vbase = (unsigned)WS_QKV + (unsigned)((64 + head) * S + 256 * (qb - 1)) * 48u;
  const unsigned vl = lane * 16;
#pragma unroll
  for (int hf = 0; hf < 2; hf++) {
    if (hf == 1 || qb > 0) {
#pragma unroll
      for (int j = 0; j < 3; j++) {
        const int pi = w * 3 + j;
        const unsigned pc = (unsigned)hf * 12288u + (unsigned)(pi % 12) * 1024;
        if (pi < 12) dma16s(ldsK + pc, vl, c.sws, kbase + pc); else dma16s(ldsV + pc, vl, c.sws, vbase + pc);
      }
    }
  }
  if (qb == 0) {
    const int tid = w * 64 + lane;
    if (tid < 256) {
      const u32x4 z = {0, 0, 0, 0};
      *(LAS u32x4*)(size_t)(ldsK + tid * 48) = z; *(LAS u32x4*)(size_t)(ldsK + tid * 48 + 16) = z; *(LAS u32x4*)(size_t)(ldsK + tid * 48 + 32) = u32x4{0, 0xf1493f80u, 0, 0};
      *(LAS u32x4*)(size_t)(ldsV + tid * 48) = z; *(LAS u32x4*)(size_t)(ldsV + tid * 48 + 16) = z; *(LAS u32x4*)(size_t)(ldsV + tid * 48 + 32) = z;
    }
  }
  const int qq = lane & 31, h = lane >> 5;
  const int q0 = 256 * qb + 32 * w;
  const unsigned qo = (unsigned)WS_QKV + (unsigned)(head * S + q0) * 48u;
  const u32x4 qf0 = ld128s(c.sws, (unsigned)qq * 48u + 16u * h, qo);
  u32x4 qf1 = ld128s(c.sws, (unsigned)qq * 48u + 32u, qo);
  if (h) qf1 = u32x4{0, 0, 0, 0};
  f32x16 cold, cdiag;
#pragma unroll
  for (int r = 0; r < 16; r++) {
    const int kk = 16 * (r >> 3) + 8 * h + (r & 7);
    cold[r] = kk > qq ? 0.f : -1e30f;
    cdiag[r] = kk <= qq ? 0.f : -1e30f;
  }
  const int key = swap23(lane & 31);
  unsigned ka0 = ldsK + (unsigned)(key + 32 * w) * 48 + 16 * h;
  unsigned ka1 = ldsK + (unsigned)(key + 32 * w) * 48 + 32;
  const int blk = (lane >> 4) & 1, q4 = (lane >> 2) & 3, pp = lane & 3;
  unsigned va = ldsV + (unsigned)(32 * w + 8 * h + q4) * 48 + 32 * blk + 8 * pp;
  asm volatile("" : "+v"(ka0), "+v"(ka1), "+v"(va), "+v"(cold), "+v"(cdiag));
  wait_vm<0>(); wait_lgkm0(); bar();
  ST(2, 2);
  const f32x16 zero = {};
  u32x4 k0[9], k1[9];
#pragma unroll
  for (int j = 0; j < 9; j++) { k0[j] = lds_u128(ka0 + j * 1536); k1[j] = lds_u128(ka1 + j * 1536); }
  float m = -1e30f;
  {
    f32x16 sA = mfma32(k1[0], qf1, mfma32(k0[0], qf0, cold)), sB;
#pragma unroll
    for (int j = 0; j < 9; j++) {
      f32x16& cur = (j & 1) ? sB : sA; f32x16& nxt = (j & 1) ? sA : sB;
      if (j + 1 < 9) nxt = mfma32(k1[j + 1], qf1, mfma32(k0[j + 1], qf0, j + 1 == 8 ? cdiag : zero));
      SB();
      m = j == 0 ? max16<11>(m, cur) : (j < 8 ? max16<1>(m, cur) : max16<3>(m, cur));
      SB();
    }
  }
  { const auto pr = __builtin_amdgcn_permlane32_swap(__float_as_uint(m), __float_as_uint(m), false, false); m = fmaxf(__uint_as_float(pr[0]), __uint_as_float(pr[1])); }
  if (h == 0) qf1[1] = (qf1[1] & 0xffff0000u) | (pk2(-m, 0.f) & 0xffffu);
  ST(2, 3);
  f32x16 oacc = {};
  {
    f32x16 sA = mfma32(k1[0], qf1, mfma32(k0[0], qf0, cold)), sB;
    u32x4 v0 = cat4(lds_tr(va), lds_tr(va + 4 * 48)), v1 = cat4(lds_tr(va + 16 * 48), lds_tr(va + 20 * 48));
#pragma unroll
    for (int j = 0; j < 9; j++) {
      f32x16& cur = (j & 1) ? sB : sA; f32x16& nxt = (j & 1) ? sA : sB;
      if (j + 1 < 9) nxt = mfma32(k0[j + 1], qf0, j + 1 == 8 ? cdiag : zero);
      SB();
      u32x4 p0, p1;
      {
        float e0 = cur[0]; asm volatile("" : "+v"(e0));
        p0[0] = pk2(__builtin_amdgcn_exp2f(e0), __builtin_amdgcn_exp2f(cur[1]));
#pragma unroll
        for (int e = 1; e < 4; e++) p0[e] = pk2(__builtin_amdgcn_exp2f(cur[2 * e]), __builtin_amdgcn_exp2f(cur[2 * e + 1]));
        asm volatile("" : "+v"(p0));
      }
      SB();
      if (j + 1 < 9) nxt = mfma32(k1[j + 1], qf1, nxt);
      oacc = mfma32(v0, p0, oacc);
      if (j + 1 < 9) v0 = cat4(lds_tr(va + (j + 1) * 1536), lds_tr(va + (j + 1) * 1536 + 4 * 48));
      SB();
      {
        float e8 = cur[8]; asm volatile("" : "+v"(e8));
        p1[0] = pk2(__builtin_amdgcn_exp2f(e8), __builtin_amdgcn_exp2f(cur[9]));
#pragma unroll
        for (int e = 1; e < 4; e++) p1[e] = pk2(__builtin_amdgcn_exp2f(cur[8 + 2 * e]), __builtin_amdgcn_exp2f(cur[8 + 2 * e + 1]));
        asm volatile("" : "+v"(p1));
      }
      SB();
      oacc = mfma32(v1, p1, oacc);
      if (j + 1 < 9) v1 = cat4(lds_tr(va + (j + 1) * 1536 + 16 * 48), lds_tr(va + (j + 1) * 1536 + 20 * 48));
      SB();
    }
  }
  const float l = __shfl(oacc[10], qq);
  const float inv = 1.0f / l;
  const u32x4 om = {pk2(oacc[0] * inv, oacc[1] * inv), pk2(oacc[2] * inv, oacc[3] * inv), pk2(oacc[4] * inv, oacc[5] * inv), pk2(oacc[6] * inv, oacc[7] * inv)};
  st128s(om, c.sws, (unsigned)lane * 16u, (unsigned)WS_OP + (unsigned)((q0 >> 5) * 32 + head) * 1024u);
  __builtin_amdgcn_raw_buffer_store_b32(pk2(oacc[8] * inv, oacc[9] * inv), c.sws, h == 0 ? (unsigned)qq * 4u : 0x7ff00000u, (unsigned)WS_OL + (unsigned)(head * S + q0) * 4u, SC1);
  publish(c, FL_A + qb * 32 + head, true);
}

DI void p3_prefetch(const Ctx& c, int ct) {
  const int l = c.w - 8, lane = c.lane;
  const srd_t sw = mk_srd(c.wo, D * D * 2);
  unsigned vw[3];
#pragma unroll
  for (int i = 0; i < 3; i++) { const int ch = 64 * i + lane; vw[i] = (unsigned)(ch / 12) * (D * 2) + (unsigned)(ch % 12) * 16u; }
#pragma unroll 1
  for (int gi = 0; gi < 9; gi++) {
    const int grp = l * 9 + gi;
#pragma unroll
    for (int i = 0; i < 3; i++) dma16(c.lds0 + L_WO + (unsigned)(grp * 3 + i) * 1024u, vw[i], sw, (unsigned)grp * 16u * (D * 2) + (unsigned)ct * 192u);
  }
}
DI void p3_wo(const Ctx& c, int rb, int ct) {
  const int w = c.w, lane = c.lane, rg = w & 1, kq = w >> 1; const unsigned lds0 = c.lds0;
  const int qt = rb * 2 + rg, tok = lane & 31, h = lane >> 5;
  const int row = qt * 32 + tok;
  const u32x4 xr0 = *(const u32x4*)(c.x + (size_t)row * D + ct * 96 + 16 * kq + 8 * h);
  const u32x4 xr1 = *(const u32x4*)(c.x + (size_t)row * D + ct * 96 + 16 * (kq < 2 ? kq + 4 : kq) + 8 * h);
  bar();
  ST(3, 1);
  u32x4 of[9];
  {
    const unsigned so = (unsigned)WS_OP + (unsigned)(qt * 32 + kq * 9) * 1024u;
#pragma unroll
    for (int s = 0; s < 5; s++) of[s] = ld128s(c.sws, (unsigned)lane * 16u + s * 1024u, so);
    if (kq < 3) {
#pragma unroll
      for (int s = 5; s < 9; s++) of[s] = ld128s(c.sws, (unsigned)lane * 16u + s * 1024u, so);
    } else {
#pragma unroll
      for (int G = 0; G < 4; G++)
#pragma unroll
        for (int i = 0; i < 4; i++)
          of[5 + G][i] = __builtin_amdgcn_raw_buffer_load_b32(c.sws, (unsigned)((4 * h + i) * S + tok) * 4u, (unsigned)WS_OL + (unsigned)(8 * G * S + qt * 32) * 4u, SC1);
    }
  }
  wait_vm<0>(); bar();
  ST(3, 2);
  const int blk = (lane >> 4) & 1, q4 = (lane >> 2) & 3, pp = lane & 3;
  const unsigned colb = lds0 + L_WO + (unsigned)(16 * blk + 8 * (pp & 1) + 4 * (pp >> 1)) * 2u;
  f32x16 acc[3] = {};
#pragma unroll
  for (int s = 0; s < 9; s++) {
    unsigned r0, r1;
    if (s < 5 || kq < 3) { const int hd = kq * 9 + s; r0 = (unsigned)(18 * hd + 4 * h + q4); r1 = r0 + 8; }
    else { const int G = s - 5; r0 = (unsigned)(18 * (8 * G + 4 * h + (q4 >> 1)) + 16 + (q4 & 1)); r1 = r0 + 36; }
    const unsigned a0 = colb + r0 * 192u, a1 = colb + r1 * 192u;
#pragma unroll
    for (int n = 0; n < 3; n++) acc[n] = mfma32(cat4(lds_tr(a0 + n * 64), lds_tr(a1 + n * 64)), of[s], acc[n]);
  }
  ST(3, 3);
  wait_lgkm0(); bar();
  const unsigned xb = lds0 + L_WO + (unsigned)rg * 49152u + (unsigned)lane * 16u;
#pragma unroll
  for (int f = 0; f < 6; f++) {
    const int r8 = 8 * (f & 1);
    lds_w128(xb + (unsigned)(f * 4 + kq) * 2048u, f32x4{acc[f >> 1][r8], acc[f >> 1][r8 + 1], acc[f >> 1][r8 + 2], acc[f >> 1][r8 + 3]});
    lds_w128(xb + (unsigned)(f * 4 + kq) * 2048u + 1024u, f32x4{acc[f >> 1][r8 + 4], acc[f >> 1][r8 + 5], acc[f >> 1][r8 + 6], acc[f >> 1][r8 + 7]});
  }
  wait_lgkm0(); bar();
  float ssum = 0, ssq = 0;
  const int nf = kq < 2 ? 2 : 1;
  for (int i = 0; i < nf; i++) {
    const int f = kq + 4 * i;
    f32x4 a = {0, 0, 0, 0}, cc = {0, 0, 0, 0};
#pragma unroll
    for (int k = 0; k < 4; k++) { a += lds_f128(xb + (unsigned)(f * 4 + k) * 2048u); cc += lds_f128(xb + (unsigned)(f * 4 + k) * 2048u + 1024u); }
    const u32x4 xr = i ? xr1 : xr0;
    u32x4 o;
    o[0] = pk2(bflo(xr[0]) + a[0], bfhi(xr[0]) + a[1]); o[1] = pk2(bflo(xr[1]) + a[2], bfhi(xr[1]) + a[3]);
    o[2] = pk2(bflo(xr[2]) + cc[0], bfhi(xr[2]) + cc[1]); o[3] = pk2(bflo(xr[3]) + cc[2], bfhi(xr[3]) + cc[3]);
    st128s(o, c.sws, (unsigned)lane * 16u, (unsigned)WS_HP + (unsigned)(qt * 36 + ct * 6 + f) * 1024u);
#pragma unroll
    for (int e = 0; e < 4; e++) { ssum = dot2(o[e], 0x3f803f80u, ssum); ssq = dot2(o[e], o[e], ssq); }
  }
  { const auto a = __builtin_amdgcn_permlane32_swap(__float_as_uint(ssum), __float_as_uint(ssum), false, false); ssum = __uint_as_float(a[0]) + __uint_as_float(a[1]);
    const auto b2 = __builtin_amdgcn_permlane32_swap(__float_as_uint(ssq), __float_as_uint(ssq), false, false); ssq = __uint_as_float(b2[0]) + __uint_as_float(b2[1]); }
  const unsigned stb = lds0 + L_WO + 98304u;
  if (h == 0) *(LAS f32x2*)(size_t)(stb + (unsigned)(rg * 32 + tok) * 32u + kq * 8u) = f32x2{ssum, ssq};
  wait_lgkm0(); bar();
  if (w == 0) {
    const f32x4 a = lds_f128(stb + lane * 32u), cc = lds_f128(stb + lane * 32u + 16);
    const u32x2 v = {__float_as_uint((a[0] + a[2]) + (cc[0] + cc[2])), __float_as_uint((a[1] + a[3]) + (cc[1] + cc[3]))};
    __builtin_amdgcn_raw_buffer_store_b64(v, c.sws, (unsigned)lane * 8u, (unsigned)WS_ST2 + (unsigned)(rb * 6 + ct) * 512u, SC1);
  }
  publish(c, FL_H + rb * 6 + ct, true);
}

constexpr float GC0 = -2.0f * 0.7978845608028654f * 1.4426950408889634f, GC1 = GC0 * 0.044715f;
DI f32x2 gelu2(f32x2 x) {
  const f32x2 t = x * (GC1 * (x * x) + GC0);
  f32x2 e; e[0] = __builtin_amdgcn_exp2f(t[0]); e[1] = __builtin_amdgcn_exp2f(t[1]);
  const f32x2 d = e + 1.0f;
  f32x2 r; r[0] = __builtin_amdgcn_rcpf(d[0]); r[1] = __builtin_amdgcn_rcpf(d[1]);
  return x * r;
}
struct G2 { float x0, x1, t0, t1; };
DI void g2_s1(G2& g, float a0, float a1, float p0, float p1) {
  asm volatile("v_add_f32_e32 %0, %4, %6\n\tv_add_f32_e32 %1, %5, %7\n\tv_mul_f32_e32 %2, %0, %0\n\tv_mul_f32_e32 %3, %1, %1"
               : "=&v"(g.x0), "=&v"(g.x1), "=&v"(g.t0), "=&v"(g.t1) : "v"(a0), "v"(a1), "v"(p0), "v"(p1));
}
DI void g2_s2(G2& g, float vc1) {
  asm volatile("v_fmaak_f32 %0, %0, %4, 0xc0135761\n\tv_fmaak_f32 %1, %1, %4, 0xc0135761\n\tv_mul_f32_e32 %0, %2, %0\n\tv_mul_f32_e32 %1, %3, %1"
               : "+v"(g.t0), "+v"(g.t1) : "v"(g.x0), "v"(g.x1), "v"(vc1));
}
DI void g2_s3(G2& g) {
  asm volatile("v_exp_f32_e32 %0, %0\n\tv_exp_f32_e32 %1, %1\n\tv_add_f32_e32 %0, 1.0, %0\n\tv_add_f32_e32 %1, 1.0, %1" : "+v"(g.t0), "+v"(g.t1));
}
DI unsigned g2_s4(G2& g) {
  unsigned o;
  asm volatile("v_rcp_f32_e32 %1, %1\n\tv_rcp_f32_e32 %2, %2\n\tv_mul_f32_e32 %1, %3, %1\n\tv_mul_f32_e32 %2, %4, %2\n\tv_cvt_pk_bf16_f32 %0, %1, %2"
               : "=v"(o), "+v"(g.t0), "+v"(g.t1) : "v"(g.x0), "v"(g.x1));
  return o;
}

#define ABL_LW 1
#define ABL_XW 1
#define ABL_ST 1
#define ABL_GELU 1
#define ABL_H 1
#define FUSE0 0
#define W2T_EARLY 1
constexpr int F_SLOT = 36864, F1_XCH = 3 * F_SLOT, F1_UV = F1_XCH + 32768, F_LDS = F1_UV + 1152;
DI void f_issue(const Ctx& c, unsigned gbase, unsigned ring) {
  const unsigned o = (unsigned)(c.w - 8) * 9216u, vl = c.lane * 16;
#pragma unroll
  for (int i = 0; i < 9; i++) dma16s(c.lds0 + ring + o + i * 1024u, vl, c.sws, gbase + o + i * 1024u);
}
DI void p45_loader(const Ctx& c, int rb, int fc) {
  const int l = c.w - 8, lane = c.lane, ks = fc >> 2, ct = fc & 3;
  const unsigned w1g = (unsigned)WS_W1P + (unsigned)fc * 9 * F_SLOT, w2g = (unsigned)WS_W2P + (unsigned)(ks * 4 + ct) * 9 * F_SLOT;
  f_issue(c, w1g + F_SLOT, F_SLOT);
  if (l == 0) poll(c, lane < 12 ? FL_H + (2 * rb + lane / 6) * 6 + lane % 6 : FL_W2 + (ks * 4 + ct) * 4 + (lane & 3), lane < 16);
  bar();
  unsigned ring = 2 * F_SLOT;
#pragma unroll 1
  for (int t = 0; t < 9; t++) {
    if (ABL_LW) wait_vm<0>();
    bar();
    if (t == 1 || t == 2) {
      const unsigned ln = (unsigned)((l * 2 + t - 1) * 64 + lane);
      touch128(c.lds0 + L_DUMP, ln < 324u ? ln * 128u : 0x7ff00000u, c.sws, w2g + (unsigned)(c.bi & 7) * 41472u);
    }
    f_issue(c, t < 7 ? w1g + (unsigned)(t + 2) * F_SLOT : w2g + (unsigned)(t - 7) * F_SLOT, ring);
    ring = ring == 2u * F_SLOT ? 0u : ring + F_SLOT;
  }
  bar(); bar();
  bar();
#pragma unroll 1
  for (int t = 0; t < 9; t++) {
    if (t == 1 && l == 0) poll(c, FL_H1 + rb * 16 + ks * 4 + (lane & 3), lane < 4);
    if (ABL_LW) wait_vm<0>();
    bar();
    if (t < 7) f_issue(c, w2g + (unsigned)(t + 2) * F_SLOT, ring);
    ring = ring == 2u * F_SLOT ? 0u : ring + F_SLOT;
  }
  bar(); bar();
  bar();
}
template <int KH>
DI void p45_ffn(const Ctx& c, int rb, int fc) {
  constexpr int kh = KH;
  const int w = c.w, lane = c.lane, rg = w & 3; const unsigned lds0 = c.lds0;
  const unsigned vl = lane * 16;
  const int ks = fc >> 2, ct = fc & 3;
  const int tok = lane & 31, h = lane >> 5;
  if (threadIdx.x < 288) *(LAS float*)(size_t)(lds0 + F1_UV + threadIdx.x * 4) = __uint_as_float((unsigned)c.b1[fc * 288 + threadIdx.x] << 16);
  wait_lgkm0(); bar();
  ST(4, 1);
  u32x2 stv[6];
#pragma unroll
  for (int e = 0; e < 6; e++)
    asm volatile("buffer_load_dwordx2 %0, %1, %2, %3 offen offset:%4 sc1" : "=&v"(stv[e]) : "v"((unsigned)tok * 8u), "s"(c.sws),
                 "s"((unsigned)WS_ST2 + (unsigned)((2 * rb + (rg >> 1)) * 6) * 512u + (unsigned)(rg & 1) * 256u), "i"(e * 512) : "memory");
  u32x4 xf[18];
  {
    const unsigned so = (unsigned)WS_HP + ((unsigned)(rb * 4 + rg) * 36u + (unsigned)kh * 18u) * 1024u;
#pragma unroll
    for (int s = 0; s < 18; s++)
      asm volatile("buffer_load_dwordx4 %0, %1, %2, %3 offen offset:%4 sc1" : "=&v"(xf[s]) : "v"(vl), "s"(c.sws), "s"(so + (s >> 2) * 4096u), "i"((s & 3) * 1024) : "memory");
  }
  float ra, rc;
  {
    asm volatile("s_waitcnt vmcnt(18)" : "+v"(stv[0]), "+v"(stv[1]), "+v"(stv[2]), "+v"(stv[3]), "+v"(stv[4]), "+v"(stv[5]) :: "memory");
    float s = 0, q = 0;
#pragma unroll
    for (int e = 0; e < 6; e++) { s += __uint_as_float(stv[e][0]); q += __uint_as_float(stv[e][1]); }
    const float mean = s * (1.0f / D), var = fmaxf(q * (1.0f / D) - mean * mean, 0.f), rstd = rsqrtf(var + LN_EPS);
    ra = rstd; rc = -mean * rstd;
  }
  const unsigned rd = lds0 + (unsigned)kh * 18432u + vl;
  const unsigned hvo = (unsigned)(tok >> 4) * 1024u + (unsigned)h * 256u + (unsigned)(tok & 15) * 16u;
  const unsigned hbase = (unsigned)WS_H1P + ((unsigned)(rb * 4 + rg) * 144u + (unsigned)fc * 9u) * 2048u;
  const unsigned bia = lds0 + F1_UV + (unsigned)h * 32u;
  if (!FUSE0) {
#pragma unroll
    for (int s = 0; s < 18; s++) {
      asm volatile("s_waitcnt vmcnt(%1)" : "+v"(xf[s]) : "i"(17 - s) : "memory");
#pragma unroll
      for (int e = 0; e < 4; e++) {
        const f32x2 v = f32x2{bflo(xf[s][e]), bfhi(xf[s][e])} * ra + rc;
        xf[s][e] = pk2(v[0], v[1]);
      }
      asm volatile("" : "+v"(xf[s]));
      SB();
    }
  }
  bar();
  ST(4, 2);
  constexpr int PF = 3;
  u32x4 wf[PF];
#pragma unroll
  for (int i = 0; i < PF; i++) wf[i] = lds_u128(rd + i * 1024);
  const f32x16 zero = {};
#define F1_BIAS(ACC, ba_) do { const f32x4 b0_ = lds_f128(ba_), b1_ = lds_f128(ba_ + 16), b2_ = lds_f128(ba_ + 64), b3_ = lds_f128(ba_ + 80); \
    ACC = f32x16{b0_[0], b0_[1], b0_[2], b0_[3], b1_[0], b1_[1], b1_[2], b1_[3], b2_[0], b2_[1], b2_[2], b2_[3], b3_[0], b3_[1], b3_[2], b3_[3]}; } while (0)
  f32x16 accG, accP, accQ;
  F1_BIAS(accG, bia + (unsigned)(1 - kh) * 128u);
  float vc1 = GC1; asm volatile("" : "+v"(vc1));
  unsigned r0 = 0, r1 = F_SLOT, r2 = 2 * F_SLOT;
  const unsigned xwr = lds0 + F1_XCH + (unsigned)(kh ^ 1) * 16384u + (unsigned)rg * 4096u + vl;
  const unsigned xrd = lds0 + F1_XCH + (unsigned)kh * 16384u + (unsigned)rg * 4096u + vl;
  f32x4 pp[4] = {};
#define F1_STAGE(q, G, SRC, R0, O) do { const int p_ = (q) >> 2, st_ = (q) & 3, r_ = (R0) + 2 * p_; \
      if (st_ == 0) g2_s1(G, SRC[r_], SRC[r_ + 1], pp[r_ >> 2][r_ & 3], pp[r_ >> 2][(r_ & 3) + 1]); \
      if (st_ == 1) g2_s2(G, vc1); if (st_ == 2) g2_s3(G); if (st_ == 3) O[p_] = g2_s4(G); } while (0)
#define F1_PERIOD(t, ACC, PREV, GELU, HALF) do { \
    bar(); \
    unsigned sb = rd + r0, sn = rd + r1; \
    asm volatile("" : "+v"(sb), "+v"(sn)); \
    u32x4 o; G2 g; \
    const int gt_ = ((t) - 2 - (HALF)) + kh;      \
    if ((GELU) && (HALF) == 0) { _Pragma("unroll") for (int e = 0; e < 4; e++) pp[e] = lds_f128(xrd + e * 1024); } \
    SB(); \
    _Pragma("unroll") for (int s = 0; s < 18; s++) { \
      const u32x4 cur = wf[s % PF]; \
      wf[s % PF] = lds_u128(s + PF < 18 ? sb + (s + PF) * 1024 : sn + (s + PF - 18) * 1024); \
      ACC = mfma32(cur, xf[s], (s == 0 && (HALF) == 1) ? zero : ACC); \
      if (ABL_XW && (HALF) == 1 && s % 4 == 1 && s / 4 < 4) { const int e = s / 4; lds_w128(xwr + e * 1024, f32x4{accG[4 * e], accG[4 * e + 1], accG[4 * e + 2], accG[4 * e + 3]}); } \
      if (ABL_GELU && (GELU)) { \
        if (s >= 2 && s <= 17) F1_STAGE(s - 2, g, PREV, 8 * (HALF), o); \
        if (ABL_ST && s == 17) st128s(o, c.sws, hvo + (HALF) * 512u, hbase + (unsigned)gt_ * 2048u); \
      } \
      if ((HALF) == 1 && s == 15) {     \
        const int nt_ = (t) + 1 == 8 ? 8 : (t) + 1 + (1 - kh); \
        if ((t) + 1 == 8 && kh == 0) accG = zero; \
        else { unsigned ba_ = bia + (unsigned)nt_ * 128u; asm volatile("" : "+v"(ba_)); F1_BIAS(accG, ba_); } \
      } \
      SB(); \
    } \
    asm volatile("" :: "v"(ACC)); \
    const unsigned rr_ = r0; r0 = r1; r1 = r2; r2 = rr_; \
  } while (0)
  const unsigned hoff0 = (unsigned)WS_H1P + ((unsigned)(rb * 4 + rg) * 144u + (unsigned)ks * 36u) * 2048u;
  u32x4 hA[4], hB[4], hC[4];
#define HLOAD1(dst, t, i) do { const int p_ = 4 * (t) + 2 * kh + ((i) >> 1) + 9 * ct; const unsigned so_ = hoff0 + (unsigned)(p_ >= 36 ? p_ - 36 : p_) * 2048u; \
    if (((i) & 1) == 0) asm volatile("buffer_load_dwordx4 %0, %1, %2, %3 offen sc1" : "=&v"(dst[i]) : "v"(vl), "s"(c.sws), "s"(so_) : "memory"); \
    else asm volatile("buffer_load_dwordx4 %0, %1, %2, %3 offen offset:1024 sc1" : "=&v"(dst[i]) : "v"(vl), "s"(c.sws), "s"(so_) : "memory"); } while (0)
#define HWAIT(N, dst) asm volatile("s_waitcnt vmcnt(" #N ")" : "+v"(dst[0]), "+v"(dst[1]), "+v"(dst[2]), "+v"(dst[3]) :: "memory")
  {
    {
      unsigned sb = rd, sn = rd + F_SLOT;
      asm volatile("" : "+v"(sb), "+v"(sn));
#pragma unroll
      for (int s = 0; s < 18; s++) {
        if (FUSE0) {
          asm volatile("s_waitcnt vmcnt(%1)" : "+v"(xf[s]) : "i"(17 - s) : "memory");
#pragma unroll
          for (int e = 0; e < 4; e++) {
            const f32x2 v = f32x2{bflo(xf[s][e]), bfhi(xf[s][e])} * ra + rc;
            xf[s][e] = pk2(v[0], v[1]);
          }
          asm volatile("" : "+v"(xf[s]));
        }
        const u32x4 cur = wf[s % PF];
        wf[s % PF] = lds_u128(s + PF < 18 ? sb + (s + PF) * 1024 : sn + (s + PF - 18) * 1024);
        accG = mfma32(cur, xf[s], accG);
        SB();
      }
      const unsigned rr_ = r0; r0 = r1; r1 = r2; r2 = rr_;
    }
    F1_PERIOD(1, accP, accQ, false, 1);
    F1_PERIOD(2, accG, accP, true, 0);
    F1_PERIOD(3, accQ, accP, true, 1);
    F1_PERIOD(4, accG, accQ, true, 0);
    F1_PERIOD(5, accP, accQ, true, 1);
    F1_PERIOD(6, accG, accP, true, 0);
    F1_PERIOD(7, accQ, accP, true, 1);
    {
      wait_vm<2>();
      bar();
      unsigned sb = rd + r0, sn = rd + r1;
      asm volatile("" : "+v"(sb), "+v"(sn));
      u32x4 o, o2; G2 g, g2;
      const int gt_ = 6 + kh;
#pragma unroll
      for (int e = 0; e < 4; e++) pp[e] = lds_f128(xrd + e * 1024);
      SB();
#pragma unroll
      for (int s = 0; s < 18; s++) {
        const u32x4 cur = wf[s % PF];
        wf[s % PF] = lds_u128(s + PF < 18 ? sb + (s + PF) * 1024 : sn + (s + PF - 18) * 1024);
        accG = mfma32(cur, xf[s], accG);
        if (s == 10 + KH) HLOAD1(hA, 0, 0); if (s == 12 + KH) HLOAD1(hA, 0, 1); if (s == 14 + KH) HLOAD1(hA, 0, 2); if (s == 16 + KH) HLOAD1(hA, 0, 3);
        if (s >= 2 && s <= 17) { F1_STAGE(s - 2, g, accQ, 0, o); F1_STAGE(s - 2, g2, accQ, 8, o2); }
        if (s == 17) { st128s(o, c.sws, hvo, hbase + (unsigned)gt_ * 2048u); st128s(o2, c.sws, hvo + 512u, hbase + (unsigned)gt_ * 2048u); }
        SB();
      }
      const unsigned rr_ = r0; r0 = r1; r1 = r2; r2 = rr_;
      ST(4, 3);
      bar();
      lds_w128(xwr, kh ? f32x4{accG[0], accG[1], accG[2], accG[3]} : f32x4{accG[8], accG[9], accG[10], accG[11]});
      lds_w128(xwr + 1024, kh ? f32x4{accG[4], accG[5], accG[6], accG[7]} : f32x4{accG[12], accG[13], accG[14], accG[15]});
      wait_lgkm0(); bar();
      const f32x4 q0 = lds_f128(xrd), q1 = lds_f128(xrd + 1024);
      const f32x4 k0 = (kh ? f32x4{accG[8], accG[9], accG[10], accG[11]} : f32x4{accG[0], accG[1], accG[2], accG[3]}) + q0;
      const f32x4 k1 = (kh ? f32x4{accG[12], accG[13], accG[14], accG[15]} : f32x4{accG[4], accG[5], accG[6], accG[7]}) + q1;
      const f32x2 z0 = gelu2(f32x2{k0[0], k0[1]}), z1 = gelu2(f32x2{k0[2], k0[3]}), z2 = gelu2(f32x2{k1[0], k1[1]}), z3 = gelu2(f32x2{k1[2], k1[3]});
      o = u32x4{pk2(z0[0], z0[1]), pk2(z1[0], z1[1]), pk2(z2[0], z2[1]), pk2(z3[0], z3[1])};
      st128s(o, c.sws, hvo + (unsigned)kh * 512u, hbase + 8u * 2048u);
    }
  }
  publish(c, FL_H1 + rb * 16 + fc, true);
  ST(4, 4);
  ST(5, 1);
  HLOAD1(hB, 1, 0); HLOAD1(hB, 1, 1); HLOAD1(hB, 1, 2); HLOAD1(hB, 1, 3);
  f32x4 acc[2][9];
#pragma unroll
  for (int a = 0; a < 2; a++)
#pragma unroll
    for (int n = 0; n < 9; n++) acc[a][n] = f32x4{0, 0, 0, 0};
#define F2_PERIOD(t, HC, HN, HM, VMW, MODE) do { \
    HWAIT(VMW, HC); \
    bar(); \
    unsigned sb = rd + r0, sn = rd + r1; \
    asm volatile("" : "+v"(sb), "+v"(sn)); \
    _Pragma("unroll") for (int s = 0; s < 18; s++) { \
      const u32x4 cur = wf[s % PF]; \
      wf[s % PF] = lds_u128(s + PF < 18 ? sb + (s + PF) * 1024 : sn + (s + PF - 18) * 1024); \
      const int k = s / 9, nt = s % 9; \
      acc[0][nt] = mfma16(cur, HC[2 * k], acc[0][nt]); \
      acc[1][nt] = mfma16(cur, HC[2 * k + 1], acc[1][nt]); \
      if ((MODE) == 3) { if (s == KH) HLOAD1(HM, (t) + 1, 0); if (s == 2 + KH) HLOAD1(HM, (t) + 1, 1); if (s == 4 + KH) HLOAD1(HM, (t) + 1, 2); if (s == 6 + KH) HLOAD1(HM, (t) + 1, 3); } \
      if (ABL_H && ((MODE) == 0 || (MODE) == 3)) { if (s == 9 + KH) HLOAD1(HN, (t) + 2, 0); if (s == 11 + KH) HLOAD1(HN, (t) + 2, 1); if (s == 13 + KH) HLOAD1(HN, (t) + 2, 2); if (s == 15 + KH) HLOAD1(HN, (t) + 2, 3); } \
      SB(); \
    } \
    const unsigned rr_ = r0; r0 = r1; r1 = r2; r2 = rr_; \
  } while (0)
  F2_PERIOD(0, hA, hC, hC, 4, 1);
  ST(5, 2);
  F2_PERIOD(1, hB, hA, hC, 0, 3);
  F2_PERIOD(2, hC, hB, hB, 4, 0);
  F2_PERIOD(3, hA, hC, hC, 4, 0);
  F2_PERIOD(4, hB, hA, hA, 4, 0);
  F2_PERIOD(5, hC, hB, hB, 4, 0);
  F2_PERIOD(6, hA, hC, hC, 4, 0);
  F2_PERIOD(7, hB, hA, hA, 4, 1);
  F2_PERIOD(8, hC, hB, hB, 0, 1);
  ST(5, 3);
  wait_lgkm0(); bar();
  const unsigned xb = lds0 + (unsigned)rg * 18432u + vl;
#pragma unroll
  for (int n = 0; n < 9; n++) lds_w128(xb + (unsigned)(kh ^ 1) * 9216u + n * 1024, kh ? acc[0][n] : acc[1][n]);
  wait_lgkm0(); bar();
  f32x4 o[9];
#pragma unroll
  for (int n = 0; n < 9; n++) o[n] = lds_f128(xb + kh * 9216u + n * 1024) + (kh ? acc[1][n] : acc[0][n]);
  const unsigned xg = (unsigned)WS_XCH + (unsigned)((rb * 4 + ct) * 16) * 9216u + (unsigned)kh * 4608u;
  if (rg != ks) {
    const unsigned d = xg + (unsigned)(rg * 4 + ks) * 9216u;
#pragma unroll
    for (int T = 0; T < 4; T++)
      st128s(u32x4{pk2(o[2 * T][0], o[2 * T][1]), pk2(o[2 * T][2], o[2 * T][3]), pk2(o[2 * T + 1][0], o[2 * T + 1][1]), pk2(o[2 * T + 1][2], o[2 * T + 1][3])}, c.sws, vl + T * 1024u, d);
    __builtin_amdgcn_raw_buffer_store_b64(u32x2{pk2(o[8][0], o[8][1]), pk2(o[8][2], o[8][3])}, c.sws, 4096u + lane * 8u, d, SC1);
  }
  publish(c, FL_P + (rb * 4 + ct) * 4 + ks, true);
  ST(5, 4);
  if (rg == ks) {
    const int tk = lane & 15, g = lane >> 4;
    const int row = rb * 128 + rg * 32 + kh * 16 + tk;
    u32x4 hv[4], bv[4]; u32x2 hv8, bv8;
#pragma unroll
    for (int T = 0; T < 4; T++) {
      const int col = ct * 144 + 32 * T + 8 * g;
      hv[T] = ld128s(c.sws, (unsigned)(((col >> 3) & 1) * 32 + kh * 16 + tk) * 16u + (unsigned)(col >> 4) * 1024u, (unsigned)WS_HP + (unsigned)(rb * 4 + rg) * 36u * 1024u);
      bv[T] = *(const u32x4*)(c.b2 + col);
    }
    {
      const int col = ct * 144 + 128 + 4 * g;
      hv8 = __builtin_amdgcn_raw_buffer_load_b64(c.sws, (unsigned)(((col >> 3) & 1) * 32 + kh * 16 + tk) * 16u + (unsigned)(col >> 4) * 1024u + (unsigned)(col & 7) * 2u,
                                                 (unsigned)WS_HP + (unsigned)(rb * 4 + rg) * 36u * 1024u, SC1);
      bv8 = *(const u32x2*)(c.b2 + col);
    }
    poll(c, FL_P + (rb * 4 + ct) * 4 + (lane & 3), lane < 4);
    asm volatile("" ::: "memory");
#pragma unroll
    for (int src = 0; src < 4; src++) {
      if (src != ks) {
        const unsigned d = xg + (unsigned)(rg * 4 + src) * 9216u;
        u32x4 pv[4];
#pragma unroll
        for (int T = 0; T < 4; T++) pv[T] = ld128s(c.sws, vl + T * 1024u, d);
        const u32x2 p8 = __builtin_amdgcn_raw_buffer_load_b64(c.sws, 4096u + lane * 8u, d, SC1);
#pragma unroll
        for (int T = 0; T < 4; T++) {
          o[2 * T] += f32x4{bflo(pv[T][0]), bfhi(pv[T][0]), bflo(pv[T][1]), bfhi(pv[T][1])};
          o[2 * T + 1] += f32x4{bflo(pv[T][2]), bfhi(pv[T][2]), bflo(pv[T][3]), bfhi(pv[T][3])};
        }
        o[8] += f32x4{bflo(p8[0]), bfhi(p8[0]), bflo(p8[1]), bfhi(p8[1])};
      }
    }
    u16* orow = c.out + (size_t)row * D + ct * 144;
#pragma unroll
    for (int T = 0; T < 4; T++) {
      u32x4 r;
      r[0] = pk2(bflo(hv[T][0]) + (o[2 * T][0] + bflo(bv[T][0])), bfhi(hv[T][0]) + (o[2 * T][1] + bfhi(bv[T][0])));
      r[1] = pk2(bflo(hv[T][1]) + (o[2 * T][2] + bflo(bv[T][1])), bfhi(hv[T][1]) + (o[2 * T][3] + bfhi(bv[T][1])));
      r[2] = pk2(bflo(hv[T][2]) + (o[2 * T + 1][0] + bflo(bv[T][2])), bfhi(hv[T][2]) + (o[2 * T + 1][1] + bfhi(bv[T][2])));
      r[3] = pk2(bflo(hv[T][3]) + (o[2 * T + 1][2] + bflo(bv[T][3])), bfhi(hv[T][3]) + (o[2 * T + 1][3] + bfhi(bv[T][3])));
      *(u32x4*)(orow + 32 * T + 8 * g) = r;
    }
    u32x2 r8;
    r8[0] = pk2(bflo(hv8[0]) + (o[8][0] + bflo(bv8[0])), bfhi(hv8[0]) + (o[8][1] + bfhi(bv8[0])));
    r8[1] = pk2(bflo(hv8[1]) + (o[8][2] + bflo(bv8[1])), bfhi(hv8[1]) + (o[8][3] + bfhi(bv8[1])));
    *(u32x2*)(orow + 128 + 4 * g) = r8;
  }
  ST(5, 5);
}

constexpr int M_LDS = 163840;
static_assert(L_WO + 110592 <= L_DUMP && 4 * Q_SLOT + 640 <= L_DUMP && F_LDS <= L_DUMP, "LDS map");
__global__ KATTR void fused_block(const u16* __restrict__ x, const u16* __restrict__ wqkv, const u16* __restrict__ wo, const u16* __restrict__ w1,
                                  const u16* __restrict__ b1, const u16* __restrict__ w2, const u16* __restrict__ b2, u16* __restrict__ out, char* __restrict__ ws) {
  extern __shared__ __attribute__((aligned(16))) char smem[];
  if (ENT_CODE) entry_code((unsigned)(size_t)smem, rfl(threadIdx.x >> 6), threadIdx.x & 63);
  Ctx c;
  c.x = x; c.wqkv = wqkv; c.wo = wo; c.w1 = w1; c.b1 = b1; c.w2 = w2; c.b2 = b2; c.out = out; c.ws = ws;
  c.sws = mk_srd(ws, WS_BYTES);
  c.lds0 = (unsigned)(size_t)smem;
  c.b = blockIdx.x; c.xcd = c.b & 7; c.bi = c.b >> 3;
  c.w = rfl(threadIdx.x >> 6); c.lane = threadIdx.x & 63;
  ST(1, 0);
  const int xcd = c.xcd, bi = c.bi, w = c.w, lane = c.lane;
  const int rb4 = (xcd >> 2) * 8 + (bi & 7), fc4 = (xcd & 3) * 4 + (bi >> 3);
  if (bi < 24) {
    p1_qkv(c, (xcd >> 1) * 4 + (bi & 3), (xcd & 1) * 6 + (bi >> 2));
  } else {
    const int j = bi - 24, fc = (xcd & 3) * 4 + (j >> 1), part = (xcd >> 2) * 2 + (j & 1);
    helper_stats(c, xcd * 8 + j);
    relayout_w1(c, fc, part);
    publish(c, FL_W1 + fc * 4 + part, true);
    if (w >= 8) {
      const srd_t sw2 = mk_srd(w2, D * F * 2);
      const int ks = xcd & 3, ct2 = j >> 1, row0 = (ks * 36 + part * 9) * 32;
#pragma unroll
      for (int i = 0; i < 4; i++) {
        const int l = ((w - 8) * 4 + i) * 64 + lane, row = l / 3;
        touch128(c.lds0 + L_DUMP, l < 864 ? (unsigned)row * (D * 2) + (unsigned)(l - 3 * row) * 128u : 0x7ff00000u, sw2, (unsigned)row0 * (D * 2) + (((unsigned)ct2 * 288u) & ~127u));
      }
    }
  }
  ST(1, 4);
  if (bi < 24 && w >= 8) p3_prefetch(c, bi >> 2);
  p2_attn(c, xcd * 4 + (bi & 3), bi >> 2);
  ST(2, 4);
  if (w >= 8) {
    if (w == 8 && bi < 24) poll(c, FL_A + xcd * 32 + (lane & 31), lane < 32);
    if (w == 9) poll(c, FL_W1 + fc4 * 4 + (lane & 3), lane < 4);
    if (w == 10 && c.b == 0) {
#pragma unroll
      for (int i = 0; i < 4; i++) poll(c, FL_D + i * 64 + lane, true);
      if (lane == 0) __hip_atomic_store((unsigned long long*)(ws + FL_N * 128), c.nonce, __ATOMIC_RELAXED, __HIP_MEMORY_SCOPE_AGENT);
    }
    bar();
    f_issue(c, (unsigned)WS_W1P + (unsigned)fc4 * 9 * F_SLOT, 0u);
    {
#pragma unroll
      for (int i = 0; i < 2; i++) { const unsigned l = (unsigned)(((w - 8) * 2 + i) * 64 + lane); touch128(c.lds0 + L_DUMP, l < 288u ? l * 128u : 0x7ff00000u, c.sws, (unsigned)WS_W1P + (unsigned)(fc4 * 9 + 1) * F_SLOT + (unsigned)(bi & 7) * 36864u); }
    }
  }
  if (bi < 24) {
    if (w >= 8) {
      wait_vm<11>(); bar();
      bar(); bar(); bar();
      publish(c, FL_H + (xcd * 4 + (bi & 3)) * 6 + (bi >> 2), false);
    } else {
      p3_wo(c, xcd * 4 + (bi & 3), bi >> 2);
    }
  } else {
    if (w < 8) bar();
    const int j = bi - 24, ks = xcd & 3, ct = j >> 1, part = (xcd >> 2) * 2 + (j & 1);
    if (w < 9) relayout_w2(c, c.lds0 + L_WO + (unsigned)w * 9216u, ks * 36 + part * 9 + w, ct);
    publish(c, FL_W2 + (ks * 4 + ct) * 4 + part, w < 9);
  }
  ST(3, 4);
  if (w >= 8) p45_loader(c, rb4, fc4); else if (w < 4) p45_ffn<0>(c, rb4, fc4); else p45_ffn<1>(c, rb4, fc4);
  asm volatile(".Lfused_code_end:");
}

extern "C" void kernel_launch(void* const* d_in, const int* in_sizes, int n_in, void* d_out, int out_size, void* d_ws, size_t ws_size,
                              hipStream_t stream) {
  (void)hipFuncSetAttribute((const void*)fused_block, hipFuncAttributeMaxDynamicSharedMemorySize, M_LDS);
  fused_block<<<256, 768, M_LDS, stream>>>((const u16*)d_in[0], (const u16*)d_in[1], (const u16*)d_in[2], (const u16*)d_in[4], (const u16*)d_in[5],
                                           (const u16*)d_in[6], (const u16*)d_in[7], (u16*)d_out, (char*)d_ws);
}
```

```cpp
#include <hip/hip_runtime.h>
#include <stdint.h>

typedef unsigned short u16;
typedef __bf16 bf16x8 __attribute__((ext_vector_type(8)));
typedef __bf16 bf16x4 __attribute__((ext_vector_type(4)));
typedef __bf16 bf16x2 __attribute__((ext_vector_type(2)));
typedef float f32x2 __attribute__((ext_vector_type(2)));
typedef float f32x4 __attribute__((ext_vector_type(4)));
typedef float f32x16 __attribute__((ext_vector_type(16)));
typedef unsigned u32x2 __attribute__((ext_vector_type(2)));
typedef unsigned u32x4 __attribute__((ext_vector_type(4)));
#define LAS __attribute__((address_space(3)))
#define DI __device__ __forceinline__
#define SB() __builtin_amdgcn_sched_barrier(0)
#define KATTR __launch_bounds__(768, 3) __attribute__((amdgpu_waves_per_eu(3, 3)))

constexpr int S = 2048, D = 576, H = 32, DH = 18, F = 4608, NQKV = 1728;
constexpr float LN_EPS = 1e-5f;

constexpr size_t WS_FLG = 0;
constexpr int FL_N = 0, FL_D = 1, FL_Q = FL_D + 256, FL_W1 = FL_Q + 192, FL_A = FL_W1 + 64, FL_W2 = FL_A + 256, FL_H = FL_W2 + 64, FL_H1 = FL_H + 192,
              FL_P = FL_H1 + 256, FL_S1 = FL_P + 256, FL_END = FL_S1 + 64;
constexpr size_t WS_QKV = 262144;
static_assert(FL_END * 128 <= WS_QKV, "flag area");
constexpr size_t WS_QKV_SZ = (size_t)3 * H * S * 24 * 2;
constexpr size_t WS_OP = WS_QKV + WS_QKV_SZ;
constexpr size_t WS_OL = WS_OP + (size_t)64 * 32 * 1024;
constexpr size_t WS_HP = WS_OL + (size_t)H * S * 4;
constexpr size_t WS_ST2 = WS_HP + (size_t)S * D * 2;
constexpr size_t WS_W1P = WS_ST2 + (size_t)S * 48;
constexpr size_t WS_W2P = WS_W1P + (size_t)D * F * 2;
constexpr size_t WS_H1P = WS_W2P + (size_t)D * F * 2;
constexpr size_t WS_XCH = WS_H1P + (size_t)S * F * 2;
constexpr size_t WS_ST1 = WS_XCH + (size_t)16 * 4 * 4 * 4 * 2 * 4608;
constexpr size_t WS_CSQ = WS_ST1 + (size_t)S * 8;
constexpr size_t WS_END = WS_CSQ + (size_t)(NQKV + 64) * 4;
constexpr unsigned WS_BYTES = (unsigned)WS_END;

#define ST(kid, idx) asm volatile("; MARK " #kid " " #idx)

DI unsigned rfl(unsigned x) { return __builtin_amdgcn_readfirstlane(x); }
DI float bflo(unsigned w) { return __uint_as_float(w << 16); }
DI float bfhi(unsigned w) { return __uint_as_float(w & 0xffff0000u); }
DI unsigned pk2(float lo, float hi) { f32x2 v = {lo, hi}; bf16x2 b = __builtin_convertvector(v, bf16x2); return __builtin_bit_cast(unsigned, b); }
DI float dot2(unsigned a, unsigned b, float c) { return __builtin_amdgcn_fdot2_f32_bf16(__builtin_bit_cast(bf16x2, a), __builtin_bit_cast(bf16x2, b), c, false); }

typedef __amdgpu_buffer_rsrc_t srd_t;
DI srd_t mk_srd(const void* p, unsigned bytes) { return __builtin_amdgcn_make_buffer_rsrc((void*)p, (short)0, (int)bytes, 0x00020000); }
DI void dma16(unsigned ldsaddr, unsigned voff, srd_t srd, unsigned soff) {
  asm volatile("s_mov_b32 m0, %0\n\ts_nop 0\n\tbuffer_load_dwordx4 %1, %2, %3 offen lds" :: "s"(ldsaddr), "v"(voff), "s"(srd), "s"(soff) : "memory");
}
DI void dma16h(unsigned ldsaddr, unsigned voff, srd_t srd, unsigned soff) {
  asm volatile("s_mov_b32 m0, %0\n\ts_mov_b32 exec_hi, 0\n\tbuffer_load_dwordx4 %1, %2, %3 offen lds\n\ts_mov_b32 exec_hi, -1" :: "s"(ldsaddr), "v"(voff), "s"(srd), "s"(soff) : "memory");
}
DI void dma16q(unsigned ldsaddr, unsigned voff, srd_t srd, unsigned soff) {
  asm volatile("s_mov_b32 m0, %0\n\ts_mov_b64 exec, 0xffff\n\tbuffer_load_dwordx4 %1, %2, %3 offen lds\n\ts_mov_b64 exec, -1" :: "s"(ldsaddr), "v"(voff), "s"(srd), "s"(soff) : "memory");
}
DI void touch128(unsigned ldsdump, unsigned voff, srd_t srd, unsigned soff) {
  asm volatile("s_mov_b32 m0, %0\n\ts_nop 0\n\tbuffer_load_dword %1, %2, %3 offen lds" :: "s"(ldsdump), "v"(voff), "s"(srd), "s"(soff) : "memory");
}
template <int N> DI void wait_vm() { asm volatile("s_waitcnt vmcnt(%0)" :: "i"(N) : "memory"); }
DI void wait_lgkm0() { asm volatile("s_waitcnt lgkmcnt(0)" ::: "memory"); }
DI void bar() { asm volatile("" ::: "memory"); __builtin_amdgcn_s_barrier(); asm volatile("" ::: "memory"); }
DI u32x4 lds_u128(unsigned addr) { return *(const LAS u32x4*)(size_t)addr; }
DI f32x4 lds_f128(unsigned addr) { return *(const LAS f32x4*)(size_t)addr; }
DI void lds_w128(unsigned addr, f32x4 v) { *(LAS f32x4*)(size_t)addr = v; }
DI bf16x8 asbf(u32x4 v) { return __builtin_bit_cast(bf16x8, v); }
DI u32x2 lds_tr(unsigned addr) { return __builtin_bit_cast(u32x2, __builtin_amdgcn_ds_read_tr16_b64_v4bf16((LAS bf16x4*)(size_t)addr)); }
DI u32x4 cat4(u32x2 a, u32x2 b) { return u32x4{a[0], a[1], b[0], b[1]}; }
DI f32x4 mfma16(u32x4 a, u32x4 b, f32x4 c) { return __builtin_amdgcn_mfma_f32_16x16x32_bf16(asbf(a), asbf(b), c, 0, 0, 0); }
DI f32x16 mfma32(u32x4 a, u32x4 b, f32x16 c) { return __builtin_amdgcn_mfma_f32_32x32x16_bf16(asbf(a), asbf(b), c, 0, 0, 0); }
template <int NOP> DI float max16(float m, const f32x16& t) {
  float u;
  asm volatile("s_nop %18\n\tv_max3_f32 %1, %2, %3, %4\n\tv_max3_f32 %0, %0, %5, %6\n\tv_max3_f32 %1, %1, %7, %8\n\tv_max3_f32 %0, %0, %9, %10\n\t"
               "v_max3_f32 %1, %1, %11, %12\n\tv_max3_f32 %0, %0, %13, %14\n\tv_max3_f32 %1, %1, %15, %16\n\tv_max3_f32 %0, %0, %1, %17"
               : "+v"(m), "=&v"(u)
               : "v"(t[0]), "v"(t[1]), "v"(t[2]), "v"(t[3]), "v"(t[4]), "v"(t[5]), "v"(t[6]), "v"(t[7]), "v"(t[8]), "v"(t[9]), "v"(t[10]), "v"(t[11]), "v"(t[12]),
                 "v"(t[13]), "v"(t[14]), "v"(t[15]), "i"(NOP));
  return m;
}
template <int CTRL, int RM> DI float dpp_add(float v) { return v + __builtin_bit_cast(float, __builtin_amdgcn_update_dpp(0, __builtin_bit_cast(int, v), CTRL, RM, 0xf, true)); }
DI float wave_sum63(float v) { v = dpp_add<0x111, 0xf>(v); v = dpp_add<0x112, 0xf>(v); v = dpp_add<0x114, 0xf>(v); v = dpp_add<0x118, 0xf>(v); v = dpp_add<0x142, 0xa>(v); return dpp_add<0x143, 0xc>(v); }
DI float sum_x32(float v) { const auto a = __builtin_amdgcn_permlane32_swap(__float_as_uint(v), __float_as_uint(v), false, false); return __uint_as_float(a[0]) + __uint_as_float(a[1]); }
DI float sum_x16(float v) { const auto a = __builtin_amdgcn_permlane16_swap(__float_as_uint(v), __float_as_uint(v), false, false); return __uint_as_float(a[0]) + __uint_as_float(a[1]); }
DI float sum_x8(float v) { return dpp_add<0x128, 0xf>(v); }
DI int swap23(int i) { return (i & 19) | ((i & 4) << 1) | ((i & 8) >> 1); }

constexpr int SC1 = 16;
DI void dma16s(unsigned ldsaddr, unsigned voff, srd_t srd, unsigned soff) {
  asm volatile("s_mov_b32 m0, %0\n\ts_nop 0\n\tbuffer_load_dwordx4 %1, %2, %3 offen sc1 lds" :: "s"(ldsaddr), "v"(voff), "s"(srd), "s"(soff) : "memory");
}
DI void dma16hs(unsigned ldsaddr, unsigned voff, srd_t srd, unsigned soff) {
  asm volatile("s_mov_b32 m0, %0\n\ts_mov_b32 exec_hi, 0\n\tbuffer_load_dwordx4 %1, %2, %3 offen sc1 lds\n\ts_mov_b32 exec_hi, -1" :: "s"(ldsaddr), "v"(voff), "s"(srd), "s"(soff) : "memory");
}
DI void st128s(u32x4 v, srd_t srd, unsigned voff, unsigned soff) {
  asm volatile("buffer_store_dwordx4 %0, %1, %2, %3 offen sc1\n\ts_nop 1" :: "v"(v), "v"(voff), "s"(srd), "s"(soff) : "memory");
}
DI u32x4 ld128s(srd_t srd, unsigned voff, unsigned soff) { return __builtin_amdgcn_raw_buffer_load_b128(srd, voff, soff, SC1); }

struct Ctx {
  const u16 *x, *wqkv, *wo, *w1, *b1, *w2, *b2;
  u16* out;
  char* ws;
  srd_t sws;
  unsigned lds0;
  unsigned long long nonce;
  int b, xcd, bi, w, lane;
};
DI void publish(const Ctx& c, int flag, bool stored) {
  if (stored) wait_vm<0>();
  bar();
  if (threadIdx.x == 0) __hip_atomic_store((unsigned long long*)(c.ws + (size_t)flag * 128), c.nonce, __ATOMIC_RELAXED, __HIP_MEMORY_SCOPE_AGENT);
}
DI void poll(const Ctx& c, int flag, bool active) {
  const unsigned long long* p = (const unsigned long long*)(c.ws + (size_t)flag * 128);
  const unsigned long long t0 = __builtin_amdgcn_s_memrealtime();
  for (;;) {
    const unsigned long long v = active ? __hip_atomic_load(p, __ATOMIC_RELAXED, __HIP_MEMORY_SCOPE_AGENT) : c.nonce;
    if (__all(v == c.nonce)) break;
    if (__builtin_amdgcn_s_memrealtime() - t0 > 2000000ull) break;
  }
}

#define ENT_CODE 1
constexpr int L_DUMP = 163840 - 256;
DI u32x2 entry_load(const Ctx& c) {
  u32x2 nv;
  asm volatile("buffer_load_dwordx2 %0, %1, %2, 0 offen sc1" : "=&v"(nv) : "v"((unsigned)(FL_N * 128)), "s"(c.sws) : "memory");
  return nv;
}
DI void entry_code(unsigned lds0, int w, int lane) {
  unsigned long long pc; unsigned len;
  asm volatile("s_getpc_b64 %0\n.Lcp%=:\n\ts_mov_b32 %1, .Lfused_code_end-.Lcp%=" : "=s"(pc), "=s"(len));
  touch128(lds0 + L_DUMP, (unsigned)(w * 64 + lane) * 128u, mk_srd((const void*)pc, len), 0u);
}
DI void entry_prefetch(const Ctx& c) {
  const int l = c.w - 8;
  const srd_t st = l == 1 ? mk_srd(c.out, S * D * 2) : c.sws;
  touch128(c.lds0 + L_DUMP, l == 0 ? (c.lane < 24 ? ((unsigned)c.bi * 24u + (unsigned)c.lane) * 65536u : 0x7ff00000u) : (l == 1 ? (unsigned)c.lane * 65536u : 0x7ff00000u), st, 0u);
}
DI void entry_nonce(Ctx& c, u32x2 nv) {
  asm volatile("" : "+v"(nv));
  c.nonce = (((unsigned long long)rfl(nv[1]) << 32) | rfl(nv[0])) + 1ull;
  if (threadIdx.x == 0) __hip_atomic_store((unsigned long long*)(c.ws + (size_t)(FL_D + c.b) * 128), c.nonce, __ATOMIC_RELAXED, __HIP_MEMORY_SCOPE_AGENT);
}

constexpr int L_W1 = 4096;
DI void relayout_w1_fetch(const Ctx& c, int fc, int part) {
  const int l = c.w - 8, lane = c.lane;
  const srd_t sw = mk_srd(c.w1, D * F * 2);
#pragma unroll 1
  for (int pc = l; pc < 81; pc += 4) {
    const int ch = pc * 64 + lane, row = ch / 36, cc = ch % 36;
    dma16(c.lds0 + L_W1 + (unsigned)pc * 1024u, (unsigned)row * (F * 2) + (unsigned)cc * 16u, sw, (unsigned)part * 144u * (F * 2) + (unsigned)fc * 576u);
  }
}
DI void helper_stats(Ctx& c, int hb, int fc, int part) {
  const int w = c.w, lane = c.lane;
  const srd_t sx = mk_srd(c.x, S * D * 2), sw = mk_srd(c.wqkv, hb < 27 ? D * NQKV * 2 : 0);
  if (w >= 8) {
    const u32x2 nv = entry_load(c);
    entry_prefetch(c);
    wait_vm<1>();
    entry_nonce(c, nv);
    bar();
    publish(c, FL_S1 + hb, false);
    relayout_w1_fetch(c, fc, part);
    return;
  }
  const int tid = w * 64 + lane, ch = tid & 7, rg = tid >> 3;
  u32x4 a[4], b2[4], v9[9];
#pragma unroll
  for (int r = 0; r < 4; r++) {
    const unsigned so = (unsigned)(hb * 32 + w * 4 + r) * (D * 2);
    a[r] = __builtin_amdgcn_raw_buffer_load_b128(sx, (unsigned)lane * 16u, so, 0);
    b2[r] = __builtin_amdgcn_raw_buffer_load_b128(sx, lane < 8 ? 1024u + (unsigned)lane * 16u : 0x7ff00000u, so, 0);
  }
#pragma unroll
  for (int i = 0; i < 9; i++) v9[i] = __builtin_amdgcn_raw_buffer_load_b128(sw, (unsigned)(rg * 9 + i) * (NQKV * 2) + (unsigned)ch * 16u, (unsigned)hb * 128u, 0);
  const u32x2 nv = entry_load(c);
  float sm[4], sq[4];
#pragma unroll
  for (int r = 0; r < 4; r++) {
    sm[r] = 0; sq[r] = 0;
#pragma unroll
    for (int e = 0; e < 4; e++) { sm[r] = dot2(a[r][e], 0x3f803f80u, sm[r]); sq[r] = dot2(a[r][e], a[r][e], sq[r]); sm[r] = dot2(b2[r][e], 0x3f803f80u, sm[r]); sq[r] = dot2(b2[r][e], b2[r][e], sq[r]); }
  }
#pragma unroll
  for (int r = 0; r < 4; r++) {
    const float s1 = __builtin_bit_cast(float, __builtin_amdgcn_readlane(__builtin_bit_cast(int, wave_sum63(sm[r])), 63));
    const float s2 = __builtin_bit_cast(float, __builtin_amdgcn_readlane(__builtin_bit_cast(int, wave_sum63(sq[r])), 63));
    const float mean = s1 * (1.0f / D), var = fmaxf(s2 * (1.0f / D) - mean * mean, 0.f);
    sm[r] = mean; sq[r] = rsqrtf(var + LN_EPS);
  }
  {
    const int r = lane & 3;
    const u32x2 v = {__float_as_uint(r == 0 ? sm[0] : r == 1 ? sm[1] : r == 2 ? sm[2] : sm[3]), __float_as_uint(r == 0 ? sq[0] : r == 1 ? sq[1] : r == 2 ? sq[2] : sq[3])};
    __builtin_amdgcn_raw_buffer_store_b64(v, c.sws, lane < 4 ? (unsigned)lane * 8u : 0x7ff00000u, (unsigned)WS_ST1 + (unsigned)(hb * 32 + w * 4) * 8u, SC1);
  }
  float s8[8] = {0, 0, 0, 0, 0, 0, 0, 0};
#pragma unroll
  for (int i = 0; i < 9; i++)
#pragma unroll
    for (int e = 0; e < 4; e++) { s8[2 * e] += bflo(v9[i][e]); s8[2 * e + 1] += bfhi(v9[i][e]); }
#pragma unroll
  for (int e = 0; e < 8; e++) s8[e] = sum_x32(sum_x16(sum_x8(s8[e])));
  if (lane < 8) { lds_w128(c.lds0 + (unsigned)(w * 64 + lane * 8) * 4u, f32x4{s8[0], s8[1], s8[2], s8[3]}); lds_w128(c.lds0 + (unsigned)(w * 64 + lane * 8) * 4u + 16, f32x4{s8[4], s8[5], s8[6], s8[7]}); }
  wait_lgkm0(); bar();
  if (w == 0) {
    float t = 0;
#pragma unroll
    for (int k = 0; k < 8; k++) t += *(const LAS float*)(size_t)(c.lds0 + (unsigned)(k * 64 + lane) * 4u);
    __builtin_amdgcn_raw_buffer_store_b32(__float_as_uint(t), c.sws, hb < 27 ? (unsigned)lane * 4u : 0x7ff00000u, (unsigned)WS_CSQ + (unsigned)hb * 256u, SC1);
  }
  wait_lgkm0();
  wait_vm<0>();
  entry_nonce(c, nv);
  publish(c, FL_S1 + hb, true);
}

DI void relayout_w1(const Ctx& c, int fc, int part) {
  const int w = c.w, lane = c.lane; const unsigned lds0 = c.lds0 + L_W1;
  const int h = lane >> 5, blk = (lane >> 4) & 1, q = (lane >> 2) & 3, p = lane & 3;
  const unsigned ra = lds0 + (unsigned)(8 * h + q) * 576u + (unsigned)(16 * blk + 8 * (p & 1) + 4 * (p >> 1)) * 2u;
  for (int fr = w; fr < 81; fr += 12) {
    const int sl = fr / 9, tile = fr % 9, s = part * 9 + sl, kh = s / 18;
    const unsigned a = ra + (unsigned)sl * 16u * 576u + (unsigned)tile * 64u;
    const u32x4 v = cat4(lds_tr(a), lds_tr(a + 4 * 576));
    const int slot = tile == 8 ? 8 : (kh == 0 ? (tile ^ 1) : tile);
    const int s18 = s % 18;
    st128s(v, c.sws, (unsigned)lane * 16u, (unsigned)WS_W1P + ((unsigned)(fc * 9 + slot) * 36u + (unsigned)((s18 / 9) * 18 + kh * 9 + s18 % 9)) * 1024u);
  }
}
DI void relayout_w2(const Ctx& c, unsigned ldsw, int kappa, int ct) {
  const int lane = c.lane;
  const srd_t sw = mk_srd(c.w2, D * F * 2);
#pragma unroll
  for (int pc = 0; pc < 9; pc++) {
    const int ch = pc * 64 + lane, row = ch / 18, cc = ch % 18;
    dma16(ldsw + (unsigned)pc * 1024u, (unsigned)row * (D * 2) + (unsigned)cc * 16u, sw, (unsigned)kappa * 32u * (D * 2) + (unsigned)ct * 288u);
  }
  wait_vm<0>();
  const int g = lane >> 4, q = (lane >> 2) & 3, p = lane & 3;
  const unsigned ra = ldsw + (unsigned)(8 * g + q) * 288u;
  const int ks = kappa / 36, kst = (kappa % 36 + 36 - 9 * ct) % 36;
#pragma unroll
  for (int nt = 0; nt < 9; nt++) {
    const unsigned col = nt < 8 ? (unsigned)(32 * (nt >> 1) + 8 * p + 4 * (nt & 1)) * 2u : (unsigned)(128 + 4 * p) * 2u;
    const u32x4 v = cat4(lds_tr(ra + col), lds_tr(ra + col + 4 * 288));
    st128s(v, c.sws, (unsigned)lane * 16u, (unsigned)WS_W2P + ((unsigned)(ks * 4 + ct) * 324u + (unsigned)(((kst >> 2) * 2 + (kst & 1)) * 18 + ((kst >> 1) & 1) * 9 + nt)) * 1024u);
  }
  wait_lgkm0();
}

#define ABL_QW 1
#define ABL_QX 1
constexpr int Q_SLOT = 36864;
constexpr int Q_CT = 4 * Q_SLOT;
DI unsigned q_voff(int p, int lane) {
  if (p < 16) return (unsigned)(lane >> 3) * (D * 2) + (unsigned)(((lane & 7) ^ ((4 * (p & 1) + (lane >> 4)) & 7)) * 16);
  const int ch = 64 * (p - 16) + lane, row = ch / 20, cc = ch % 20;
  return (unsigned)row * (NQKV * 2) + (unsigned)cc * 16u;
}
DI void q_issue(const Ctx& c, srd_t sx, srd_t sw, int rb, int ct, int p, unsigned voff, int t, unsigned ring) {
  if (p < 16) dma16(c.lds0 + ring + (unsigned)p * 1024u, voff, sx, ((unsigned)rb * 128u + (unsigned)p * 8u) * (D * 2) + (unsigned)t * 128u);
  else dma16(c.lds0 + ring + (unsigned)p * 1024u, voff, sw, (unsigned)ct * 288u + (unsigned)t * 64u * (NQKV * 2));
}
DI void p1_loader(Ctx& c, int rb, int ct) {
  const int w = c.w, lane = c.lane, l = w - 8;
  const srd_t sx = mk_srd(c.x, S * D * 2), sw = mk_srd(c.wqkv, D * NQKV * 2);
  const u32x2 nv = entry_load(c);
#pragma unroll
  for (int t = 0; t < 2; t++)
#pragma unroll
    for (int i = 0; i < 3; i++) q_issue(c, sx, sw, rb, ct, w + 12 * i, q_voff(w + 12 * i, lane), t, (unsigned)t * Q_SLOT);
  unsigned vw[5];
#pragma unroll
  for (int i = 0; i < 5; i++) vw[i] = q_voff(16 + l + 4 * i, lane);
#define Q_W_ISSUE(t, ring) do { if (ABL_QW || (t) < 3) \
    _Pragma("unroll") for (int i = 0; i < 5; i++) dma16(c.lds0 + (ring) + (unsigned)(16 + l + 4 * i) * 1024u, vw[i], sw, (unsigned)ct * 288u + (unsigned)(t) * 64u * (NQKV * 2)); } while (0)
  Q_W_ISSUE(2, 2u * Q_SLOT);
  entry_prefetch(c);
  wait_vm<6>();
  entry_nonce(c, nv);
  bar();
  Q_W_ISSUE(3, 3u * Q_SLOT);
#pragma unroll 1
  for (int t = 1; t < 6; t++) {
    wait_vm<5>(); bar();
    Q_W_ISSUE(t + 3, (unsigned)((t + 3) & 3) * Q_SLOT);
  }
  wait_vm<5>(); bar();
  wait_vm<0>(); bar();
  if (l == 0) { const int g0 = (144 * ct) >> 6; poll(c, FL_S1 + (lane < 4 ? 4 * rb + lane : min(g0 + (lane & 3), 26)), lane < 8); }
  bar();
  bar();
  bar();
}
template <int NT, int T0>
DI void p1_body(Ctx& c, int rb, int ct) {
  const int w = c.w, lane = c.lane, rg = w & 3; const unsigned lds0 = c.lds0;
  constexpr bool XI = NT == 2;
  const srd_t sx = mk_srd(c.x, S * D * 2);
  const unsigned vx = q_voff(rg, lane);
#define Q_X_ISSUE1(t, ring, i) dma16(lds0 + (ring) + (unsigned)(rg + 4 * (i)) * 1024u, vx, sx, ((unsigned)rb * 128u + (unsigned)(rg + 4 * (i)) * 8u) * (D * 2) + (unsigned)(t) * 128u)
  {
    const srd_t sw = mk_srd(c.wqkv, D * NQKV * 2);
    const u32x2 nv = entry_load(c);
#pragma unroll
    for (int t = 0; t < 2; t++)
#pragma unroll
      for (int i = 0; i < 3; i++) q_issue(c, sx, sw, rb, ct, w + 12 * i, q_voff(w + 12 * i, lane), t, (unsigned)t * Q_SLOT);
    if (XI) { Q_X_ISSUE1(2, 2u * Q_SLOT, 0); Q_X_ISSUE1(2, 2u * Q_SLOT, 1); Q_X_ISSUE1(2, 2u * Q_SLOT, 2); Q_X_ISSUE1(2, 2u * Q_SLOT, 3); wait_vm<4>(); } else wait_vm<0>();
    entry_nonce(c, nv);
  }
  const int tok = lane & 31, h = lane >> 5, blk = (lane >> 4) & 1, q4 = (lane >> 2) & 3, pp = lane & 3;
  const unsigned xrow = lds0 + (unsigned)(rg * 32 + tok) * 128u;
  unsigned xj[4];
#pragma unroll
  for (int j = 0; j < 4; j++) xj[j] = xrow + (unsigned)(((2 * j + h) ^ ((tok >> 1) & 7)) * 16);
  const unsigned wj = lds0 + 16384u + (unsigned)(8 * h + q4) * 320u + (unsigned)(32 * T0 + 16 * blk + 8 * (pp & 1) + 4 * (pp >> 1)) * 2u;
  f32x16 acc[NT];
#pragma unroll
  for (int n = 0; n < NT; n++) acc[n] = f32x16{};
  ST(1, 1);
  bar();
  ST(1, 2);
  u32x4 wf[4][NT], xf[4];
#pragma unroll
  for (int j = 0; j < 2; j++) {
    xf[j] = lds_u128(xj[j]);
#pragma unroll
    for (int n = 0; n < NT; n++) wf[j][n] = cat4(lds_tr(wj + j * 5120 + n * 64), lds_tr(wj + j * 5120 + n * 64 + 4 * 320));
  }
  unsigned r0 = 0, r1 = Q_SLOT, r3 = 3 * Q_SLOT;
#define Q_PERIOD(t, SYNC, ISSUE, EXTRA) do { \
    if (XI && (SYNC) == 2) wait_vm<4>(); \
    if (XI && (SYNC) == 3) wait_vm<0>(); \
    if (SYNC) bar(); \
    EXTRA; \
    unsigned wa_ = wj + r0, wn_ = wj + r1, xa_[4]; \
    _Pragma("unroll") for (int j = 0; j < 4; j++) { xa_[j] = xj[(j + 2) & 3] + (j < 2 ? r0 : r1); asm volatile("" : "+v"(xa_[j])); } \
    asm volatile("" : "+v"(wa_), "+v"(wn_)); \
    _Pragma("unroll") for (int j = 0; j < 4; j++) { \
      const u32x4 xc = xf[j]; \
      xf[(j + 2) & 3] = lds_u128(xa_[j]); \
      _Pragma("unroll") for (int n = 0; n < NT; n++) { \
        const u32x4 cur = wf[j][n]; \
        const unsigned a_ = (j < 2 ? wa_ : wn_) + ((j + 2) & 3) * 5120 + n * 64; \
        wf[(j + 2) & 3][n] = cat4(lds_tr(a_), lds_tr(a_ + 4 * 320)); \
        acc[n] = mfma32(cur, xc, acc[n]); \
        if (ABL_QX && XI && (ISSUE) && n == 0) Q_X_ISSUE1((t) + 3, r3, j); \
        SB(); \
      } \
    } \
    r0 = r1; r1 = r1 == 3u * Q_SLOT ? 0u : r1 + Q_SLOT; r3 = r3 == 3u * Q_SLOT ? 0u : r3 + Q_SLOT; \
  } while (0)
  Q_PERIOD(0, 0, true, (void)0);
#pragma unroll 1
  for (int t = 1; t < 6; t++) Q_PERIOD(t, 2, true, (void)0);
  const int tl = rg * 32 + tok, type = ct >> 2;
  u32x2 stv; u32x4 csv;
  Q_PERIOD(6, 2, false, (void)0);
  Q_PERIOD(7, 3, false, (void)0);
  ST(6, 1);
  Q_PERIOD(8, 1, false, (
    { asm volatile("buffer_load_dwordx2 %0, %1, %2, %3 offen sc1" : "=&v"(stv) : "v"((unsigned)tl * 8u), "s"(c.sws), "s"((unsigned)WS_ST1 + (unsigned)rb * 1024u) : "memory");
      asm volatile("buffer_load_dwordx4 %0, %1, %2, %3 offen sc1" : "=&v"(csv) : "v"((unsigned)min(lane, 39) * 16u), "s"(c.sws), "s"((unsigned)WS_CSQ + (unsigned)ct * 576u) : "memory"); }));
  asm volatile("s_waitcnt vmcnt(0)" : "+v"(stv), "+v"(csv) :: "memory");
  ST(6, 2);
  if (w == 0 && lane < 40) *(LAS u32x4*)(size_t)(lds0 + Q_CT + (unsigned)lane * 16u) = csv;
  ST(1, 3);
  wait_lgkm0(); bar();
  ST(1, 5);
  f32x2 st;
  st[0] = __uint_as_float(stv[1]) * (type == 0 ? 0.23570226039551584f * 1.4426950408889634f : 1.0f);
  st[1] = -__uint_as_float(stv[0]) * st[0];
#pragma unroll
  for (int n = 0; n < NT; n++)
#pragma unroll
    for (int hf = 0; hf < 2; hf++) {
      if (T0 + n == 4 && hf == 1) continue;
      const int c0 = 32 * (T0 + n) + 16 * hf;
      const f32x4 ca = lds_f128(lds0 + Q_CT + (unsigned)(c0 + 8 * h) * 4u), cb = lds_f128(lds0 + Q_CT + (unsigned)(c0 + 8 * h) * 4u + 16);
      u32x4 o;
#pragma unroll
      for (int e = 0; e < 4; e++) {
        const float c0v = e < 2 ? ca[2 * e] : cb[2 * e - 4], c1v = e < 2 ? ca[2 * e + 1] : cb[2 * e - 3];
        o[e] = pk2(__builtin_fmaf(acc[n][8 * hf + 2 * e], st[0], c0v * st[1]), __builtin_fmaf(acc[n][8 * hf + 2 * e + 1], st[0], c1v * st[1]));
      }
      *(LAS u32x4*)(size_t)(lds0 + (unsigned)tl * 304u + (unsigned)(c0 + 8 * h) * 2u) = o;
    }
  wait_lgkm0(); bar();
  ST(1, 6);
}
DI void p1_qkv(Ctx& c, int rb, int ct) {
  if (c.w >= 8) p1_loader(c, rb, ct); else if (c.w < 4) p1_body<3, 0>(c, rb, ct); else p1_body<2, 3>(c, rb, ct);
  {
    const int tid = c.w * 64 + c.lane, tg = tid >= 384, t = tid - 384 * tg, tk = t / 3, part = t - 3 * tk, type = ct >> 2;
    const unsigned src = c.lds0 + (unsigned)tk * 304u + (unsigned)part * 16u + (unsigned)tg * 144u;
    const unsigned keep = part < 2 ? 0xffffffffu : 0u, padv = part < 2 ? 0u : (type == 0 ? 0x3f800000u : 0x00003f80u);
    const unsigned dst = (unsigned)WS_QKV + ((unsigned)(type * 32 + (ct & 3) * 8) * S + (unsigned)rb * 128u) * 48u;
    u32x4 v[4];
#pragma unroll
    for (int hh = 0; hh < 4; hh++)
#pragma unroll
      for (int e = 0; e < 4; e++) v[hh][e] = *(const LAS unsigned*)(size_t)(src + hh * 36 + e * 4);
#pragma unroll
    for (int hh = 0; hh < 4; hh++) {
      v[hh][1] = (v[hh][1] & keep) | padv; v[hh][2] &= keep; v[hh][3] &= keep;
      st128s(v[hh], c.sws, (unsigned)t * 16u + (unsigned)(4 * tg + hh) * (S * 48), dst);
    }
  }
  wait_lgkm0();
  ST(1, 7);
  publish(c, FL_Q + rb * 12 + ct, true);
}

constexpr int L_K = 0, L_V = 24576, L_WO = 49216;
DI void p2_attn(const Ctx& c, int head, int qb) {
  const int w = c.w, lane = c.lane; const unsigned lds0 = c.lds0;
  const unsigned ldsK = lds0 + L_K, ldsV = lds0 + L_V;
  if (w == 0) {
    const int hg = head >> 3, l = lane;
    const int ty = l < 2 ? 0 : (l < 6 ? 1 : 2), rbl = l < 2 ? 2 * qb + l : 2 * qb - 2 + ((l - 2) & 3);
    poll(c, FL_Q + rbl * 12 + ty * 4 + hg, l < 10 && rbl >= 0);
  }
  bar();
  ST(2, 1);
  if (w >= 8) { bar(); publish(c, FL_A + qb * 32 + head, false); return; }
  const unsigned kbase = (unsigned)WS_QKV + (unsigned)((32 + head) * S + 256 * (qb - 1)) * 48u, vbase = (unsigned)WS_QKV + (unsigned)((64 + head) * S + 256 * (qb - 1)) * 48u;
  const unsigned vl = lane * 16;
#pragma unroll
  for (int hf = 0; hf < 2; hf++) {
    if (hf == 1 || qb > 0) {
#pragma unroll
      for (int j = 0; j < 3; j++) {
        const int pi = w * 3 + j;
        const unsigned pc = (unsigned)hf * 12288u + (unsigned)(pi % 12) * 1024;
        if (pi < 12) dma16s(ldsK + pc, vl, c.sws, kbase + pc); else dma16s(ldsV + pc, vl, c.sws, vbase + pc);
      }
    }
  }
  if (qb == 0) {
    const int tid = w * 64 + lane;
    if (tid < 256) {
      const u32x4 z = {0, 0, 0, 0};
      *(LAS u32x4*)(size_t)(ldsK + tid * 48) = z; *(LAS u32x4*)(size_t)(ldsK + tid * 48 + 16) = z; *(LAS u32x4*)(size_t)(ldsK + tid * 48 + 32) = u32x4{0, 0xf1493f80u, 0, 0};
      *(LAS u32x4*)(size_t)(ldsV + tid * 48) = z; *(LAS u32x4*)(size_t)(ldsV + tid * 48 + 16) = z; *(LAS u32x4*)(size_t)(ldsV + tid * 48 + 32) = z;
    }
  }
  const int qq = lane & 31, h = lane >> 5;
  const int q0 = 256 * qb + 32 * w;
  const unsigned qo = (unsigned)WS_QKV + (unsigned)(head * S + q0) * 48u;
  const u32x4 qf0 = ld128s(c.sws, (unsigned)qq * 48u + 16u * h, qo);
  u32x4 qf1 = ld128s(c.sws, (unsigned)qq * 48u + 32u, qo);
  if (h) qf1 = u32x4{0, 0, 0, 0};
  f32x16 cold, cdiag;
#pragma unroll
  for (int r = 0; r < 16; r++) {
    const int kk = 16 * (r >> 3) + 8 * h + (r & 7);
    cold[r] = kk > qq ? 0.f : -1e30f;
    cdiag[r] = kk <= qq ? 0.f : -1e30f;
  }
  const int key = swap23(lane & 31);
  unsigned ka0 = ldsK + (unsigned)(key + 32 * w) * 48 + 16 * h;
  unsigned ka1 = ldsK + (unsigned)(key + 32 * w) * 48 + 32;
  const int blk = (lane >> 4) & 1, q4 = (lane >> 2) & 3, pp = lane & 3;
  unsigned va = ldsV + (unsigned)(32 * w + 8 * h + q4) * 48 + 32 * blk + 8 * pp;
  asm volatile("" : "+v"(ka0), "+v"(ka1), "+v"(va), "+v"(cold), "+v"(cdiag));
  wait_vm<0>(); wait_lgkm0(); bar();
  ST(2, 2);
  const f32x16 zero = {};
  u32x4 k0[9], k1[9];
#pragma unroll
  for (int j = 0; j < 9; j++) { k0[j] = lds_u128(ka0 + j * 1536); k1[j] = lds_u128(ka1 + j * 1536); }
  float m = -1e30f;
  {
    f32x16 sA = mfma32(k1[0], qf1, mfma32(k0[0], qf0, cold)), sB;
#pragma unroll
    for (int j = 0; j < 9; j++) {
      f32x16& cur = (j & 1) ? sB : sA; f32x16& nxt = (j & 1) ? sA : sB;
      if (j + 1 < 9) nxt = mfma32(k1[j + 1], qf1, mfma32(k0[j + 1], qf0, j + 1 == 8 ? cdiag : zero));
      SB();
      m = j == 0 ? max16<11>(m, cur) : (j < 8 ? max16<1>(m, cur) : max16<3>(m, cur));
      SB();
    }
  }
  { const auto pr = __builtin_amdgcn_permlane32_swap(__float_as_uint(m), __float_as_uint(m), false, false); m = fmaxf(__uint_as_float(pr[0]), __uint_as_float(pr[1])); }
  if (h == 0) qf1[1] = (qf1[1] & 0xffff0000u) | (pk2(-m, 0.f) & 0xffffu);
  ST(2, 3);
  f32x16 oacc = {};
  {
    f32x16 sA = mfma32(k1[0], qf1, mfma32(k0[0], qf0, cold)), sB;
    u32x4 v0 = cat4(lds_tr(va), lds_tr(va + 4 * 48)), v1 = cat4(lds_tr(va + 16 * 48), lds_tr(va + 20 * 48));
#pragma unroll
    for (int j = 0; j < 9; j++) {
      f32x16& cur = (j & 1) ? sB : sA; f32x16& nxt = (j & 1) ? sA : sB;
      if (j + 1 < 9) nxt = mfma32(k0[j + 1], qf0, j + 1 == 8 ? cdiag : zero);
      SB();
      u32x4 p0, p1;
      {
        float e0 = cur[0]; asm volatile("" : "+v"(e0));
        p0[0] = pk2(__builtin_amdgcn_exp2f(e0), __builtin_amdgcn_exp2f(cur[1]));
#pragma unroll
        for (int e = 1; e < 4; e++) p0[e] = pk2(__builtin_amdgcn_exp2f(cur[2 * e]), __builtin_amdgcn_exp2f(cur[2 * e + 1]));
        asm volatile("" : "+v"(p0));
      }
      SB();
      if (j + 1 < 9) nxt = mfma32(k1[j + 1], qf1, nxt);
      oacc = mfma32(v0, p0, oacc);
      if (j + 1 < 9) v0 = cat4(lds_tr(va + (j + 1) * 1536), lds_tr(va + (j + 1) * 1536 + 4 * 48));
      SB();
      {
        float e8 = cur[8]; asm volatile("" : "+v"(e8));
        p1[0] = pk2(__builtin_amdgcn_exp2f(e8), __builtin_amdgcn_exp2f(cur[9]));
#pragma unroll
        for (int e = 1; e < 4; e++) p1[e] = pk2(__builtin_amdgcn_exp2f(cur[8 + 2 * e]), __builtin_amdgcn_exp2f(cur[8 + 2 * e + 1]));
        asm volatile("" : "+v"(p1));
      }
      SB();
      oacc = mfma32(v1, p1, oacc);
      if (j + 1 < 9) v1 = cat4(lds_tr(va + (j + 1) * 1536 + 16 * 48), lds_tr(va + (j + 1) * 1536 + 20 * 48));
      SB();
    }
  }
  const float l = __uint_as_float(__builtin_amdgcn_permlane32_swap(__float_as_uint(oacc[10]), __float_as_uint(oacc[10]), false, false)[0]);
  const float inv = __builtin_amdgcn_rcpf(l);
  const u32x4 om = {pk2(oacc[0] * inv, oacc[1] * inv), pk2(oacc[2] * inv, oacc[3] * inv), pk2(oacc[4] * inv, oacc[5] * inv), pk2(oacc[6] * inv, oacc[7] * inv)};
  st128s(om, c.sws, (unsigned)lane * 16u, (unsigned)WS_OP + (unsigned)((q0 >> 5) * 32 + head) * 1024u);
  __builtin_amdgcn_raw_buffer_store_b32(pk2(oacc[8] * inv, oacc[9] * inv), c.sws, h == 0 ? (unsigned)qq * 4u : 0x7ff00000u, (unsigned)WS_OL + (unsigned)(head * S + q0) * 4u, SC1);
  publish(c, FL_A + qb * 32 + head, true);
}

DI void p3_prefetch(const Ctx& c, int ct) {
  const int l = c.w - 8, lane = c.lane;
  const srd_t sw = mk_srd(c.wo, D * D * 2);
  unsigned vw[3];
#pragma unroll
  for (int i = 0; i < 3; i++) { const int ch = 64 * i + lane; vw[i] = (unsigned)(ch / 12) * (D * 2) + (unsigned)(ch % 12) * 16u; }
#pragma unroll 1
  for (int gi = 0; gi < 9; gi++) {
    const int grp = l * 9 + gi;
#pragma unroll
    for (int i = 0; i < 3; i++) dma16(c.lds0 + L_WO + (unsigned)(grp * 3 + i) * 1024u, vw[i], sw, (unsigned)grp * 16u * (D * 2) + (unsigned)ct * 192u);
  }
}
template <int KQ>
DI void p3_body(const Ctx& c, int rb, int ct) {
  constexpr int kq = KQ;
  const int lane = c.lane, rg = c.w & 1; const unsigned lds0 = c.lds0;
  const int qt = rb * 2 + rg, tok = lane & 31, h = lane >> 5;
  const int row = qt * 32 + tok;
  const u32x4 xr0 = *(const u32x4*)(c.x + (size_t)row * D + ct * 96 + 16 * kq + 8 * h);
  const u32x4 xr1 = *(const u32x4*)(c.x + (size_t)row * D + ct * 96 + 16 * (kq < 2 ? kq + 4 : kq) + 8 * h);
  const int blk = (lane >> 4) & 1, q4 = (lane >> 2) & 3, pp = lane & 3;
  const unsigned colb = lds0 + L_WO + (unsigned)(16 * blk + 8 * (pp & 1) + 4 * (pp >> 1)) * 2u;
  unsigned ah = colb + (unsigned)(18 * 9 * kq + 4 * h + q4) * 192u;
  unsigned al = colb + (unsigned)(18 * (4 * h + (q4 >> 1)) + 16 + (q4 & 1)) * 192u;
  asm volatile("" : "+v"(ah), "+v"(al));
#define P3_W(s, n) ((kq < 3 || (s) < 5) ? cat4(lds_tr(ah + (s) * 3456 + (n) * 64), lds_tr(ah + (s) * 3456 + 1536 + (n) * 64)) \
                                        : cat4(lds_tr(al + ((s) - 5) * 27648 + (n) * 64), lds_tr(al + ((s) - 5) * 27648 + 6912 + (n) * 64)))
  bar();
  ST(3, 1);
  u32x4 of[9];
  {
    const unsigned so = (unsigned)WS_OP + (unsigned)(qt * 32 + kq * 9) * 1024u;
#pragma unroll
    for (int s = 0; s < (kq < 3 ? 9 : 5); s++) of[s] = ld128s(c.sws, (unsigned)lane * 16u + s * 1024u, so);
    if (kq == 3) {
#pragma unroll
      for (int G = 0; G < 4; G++)
#pragma unroll
        for (int i = 0; i < 4; i++)
          of[5 + G][i] = __builtin_amdgcn_raw_buffer_load_b32(c.sws, (unsigned)((4 * h + i) * S + tok) * 4u, (unsigned)WS_OL + (unsigned)(8 * G * S + qt * 32) * 4u, SC1);
    }
  }
  u32x4 wf[3][3];
#pragma unroll
  for (int s = 0; s < 2; s++)
#pragma unroll
    for (int n = 0; n < 3; n++) wf[s][n] = P3_W(s, n);
  SB();
  ST(3, 2);
  f32x16 acc[3] = {};
#pragma unroll
  for (int s = 0; s < 9; s++) {
#pragma unroll
    for (int n = 0; n < 3; n++) {
      if (s + 2 < 9) wf[(s + 2) % 3][n] = P3_W(s + 2, n);
      acc[n] = mfma32(wf[s % 3][n], of[s], acc[n]);
    }
    SB();
  }
  ST(3, 3);
  wait_lgkm0(); bar();
  const unsigned xb = lds0 + L_WO + (unsigned)rg * 49152u + (unsigned)lane * 16u;
#pragma unroll
  for (int f = 0; f < 6; f++) {
    if ((f & 3) == kq) continue;
    const int r8 = 8 * (f & 1);
    lds_w128(xb + (unsigned)(f * 4 + kq) * 2048u, f32x4{acc[f >> 1][r8], acc[f >> 1][r8 + 1], acc[f >> 1][r8 + 2], acc[f >> 1][r8 + 3]});
    lds_w128(xb + (unsigned)(f * 4 + kq) * 2048u + 1024u, f32x4{acc[f >> 1][r8 + 4], acc[f >> 1][r8 + 5], acc[f >> 1][r8 + 6], acc[f >> 1][r8 + 7]});
  }
  wait_lgkm0(); bar();
  float ssum = 0, ssq = 0;
#pragma unroll
  for (int i = 0; i < (kq < 2 ? 2 : 1); i++) {
    const int f = kq + 4 * i, r8 = 8 * (f & 1);
    f32x4 a = {acc[f >> 1][r8], acc[f >> 1][r8 + 1], acc[f >> 1][r8 + 2], acc[f >> 1][r8 + 3]}, cc = {acc[f >> 1][r8 + 4], acc[f >> 1][r8 + 5], acc[f >> 1][r8 + 6], acc[f >> 1][r8 + 7]};
#pragma unroll
    for (int k = 0; k < 4; k++)
      if (k != kq) { a += lds_f128(xb + (unsigned)(f * 4 + k) * 2048u); cc += lds_f128(xb + (unsigned)(f * 4 + k) * 2048u + 1024u); }
    const u32x4 xr = i ? xr1 : xr0;
    u32x4 o;
    o[0] = pk2(bflo(xr[0]) + a[0], bfhi(xr[0]) + a[1]); o[1] = pk2(bflo(xr[1]) + a[2], bfhi(xr[1]) + a[3]);
    o[2] = pk2(bflo(xr[2]) + cc[0], bfhi(xr[2]) + cc[1]); o[3] = pk2(bflo(xr[3]) + cc[2], bfhi(xr[3]) + cc[3]);
    st128s(o, c.sws, (unsigned)lane * 16u, (unsigned)WS_HP + (unsigned)(qt * 36 + ct * 6 + f) * 1024u);
#pragma unroll
    for (int e = 0; e < 4; e++) { ssum = dot2(o[e], 0x3f803f80u, ssum); ssq = dot2(o[e], o[e], ssq); }
  }
  { const auto a = __builtin_amdgcn_permlane32_swap(__float_as_uint(ssum), __float_as_uint(ssum), false, false); ssum = __uint_as_float(a[0]) + __uint_as_float(a[1]);
    const auto b2 = __builtin_amdgcn_permlane32_swap(__float_as_uint(ssq), __float_as_uint(ssq), false, false); ssq = __uint_as_float(b2[0]) + __uint_as_float(b2[1]); }
  const unsigned stb = lds0 + L_WO + 98304u;
  if (h == 0) *(LAS f32x2*)(size_t)(stb + (unsigned)(rg * 32 + tok) * 32u + kq * 8u) = f32x2{ssum, ssq};
  wait_lgkm0(); bar();
  if (c.w == 0) {
    const f32x4 a = lds_f128(stb + lane * 32u), cc = lds_f128(stb + lane * 32u + 16);
    const u32x2 v = {__float_as_uint((a[0] + a[2]) + (cc[0] + cc[2])), __float_as_uint((a[1] + a[3]) + (cc[1] + cc[3]))};
    __builtin_amdgcn_raw_buffer_store_b64(v, c.sws, (unsigned)lane * 8u, (unsigned)WS_ST2 + (unsigned)(rb * 6 + ct) * 512u, SC1);
  }
}
DI void p3_wo(const Ctx& c, int rb, int ct) {
  const int kq = c.w >> 1;
  if (kq == 0) p3_body<0>(c, rb, ct); else if (kq == 1) p3_body<1>(c, rb, ct); else if (kq == 2) p3_body<2>(c, rb, ct); else p3_body<3>(c, rb, ct);
  publish(c, FL_H + rb * 6 + ct, true);
}

constexpr float GC0 = -2.0f * 0.7978845608028654f * 1.4426950408889634f, GC1 = GC0 * 0.044715f;
DI f32x2 gelu2(f32x2 x) {
  const f32x2 t = x * (GC1 * (x * x) + GC0);
  f32x2 e; e[0] = __builtin_amdgcn_exp2f(t[0]); e[1] = __builtin_amdgcn_exp2f(t[1]);
  const f32x2 d = e + 1.0f;
  f32x2 r; r[0] = __builtin_amdgcn_rcpf(d[0]); r[1] = __builtin_amdgcn_rcpf(d[1]);
  return x * r;
}
struct G2 { float x0, x1, t0, t1; };
DI void g2_s1(G2& g, float a0, float a1, float p0, float p1) {
  asm volatile("v_add_f32_e32 %0, %4, %6\n\tv_add_f32_e32 %1, %5, %7\n\tv_mul_f32_e32 %2, %0, %0\n\tv_mul_f32_e32 %3, %1, %1"
               : "=&v"(g.x0), "=&v"(g.x1), "=&v"(g.t0), "=&v"(g.t1) : "v"(a0), "v"(a1), "v"(p0), "v"(p1));
}
DI void g2_s2(G2& g, float vc1) {
  asm volatile("v_fmaak_f32 %0, %0, %4, 0xc0135761\n\tv_fmaak_f32 %1, %1, %4, 0xc0135761\n\tv_mul_f32_e32 %0, %2, %0\n\tv_mul_f32_e32 %1, %3, %1"
               : "+v"(g.t0), "+v"(g.t1) : "v"(g.x0), "v"(g.x1), "v"(vc1));
}
DI void g2_s3(G2& g) {
  asm volatile("v_exp_f32_e32 %0, %0\n\tv_exp_f32_e32 %1, %1\n\tv_add_f32_e32 %0, 1.0, %0\n\tv_add_f32_e32 %1, 1.0, %1" : "+v"(g.t0), "+v"(g.t1));
}
DI unsigned g2_s4(G2& g) {
  unsigned o;
  asm volatile("v_rcp_f32_e32 %1, %1\n\tv_rcp_f32_e32 %2, %2\n\tv_mul_f32_e32 %1, %3, %1\n\tv_mul_f32_e32 %2, %4, %2\n\tv_cvt_pk_bf16_f32 %0, %1, %2"
               : "=v"(o), "+v"(g.t0), "+v"(g.t1) : "v"(g.x0), "v"(g.x1));
  return o;
}

#define FPF 3
#define XSTR 4
#define XOFF 1
#define ABL_LW 1
#define ABL_XW 1
#define ABL_ST 1
#define ABL_GELU 1
#define ABL_H 1
#define FUSE0 0
#define W2T_EARLY 1
constexpr int F_SLOT = 36864, H_SLOT = 18432, F1_XCH = 6 * H_SLOT, F1_UV = F1_XCH + 32768, F_LDS = F1_UV + 1152;
DI unsigned f_stream(int fc, int g) {
  return g < 18 ? (unsigned)WS_W1P + (unsigned)(fc * 18 + g) * H_SLOT : (unsigned)WS_W2P + (unsigned)(fc * 18 + g - 18) * H_SLOT;
}
DI void f_issue(const Ctx& c, int fc, int g) {
  const int l = c.w - 8;
  const unsigned o = (unsigned)(l < 2 ? 5 * l : 2 + 4 * l) * 1024u, vl = c.lane * 16, gb = f_stream(fc, g) + o, lb = c.lds0 + (unsigned)(g % 6) * H_SLOT + o;
#pragma unroll
  for (int i = 0; i < 4; i++) dma16s(lb + i * 1024u, vl, c.sws, gb + i * 1024u);
  if (l < 2) dma16s(lb + 4096u, vl, c.sws, gb + 4096u);
}
template <int N> DI void f_wait(int l) { if (l < 2) wait_vm<5 * N>(); else wait_vm<4 * N>(); }
constexpr int F_FIN = 4 * H_SLOT;
DI void p6_finish(const Ctx& c, int rb, int fc) {
  const int w = c.w, lane = c.lane, ks = fc >> 2, ct = fc & 3, tk = lane & 15, g = lane >> 4;
  const bool pair = w < 8;
  const int T = w & 3, rt = pair ? w >> 2 : w - 8;
  const int col = ct * 144 + (pair ? 32 * T + 8 * g : 128 + 4 * g), row = rb * 128 + ks * 32 + rt * 16 + tk;
  u32x4 hv = {}, bv = {};
  const unsigned ho = (unsigned)(((col >> 3) & 1) * 32 + rt * 16 + tk) * 16u + (unsigned)(col >> 4) * 1024u + (unsigned)(col & 7) * 2u, hb = (unsigned)WS_HP + (unsigned)(rb * 4 + ks) * 36u * 1024u;
  if (w < 10) {
    if (pair) { hv = ld128s(c.sws, ho, hb); bv = *(const u32x4*)(c.b2 + col); }
    else { const u32x2 a = __builtin_amdgcn_raw_buffer_load_b64(c.sws, ho, hb, SC1), b2 = *(const u32x2*)(c.b2 + col); hv[0] = a[0]; hv[1] = a[1]; bv[0] = b2[0]; bv[1] = b2[1]; }
  }
  if (w == 10) poll(c, FL_P + (rb * 4 + ct) * 4 + (lane & 3), lane < 4);
  bar();
  if (w >= 10) return;
  const unsigned xg = (unsigned)WS_XCH + (unsigned)((rb * 4 + ct) * 16 + ks * 4) * 9216u + (unsigned)rt * 4608u;
  const unsigned fin = c.lds0 + F_FIN + (unsigned)rt * 9216u + (unsigned)lane * 16u;
  if (pair) {
    u32x4 pv[3];
#pragma unroll
    for (int i = 0; i < 3; i++) pv[i] = ld128s(c.sws, (unsigned)T * 1024u + (unsigned)lane * 16u, xg + (unsigned)(i + (i >= ks)) * 9216u);
    f32x4 a = lds_f128(fin + (unsigned)T * 2048u), b2 = lds_f128(fin + (unsigned)T * 2048u + 1024u);
#pragma unroll
    for (int i = 0; i < 3; i++) { a += f32x4{bflo(pv[i][0]), bfhi(pv[i][0]), bflo(pv[i][1]), bfhi(pv[i][1])}; b2 += f32x4{bflo(pv[i][2]), bfhi(pv[i][2]), bflo(pv[i][3]), bfhi(pv[i][3])}; }
    u32x4 r;
    r[0] = pk2(bflo(hv[0]) + (a[0] + bflo(bv[0])), bfhi(hv[0]) + (a[1] + bfhi(bv[0])));
    r[1] = pk2(bflo(hv[1]) + (a[2] + bflo(bv[1])), bfhi(hv[1]) + (a[3] + bfhi(bv[1])));
    r[2] = pk2(bflo(hv[2]) + (b2[0] + bflo(bv[2])), bfhi(hv[2]) + (b2[1] + bfhi(bv[2])));
    r[3] = pk2(bflo(hv[3]) + (b2[2] + bflo(bv[3])), bfhi(hv[3]) + (b2[3] + bfhi(bv[3])));
    *(u32x4*)(c.out + (size_t)row * D + col) = r;
  } else {
    u32x2 pv[3];
#pragma unroll
    for (int i = 0; i < 3; i++) pv[i] = __builtin_amdgcn_raw_buffer_load_b64(c.sws, 4096u + (unsigned)lane * 8u, xg + (unsigned)(i + (i >= ks)) * 9216u, SC1);
    f32x4 a = lds_f128(fin + 8192u);
#pragma unroll
    for (int i = 0; i < 3; i++) a += f32x4{bflo(pv[i][0]), bfhi(pv[i][0]), bflo(pv[i][1]), bfhi(pv[i][1])};
    u32x2 r;
    r[0] = pk2(bflo(hv[0]) + (a[0] + bflo(bv[0])), bfhi(hv[0]) + (a[1] + bfhi(bv[0])));
    r[1] = pk2(bflo(hv[1]) + (a[2] + bflo(bv[1])), bfhi(hv[1]) + (a[3] + bfhi(bv[1])));
    *(u32x2*)(c.out + (size_t)row * D + col) = r;
  }
}
DI void p45_loader(const Ctx& c, int rb, int fc) {
  const int l = c.w - 8, lane = c.lane, ks = fc >> 2, ct = fc & 3;
  f_issue(c, fc, 2); f_issue(c, fc, 3); f_issue(c, fc, 4);
  if (l == 0) poll(c, FL_H + (2 * rb + lane / 6) * 6 + lane % 6, lane < 12);
  bar();
  u32x2 fd[4] = {};
#pragma unroll 1
  for (int g = 0; g < 36; g++) {
    if (g == 18) { bar(); bar(); bar(); }
    if (g == 8 && l == 0) poll(c, FL_W2 + (ks * 4 + ct) * 4 + (lane & 3), lane < 4);
    if (g == 20 && l == 0) poll(c, FL_H1 + rb * 16 + ks * 4 + (lane & 3), lane < 4);
    if (g < 32) f_wait<3>(l); else if (g == 32) f_wait<2>(l); else if (g == 33) f_wait<1>(l); else wait_vm<0>();
    bar();
    if (g == 8 || g == 10) {
      const unsigned ln = (unsigned)((l * 2 + ((g >> 1) & 1)) * 64 + lane);
      touch128(c.lds0 + L_DUMP, ln < 324u ? ln * 128u : 0x7ff00000u, c.sws, f_stream(fc, 18) + (unsigned)(c.bi & 7) * 41472u);
    }
    if (g == 6 && l == 3 && c.b == 0) {
#pragma unroll
      for (int i = 0; i < 4; i++) asm volatile("buffer_load_dwordx2 %0, %1, %2, 0 offen sc1" : "=&v"(fd[i]) : "v"((unsigned)(FL_D + i * 64 + lane) * 128u), "s"(c.sws) : "memory");
    }
    if (g == 12 && l == 3 && c.b == 0) {
      asm volatile("" : "+v"(fd[0]), "+v"(fd[1]), "+v"(fd[2]), "+v"(fd[3]));
      const unsigned n0 = (unsigned)c.nonce, n1 = (unsigned)(c.nonce >> 32);
      if (!__all(fd[0][0] == n0 && fd[0][1] == n1 && fd[1][0] == n0 && fd[1][1] == n1 && fd[2][0] == n0 && fd[2][1] == n1 && fd[3][0] == n0 && fd[3][1] == n1)) {
#pragma unroll 1
        for (int i = 0; i < 4; i++) poll(c, FL_D + i * 64 + lane, true);
      }
      if (lane == 0) __hip_atomic_store((unsigned long long*)(c.ws + FL_N * 128), c.nonce, __ATOMIC_RELAXED, __HIP_MEMORY_SCOPE_AGENT);
    }
    if (g + 5 < 36) f_issue(c, fc, g + 5);
  }
  bar(); bar();
  bar();
  p6_finish(c, rb, fc);
}
template <int KH>
DI void p45_ffn(const Ctx& c, int rb, int fc) {
  constexpr int kh = KH;
  const int w = c.w, lane = c.lane, rg = w & 3; const unsigned lds0 = c.lds0;
  const unsigned vl = lane * 16;
  const int ks = fc >> 2, ct = fc & 3;
  const int tok = lane & 31, h = lane >> 5;
  if (threadIdx.x < 288) *(LAS float*)(size_t)(lds0 + F1_UV + threadIdx.x * 4) = __uint_as_float((unsigned)c.b1[fc * 288 + threadIdx.x] << 16);
  wait_lgkm0(); bar();
  ST(4, 1);
  u32x2 stv[6];
#pragma unroll
  for (int e = 0; e < 6; e++)
    asm volatile("buffer_load_dwordx2 %0, %1, %2, %3 offen offset:%4 sc1" : "=&v"(stv[e]) : "v"((unsigned)tok * 8u), "s"(c.sws),
                 "s"((unsigned)WS_ST2 + (unsigned)((2 * rb + (rg >> 1)) * 6) * 512u + (unsigned)(rg & 1) * 256u), "i"(e * 512) : "memory");
  u32x4 xf[18];
  {
    const unsigned so = (unsigned)WS_HP + ((unsigned)(rb * 4 + rg) * 36u + (unsigned)kh * 18u) * 1024u;
#pragma unroll
    for (int s = 0; s < 18; s++)
      asm volatile("buffer_load_dwordx4 %0, %1, %2, %3 offen offset:%4 sc1" : "=&v"(xf[s]) : "v"(vl), "s"(c.sws), "s"(so + (s >> 2) * 4096u), "i"((s & 3) * 1024) : "memory");
  }
  float ra, rc;
  {
    asm volatile("s_waitcnt vmcnt(18)" : "+v"(stv[0]), "+v"(stv[1]), "+v"(stv[2]), "+v"(stv[3]), "+v"(stv[4]), "+v"(stv[5]) :: "memory");
    float s = 0, q = 0;
#pragma unroll
    for (int e = 0; e < 6; e++) { s += __uint_as_float(stv[e][0]); q += __uint_as_float(stv[e][1]); }
    const float mean = s * (1.0f / D), var = fmaxf(q * (1.0f / D) - mean * mean, 0.f), rstd = rsqrtf(var + LN_EPS);
    ra = rstd; rc = -mean * rstd;
  }
  const unsigned rd = lds0 + (unsigned)kh * 9216u + vl;
  const unsigned hvo = (unsigned)(tok >> 4) * 1024u + (unsigned)h * 256u + (unsigned)(tok & 15) * 16u;
  const unsigned hbase = (unsigned)WS_H1P + ((unsigned)(rb * 4 + rg) * 144u + (unsigned)fc * 9u) * 2048u;
  const unsigned bia = lds0 + F1_UV + (unsigned)h * 32u;
  {
#pragma unroll
    for (int s = 0; s < 18; s++) {
      asm volatile("s_waitcnt vmcnt(%1)" : "+v"(xf[s]) : "i"(17 - s) : "memory");
#pragma unroll
      for (int e = 0; e < 4; e++) {
        const f32x2 v = f32x2{bflo(xf[s][e]), bfhi(xf[s][e])} * ra + rc;
        xf[s][e] = pk2(v[0], v[1]);
      }
      asm volatile("" : "+v"(xf[s]));
      SB();
    }
  }
  ST(4, 2);
  constexpr int PF = FPF;
  u32x4 wf[PF];
  const f32x16 zero = {};
#define F1_BIAS(ACC, ba_) do { const f32x4 b0_ = lds_f128(ba_), b1_ = lds_f128(ba_ + 16), b2_ = lds_f128(ba_ + 64), b3_ = lds_f128(ba_ + 80); \
    ACC = f32x16{b0_[0], b0_[1], b0_[2], b0_[3], b1_[0], b1_[1], b1_[2], b1_[3], b2_[0], b2_[1], b2_[2], b2_[3], b3_[0], b3_[1], b3_[2], b3_[3]}; } while (0)
  f32x16 accG, accP, accQ;
  F1_BIAS(accG, bia + (unsigned)(1 - kh) * 128u);
  float vc1 = GC1; asm volatile("" : "+v"(vc1));
  unsigned r0 = 0;
#define F_NEXT(r) ((r) == 5u * H_SLOT ? 0u : (r) + H_SLOT)
  const unsigned xwr = lds0 + F1_XCH + (unsigned)(kh ^ 1) * 16384u + (unsigned)rg * 4096u + vl;
  const unsigned xrd = lds0 + F1_XCH + (unsigned)kh * 16384u + (unsigned)rg * 4096u + vl;
  f32x4 pp[4] = {};
#define F1_STAGE(q, G, SRC, R0, O) do { const int p_ = (q) >> 2, st_ = (q) & 3, r_ = (R0) + 2 * p_; \
      if (st_ == 0) g2_s1(G, SRC[r_], SRC[r_ + 1], pp[r_ >> 2][r_ & 3], pp[r_ >> 2][(r_ & 3) + 1]); \
      if (st_ == 1) g2_s2(G, vc1); if (st_ == 2) g2_s3(G); if (st_ == 3) O[p_] = g2_s4(G); } while (0)
#define F_HALF_BEGIN(s) do { if ((s) % 9 == 0) { bar(); sb = rd + r0; r0 = F_NEXT(r0); sn = rd + r0; asm volatile("" : "+v"(sb), "+v"(sn)); } } while (0)
#define F_FRAG(s) lds_u128((s) % 9 + PF < 9 ? sb + ((s) % 9 + PF) * 1024 : sn + ((s) % 9 + PF - 9) * 1024)
#define F1_PERIOD(t, ACC, PREV, GELU, HALF) do { \
    unsigned sb, sn; \
    u32x4 o; G2 g; \
    const int gt_ = ((t) - 2 - (HALF)) + kh;      \
    _Pragma("unroll") for (int s = 0; s < 18; s++) { \
      F_HALF_BEGIN(s); \
      if (s == 0 && (t) == 0) { _Pragma("unroll") for (int i = 0; i < PF; i++) wf[i] = lds_u128(sb + i * 1024); } \
      if (s == 0 && (GELU) && (HALF) == 0) { _Pragma("unroll") for (int e = 0; e < 4; e++) pp[e] = lds_f128(xrd + e * 1024); SB(); } \
      const u32x4 cur = wf[s % PF]; \
      wf[s % PF] = F_FRAG(s); \
      ACC = mfma32(cur, xf[s], (s == 0 && (HALF) == 1) ? zero : ACC); \
      if (ABL_XW && (HALF) == 1 && s % XSTR == XOFF && s / XSTR < 4) { const int e = s / XSTR; lds_w128(xwr + e * 1024, f32x4{accG[4 * e], accG[4 * e + 1], accG[4 * e + 2], accG[4 * e + 3]}); } \
      if (ABL_GELU && (GELU)) { \
        if (s >= 2 && s <= 17) F1_STAGE(s - 2, g, PREV, 8 * (HALF), o); \
        if (ABL_ST && s == 17) st128s(o, c.sws, hvo + (HALF) * 512u, hbase + (unsigned)gt_ * 2048u); \
      } \
      if ((HALF) == 1 && s == 15) {     \
        const int nt_ = (t) + 1 == 8 ? 8 : (t) + 1 + (1 - kh); \
        if ((t) + 1 == 8 && kh == 0) accG = zero; \
        else { unsigned ba_ = bia + (unsigned)nt_ * 128u; asm volatile("" : "+v"(ba_)); F1_BIAS(accG, ba_); } \
      } \
      SB(); \
    } \
    asm volatile("" :: "v"(ACC)); \
  } while (0)
  const unsigned hoff0 = (unsigned)WS_H1P + ((unsigned)(rb * 4 + rg) * 144u + (unsigned)ks * 36u) * 2048u;
  u32x4 hA[4], hB[4], hC[4];
#define HLOAD1(dst, t, i) do { const int p_ = 4 * (t) + 2 * kh + ((i) >> 1) + 9 * ct; const unsigned so_ = hoff0 + (unsigned)(p_ >= 36 ? p_ - 36 : p_) * 2048u; \
    if (((i) & 1) == 0) asm volatile("buffer_load_dwordx4 %0, %1, %2, %3 offen sc1" : "=&v"(dst[i]) : "v"(vl), "s"(c.sws), "s"(so_) : "memory"); \
    else asm volatile("buffer_load_dwordx4 %0, %1, %2, %3 offen offset:1024 sc1" : "=&v"(dst[i]) : "v"(vl), "s"(c.sws), "s"(so_) : "memory"); } while (0)
#define HWAIT(N, dst) asm volatile("s_waitcnt vmcnt(" #N ")" : "+v"(dst[0]), "+v"(dst[1]), "+v"(dst[2]), "+v"(dst[3]) :: "memory")
  {
    F1_PERIOD(0, accG, accQ, false, 0);
    F1_PERIOD(1, accP, accQ, false, 1);
    F1_PERIOD(2, accG, accP, true, 0);
    F1_PERIOD(3, accQ, accP, true, 1);
    F1_PERIOD(4, accG, accQ, true, 0);
    F1_PERIOD(5, accP, accQ, true, 1);
    F1_PERIOD(6, accG, accP, true, 0);
    F1_PERIOD(7, accQ, accP, true, 1);
    {
      wait_vm<2>();
      unsigned sb, sn;
      u32x4 o, o2; G2 g, g2;
      const int gt_ = 6 + kh;
#pragma unroll
      for (int s = 0; s < 18; s++) {
        F_HALF_BEGIN(s);
        if (s == 0) {
#pragma unroll
          for (int e = 0; e < 4; e++) pp[e] = lds_f128(xrd + e * 1024);
          SB();
        }
        const u32x4 cur = wf[s % PF];
        wf[s % PF] = F_FRAG(s);
        accG = mfma32(cur, xf[s], accG);
        if (s == 10 + KH) HLOAD1(hA, 0, 0); if (s == 12 + KH) HLOAD1(hA, 0, 1); if (s == 14 + KH) HLOAD1(hA, 0, 2); if (s == 16 + KH) HLOAD1(hA, 0, 3);
        if (s >= 2 && s <= 17) { F1_STAGE(s - 2, g, accQ, 0, o); F1_STAGE(s - 2, g2, accQ, 8, o2); }
        if (s == 17) { st128s(o, c.sws, hvo, hbase + (unsigned)gt_ * 2048u); st128s(o2, c.sws, hvo + 512u, hbase + (unsigned)gt_ * 2048u); }
        SB();
      }
      ST(4, 3);
      bar();
      lds_w128(xwr, kh ? f32x4{accG[0], accG[1], accG[2], accG[3]} : f32x4{accG[8], accG[9], accG[10], accG[11]});
      lds_w128(xwr + 1024, kh ? f32x4{accG[4], accG[5], accG[6], accG[7]} : f32x4{accG[12], accG[13], accG[14], accG[15]});
      wait_lgkm0(); bar();
      const f32x4 q0 = lds_f128(xrd), q1 = lds_f128(xrd + 1024);
      const f32x4 k0 = (kh ? f32x4{accG[8], accG[9], accG[10], accG[11]} : f32x4{accG[0], accG[1], accG[2], accG[3]}) + q0;
      const f32x4 k1 = (kh ? f32x4{accG[12], accG[13], accG[14], accG[15]} : f32x4{accG[4], accG[5], accG[6], accG[7]}) + q1;
      const f32x2 z0 = gelu2(f32x2{k0[0], k0[1]}), z1 = gelu2(f32x2{k0[2], k0[3]}), z2 = gelu2(f32x2{k1[0], k1[1]}), z3 = gelu2(f32x2{k1[2], k1[3]});
      o = u32x4{pk2(z0[0], z0[1]), pk2(z1[0], z1[1]), pk2(z2[0], z2[1]), pk2(z3[0], z3[1])};
      st128s(o, c.sws, hvo + (unsigned)kh * 512u, hbase + 8u * 2048u);
    }
  }
  publish(c, FL_H1 + rb * 16 + fc, true);
  ST(4, 4);
  ST(5, 1);
  HLOAD1(hB, 1, 0); HLOAD1(hB, 1, 1); HLOAD1(hB, 1, 2); HLOAD1(hB, 1, 3);
  f32x4 acc[2][9];
#pragma unroll
  for (int a = 0; a < 2; a++)
#pragma unroll
    for (int n = 0; n < 9; n++) acc[a][n] = f32x4{0, 0, 0, 0};
#define F2_PERIOD(t, HC, HN, HM, VMW, MODE) do { \
    HWAIT(VMW, HC); \
    unsigned sb, sn; \
    _Pragma("unroll") for (int s = 0; s < 18; s++) { \
      F_HALF_BEGIN(s); \
      const u32x4 cur = wf[s % PF]; \
      wf[s % PF] = F_FRAG(s); \
      const int k = s / 9, nt = s % 9; \
      acc[0][nt] = mfma16(cur, HC[2 * k], acc[0][nt]); \
      acc[1][nt] = mfma16(cur, HC[2 * k + 1], acc[1][nt]); \
      if ((MODE) == 3) { if (s == KH) HLOAD1(HM, (t) + 1, 0); if (s == 2 + KH) HLOAD1(HM, (t) + 1, 1); if (s == 4 + KH) HLOAD1(HM, (t) + 1, 2); if (s == 6 + KH) HLOAD1(HM, (t) + 1, 3); } \
      if (ABL_H && ((MODE) == 0 || (MODE) == 3)) { if (s == 9 + KH) HLOAD1(HN, (t) + 2, 0); if (s == 11 + KH) HLOAD1(HN, (t) + 2, 1); if (s == 13 + KH) HLOAD1(HN, (t) + 2, 2); if (s == 15 + KH) HLOAD1(HN, (t) + 2, 3); } \
      SB(); \
    } \
  } while (0)
  F2_PERIOD(0, hA, hC, hC, 4, 1);
  ST(5, 2);
  F2_PERIOD(1, hB, hA, hC, 0, 3);
  F2_PERIOD(2, hC, hB, hB, 4, 0);
  F2_PERIOD(3, hA, hC, hC, 4, 0);
  F2_PERIOD(4, hB, hA, hA, 4, 0);
  F2_PERIOD(5, hC, hB, hB, 4, 0);
  F2_PERIOD(6, hA, hC, hC, 4, 0);
  F2_PERIOD(7, hB, hA, hA, 4, 1);
  F2_PERIOD(8, hC, hB, hB, 0, 1);
  ST(5, 3);
  wait_lgkm0(); bar();
  const unsigned xb = lds0 + (unsigned)rg * 18432u + vl;
#pragma unroll
  for (int n = 0; n < 9; n++) lds_w128(xb + (unsigned)(kh ^ 1) * 9216u + n * 1024, kh ? acc[0][n] : acc[1][n]);
  wait_lgkm0(); bar();
  f32x4 o[9];
#pragma unroll
  for (int n = 0; n < 9; n++) o[n] = lds_f128(xb + kh * 9216u + n * 1024) + (kh ? acc[1][n] : acc[0][n]);
  const unsigned xg = (unsigned)WS_XCH + (unsigned)((rb * 4 + ct) * 16) * 9216u + (unsigned)kh * 4608u;
  if (rg != ks) {
    const unsigned d = xg + (unsigned)(rg * 4 + ks) * 9216u;
#pragma unroll
    for (int T = 0; T < 4; T++)
      st128s(u32x4{pk2(o[2 * T][0], o[2 * T][1]), pk2(o[2 * T][2], o[2 * T][3]), pk2(o[2 * T + 1][0], o[2 * T + 1][1]), pk2(o[2 * T + 1][2], o[2 * T + 1][3])}, c.sws, vl + T * 1024u, d);
    __builtin_amdgcn_raw_buffer_store_b64(u32x2{pk2(o[8][0], o[8][1]), pk2(o[8][2], o[8][3])}, c.sws, 4096u + lane * 8u, d, SC1);
  } else {
#pragma unroll
    for (int n = 0; n < 9; n++) lds_w128(lds0 + F_FIN + (unsigned)kh * 9216u + n * 1024u + vl, o[n]);
  }
  wait_lgkm0();
  publish(c, FL_P + (rb * 4 + ct) * 4 + ks, true);
  ST(5, 4);
  p6_finish(c, rb, fc);
  ST(5, 5);
}

constexpr int M_LDS = 163840;
static_assert(L_WO + 110592 <= L_DUMP && 4 * Q_SLOT + 640 <= L_DUMP && F_LDS <= L_DUMP, "LDS map");
__global__ KATTR void fused_block(const u16* __restrict__ x, const u16* __restrict__ wqkv, const u16* __restrict__ wo, const u16* __restrict__ w1,
                                  const u16* __restrict__ b1, const u16* __restrict__ w2, const u16* __restrict__ b2, u16* __restrict__ out, char* __restrict__ ws) {
  extern __shared__ __attribute__((aligned(16))) char smem[];
  if (ENT_CODE) entry_code((unsigned)(size_t)smem, rfl(threadIdx.x >> 6), threadIdx.x & 63);
  Ctx c;
  c.x = x; c.wqkv = wqkv; c.wo = wo; c.w1 = w1; c.b1 = b1; c.w2 = w2; c.b2 = b2; c.out = out; c.ws = ws;
  c.sws = mk_srd(ws, WS_BYTES);
  c.lds0 = (unsigned)(size_t)smem;
  c.b = blockIdx.x; c.xcd = c.b & 7; c.bi = c.b >> 3;
  c.w = rfl(threadIdx.x >> 6); c.lane = threadIdx.x & 63;
  ST(1, 0);
  const int xcd = c.xcd, bi = c.bi, w = c.w, lane = c.lane;
  const int rb4 = (xcd >> 2) * 8 + (bi & 7), fc4 = (xcd & 3) * 4 + (bi >> 3);
  if (bi < 24) {
    p1_qkv(c, (xcd >> 1) * 4 + (bi & 3), (xcd & 1) * 6 + (bi >> 2));
  } else {
    const int j = bi - 24, fc = (xcd & 3) * 4 + (j >> 1), part = (xcd >> 2) * 2 + (j & 1);
    helper_stats(c, xcd * 8 + j, fc, part);
    ST(6, 0);
    if (w >= 8) wait_vm<0>();
    bar();
    relayout_w1(c, fc, part);
    publish(c, FL_W1 + fc * 4 + part, true);
    ST(6, 3);
    if (w >= 8) {
      const srd_t sw2 = mk_srd(w2, D * F * 2);
      const int ks = xcd & 3, ct2 = j >> 1, row0 = (ks * 36 + part * 9) * 32;
#pragma unroll
      for (int i = 0; i < 4; i++) {
        const int l = ((w - 8) * 4 + i) * 64 + lane, row = l / 3;
        touch128(c.lds0 + L_DUMP, l < 864 ? (unsigned)row * (D * 2) + (unsigned)(l - 3 * row) * 128u : 0x7ff00000u, sw2, (unsigned)row0 * (D * 2) + (((unsigned)ct2 * 288u) & ~127u));
      }
    }
  }
  ST(1, 4);
  if (bi < 24 && w >= 8) p3_prefetch(c, bi >> 2);
  p2_attn(c, xcd * 4 + (bi & 3), bi >> 2);
  ST(2, 4);
  if (w >= 8) {
    if (w == 8 && bi < 24) poll(c, FL_A + xcd * 32 + (lane & 31), lane < 32);
    if (w == 9) poll(c, FL_W1 + fc4 * 4 + (lane & 3), lane < 4);
    if (bi < 24) wait_vm<0>();
    bar();
    f_issue(c, fc4, 0); f_issue(c, fc4, 1);
    {
#pragma unroll
      for (int i = 0; i < 2; i++) { const unsigned l = (unsigned)(((w - 8) * 2 + i) * 64 + lane); touch128(c.lds0 + L_DUMP, l < 288u ? l * 128u : 0x7ff00000u, c.sws, (unsigned)WS_W1P + (unsigned)(fc4 * 9 + 1) * F_SLOT + (unsigned)(bi & 7) * 36864u); }
    }
  }
  if (bi < 24) {
    if (w >= 8) {
      bar(); bar(); bar();
      publish(c, FL_H + (xcd * 4 + (bi & 3)) * 6 + (bi >> 2), false);
    } else {
      p3_wo(c, xcd * 4 + (bi & 3), bi >> 2);
    }
  } else {
    if (w < 8) bar();
    const int j = bi - 24, ks = xcd & 3, ct = j >> 1, part = (xcd >> 2) * 2 + (j & 1);
    if (w < 9) relayout_w2(c, c.lds0 + L_WO + (unsigned)w * 9216u, ks * 36 + part * 9 + w, ct);
    publish(c, FL_W2 + (ks * 4 + ct) * 4 + part, w < 9);
  }
  ST(3, 4);
  if (w >= 8) p45_loader(c, rb4, fc4); else if (w < 4) p45_ffn<0>(c, rb4, fc4); else p45_ffn<1>(c, rb4, fc4);
  asm volatile(".Lfused_code_end:");
}

extern "C" void kernel_launch(void* const* d_in, const int* in_sizes, int n_in, void* d_out, int out_size, void* d_ws, size_t ws_size,
                              hipStream_t stream) {
  (void)hipFuncSetAttribute((const void*)fused_block, hipFuncAttributeMaxDynamicSharedMemorySize, M_LDS);
  fused_block<<<256, 768, M_LDS, stream>>>((const u16*)d_in[0], (const u16*)d_in[1], (const u16*)d_in[2], (const u16*)d_in[4], (const u16*)d_in[5],
                                           (const u16*)d_in[6], (const u16*)d_in[7], (u16*)d_out, (char*)d_ws);
}
```

```cpp
#include <hip/hip_runtime.h>
#include <stdint.h>

typedef unsigned short u16;
typedef __bf16 bf16x8 __attribute__((ext_vector_type(8)));
typedef __bf16 bf16x4 __attribute__((ext_vector_type(4)));
typedef __bf16 bf16x2 __attribute__((ext_vector_type(2)));
typedef float f32x2 __attribute__((ext_vector_type(2)));
typedef float f32x4 __attribute__((ext_vector_type(4)));
typedef float f32x16 __attribute__((ext_vector_type(16)));
typedef unsigned u32x2 __attribute__((ext_vector_type(2)));
typedef unsigned u32x4 __attribute__((ext_vector_type(4)));
#define LAS __attribute__((address_space(3)))
#define DI __device__ __forceinline__
#define SB() __builtin_amdgcn_sched_barrier(0)
#define KATTR __launch_bounds__(768, 3) __attribute__((amdgpu_waves_per_eu(3, 3)))

constexpr int S = 2048, D = 576, H = 32, DH = 18, F = 4608, NQKV = 1728;
constexpr float LN_EPS = 1e-5f;

constexpr size_t WS_FLG = 0;
constexpr int FL_N = 0, FL_D = 1, FL_Q = FL_D + 256, FL_W1 = FL_Q + 192, FL_A = FL_W1 + 64, FL_W2 = FL_A + 256, FL_H = FL_W2 + 64, FL_H1 = FL_H + 192,
              FL_P = FL_H1 + 256, FL_S1 = FL_P + 256, FL_END = FL_S1 + 64;
constexpr size_t WS_QKV = 262144;
static_assert(FL_END * 128 <= WS_QKV, "flag area");
constexpr size_t WS_QKV_SZ = (size_t)3 * H * S * 24 * 2;
constexpr size_t WS_OP = WS_QKV + WS_QKV_SZ;
constexpr size_t WS_OL = WS_OP + (size_t)64 * 32 * 1024;
constexpr size_t WS_HP = WS_OL + (size_t)H * S * 4;
constexpr size_t WS_ST2 = WS_HP + (size_t)S * D * 2;
constexpr size_t WS_W1P = WS_ST2 + (size_t)S * 48;
constexpr size_t WS_W2P = WS_W1P + (size_t)D * F * 2;
constexpr size_t WS_H1P = WS_W2P + (size_t)D * F * 2;
constexpr size_t WS_XCH = WS_H1P + (size_t)S * F * 2;
constexpr size_t WS_ST1 = WS_XCH + (size_t)16 * 4 * 4 * 4 * 2 * 4608;
constexpr size_t WS_CSQ = WS_ST1 + (size_t)S * 8;
constexpr size_t WS_END = WS_CSQ + (size_t)(NQKV + 64) * 4;
constexpr unsigned WS_BYTES = (unsigned)WS_END;

#define ST(kid, idx) asm volatile("; MARK " #kid " " #idx)

DI unsigned rfl(unsigned x) { return __builtin_amdgcn_readfirstlane(x); }
DI float bflo(unsigned w) { return __uint_as_float(w << 16); }
DI float bfhi(unsigned w) { return __uint_as_float(w & 0xffff0000u); }
DI unsigned pk2(float lo, float hi) { f32x2 v = {lo, hi}; bf16x2 b = __builtin_convertvector(v, bf16x2); return __builtin_bit_cast(unsigned, b); }
DI float dot2(unsigned a, unsigned b, float c) { return __builtin_amdgcn_fdot2_f32_bf16(__builtin_bit_cast(bf16x2, a), __builtin_bit_cast(bf16x2, b), c, false); }

typedef __amdgpu_buffer_rsrc_t srd_t;
DI srd_t mk_srd(const void* p, unsigned bytes) { return __builtin_amdgcn_make_buffer_rsrc((void*)p, (short)0, (int)bytes, 0x00020000); }
DI void dma16(unsigned ldsaddr, unsigned voff, srd_t srd, unsigned soff) {
  asm volatile("s_mov_b32 m0, %0\n\ts_nop 0\n\tbuffer_load_dwordx4 %1, %2, %3 offen lds" :: "s"(ldsaddr), "v"(voff), "s"(srd), "s"(soff) : "memory");
}
DI void dma16h(unsigned ldsaddr, unsigned voff, srd_t srd, unsigned soff) {
  asm volatile("s_mov_b32 m0, %0\n\ts_mov_b32 exec_hi, 0\n\tbuffer_load_dwordx4 %1, %2, %3 offen lds\n\ts_mov_b32 exec_hi, -1" :: "s"(ldsaddr), "v"(voff), "s"(srd), "s"(soff) : "memory");
}
DI void dma16q(unsigned ldsaddr, unsigned voff, srd_t srd, unsigned soff) {
  asm volatile("s_mov_b32 m0, %0\n\ts_mov_b64 exec, 0xffff\n\tbuffer_load_dwordx4 %1, %2, %3 offen lds\n\ts_mov_b64 exec, -1" :: "s"(ldsaddr), "v"(voff), "s"(srd), "s"(soff) : "memory");
}
DI void touch128(unsigned ldsdump, unsigned voff, srd_t srd, unsigned soff) {
  asm volatile("s_mov_b32 m0, %0\n\ts_nop 0\n\tbuffer_load_dword %1, %2, %3 offen lds" :: "s"(ldsdump), "v"(voff), "s"(srd), "s"(soff) : "memory");
}
template <int N> DI void wait_vm() { asm volatile("s_waitcnt vmcnt(%0)" :: "i"(N) : "memory"); }
DI void wait_lgkm0() { asm volatile("s_waitcnt lgkmcnt(0)" ::: "memory"); }
DI void bar() { asm volatile("" ::: "memory"); __builtin_amdgcn_s_barrier(); asm volatile("" ::: "memory"); }
DI u32x4 lds_u128(unsigned addr) { return *(const LAS u32x4*)(size_t)addr; }
DI f32x4 lds_f128(unsigned addr) { return *(const LAS f32x4*)(size_t)addr; }
DI void lds_w128(unsigned addr, f32x4 v) { *(LAS f32x4*)(size_t)addr = v; }
DI bf16x8 asbf(u32x4 v) { return __builtin_bit_cast(bf16x8, v); }
DI u32x2 lds_tr(unsigned addr) { return __builtin_bit_cast(u32x2, __builtin_amdgcn_ds_read_tr16_b64_v4bf16((LAS bf16x4*)(size_t)addr)); }
DI u32x4 cat4(u32x2 a, u32x2 b) { return u32x4{a[0], a[1], b[0], b[1]}; }
DI f32x4 mfma16(u32x4 a, u32x4 b, f32x4 c) { return __builtin_amdgcn_mfma_f32_16x16x32_bf16(asbf(a), asbf(b), c, 0, 0, 0); }
DI f32x16 mfma32(u32x4 a, u32x4 b, f32x16 c) { return __builtin_amdgcn_mfma_f32_32x32x16_bf16(asbf(a), asbf(b), c, 0, 0, 0); }
template <int NOP> DI float max16(float m, const f32x16& t) {
  float u;
  asm volatile("s_nop %18\n\tv_max3_f32 %1, %2, %3, %4\n\tv_max3_f32 %0, %0, %5, %6\n\tv_max3_f32 %1, %1, %7, %8\n\tv_max3_f32 %0, %0, %9, %10\n\t"
               "v_max3_f32 %1, %1, %11, %12\n\tv_max3_f32 %0, %0, %13, %14\n\tv_max3_f32 %1, %1, %15, %16\n\tv_max3_f32 %0, %0, %1, %17"
               : "+v"(m), "=&v"(u)
               : "v"(t[0]), "v"(t[1]), "v"(t[2]), "v"(t[3]), "v"(t[4]), "v"(t[5]), "v"(t[6]), "v"(t[7]), "v"(t[8]), "v"(t[9]), "v"(t[10]), "v"(t[11]), "v"(t[12]),
                 "v"(t[13]), "v"(t[14]), "v"(t[15]), "i"(NOP));
  return m;
}
template <int CTRL, int RM> DI float dpp_add(float v) { return v + __builtin_bit_cast(float, __builtin_amdgcn_update_dpp(0, __builtin_bit_cast(int, v), CTRL, RM, 0xf, true)); }
DI float wave_sum63(float v) { v = dpp_add<0x111, 0xf>(v); v = dpp_add<0x112, 0xf>(v); v = dpp_add<0x114, 0xf>(v); v = dpp_add<0x118, 0xf>(v); v = dpp_add<0x142, 0xa>(v); return dpp_add<0x143, 0xc>(v); }
DI float sum_x32(float v) { const auto a = __builtin_amdgcn_permlane32_swap(__float_as_uint(v), __float_as_uint(v), false, false); return __uint_as_float(a[0]) + __uint_as_float(a[1]); }
DI float sum_x16(float v) { const auto a = __builtin_amdgcn_permlane16_swap(__float_as_uint(v), __float_as_uint(v), false, false); return __uint_as_float(a[0]) + __uint_as_float(a[1]); }
DI float sum_x8(float v) { return dpp_add<0x128, 0xf>(v); }
DI int swap23(int i) { return (i & 19) | ((i & 4) << 1) | ((i & 8) >> 1); }

constexpr int SC1 = 16;
DI void dma16s(unsigned ldsaddr, unsigned voff, srd_t srd, unsigned soff) {
  asm volatile("s_mov_b32 m0, %0\n\ts_nop 0\n\tbuffer_load_dwordx4 %1, %2, %3 offen sc1 lds" :: "s"(ldsaddr), "v"(voff), "s"(srd), "s"(soff) : "memory");
}
DI void dma16hs(unsigned ldsaddr, unsigned voff, srd_t srd, unsigned soff) {
  asm volatile("s_mov_b32 m0, %0\n\ts_mov_b32 exec_hi, 0\n\tbuffer_load_dwordx4 %1, %2, %3 offen sc1 lds\n\ts_mov_b32 exec_hi, -1" :: "s"(ldsaddr), "v"(voff), "s"(srd), "s"(soff) : "memory");
}
DI void st128s(u32x4 v, srd_t srd, unsigned voff, unsigned soff) {
  asm volatile("buffer_store_dwordx4 %0, %1, %2, %3 offen sc1\n\ts_nop 1" :: "v"(v), "v"(voff), "s"(srd), "s"(soff) : "memory");
}
DI u32x4 ld128s(srd_t srd, unsigned voff, unsigned soff) { return __builtin_amdgcn_raw_buffer_load_b128(srd, voff, soff, SC1); }

struct Ctx {
  const u16 *x, *wqkv, *wo, *w1, *b1, *w2, *b2;
  u16* out;
  char* ws;
  srd_t sws;
  unsigned lds0;
  unsigned long long nonce;
  int b, xcd, bi, w, lane;
};
DI void publish(const Ctx& c, int flag, bool stored) {
  if (stored) wait_vm<0>();
  bar();
  if (threadIdx.x == 0) __hip_atomic_store((unsigned long long*)(c.ws + (size_t)flag * 128), c.nonce, __ATOMIC_RELAXED, __HIP_MEMORY_SCOPE_AGENT);
}
DI void poll(const Ctx& c, int flag, bool active) {
  const unsigned long long* p = (const unsigned long long*)(c.ws + (size_t)flag * 128);
  const unsigned long long t0 = __builtin_amdgcn_s_memrealtime();
  for (;;) {
    const unsigned long long v = active ? __hip_atomic_load(p, __ATOMIC_RELAXED, __HIP_MEMORY_SCOPE_AGENT) : c.nonce;
    if (__all(v == c.nonce)) break;
    if (__builtin_amdgcn_s_memrealtime() - t0 > 2000000ull) break;
  }
}

#define ENT_CODE 0
#define CODE_TOUCH 0
constexpr int L_DUMP = 163840 - 256;
DI u32x2 entry_load(const Ctx& c) {
  u32x2 nv;
  asm volatile("buffer_load_dwordx2 %0, %1, %2, 0 offen sc1" : "=&v"(nv) : "v"((unsigned)(FL_N * 128)), "s"(c.sws) : "memory");
  return nv;
}
DI void entry_code(unsigned lds0, int w, int lane) {
  unsigned long long pc; unsigned len;
  asm volatile("s_getpc_b64 %0\n.Lcp%=:\n\ts_mov_b32 %1, .Lfused_code_end-.Lcp%=" : "=s"(pc), "=s"(len));
  touch128(lds0 + L_DUMP, (unsigned)(w * 64 + lane) * 128u, mk_srd((const void*)pc, len), 0u);
}
DI void code_touch(const Ctx& c) {
}
DI void entry_prefetch(const Ctx& c) {
  const int l = c.w - 8;
  const srd_t st = l == 1 ? mk_srd(c.out, S * D * 2) : c.sws;
  touch128(c.lds0 + L_DUMP, l == 0 ? (c.lane < 24 ? ((unsigned)c.bi * 24u + (unsigned)c.lane) * 65536u : 0x7ff00000u) : (l == 1 ? (unsigned)c.lane * 65536u : 0x7ff00000u), st, 0u);
}
DI void entry_nonce(Ctx& c, u32x2 nv) {
  asm volatile("" : "+v"(nv));
  c.nonce = (((unsigned long long)rfl(nv[1]) << 32) | rfl(nv[0])) + 1ull;
  if (threadIdx.x == 0) __hip_atomic_store((unsigned long long*)(c.ws + (size_t)(FL_D + c.b) * 128), c.nonce, __ATOMIC_RELAXED, __HIP_MEMORY_SCOPE_AGENT);
}

constexpr int L_W1 = 4096;
DI void relayout_w1_fetch(const Ctx& c, int fc, int part) {
  const int l = c.w - 8, lane = c.lane;
  const srd_t sw = mk_srd(c.w1, D * F * 2);
#pragma unroll 1
  for (int pc = l; pc < 81; pc += 4) {
    const int ch = pc * 64 + lane, row = ch / 36, cc = ch % 36;
    dma16(c.lds0 + L_W1 + (unsigned)pc * 1024u, (unsigned)row * (F * 2) + (unsigned)cc * 16u, sw, (unsigned)part * 144u * (F * 2) + (unsigned)fc * 576u);
  }
}
DI void helper_stats(Ctx& c, int hb, int fc, int part) {
  const int w = c.w, lane = c.lane;
  const srd_t sx = mk_srd(c.x, S * D * 2), sw = mk_srd(c.wqkv, hb < 27 ? D * NQKV * 2 : 0);
  if (w >= 8) {
    const u32x2 nv = entry_load(c);
    entry_prefetch(c);
    wait_vm<1>();
    entry_nonce(c, nv);
    bar();
    publish(c, FL_S1 + hb, false);
    relayout_w1_fetch(c, fc, part);
    return;
  }
  const int tid = w * 64 + lane, ch = tid & 7, rg = tid >> 3;
  u32x4 a[4], b2[4], v9[9];
#pragma unroll
  for (int r = 0; r < 4; r++) {
    const unsigned so = (unsigned)(hb * 32 + w * 4 + r) * (D * 2);
    a[r] = __builtin_amdgcn_raw_buffer_load_b128(sx, (unsigned)lane * 16u, so, 0);
    b2[r] = __builtin_amdgcn_raw_buffer_load_b128(sx, lane < 8 ? 1024u + (unsigned)lane * 16u : 0x7ff00000u, so, 0);
  }
#pragma unroll
  for (int i = 0; i < 9; i++) v9[i] = __builtin_amdgcn_raw_buffer_load_b128(sw, (unsigned)(rg * 9 + i) * (NQKV * 2) + (unsigned)ch * 16u, (unsigned)hb * 128u, 0);
  const u32x2 nv = entry_load(c);
  float sm[4], sq[4];
#pragma unroll
  for (int r = 0; r < 4; r++) {
    sm[r] = 0; sq[r] = 0;
#pragma unroll
    for (int e = 0; e < 4; e++) { sm[r] = dot2(a[r][e], 0x3f803f80u, sm[r]); sq[r] = dot2(a[r][e], a[r][e], sq[r]); sm[r] = dot2(b2[r][e], 0x3f803f80u, sm[r]); sq[r] = dot2(b2[r][e], b2[r][e], sq[r]); }
  }
#pragma unroll
  for (int r = 0; r < 4; r++) {
    const float s1 = __builtin_bit_cast(float, __builtin_amdgcn_readlane(__builtin_bit_cast(int, wave_sum63(sm[r])), 63));
    const float s2 = __builtin_bit_cast(float, __builtin_amdgcn_readlane(__builtin_bit_cast(int, wave_sum63(sq[r])), 63));
    const float mean = s1 * (1.0f / D), var = fmaxf(s2 * (1.0f / D) - mean * mean, 0.f);
    sm[r] = mean; sq[r] = rsqrtf(var + LN_EPS);
  }
  {
    const int r = lane & 3;
    const u32x2 v = {__float_as_uint(r == 0 ? sm[0] : r == 1 ? sm[1] : r == 2 ? sm[2] : sm[3]), __float_as_uint(r == 0 ? sq[0] : r == 1 ? sq[1] : r == 2 ? sq[2] : sq[3])};
    __builtin_amdgcn_raw_buffer_store_b64(v, c.sws, lane < 4 ? (unsigned)lane * 8u : 0x7ff00000u, (unsigned)WS_ST1 + (unsigned)(hb * 32 + w * 4) * 8u, SC1);
  }
  float s8[8] = {0, 0, 0, 0, 0, 0, 0, 0};
#pragma unroll
  for (int i = 0; i < 9; i++)
#pragma unroll
    for (int e = 0; e < 4; e++) { s8[2 * e] += bflo(v9[i][e]); s8[2 * e + 1] += bfhi(v9[i][e]); }
#pragma unroll
  for (int e = 0; e < 8; e++) s8[e] = sum_x32(sum_x16(sum_x8(s8[e])));
  if (lane < 8) { lds_w128(c.lds0 + (unsigned)(w * 64 + lane * 8) * 4u, f32x4{s8[0], s8[1], s8[2], s8[3]}); lds_w128(c.lds0 + (unsigned)(w * 64 + lane * 8) * 4u + 16, f32x4{s8[4], s8[5], s8[6], s8[7]}); }
  wait_lgkm0(); bar();
  if (w == 0) {
    float t = 0;
#pragma unroll
    for (int k = 0; k < 8; k++) t += *(const LAS float*)(size_t)(c.lds0 + (unsigned)(k * 64 + lane) * 4u);
    __builtin_amdgcn_raw_buffer_store_b32(__float_as_uint(t), c.sws, hb < 27 ? (unsigned)lane * 4u : 0x7ff00000u, (unsigned)WS_CSQ + (unsigned)hb * 256u, SC1);
  }
  wait_lgkm0();
  wait_vm<0>();
  entry_nonce(c, nv);
  publish(c, FL_S1 + hb, true);
}

DI void relayout_w1(const Ctx& c, int fc, int part) {
  const int w = c.w, lane = c.lane; const unsigned lds0 = c.lds0 + L_W1;
  const int h = lane >> 5, blk = (lane >> 4) & 1, q = (lane >> 2) & 3, p = lane & 3;
  const unsigned ra = lds0 + (unsigned)(8 * h + q) * 576u + (unsigned)(16 * blk + 8 * (p & 1) + 4 * (p >> 1)) * 2u;
  for (int fr = w; fr < 81; fr += 12) {
    const int sl = fr / 9, tile = fr % 9, s = part * 9 + sl, kh = s / 18;
    const unsigned a = ra + (unsigned)sl * 16u * 576u + (unsigned)tile * 64u;
    const u32x4 v = cat4(lds_tr(a), lds_tr(a + 4 * 576));
    const int slot = tile == 8 ? 8 : (kh == 0 ? (tile ^ 1) : tile);
    const int s18 = s % 18;
    st128s(v, c.sws, (unsigned)lane * 16u, (unsigned)WS_W1P + ((unsigned)(fc * 9 + slot) * 36u + (unsigned)((s18 / 9) * 18 + kh * 9 + s18 % 9)) * 1024u);
  }
}
DI void relayout_w2(const Ctx& c, unsigned ldsw, int kappa, int ct) {
  const int lane = c.lane;
  const srd_t sw = mk_srd(c.w2, D * F * 2);
#pragma unroll
  for (int pc = 0; pc < 9; pc++) {
    const int ch = pc * 64 + lane, row = ch / 18, cc = ch % 18;
    dma16(ldsw + (unsigned)pc * 1024u, (unsigned)row * (D * 2) + (unsigned)cc * 16u, sw, (unsigned)kappa * 32u * (D * 2) + (unsigned)ct * 288u);
  }
  wait_vm<0>();
  const int g = lane >> 4, q = (lane >> 2) & 3, p = lane & 3;
  const unsigned ra = ldsw + (unsigned)(8 * g + q) * 288u;
  const int ks = kappa / 36, kst = (kappa % 36 + 36 - 9 * ct) % 36;
#pragma unroll
  for (int nt = 0; nt < 9; nt++) {
    const unsigned col = nt < 8 ? (unsigned)(32 * (nt >> 1) + 8 * p + 4 * (nt & 1)) * 2u : (unsigned)(128 + 4 * p) * 2u;
    const u32x4 v = cat4(lds_tr(ra + col), lds_tr(ra + col + 4 * 288));
    st128s(v, c.sws, (unsigned)lane * 16u, (unsigned)WS_W2P + ((unsigned)(ks * 4 + ct) * 324u + (unsigned)(((kst >> 2) * 2 + (kst & 1)) * 18 + ((kst >> 1) & 1) * 9 + nt)) * 1024u);
  }
  wait_lgkm0();
}

#define ABL_QW 1
#define ABL_QX 1
constexpr int Q_SLOT = 36864;
constexpr int Q_CT = 4 * Q_SLOT;
DI unsigned q_voff(int p, int lane) {
  if (p < 16) return (unsigned)(lane >> 3) * (D * 2) + (unsigned)(((lane & 7) ^ ((4 * (p & 1) + (lane >> 4)) & 7)) * 16);
  const int ch = 64 * (p - 16) + lane, row = ch / 20, cc = ch % 20;
  return (unsigned)row * (NQKV * 2) + (unsigned)cc * 16u;
}
DI void q_issue(const Ctx& c, srd_t sx, srd_t sw, int rb, int ct, int p, unsigned voff, int t, unsigned ring) {
  if (p < 16) dma16(c.lds0 + ring + (unsigned)p * 1024u, voff, sx, ((unsigned)rb * 128u + (unsigned)p * 8u) * (D * 2) + (unsigned)t * 128u);
  else dma16(c.lds0 + ring + (unsigned)p * 1024u, voff, sw, (unsigned)ct * 288u + (unsigned)t * 64u * (NQKV * 2));
}
DI void p1_loader(Ctx& c, int rb, int ct) {
  const int w = c.w, lane = c.lane, l = w - 8;
  const srd_t sx = mk_srd(c.x, S * D * 2), sw = mk_srd(c.wqkv, D * NQKV * 2);
  const u32x2 nv = entry_load(c);
#pragma unroll
  for (int t = 0; t < 2; t++)
#pragma unroll
    for (int i = 0; i < 3; i++) q_issue(c, sx, sw, rb, ct, w + 12 * i, q_voff(w + 12 * i, lane), t, (unsigned)t * Q_SLOT);
  unsigned vw[5];
#pragma unroll
  for (int i = 0; i < 5; i++) vw[i] = q_voff(16 + l + 4 * i, lane);
#define Q_W_ISSUE(t, ring) do { if (ABL_QW || (t) < 3) \
    _Pragma("unroll") for (int i = 0; i < 5; i++) dma16(c.lds0 + (ring) + (unsigned)(16 + l + 4 * i) * 1024u, vw[i], sw, (unsigned)ct * 288u + (unsigned)(t) * 64u * (NQKV * 2)); } while (0)
  Q_W_ISSUE(2, 2u * Q_SLOT);
  entry_prefetch(c);
  wait_vm<6>();
  entry_nonce(c, nv);
  bar();
  Q_W_ISSUE(3, 3u * Q_SLOT);
#pragma unroll 1
  for (int t = 1; t < 6; t++) {
    wait_vm<5>(); bar();
    Q_W_ISSUE(t + 3, (unsigned)((t + 3) & 3) * Q_SLOT);
  }
  wait_vm<5>(); bar();
  wait_vm<0>(); bar();
  if (l == 0) { const int g0 = (144 * ct) >> 6; poll(c, FL_S1 + (lane < 4 ? 4 * rb + lane : min(g0 + (lane & 3), 26)), lane < 8); }
  bar();
  bar();
  bar();
}
template <int NT, int T0>
DI void p1_body(Ctx& c, int rb, int ct) {
  const int w = c.w, lane = c.lane, rg = w & 3; const unsigned lds0 = c.lds0;
  constexpr bool XI = NT == 2;
  const srd_t sx = mk_srd(c.x, S * D * 2);
  const unsigned vx = q_voff(rg, lane);
#define Q_X_ISSUE1(t, ring, i) dma16(lds0 + (ring) + (unsigned)(rg + 4 * (i)) * 1024u, vx, sx, ((unsigned)rb * 128u + (unsigned)(rg + 4 * (i)) * 8u) * (D * 2) + (unsigned)(t) * 128u)
  {
    const srd_t sw = mk_srd(c.wqkv, D * NQKV * 2);
    const u32x2 nv = entry_load(c);
#pragma unroll
    for (int t = 0; t < 2; t++)
#pragma unroll
      for (int i = 0; i < 3; i++) q_issue(c, sx, sw, rb, ct, w + 12 * i, q_voff(w + 12 * i, lane), t, (unsigned)t * Q_SLOT);
    if (XI) { Q_X_ISSUE1(2, 2u * Q_SLOT, 0); Q_X_ISSUE1(2, 2u * Q_SLOT, 1); Q_X_ISSUE1(2, 2u * Q_SLOT, 2); Q_X_ISSUE1(2, 2u * Q_SLOT, 3); wait_vm<4>(); } else wait_vm<0>();
    entry_nonce(c, nv);
  }
  const int tok = lane & 31, h = lane >> 5, blk = (lane >> 4) & 1, q4 = (lane >> 2) & 3, pp = lane & 3;
  const unsigned xrow = lds0 + (unsigned)(rg * 32 + tok) * 128u;
  unsigned xj[4];
#pragma unroll
  for (int j = 0; j < 4; j++) xj[j] = xrow + (unsigned)(((2 * j + h) ^ ((tok >> 1) & 7)) * 16);
  const unsigned wj = lds0 + 16384u + (unsigned)(8 * h + q4) * 320u + (unsigned)(32 * T0 + 16 * blk + 8 * (pp & 1) + 4 * (pp >> 1)) * 2u;
  f32x16 acc[NT];
#pragma unroll
  for (int n = 0; n < NT; n++) acc[n] = f32x16{};
  ST(1, 1);
  bar();
  ST(1, 2);
  u32x4 wf[4][NT], xf[4];
#pragma unroll
  for (int j = 0; j < 2; j++) {
    xf[j] = lds_u128(xj[j]);
#pragma unroll
    for (int n = 0; n < NT; n++) wf[j][n] = cat4(lds_tr(wj + j * 5120 + n * 64), lds_tr(wj + j * 5120 + n * 64 + 4 * 320));
  }
  unsigned r0 = 0, r1 = Q_SLOT, r3 = 3 * Q_SLOT;
#define Q_PERIOD(t, SYNC, ISSUE, EXTRA) do { \
    if (XI && (SYNC) == 2) wait_vm<4>(); \
    if (XI && (SYNC) == 3) wait_vm<0>(); \
    if (SYNC) bar(); \
    EXTRA; \
    unsigned wa_ = wj + r0, wn_ = wj + r1, xa_[4]; \
    _Pragma("unroll") for (int j = 0; j < 4; j++) { xa_[j] = xj[(j + 2) & 3] + (j < 2 ? r0 : r1); asm volatile("" : "+v"(xa_[j])); } \
    asm volatile("" : "+v"(wa_), "+v"(wn_)); \
    _Pragma("unroll") for (int j = 0; j < 4; j++) { \
      const u32x4 xc = xf[j]; \
      xf[(j + 2) & 3] = lds_u128(xa_[j]); \
      _Pragma("unroll") for (int n = 0; n < NT; n++) { \
        const u32x4 cur = wf[j][n]; \
        const unsigned a_ = (j < 2 ? wa_ : wn_) + ((j + 2) & 3) * 5120 + n * 64; \
        wf[(j + 2) & 3][n] = cat4(lds_tr(a_), lds_tr(a_ + 4 * 320)); \
        acc[n] = mfma32(cur, xc, acc[n]); \
        if (ABL_QX && XI && (ISSUE) && n == 0) Q_X_ISSUE1((t) + 3, r3, j); \
        SB(); \
      } \
    } \
    r0 = r1; r1 = r1 == 3u * Q_SLOT ? 0u : r1 + Q_SLOT; r3 = r3 == 3u * Q_SLOT ? 0u : r3 + Q_SLOT; \
  } while (0)
  Q_PERIOD(0, 0, true, (void)0);
#pragma unroll 1
  for (int t = 1; t < 6; t++) Q_PERIOD(t, 2, true, (void)0);
  const int tl = rg * 32 + tok, type = ct >> 2;
  u32x2 stv; u32x4 csv;
  Q_PERIOD(6, 2, false, (void)0);
  Q_PERIOD(7, 3, false, (void)0);
  ST(6, 1);
  Q_PERIOD(8, 1, false, (
    { asm volatile("buffer_load_dwordx2 %0, %1, %2, %3 offen sc1" : "=&v"(stv) : "v"((unsigned)tl * 8u), "s"(c.sws), "s"((unsigned)WS_ST1 + (unsigned)rb * 1024u) : "memory");
      asm volatile("buffer_load_dwordx4 %0, %1, %2, %3 offen sc1" : "=&v"(csv) : "v"((unsigned)min(lane, 39) * 16u), "s"(c.sws), "s"((unsigned)WS_CSQ + (unsigned)ct * 576u) : "memory"); }));
  asm volatile("s_waitcnt vmcnt(0)" : "+v"(stv), "+v"(csv) :: "memory");
  ST(6, 2);
  if (w == 0 && lane < 40) *(LAS u32x4*)(size_t)(lds0 + Q_CT + (unsigned)lane * 16u) = csv;
  ST(1, 3);
  wait_lgkm0(); bar();
  ST(1, 5);
  f32x2 st;
  st[0] = __uint_as_float(stv[1]) * (type == 0 ? 0.23570226039551584f * 1.4426950408889634f : 1.0f);
  st[1] = -__uint_as_float(stv[0]) * st[0];
#pragma unroll
  for (int n = 0; n < NT; n++)
#pragma unroll
    for (int hf = 0; hf < 2; hf++) {
      if (T0 + n == 4 && hf == 1) continue;
      const int c0 = 32 * (T0 + n) + 16 * hf;
      const f32x4 ca = lds_f128(lds0 + Q_CT + (unsigned)(c0 + 8 * h) * 4u), cb = lds_f128(lds0 + Q_CT + (unsigned)(c0 + 8 * h) * 4u + 16);
      u32x4 o;
#pragma unroll
      for (int e = 0; e < 4; e++) {
        const float c0v = e < 2 ? ca[2 * e] : cb[2 * e - 4], c1v = e < 2 ? ca[2 * e + 1] : cb[2 * e - 3];
        o[e] = pk2(__builtin_fmaf(acc[n][8 * hf + 2 * e], st[0], c0v * st[1]), __builtin_fmaf(acc[n][8 * hf + 2 * e + 1], st[0], c1v * st[1]));
      }
      *(LAS u32x4*)(size_t)(lds0 + (unsigned)tl * 304u + (unsigned)(c0 + 8 * h) * 2u) = o;
    }
  wait_lgkm0(); bar();
  ST(1, 6);
}
DI void p1_qkv(Ctx& c, int rb, int ct) {
  if (c.w >= 8) p1_loader(c, rb, ct); else if (c.w < 4) p1_body<3, 0>(c, rb, ct); else p1_body<2, 3>(c, rb, ct);
  {
    const int tid = c.w * 64 + c.lane, tg = tid >= 384, t = tid - 384 * tg, tk = t / 3, part = t - 3 * tk, type = ct >> 2;
    const unsigned src = c.lds0 + (unsigned)tk * 304u + (unsigned)part * 16u + (unsigned)tg * 144u;
    const unsigned keep = part < 2 ? 0xffffffffu : 0u, padv = part < 2 ? 0u : (type == 0 ? 0x3f800000u : 0x00003f80u);
    const unsigned dst = (unsigned)WS_QKV + ((unsigned)(type * 32 + (ct & 3) * 8) * S + (unsigned)rb * 128u) * 48u;
    u32x4 v[4];
#pragma unroll
    for (int hh = 0; hh < 4; hh++)
#pragma unroll
      for (int e = 0; e < 4; e++) v[hh][e] = *(const LAS unsigned*)(size_t)(src + hh * 36 + e * 4);
#pragma unroll
    for (int hh = 0; hh < 4; hh++) {
      v[hh][1] = (v[hh][1] & keep) | padv; v[hh][2] &= keep; v[hh][3] &= keep;
      st128s(v[hh], c.sws, (unsigned)t * 16u + (unsigned)(4 * tg + hh) * (S * 48), dst);
    }
  }
  wait_lgkm0();
  ST(1, 7);
  publish(c, FL_Q + rb * 12 + ct, true);
}

constexpr int L_K = 0, L_V = 24576, L_WO = 49216;
DI void p2_attn(const Ctx& c, int head, int qb) {
  const int w = c.w, lane = c.lane; const unsigned lds0 = c.lds0;
  const unsigned ldsK = lds0 + L_K, ldsV = lds0 + L_V;
  if (w == 0) {
    const int hg = head >> 3, l = lane;
    const int ty = l < 2 ? 0 : (l < 6 ? 1 : 2), rbl = l < 2 ? 2 * qb + l : 2 * qb - 2 + ((l - 2) & 3);
    poll(c, FL_Q + rbl * 12 + ty * 4 + hg, l < 10 && rbl >= 0);
  }
  bar();
  ST(2, 1);
  if (w >= 8) { bar(); publish(c, FL_A + qb * 32 + head, false); return; }
  const unsigned kbase = (unsigned)WS_QKV + (unsigned)((32 + head) * S + 256 * (qb - 1)) * 48u, vbase = (unsigned)WS_QKV + (unsigned)((64 + head) * S + 256 * (qb - 1)) * 48u;
  const unsigned vl = lane * 16;
#pragma unroll
  for (int hf = 0; hf < 2; hf++) {
    if (hf == 1 || qb > 0) {
#pragma unroll
      for (int j = 0; j < 3; j++) {
        const int pi = w * 3 + j;
        const unsigned pc = (unsigned)hf * 12288u + (unsigned)(pi % 12) * 1024;
        if (pi < 12) dma16s(ldsK + pc, vl, c.sws, kbase + pc); else dma16s(ldsV + pc, vl, c.sws, vbase + pc);
      }
    }
  }
  if (qb == 0) {
    const int tid = w * 64 + lane;
    if (tid < 256) {
      const u32x4 z = {0, 0, 0, 0};
      *(LAS u32x4*)(size_t)(ldsK + tid * 48) = z; *(LAS u32x4*)(size_t)(ldsK + tid * 48 + 16) = z; *(LAS u32x4*)(size_t)(ldsK + tid * 48 + 32) = u32x4{0, 0xf1493f80u, 0, 0};
      *(LAS u32x4*)(size_t)(ldsV + tid * 48) = z; *(LAS u32x4*)(size_t)(ldsV + tid * 48 + 16) = z; *(LAS u32x4*)(size_t)(ldsV + tid * 48 + 32) = z;
    }
  }
  const int qq = lane & 31, h = lane >> 5;
  const int q0 = 256 * qb + 32 * w;
  const unsigned qo = (unsigned)WS_QKV + (unsigned)(head * S + q0) * 48u;
  const u32x4 qf0 = ld128s(c.sws, (unsigned)qq * 48u + 16u * h, qo);
  u32x4 qf1 = ld128s(c.sws, (unsigned)qq * 48u + 32u, qo);
  if (h) qf1 = u32x4{0, 0, 0, 0};
  f32x16 cold, cdiag;
#pragma unroll
  for (int r = 0; r < 16; r++) {
    const int kk = 16 * (r >> 3) + 8 * h + (r & 7);
    cold[r] = kk > qq ? 0.f : -1e30f;
    cdiag[r] = kk <= qq ? 0.f : -1e30f;
  }
  const int key = swap23(lane & 31);
  unsigned ka0 = ldsK + (unsigned)(key + 32 * w) * 48 + 16 * h;
  unsigned ka1 = ldsK + (unsigned)(key + 32 * w) * 48 + 32;
  const int blk = (lane >> 4) & 1, q4 = (lane >> 2) & 3, pp = lane & 3;
  unsigned va = ldsV + (unsigned)(32 * w + 8 * h + q4) * 48 + 32 * blk + 8 * pp;
  asm volatile("" : "+v"(ka0), "+v"(ka1), "+v"(va), "+v"(cold), "+v"(cdiag));
  wait_vm<0>(); wait_lgkm0(); bar();
  ST(2, 2);
  const f32x16 zero = {};
  u32x4 k0[9], k1[9];
#pragma unroll
  for (int j = 0; j < 9; j++) { k0[j] = lds_u128(ka0 + j * 1536); k1[j] = lds_u128(ka1 + j * 1536); }
  float m = -1e30f;
  {
    f32x16 sA = mfma32(k1[0], qf1, mfma32(k0[0], qf0, cold)), sB;
#pragma unroll
    for (int j = 0; j < 9; j++) {
      f32x16& cur = (j & 1) ? sB : sA; f32x16& nxt = (j & 1) ? sA : sB;
      if (j + 1 < 9) nxt = mfma32(k1[j + 1], qf1, mfma32(k0[j + 1], qf0, j + 1 == 8 ? cdiag : zero));
      SB();
      m = j == 0 ? max16<11>(m, cur) : (j < 8 ? max16<1>(m, cur) : max16<3>(m, cur));
      SB();
    }
  }
  { const auto pr = __builtin_amdgcn_permlane32_swap(__float_as_uint(m), __float_as_uint(m), false, false); m = fmaxf(__uint_as_float(pr[0]), __uint_as_float(pr[1])); }
  if (h == 0) qf1[1] = (qf1[1] & 0xffff0000u) | (pk2(-m, 0.f) & 0xffffu);
  ST(2, 3);
  f32x16 oacc = {};
  {
    f32x16 sA = mfma32(k1[0], qf1, mfma32(k0[0], qf0, cold)), sB;
    u32x4 v0 = cat4(lds_tr(va), lds_tr(va + 4 * 48)), v1 = cat4(lds_tr(va + 16 * 48), lds_tr(va + 20 * 48));
#pragma unroll
    for (int j = 0; j < 9; j++) {
      f32x16& cur = (j & 1) ? sB : sA; f32x16& nxt = (j & 1) ? sA : sB;
      if (j + 1 < 9) nxt = mfma32(k0[j + 1], qf0, j + 1 == 8 ? cdiag : zero);
      SB();
      u32x4 p0, p1;
      {
        float e0 = cur[0]; asm volatile("" : "+v"(e0));
        p0[0] = pk2(__builtin_amdgcn_exp2f(e0), __builtin_amdgcn_exp2f(cur[1]));
#pragma unroll
        for (int e = 1; e < 4; e++) p0[e] = pk2(__builtin_amdgcn_exp2f(cur[2 * e]), __builtin_amdgcn_exp2f(cur[2 * e + 1]));
        asm volatile("" : "+v"(p0));
      }
      SB();
      if (j + 1 < 9) nxt = mfma32(k1[j + 1], qf1, nxt);
      oacc = mfma32(v0, p0, oacc);
      if (j + 1 < 9) v0 = cat4(lds_tr(va + (j + 1) * 1536), lds_tr(va + (j + 1) * 1536 + 4 * 48));
      SB();
      {
        float e8 = cur[8]; asm volatile("" : "+v"(e8));
        p1[0] = pk2(__builtin_amdgcn_exp2f(e8), __builtin_amdgcn_exp2f(cur[9]));
#pragma unroll
        for (int e = 1; e < 4; e++) p1[e] = pk2(__builtin_amdgcn_exp2f(cur[8 + 2 * e]), __builtin_amdgcn_exp2f(cur[8 + 2 * e + 1]));
        asm volatile("" : "+v"(p1));
      }
      SB();
      oacc = mfma32(v1, p1, oacc);
      if (j + 1 < 9) v1 = cat4(lds_tr(va + (j + 1) * 1536 + 16 * 48), lds_tr(va + (j + 1) * 1536 + 20 * 48));
      SB();
    }
  }
  const float l = __uint_as_float(__builtin_amdgcn_permlane32_swap(__float_as_uint(oacc[10]), __float_as_uint(oacc[10]), false, false)[0]);
  const float inv = __builtin_amdgcn_rcpf(l);
  const u32x4 om = {pk2(oacc[0] * inv, oacc[1] * inv), pk2(oacc[2] * inv, oacc[3] * inv), pk2(oacc[4] * inv, oacc[5] * inv), pk2(oacc[6] * inv, oacc[7] * inv)};
  st128s(om, c.sws, (unsigned)lane * 16u, (unsigned)WS_OP + (unsigned)((q0 >> 5) * 32 + head) * 1024u);
  __builtin_amdgcn_raw_buffer_store_b32(pk2(oacc[8] * inv, oacc[9] * inv), c.sws, h == 0 ? (unsigned)qq * 4u : 0x7ff00000u, (unsigned)WS_OL + (unsigned)(head * S + q0) * 4u, SC1);
  publish(c, FL_A + qb * 32 + head, true);
}

DI void p3_prefetch(const Ctx& c, int ct) {
  const int l = c.w - 8, lane = c.lane;
  const srd_t sw = mk_srd(c.wo, D * D * 2);
  unsigned vw[3];
#pragma unroll
  for (int i = 0; i < 3; i++) { const int ch = 64 * i + lane; vw[i] = (unsigned)(ch / 12) * (D * 2) + (unsigned)(ch % 12) * 16u; }
#pragma unroll 1
  for (int gi = 0; gi < 9; gi++) {
    const int grp = l * 9 + gi;
#pragma unroll
    for (int i = 0; i < 3; i++) dma16(c.lds0 + L_WO + (unsigned)(grp * 3 + i) * 1024u, vw[i], sw, (unsigned)grp * 16u * (D * 2) + (unsigned)ct * 192u);
  }
}
template <int KQ>
DI void p3_body(const Ctx& c, int rb, int ct) {
  constexpr int kq = KQ;
  const int lane = c.lane, rg = c.w & 1; const unsigned lds0 = c.lds0;
  const int qt = rb * 2 + rg, tok = lane & 31, h = lane >> 5;
  const int row = qt * 32 + tok;
  const u32x4 xr0 = *(const u32x4*)(c.x + (size_t)row * D + ct * 96 + 16 * kq + 8 * h);
  const u32x4 xr1 = *(const u32x4*)(c.x + (size_t)row * D + ct * 96 + 16 * (kq < 2 ? kq + 4 : kq) + 8 * h);
  const int blk = (lane >> 4) & 1, q4 = (lane >> 2) & 3, pp = lane & 3;
  const unsigned colb = lds0 + L_WO + (unsigned)(16 * blk + 8 * (pp & 1) + 4 * (pp >> 1)) * 2u;
  unsigned ah = colb + (unsigned)(18 * 9 * kq + 4 * h + q4) * 192u;
  unsigned al = colb + (unsigned)(18 * (4 * h + (q4 >> 1)) + 16 + (q4 & 1)) * 192u;
  asm volatile("" : "+v"(ah), "+v"(al));
#define P3_W(s, n) ((kq < 3 || (s) < 5) ? cat4(lds_tr(ah + (s) * 3456 + (n) * 64), lds_tr(ah + (s) * 3456 + 1536 + (n) * 64)) \
                                        : cat4(lds_tr(al + ((s) - 5) * 27648 + (n) * 64), lds_tr(al + ((s) - 5) * 27648 + 6912 + (n) * 64)))
  bar();
  ST(3, 1);
  u32x4 of[9];
  {
    const unsigned so = (unsigned)WS_OP + (unsigned)(qt * 32 + kq * 9) * 1024u;
#pragma unroll
    for (int s = 0; s < (kq < 3 ? 9 : 5); s++) of[s] = ld128s(c.sws, (unsigned)lane * 16u + s * 1024u, so);
    if (kq == 3) {
#pragma unroll
      for (int G = 0; G < 4; G++)
#pragma unroll
        for (int i = 0; i < 4; i++)
          of[5 + G][i] = __builtin_amdgcn_raw_buffer_load_b32(c.sws, (unsigned)((4 * h + i) * S + tok) * 4u, (unsigned)WS_OL + (unsigned)(8 * G * S + qt * 32) * 4u, SC1);
    }
  }
  u32x4 wf[3][3];
#pragma unroll
  for (int s = 0; s < 2; s++)
#pragma unroll
    for (int n = 0; n < 3; n++) wf[s][n] = P3_W(s, n);
  SB();
  ST(3, 2);
  f32x16 acc[3] = {};
#pragma unroll
  for (int s = 0; s < 9; s++) {
#pragma unroll
    for (int n = 0; n < 3; n++) {
      if (s + 2 < 9) wf[(s + 2) % 3][n] = P3_W(s + 2, n);
      acc[n] = mfma32(wf[s % 3][n], of[s], acc[n]);
    }
    SB();
  }
  ST(3, 3);
  wait_lgkm0(); bar();
  const unsigned xb = lds0 + L_WO + (unsigned)rg * 49152u + (unsigned)lane * 16u;
#pragma unroll
  for (int f = 0; f < 6; f++) {
    if ((f & 3) == kq) continue;
    const int r8 = 8 * (f & 1);
    lds_w128(xb + (unsigned)(f * 4 + kq) * 2048u, f32x4{acc[f >> 1][r8], acc[f >> 1][r8 + 1], acc[f >> 1][r8 + 2], acc[f >> 1][r8 + 3]});
    lds_w128(xb + (unsigned)(f * 4 + kq) * 2048u + 1024u, f32x4{acc[f >> 1][r8 + 4], acc[f >> 1][r8 + 5], acc[f >> 1][r8 + 6], acc[f >> 1][r8 + 7]});
  }
  wait_lgkm0(); bar();
  float ssum = 0, ssq = 0;
#pragma unroll
  for (int i = 0; i < (kq < 2 ? 2 : 1); i++) {
    const int f = kq + 4 * i, r8 = 8 * (f & 1);
    f32x4 a = {acc[f >> 1][r8], acc[f >> 1][r8 + 1], acc[f >> 1][r8 + 2], acc[f >> 1][r8 + 3]}, cc = {acc[f >> 1][r8 + 4], acc[f >> 1][r8 + 5], acc[f >> 1][r8 + 6], acc[f >> 1][r8 + 7]};
#pragma unroll
    for (int k = 0; k < 4; k++)
      if (k != kq) { a += lds_f128(xb + (unsigned)(f * 4 + k) * 2048u); cc += lds_f128(xb + (unsigned)(f * 4 + k) * 2048u + 1024u); }
    const u32x4 xr = i ? xr1 : xr0;
    u32x4 o;
    o[0] = pk2(bflo(xr[0]) + a[0], bfhi(xr[0]) + a[1]); o[1] = pk2(bflo(xr[1]) + a[2], bfhi(xr[1]) + a[3]);
    o[2] = pk2(bflo(xr[2]) + cc[0], bfhi(xr[2]) + cc[1]); o[3] = pk2(bflo(xr[3]) + cc[2], bfhi(xr[3]) + cc[3]);
    st128s(o, c.sws, (unsigned)lane * 16u, (unsigned)WS_HP + (unsigned)(qt * 36 + ct * 6 + f) * 1024u);
#pragma unroll
    for (int e = 0; e < 4; e++) { ssum = dot2(o[e], 0x3f803f80u, ssum); ssq = dot2(o[e], o[e], ssq); }
  }
  { const auto a = __builtin_amdgcn_permlane32_swap(__float_as_uint(ssum), __float_as_uint(ssum), false, false); ssum = __uint_as_float(a[0]) + __uint_as_float(a[1]);
    const auto b2 = __builtin_amdgcn_permlane32_swap(__float_as_uint(ssq), __float_as_uint(ssq), false, false); ssq = __uint_as_float(b2[0]) + __uint_as_float(b2[1]); }
  const unsigned stb = lds0 + L_WO + 98304u;
  if (h == 0) *(LAS f32x2*)(size_t)(stb + (unsigned)(rg * 32 + tok) * 32u + kq * 8u) = f32x2{ssum, ssq};
  wait_lgkm0(); bar();
  if (c.w == 0) {
    const f32x4 a = lds_f128(stb + lane * 32u), cc = lds_f128(stb + lane * 32u + 16);
    const u32x2 v = {__float_as_uint((a[0] + a[2]) + (cc[0] + cc[2])), __float_as_uint((a[1] + a[3]) + (cc[1] + cc[3]))};
    __builtin_amdgcn_raw_buffer_store_b64(v, c.sws, (unsigned)lane * 8u, (unsigned)WS_ST2 + (unsigned)(rb * 6 + ct) * 512u, SC1);
  }
}
DI void p3_wo(const Ctx& c, int rb, int ct) {
  const int kq = c.w >> 1;
  if (kq == 0) p3_body<0>(c, rb, ct); else if (kq == 1) p3_body<1>(c, rb, ct); else if (kq == 2) p3_body<2>(c, rb, ct); else p3_body<3>(c, rb, ct);
  publish(c, FL_H + rb * 6 + ct, true);
}

constexpr float GC0 = -2.0f * 0.7978845608028654f * 1.4426950408889634f, GC1 = GC0 * 0.044715f;
DI f32x2 gelu2(f32x2 x) {
  const f32x2 t = x * (GC1 * (x * x) + GC0);
  f32x2 e; e[0] = __builtin_amdgcn_exp2f(t[0]); e[1] = __builtin_amdgcn_exp2f(t[1]);
  const f32x2 d = e + 1.0f;
  f32x2 r; r[0] = __builtin_amdgcn_rcpf(d[0]); r[1] = __builtin_amdgcn_rcpf(d[1]);
  return x * r;
}
struct G2 { float x0, x1, t0, t1; };
DI void g2_s1(G2& g, float a0, float a1, float p0, float p1) {
  asm volatile("v_add_f32_e32 %0, %4, %6\n\tv_add_f32_e32 %1, %5, %7\n\tv_mul_f32_e32 %2, %0, %0\n\tv_mul_f32_e32 %3, %1, %1"
               : "=&v"(g.x0), "=&v"(g.x1), "=&v"(g.t0), "=&v"(g.t1) : "v"(a0), "v"(a1), "v"(p0), "v"(p1));
}
DI void g2_s2(G2& g, float vc1) {
  asm volatile("v_fmaak_f32 %0, %0, %4, 0xc0135761\n\tv_fmaak_f32 %1, %1, %4, 0xc0135761\n\tv_mul_f32_e32 %0, %2, %0\n\tv_mul_f32_e32 %1, %3, %1"
               : "+v"(g.t0), "+v"(g.t1) : "v"(g.x0), "v"(g.x1), "v"(vc1));
}
DI void g2_s3(G2& g) {
  asm volatile("v_exp_f32_e32 %0, %0\n\tv_exp_f32_e32 %1, %1\n\tv_add_f32_e32 %0, 1.0, %0\n\tv_add_f32_e32 %1, 1.0, %1" : "+v"(g.t0), "+v"(g.t1));
}
DI unsigned g2_s4(G2& g) {
  unsigned o;
  asm volatile("v_rcp_f32_e32 %1, %1\n\tv_rcp_f32_e32 %2, %2\n\tv_mul_f32_e32 %1, %3, %1\n\tv_mul_f32_e32 %2, %4, %2\n\tv_cvt_pk_bf16_f32 %0, %1, %2"
               : "=v"(o), "+v"(g.t0), "+v"(g.t1) : "v"(g.x0), "v"(g.x1));
  return o;
}

#define FPF 3
#define XSTR 4
#define XOFF 1
#define ABL_LW 1
#define ABL_XW 1
#define ABL_ST 1
#define ABL_GELU 1
#define ABL_H 1
#define FUSE0 0
#define W2T_EARLY 1
constexpr int F_SLOT = 36864, H_SLOT = 18432, F1_XCH = 6 * H_SLOT, F1_UV = F1_XCH + 32768, F_LDS = F1_UV + 1152;
DI unsigned f_stream(int fc, int g) {
  return g < 18 ? (unsigned)WS_W1P + (unsigned)(fc * 18 + g) * H_SLOT : (unsigned)WS_W2P + (unsigned)(fc * 18 + g - 18) * H_SLOT;
}
DI void f_issue(const Ctx& c, int fc, int g) {
  const int l = c.w - 8;
  const unsigned o = (unsigned)(l < 2 ? 5 * l : 2 + 4 * l) * 1024u, vl = c.lane * 16, gb = f_stream(fc, g) + o, lb = c.lds0 + (unsigned)(g % 6) * H_SLOT + o;
#pragma unroll
  for (int i = 0; i < 4; i++) dma16s(lb + i * 1024u, vl, c.sws, gb + i * 1024u);
  if (l < 2) dma16s(lb + 4096u, vl, c.sws, gb + 4096u);
}
template <int N> DI void f_wait(int l) { if (l < 2) wait_vm<5 * N>(); else wait_vm<4 * N>(); }
constexpr int F_FIN = 4 * H_SLOT;
DI void p6_finish(const Ctx& c, int rb, int fc) {
  const int w = c.w, lane = c.lane, ks = fc >> 2, ct = fc & 3, tk = lane & 15, g = lane >> 4;
  const bool pair = w < 8;
  const int T = w & 3, rt = pair ? w >> 2 : w - 8;
  const int col = ct * 144 + (pair ? 32 * T + 8 * g : 128 + 4 * g), row = rb * 128 + ks * 32 + rt * 16 + tk;
  u32x4 hv = {}, bv = {};
  const unsigned ho = (unsigned)(((col >> 3) & 1) * 32 + rt * 16 + tk) * 16u + (unsigned)(col >> 4) * 1024u + (unsigned)(col & 7) * 2u, hb = (unsigned)WS_HP + (unsigned)(rb * 4 + ks) * 36u * 1024u;
  if (w < 10) {
    if (pair) { hv = ld128s(c.sws, ho, hb); bv = *(const u32x4*)(c.b2 + col); }
    else { const u32x2 a = __builtin_amdgcn_raw_buffer_load_b64(c.sws, ho, hb, SC1), b2 = *(const u32x2*)(c.b2 + col); hv[0] = a[0]; hv[1] = a[1]; bv[0] = b2[0]; bv[1] = b2[1]; }
  }
  if (w == 10) poll(c, FL_P + (rb * 4 + ct) * 4 + (lane & 3), lane < 4);
  bar();
  if (w >= 10) return;
  const unsigned xg = (unsigned)WS_XCH + (unsigned)((rb * 4 + ct) * 16 + ks * 4) * 9216u + (unsigned)rt * 4608u;
  const unsigned fin = c.lds0 + F_FIN + (unsigned)rt * 9216u + (unsigned)lane * 16u;
  if (pair) {
    u32x4 pv[3];
#pragma unroll
    for (int i = 0; i < 3; i++) pv[i] = ld128s(c.sws, (unsigned)T * 1024u + (unsigned)lane * 16u, xg + (unsigned)(i + (i >= ks)) * 9216u);
    f32x4 a = lds_f128(fin + (unsigned)T * 2048u), b2 = lds_f128(fin + (unsigned)T * 2048u + 1024u);
#pragma unroll
    for (int i = 0; i < 3; i++) { a += f32x4{bflo(pv[i][0]), bfhi(pv[i][0]), bflo(pv[i][1]), bfhi(pv[i][1])}; b2 += f32x4{bflo(pv[i][2]), bfhi(pv[i][2]), bflo(pv[i][3]), bfhi(pv[i][3])}; }
    u32x4 r;
    r[0] = pk2(bflo(hv[0]) + (a[0] + bflo(bv[0])), bfhi(hv[0]) + (a[1] + bfhi(bv[0])));
    r[1] = pk2(bflo(hv[1]) + (a[2] + bflo(bv[1])), bfhi(hv[1]) + (a[3] + bfhi(bv[1])));
    r[2] = pk2(bflo(hv[2]) + (b2[0] + bflo(bv[2])), bfhi(hv[2]) + (b2[1] + bfhi(bv[2])));
    r[3] = pk2(bflo(hv[3]) + (b2[2] + bflo(bv[3])), bfhi(hv[3]) + (b2[3] + bfhi(bv[3])));
    *(u32x4*)(c.out + (size_t)row * D + col) = r;
  } else {
    u32x2 pv[3];
#pragma unroll
    for (int i = 0; i < 3; i++) pv[i] = __builtin_amdgcn_raw_buffer_load_b64(c.sws, 4096u + (unsigned)lane * 8u, xg + (unsigned)(i + (i >= ks)) * 9216u, SC1);
    f32x4 a = lds_f128(fin + 8192u);
#pragma unroll
    for (int i = 0; i < 3; i++) a += f32x4{bflo(pv[i][0]), bfhi(pv[i][0]), bflo(pv[i][1]), bfhi(pv[i][1])};
    u32x2 r;
    r[0] = pk2(bflo(hv[0]) + (a[0] + bflo(bv[0])), bfhi(hv[0]) + (a[1] + bfhi(bv[0])));
    r[1] = pk2(bflo(hv[1]) + (a[2] + bflo(bv[1])), bfhi(hv[1]) + (a[3] + bfhi(bv[1])));
    *(u32x2*)(c.out + (size_t)row * D + col) = r;
  }
}
DI void p45_loader(const Ctx& c, int rb, int fc) {
  const int l = c.w - 8, lane = c.lane, ks = fc >> 2, ct = fc & 3;
  f_issue(c, fc, 2); f_issue(c, fc, 3); f_issue(c, fc, 4);
  if (l == 0) poll(c, FL_H + (2 * rb + lane / 6) * 6 + lane % 6, lane < 12);
  bar();
  u32x2 fd[4] = {};
#pragma unroll 1
  for (int g = 0; g < 36; g++) {
    if (g == 18) { bar(); bar(); bar(); }
    if (g == 8 && l == 0) poll(c, FL_W2 + (ks * 4 + ct) * 4 + (lane & 3), lane < 4);
    if (g == 20 && l == 0) poll(c, FL_H1 + rb * 16 + ks * 4 + (lane & 3), lane < 4);
    if (g < 32) f_wait<3>(l); else if (g == 32) f_wait<2>(l); else if (g == 33) f_wait<1>(l); else wait_vm<0>();
    bar();
    if (g == 8 || g == 10) {
      const unsigned ln = (unsigned)((l * 2 + ((g >> 1) & 1)) * 64 + lane);
      touch128(c.lds0 + L_DUMP, ln < 324u ? ln * 128u : 0x7ff00000u, c.sws, f_stream(fc, 18) + (unsigned)(c.bi & 7) * 41472u);
    }
    if (g == 6 && l == 3 && c.b == 0) {
#pragma unroll
      for (int i = 0; i < 4; i++) asm volatile("buffer_load_dwordx2 %0, %1, %2, 0 offen sc1" : "=&v"(fd[i]) : "v"((unsigned)(FL_D + i * 64 + lane) * 128u), "s"(c.sws) : "memory");
    }
    if (g == 12 && l == 3 && c.b == 0) {
      asm volatile("" : "+v"(fd[0]), "+v"(fd[1]), "+v"(fd[2]), "+v"(fd[3]));
      const unsigned n0 = (unsigned)c.nonce, n1 = (unsigned)(c.nonce >> 32);
      if (!__all(fd[0][0] == n0 && fd[0][1] == n1 && fd[1][0] == n0 && fd[1][1] == n1 && fd[2][0] == n0 && fd[2][1] == n1 && fd[3][0] == n0 && fd[3][1] == n1)) {
#pragma unroll 1
        for (int i = 0; i < 4; i++) poll(c, FL_D + i * 64 + lane, true);
      }
      if (lane == 0) __hip_atomic_store((unsigned long long*)(c.ws + FL_N * 128), c.nonce, __ATOMIC_RELAXED, __HIP_MEMORY_SCOPE_AGENT);
    }
    if (g == 4 || g == 24) code_touch(c);
    if (g + 5 < 36) f_issue(c, fc, g + 5);
  }
  bar(); bar();
  bar();
  p6_finish(c, rb, fc);
}
template <int KH>
DI void p45_ffn(const Ctx& c, int rb, int fc) {
  constexpr int kh = KH;
  const int w = c.w, lane = c.lane, rg = w & 3; const unsigned lds0 = c.lds0;
  const unsigned vl = lane * 16;
  const int ks = fc >> 2, ct = fc & 3;
  const int tok = lane & 31, h = lane >> 5;
  if (threadIdx.x < 288) *(LAS float*)(size_t)(lds0 + F1_UV + threadIdx.x * 4) = __uint_as_float((unsigned)c.b1[fc * 288 + threadIdx.x] << 16);
  wait_lgkm0(); bar();
  ST(4, 1);
  u32x2 stv[6];
#pragma unroll
  for (int e = 0; e < 6; e++)
    asm volatile("buffer_load_dwordx2 %0, %1, %2, %3 offen offset:%4 sc1" : "=&v"(stv[e]) : "v"((unsigned)tok * 8u), "s"(c.sws),
                 "s"((unsigned)WS_ST2 + (unsigned)((2 * rb + (rg >> 1)) * 6) * 512u + (unsigned)(rg & 1) * 256u), "i"(e * 512) : "memory");
  u32x4 xf[18];
  {
    const unsigned so = (unsigned)WS_HP + ((unsigned)(rb * 4 + rg) * 36u + (unsigned)kh * 18u) * 1024u;
#pragma unroll
    for (int s = 0; s < 18; s++)
      asm volatile("buffer_load_dwordx4 %0, %1, %2, %3 offen offset:%4 sc1" : "=&v"(xf[s]) : "v"(vl), "s"(c.sws), "s"(so + (s >> 2) * 4096u), "i"((s & 3) * 1024) : "memory");
  }
  float ra, rc;
  {
    asm volatile("s_waitcnt vmcnt(18)" : "+v"(stv[0]), "+v"(stv[1]), "+v"(stv[2]), "+v"(stv[3]), "+v"(stv[4]), "+v"(stv[5]) :: "memory");
    float s = 0, q = 0;
#pragma unroll
    for (int e = 0; e < 6; e++) { s += __uint_as_float(stv[e][0]); q += __uint_as_float(stv[e][1]); }
    const float mean = s * (1.0f / D), var = fmaxf(q * (1.0f / D) - mean * mean, 0.f), rstd = rsqrtf(var + LN_EPS);
    ra = rstd; rc = -mean * rstd;
  }
  const unsigned rd = lds0 + (unsigned)kh * 9216u + vl;
  const unsigned hvo = (unsigned)(tok >> 4) * 1024u + (unsigned)h * 256u + (unsigned)(tok & 15) * 16u;
  const unsigned hbase = (unsigned)WS_H1P + ((unsigned)(rb * 4 + rg) * 144u + (unsigned)fc * 9u) * 2048u;
  const unsigned bia = lds0 + F1_UV + (unsigned)h * 32u;
  {
#pragma unroll
    for (int s = 0; s < 18; s++) {
      asm volatile("s_waitcnt vmcnt(%1)" : "+v"(xf[s]) : "i"(17 - s) : "memory");
#pragma unroll
      for (int e = 0; e < 4; e++) {
        const f32x2 v = f32x2{bflo(xf[s][e]), bfhi(xf[s][e])} * ra + rc;
        xf[s][e] = pk2(v[0], v[1]);
      }
      asm volatile("" : "+v"(xf[s]));
      SB();
    }
  }
  ST(4, 2);
  constexpr int PF = FPF;
  u32x4 wf[PF];
  const f32x16 zero = {};
#define F1_BIAS(ACC, ba_) do { const f32x4 b0_ = lds_f128(ba_), b1_ = lds_f128(ba_ + 16), b2_ = lds_f128(ba_ + 64), b3_ = lds_f128(ba_ + 80); \
    ACC = f32x16{b0_[0], b0_[1], b0_[2], b0_[3], b1_[0], b1_[1], b1_[2], b1_[3], b2_[0], b2_[1], b2_[2], b2_[3], b3_[0], b3_[1], b3_[2], b3_[3]}; } while (0)
  f32x16 accG, accP, accQ;
  F1_BIAS(accG, bia + (unsigned)(1 - kh) * 128u);
  float vc1 = GC1; asm volatile("" : "+v"(vc1));
  unsigned r0 = 0;
#define F_NEXT(r) ((r) == 5u * H_SLOT ? 0u : (r) + H_SLOT)
  const unsigned xwr = lds0 + F1_XCH + (unsigned)(kh ^ 1) * 16384u + (unsigned)rg * 4096u + vl;
  const unsigned xrd = lds0 + F1_XCH + (unsigned)kh * 16384u + (unsigned)rg * 4096u + vl;
  f32x4 pp[4] = {};
#define F1_STAGE(q, G, SRC, R0, O) do { const int p_ = (q) >> 2, st_ = (q) & 3, r_ = (R0) + 2 * p_; \
      if (st_ == 0) g2_s1(G, SRC[r_], SRC[r_ + 1], pp[r_ >> 2][r_ & 3], pp[r_ >> 2][(r_ & 3) + 1]); \
      if (st_ == 1) g2_s2(G, vc1); if (st_ == 2) g2_s3(G); if (st_ == 3) O[p_] = g2_s4(G); } while (0)
#define F_HALF_BEGIN(s) do { if ((s) % 9 == 0) { bar(); sb = rd + r0; r0 = F_NEXT(r0); sn = rd + r0; asm volatile("" : "+v"(sb), "+v"(sn)); } } while (0)
#define F_FRAG(s) lds_u128((s) % 9 + PF < 9 ? sb + ((s) % 9 + PF) * 1024 : sn + ((s) % 9 + PF - 9) * 1024)
#define F1_PERIOD(t, ACC, PREV, GELU, HALF) do { \
    unsigned sb, sn; \
    u32x4 o; G2 g; \
    const int gt_ = ((t) - 2 - (HALF)) + kh;      \
    _Pragma("unroll") for (int s = 0; s < 18; s++) { \
      F_HALF_BEGIN(s); \
      if (s == 0 && (t) == 0) { _Pragma("unroll") for (int i = 0; i < PF; i++) wf[i] = lds_u128(sb + i * 1024); } \
      if (s == 0 && (GELU) && (HALF) == 0) { _Pragma("unroll") for (int e = 0; e < 4; e++) pp[e] = lds_f128(xrd + e * 1024); SB(); } \
      const u32x4 cur = wf[s % PF]; \
      wf[s % PF] = F_FRAG(s); \
      ACC = mfma32(cur, xf[s], (s == 0 && (HALF) == 1) ? zero : ACC); \
      if (ABL_XW && (HALF) == 1 && s % XSTR == XOFF && s / XSTR < 4) { const int e = s / XSTR; lds_w128(xwr + e * 1024, f32x4{accG[4 * e], accG[4 * e + 1], accG[4 * e + 2], accG[4 * e + 3]}); } \
      if (ABL_GELU && (GELU)) { \
        if (s >= 2 && s <= 17) F1_STAGE(s - 2, g, PREV, 8 * (HALF), o); \
        if (ABL_ST && s == 17) st128s(o, c.sws, hvo + (HALF) * 512u, hbase + (unsigned)gt_ * 2048u); \
      } \
      if ((HALF) == 1 && s == 15) {     \
        const int nt_ = (t) + 1 == 8 ? 8 : (t) + 1 + (1 - kh); \
        if ((t) + 1 == 8 && kh == 0) accG = zero; \
        else { unsigned ba_ = bia + (unsigned)nt_ * 128u; asm volatile("" : "+v"(ba_)); F1_BIAS(accG, ba_); } \
      } \
      SB(); \
    } \
    asm volatile("" :: "v"(ACC)); \
  } while (0)
  const unsigned hoff0 = (unsigned)WS_H1P + ((unsigned)(rb * 4 + rg) * 144u + (unsigned)ks * 36u) * 2048u;
  u32x4 hA[4], hB[4], hC[4];
#define HLOAD1(dst, t, i) do { const int p_ = 4 * (t) + 2 * kh + ((i) >> 1) + 9 * ct; const unsigned so_ = hoff0 + (unsigned)(p_ >= 36 ? p_ - 36 : p_) * 2048u; \
    if (((i) & 1) == 0) asm volatile("buffer_load_dwordx4 %0, %1, %2, %3 offen sc1" : "=&v"(dst[i]) : "v"(vl), "s"(c.sws), "s"(so_) : "memory"); \
    else asm volatile("buffer_load_dwordx4 %0, %1, %2, %3 offen offset:1024 sc1" : "=&v"(dst[i]) : "v"(vl), "s"(c.sws), "s"(so_) : "memory"); } while (0)
#define HWAIT(N, dst) asm volatile("s_waitcnt vmcnt(" #N ")" : "+v"(dst[0]), "+v"(dst[1]), "+v"(dst[2]), "+v"(dst[3]) :: "memory")
  {
    F1_PERIOD(0, accG, accQ, false, 0);
    F1_PERIOD(1, accP, accQ, false, 1);
    F1_PERIOD(2, accG, accP, true, 0);
    F1_PERIOD(3, accQ, accP, true, 1);
    F1_PERIOD(4, accG, accQ, true, 0);
    F1_PERIOD(5, accP, accQ, true, 1);
    F1_PERIOD(6, accG, accP, true, 0);
    F1_PERIOD(7, accQ, accP, true, 1);
    {
      wait_vm<2>();
      unsigned sb, sn;
      u32x4 o, o2; G2 g, g2;
      const int gt_ = 6 + kh;
#pragma unroll
      for (int s = 0; s < 18; s++) {
        F_HALF_BEGIN(s);
        if (s == 0) {
#pragma unroll
          for (int e = 0; e < 4; e++) pp[e] = lds_f128(xrd + e * 1024);
          SB();
        }
        const u32x4 cur = wf[s % PF];
        wf[s % PF] = F_FRAG(s);
        accG = mfma32(cur, xf[s], accG);
        if (s == 10 + KH) HLOAD1(hA, 0, 0); if (s == 12 + KH) HLOAD1(hA, 0, 1); if (s == 14 + KH) HLOAD1(hA, 0, 2); if (s == 16 + KH) HLOAD1(hA, 0, 3);
        if (s >= 2 && s <= 17) { F1_STAGE(s - 2, g, accQ, 0, o); F1_STAGE(s - 2, g2, accQ, 8, o2); }
        if (s == 17) { st128s(o, c.sws, hvo, hbase + (unsigned)gt_ * 2048u); st128s(o2, c.sws, hvo + 512u, hbase + (unsigned)gt_ * 2048u); }
        SB();
      }
      ST(4, 3);
      bar();
      lds_w128(xwr, kh ? f32x4{accG[0], accG[1], accG[2], accG[3]} : f32x4{accG[8], accG[9], accG[10], accG[11]});
      lds_w128(xwr + 1024, kh ? f32x4{accG[4], accG[5], accG[6], accG[7]} : f32x4{accG[12], accG[13], accG[14], accG[15]});
      wait_lgkm0(); bar();
      const f32x4 q0 = lds_f128(xrd), q1 = lds_f128(xrd + 1024);
      const f32x4 k0 = (kh ? f32x4{accG[8], accG[9], accG[10], accG[11]} : f32x4{accG[0], accG[1], accG[2], accG[3]}) + q0;
      const f32x4 k1 = (kh ? f32x4{accG[12], accG[13], accG[14], accG[15]} : f32x4{accG[4], accG[5], accG[6], accG[7]}) + q1;
      const f32x2 z0 = gelu2(f32x2{k0[0], k0[1]}), z1 = gelu2(f32x2{k0[2], k0[3]}), z2 = gelu2(f32x2{k1[0], k1[1]}), z3 = gelu2(f32x2{k1[2], k1[3]});
      o = u32x4{pk2(z0[0], z0[1]), pk2(z1[0], z1[1]), pk2(z2[0], z2[1]), pk2(z3[0], z3[1])};
      st128s(o, c.sws, hvo + (unsigned)kh * 512u, hbase + 8u * 2048u);
    }
  }
  publish(c, FL_H1 + rb * 16 + fc, true);
  ST(4, 4);
  ST(5, 1);
  HLOAD1(hB, 1, 0); HLOAD1(hB, 1, 1); HLOAD1(hB, 1, 2); HLOAD1(hB, 1, 3);
  f32x4 acc[2][9];
#pragma unroll
  for (int a = 0; a < 2; a++)
#pragma unroll
    for (int n = 0; n < 9; n++) acc[a][n] = f32x4{0, 0, 0, 0};
#define F2_PERIOD(t, HC, HN, HM, VMW, MODE) do { \
    HWAIT(VMW, HC); \
    unsigned sb, sn; \
    _Pragma("unroll") for (int s = 0; s < 18; s++) { \
      F_HALF_BEGIN(s); \
      const u32x4 cur = wf[s % PF]; \
      wf[s % PF] = F_FRAG(s); \
      const int k = s / 9, nt = s % 9; \
      acc[0][nt] = mfma16(cur, HC[2 * k], acc[0][nt]); \
      acc[1][nt] = mfma16(cur, HC[2 * k + 1], acc[1][nt]); \
      if ((MODE) == 3) { if (s == KH) HLOAD1(HM, (t) + 1, 0); if (s == 2 + KH) HLOAD1(HM, (t) + 1, 1); if (s == 4 + KH) HLOAD1(HM, (t) + 1, 2); if (s == 6 + KH) HLOAD1(HM, (t) + 1, 3); } \
      if (ABL_H && ((MODE) == 0 || (MODE) == 3)) { if (s == 9 + KH) HLOAD1(HN, (t) + 2, 0); if (s == 11 + KH) HLOAD1(HN, (t) + 2, 1); if (s == 13 + KH) HLOAD1(HN, (t) + 2, 2); if (s == 15 + KH) HLOAD1(HN, (t) + 2, 3); } \
      SB(); \
    } \
  } while (0)
  F2_PERIOD(0, hA, hC, hC, 4, 1);
  ST(5, 2);
  F2_PERIOD(1, hB, hA, hC, 0, 3);
  F2_PERIOD(2, hC, hB, hB, 4, 0);
  F2_PERIOD(3, hA, hC, hC, 4, 0);
  F2_PERIOD(4, hB, hA, hA, 4, 0);
  F2_PERIOD(5, hC, hB, hB, 4, 0);
  F2_PERIOD(6, hA, hC, hC, 4, 0);
  F2_PERIOD(7, hB, hA, hA, 4, 1);
  F2_PERIOD(8, hC, hB, hB, 0, 1);
  ST(5, 3);
  wait_lgkm0(); bar();
  const unsigned xb = lds0 + (unsigned)rg * 18432u + vl;
#pragma unroll
  for (int n = 0; n < 9; n++) lds_w128(xb + (unsigned)(kh ^ 1) * 9216u + n * 1024, kh ? acc[0][n] : acc[1][n]);
  wait_lgkm0(); bar();
  f32x4 o[9];
#pragma unroll
  for (int n = 0; n < 9; n++) o[n] = lds_f128(xb + kh * 9216u + n * 1024) + (kh ? acc[1][n] : acc[0][n]);
  const unsigned xg = (unsigned)WS_XCH + (unsigned)((rb * 4 + ct) * 16) * 9216u + (unsigned)kh * 4608u;
  if (rg != ks) {
    const unsigned d = xg + (unsigned)(rg * 4 + ks) * 9216u;
#pragma unroll
    for (int T = 0; T < 4; T++)
      st128s(u32x4{pk2(o[2 * T][0], o[2 * T][1]), pk2(o[2 * T][2], o[2 * T][3]), pk2(o[2 * T + 1][0], o[2 * T + 1][1]), pk2(o[2 * T + 1][2], o[2 * T + 1][3])}, c.sws, vl + T * 1024u, d);
    __builtin_amdgcn_raw_buffer_store_b64(u32x2{pk2(o[8][0], o[8][1]), pk2(o[8][2], o[8][3])}, c.sws, 4096u + lane * 8u, d, SC1);
  } else {
#pragma unroll
    for (int n = 0; n < 9; n++) lds_w128(lds0 + F_FIN + (unsigned)kh * 9216u + n * 1024u + vl, o[n]);
  }
  wait_lgkm0();
  publish(c, FL_P + (rb * 4 + ct) * 4 + ks, true);
  ST(5, 4);
  p6_finish(c, rb, fc);
  ST(5, 5);
}

constexpr int M_LDS = 163840;
static_assert(L_WO + 110592 <= L_DUMP && 4 * Q_SLOT + 640 <= L_DUMP && F_LDS <= L_DUMP, "LDS map");
__global__ KATTR void fused_block(const u16* __restrict__ x, const u16* __restrict__ wqkv, const u16* __restrict__ wo, const u16* __restrict__ w1,
                                  const u16* __restrict__ b1, const u16* __restrict__ w2, const u16* __restrict__ b2, u16* __restrict__ out, char* __restrict__ ws) {
  extern __shared__ __attribute__((aligned(16))) char smem[];
  asm volatile(".Lfused_code_begin:");
  if (ENT_CODE) entry_code((unsigned)(size_t)smem, rfl(threadIdx.x >> 6), threadIdx.x & 63);
  Ctx c;
  c.x = x; c.wqkv = wqkv; c.wo = wo; c.w1 = w1; c.b1 = b1; c.w2 = w2; c.b2 = b2; c.out = out; c.ws = ws;
  c.sws = mk_srd(ws, WS_BYTES);
  c.lds0 = (unsigned)(size_t)smem;
  c.b = blockIdx.x; c.xcd = c.b & 7; c.bi = c.b >> 3;
  c.w = rfl(threadIdx.x >> 6); c.lane = threadIdx.x & 63;
  ST(1, 0);
  const int xcd = c.xcd, bi = c.bi, w = c.w, lane = c.lane;
  const int rb4 = (xcd >> 2) * 8 + (bi & 7), fc4 = (xcd & 3) * 4 + (bi >> 3);
  if (bi < 24) {
    p1_qkv(c, (xcd >> 1) * 4 + (bi & 3), (xcd & 1) * 6 + (bi >> 2));
  } else {
    const int j = bi - 24, fc = (xcd & 3) * 4 + (j >> 1), part = (xcd >> 2) * 2 + (j & 1);
    helper_stats(c, xcd * 8 + j, fc, part);
    ST(6, 0);
    if (w >= 8) wait_vm<0>();
    bar();
    relayout_w1(c, fc, part);
    publish(c, FL_W1 + fc * 4 + part, true);
    ST(6, 3);
    if (w >= 8) {
      const srd_t sw2 = mk_srd(w2, D * F * 2);
      const int ks = xcd & 3, ct2 = j >> 1, row0 = (ks * 36 + part * 9) * 32;
#pragma unroll
      for (int i = 0; i < 4; i++) {
        const int l = ((w - 8) * 4 + i) * 64 + lane, row = l / 3;
        touch128(c.lds0 + L_DUMP, l < 864 ? (unsigned)row * (D * 2) + (unsigned)(l - 3 * row) * 128u : 0x7ff00000u, sw2, (unsigned)row0 * (D * 2) + (((unsigned)ct2 * 288u) & ~127u));
      }
    }
  }
  ST(1, 4);
  if (bi < 24 && w >= 8) p3_prefetch(c, bi >> 2);
  if (w >= 8) code_touch(c);
  p2_attn(c, xcd * 4 + (bi & 3), bi >> 2);
  ST(2, 4);
  if (w >= 8) {
    if (w == 8 && bi < 24) poll(c, FL_A + xcd * 32 + (lane & 31), lane < 32);
    if (w == 9) poll(c, FL_W1 + fc4 * 4 + (lane & 3), lane < 4);
    if (bi < 24) wait_vm<0>();
    bar();
    code_touch(c);
    f_issue(c, fc4, 0); f_issue(c, fc4, 1);
    {
#pragma unroll
      for (int i = 0; i < 2; i++) { const unsigned l = (unsigned)(((w - 8) * 2 + i) * 64 + lane); touch128(c.lds0 + L_DUMP, l < 288u ? l * 128u : 0x7ff00000u, c.sws, (unsigned)WS_W1P + (unsigned)(fc4 * 9 + 1) * F_SLOT + (unsigned)(bi & 7) * 36864u); }
    }
  }
  if (bi < 24) {
    if (w >= 8) {
      bar(); bar(); bar();
      publish(c, FL_H + (xcd * 4 + (bi & 3)) * 6 + (bi >> 2), false);
    } else {
      p3_wo(c, xcd * 4 + (bi & 3), bi >> 2);
    }
  } else {
    if (w < 8) bar();
    const int j = bi - 24, ks = xcd & 3, ct = j >> 1, part = (xcd >> 2) * 2 + (j & 1);
    if (w < 9) relayout_w2(c, c.lds0 + L_WO + (unsigned)w * 9216u, ks * 36 + part * 9 + w, ct);
    publish(c, FL_W2 + (ks * 4 + ct) * 4 + part, w < 9);
  }
  ST(3, 4);
  if (w >= 8) p45_loader(c, rb4, fc4); else if (w < 4) p45_ffn<0>(c, rb4, fc4); else p45_ffn<1>(c, rb4, fc4);
  asm volatile(".Lfused_code_end:");
}

extern "C" void kernel_launch(void* const* d_in, const int* in_sizes, int n_in, void* d_out, int out_size, void* d_ws, size_t ws_size,
                              hipStream_t stream) {
  (void)hipFuncSetAttribute((const void*)fused_block, hipFuncAttributeMaxDynamicSharedMemorySize, M_LDS);
  fused_block<<<256, 768, M_LDS, stream>>>((const u16*)d_in[0], (const u16*)d_in[1], (const u16*)d_in[2], (const u16*)d_in[4], (const u16*)d_in[5],
                                           (const u16*)d_in[6], (const u16*)d_in[7], (u16*)d_out, (char*)d_ws);
}
```

```cpp
#include <hip/hip_runtime.h>
#include <stdint.h>

typedef unsigned short u16;
typedef __bf16 bf16x8 __attribute__((ext_vector_type(8)));
typedef __bf16 bf16x4 __attribute__((ext_vector_type(4)));
typedef __bf16 bf16x2 __attribute__((ext_vector_type(2)));
typedef float f32x2 __attribute__((ext_vector_type(2)));
typedef float f32x4 __attribute__((ext_vector_type(4)));
typedef float f32x16 __attribute__((ext_vector_type(16)));
typedef unsigned u32x2 __attribute__((ext_vector_type(2)));
typedef unsigned u32x4 __attribute__((ext_vector_type(4)));
#define LAS __attribute__((address_space(3)))
#define DI __device__ __forceinline__
#define SB() __builtin_amdgcn_sched_barrier(0)
#define KATTR __launch_bounds__(768, 3) __attribute__((amdgpu_waves_per_eu(3, 3)))

constexpr int S = 2048, D = 576, H = 32, DH = 18, F = 4608, NQKV = 1728;
constexpr float LN_EPS = 1e-5f;


constexpr size_t WS_FLG = 0;
constexpr int FL_N = 0, FL_D = 1, FL_Q = FL_D + 256, FL_W1 = FL_Q + 192, FL_A = FL_W1 + 64, FL_W2 = FL_A + 256, FL_H = FL_W2 + 64, FL_H1 = FL_H + 192,
              FL_P = FL_H1 + 256, FL_S1 = FL_P + 256, FL_END = FL_S1 + 64;
constexpr size_t WS_QKV = 262144;
static_assert(FL_END * 128 <= WS_QKV, "flag area");
constexpr size_t WS_QKV_SZ = (size_t)3 * H * S * 24 * 2;
constexpr size_t WS_OP = WS_QKV + WS_QKV_SZ;
constexpr size_t WS_OL = WS_OP + (size_t)64 * 32 * 1024;
constexpr size_t WS_HP = WS_OL + (size_t)H * S * 4;
constexpr size_t WS_ST2 = WS_HP + (size_t)S * D * 2;
constexpr size_t WS_W1P = WS_ST2 + (size_t)S * 48;
constexpr size_t WS_W2P = WS_W1P + (size_t)D * F * 2;
constexpr size_t WS_H1P = WS_W2P + (size_t)D * F * 2;
constexpr size_t WS_XCH = WS_H1P + (size_t)S * F * 2;
constexpr size_t WS_ST1 = WS_XCH + (size_t)16 * 4 * 4 * 4 * 2 * 4608;
constexpr size_t WS_CSQ = WS_ST1 + (size_t)S * 8;
constexpr size_t WS_END = WS_CSQ + (size_t)(NQKV + 64) * 4;
constexpr unsigned WS_BYTES = (unsigned)WS_END;


DI unsigned rfl(unsigned x) { return __builtin_amdgcn_readfirstlane(x); }
DI float bflo(unsigned w) { return __uint_as_float(w << 16); }
DI float bfhi(unsigned w) { return __uint_as_float(w & 0xffff0000u); }
DI unsigned pk2(float lo, float hi) { f32x2 v = {lo, hi}; bf16x2 b = __builtin_convertvector(v, bf16x2); return __builtin_bit_cast(unsigned, b); }
DI float dot2(unsigned a, unsigned b, float c) { return __builtin_amdgcn_fdot2_f32_bf16(__builtin_bit_cast(bf16x2, a), __builtin_bit_cast(bf16x2, b), c, false); }

typedef __amdgpu_buffer_rsrc_t srd_t;
DI srd_t mk_srd(const void* p, unsigned bytes) { return __builtin_amdgcn_make_buffer_rsrc((void*)p, (short)0, (int)bytes, 0x00020000); }
DI void dma16(unsigned ldsaddr, unsigned voff, srd_t srd, unsigned soff) {
  asm volatile("s_mov_b32 m0, %0\n\ts_nop 0\n\tbuffer_load_dwordx4 %1, %2, %3 offen lds" :: "s"(ldsaddr), "v"(voff), "s"(srd), "s"(soff) : "memory");
}
DI void touch128(unsigned ldsdump, unsigned voff, srd_t srd, unsigned soff) {
  asm volatile("s_mov_b32 m0, %0\n\ts_nop 0\n\tbuffer_load_dword %1, %2, %3 offen lds" :: "s"(ldsdump), "v"(voff), "s"(srd), "s"(soff) : "memory");
}
template <int N> DI void wait_vm() { asm volatile("s_waitcnt vmcnt(%0)" :: "i"(N) : "memory"); }
DI void wait_lgkm0() { asm volatile("s_waitcnt lgkmcnt(0)" ::: "memory"); }
DI void bar() { asm volatile("" ::: "memory"); __builtin_amdgcn_s_barrier(); asm volatile("" ::: "memory"); }
DI u32x4 lds_u128(unsigned addr) { return *(const LAS u32x4*)(size_t)addr; }
DI f32x4 lds_f128(unsigned addr) { return *(const LAS f32x4*)(size_t)addr; }
DI void lds_w128(unsigned addr, f32x4 v) { *(LAS f32x4*)(size_t)addr = v; }
DI bf16x8 asbf(u32x4 v) { return __builtin_bit_cast(bf16x8, v); }
DI u32x2 lds_tr(unsigned addr) { return __builtin_bit_cast(u32x2, __builtin_amdgcn_ds_read_tr16_b64_v4bf16((LAS bf16x4*)(size_t)addr)); }
DI u32x4 cat4(u32x2 a, u32x2 b) { return u32x4{a[0], a[1], b[0], b[1]}; }
DI f32x4 mfma16(u32x4 a, u32x4 b, f32x4 c) { return __builtin_amdgcn_mfma_f32_16x16x32_bf16(asbf(a), asbf(b), c, 0, 0, 0); }
DI f32x16 mfma32(u32x4 a, u32x4 b, f32x16 c) { return __builtin_amdgcn_mfma_f32_32x32x16_bf16(asbf(a), asbf(b), c, 0, 0, 0); }
template <int NOP> DI float max16(float m, const f32x16& t) {
  float u;
  asm volatile("s_nop %18\n\tv_max3_f32 %1, %2, %3, %4\n\tv_max3_f32 %0, %0, %5, %6\n\tv_max3_f32 %1, %1, %7, %8\n\tv_max3_f32 %0, %0, %9, %10\n\t"
               "v_max3_f32 %1, %1, %11, %12\n\tv_max3_f32 %0, %0, %13, %14\n\tv_max3_f32 %1, %1, %15, %16\n\tv_max3_f32 %0, %0, %1, %17"
               : "+v"(m), "=&v"(u)
               : "v"(t[0]), "v"(t[1]), "v"(t[2]), "v"(t[3]), "v"(t[4]), "v"(t[5]), "v"(t[6]), "v"(t[7]), "v"(t[8]), "v"(t[9]), "v"(t[10]), "v"(t[11]), "v"(t[12]),
                 "v"(t[13]), "v"(t[14]), "v"(t[15]), "i"(NOP));
  return m;
}
template <int CTRL, int RM> DI float dpp_add(float v) { return v + __builtin_bit_cast(float, __builtin_amdgcn_update_dpp(0, __builtin_bit_cast(int, v), CTRL, RM, 0xf, true)); }
DI float wave_sum63(float v) { v = dpp_add<0x111, 0xf>(v); v = dpp_add<0x112, 0xf>(v); v = dpp_add<0x114, 0xf>(v); v = dpp_add<0x118, 0xf>(v); v = dpp_add<0x142, 0xa>(v); return dpp_add<0x143, 0xc>(v); }
DI float sum_x32(float v) { const auto a = __builtin_amdgcn_permlane32_swap(__float_as_uint(v), __float_as_uint(v), false, false); return __uint_as_float(a[0]) + __uint_as_float(a[1]); }
DI float sum_x16(float v) { const auto a = __builtin_amdgcn_permlane16_swap(__float_as_uint(v), __float_as_uint(v), false, false); return __uint_as_float(a[0]) + __uint_as_float(a[1]); }
DI float sum_x8(float v) { return dpp_add<0x128, 0xf>(v); }
DI int swap23(int i) { return (i & 19) | ((i & 4) << 1) | ((i & 8) >> 1); }


constexpr int SC1 = 16;
DI void dma16s(unsigned ldsaddr, unsigned voff, srd_t srd, unsigned soff) {
  asm volatile("s_mov_b32 m0, %0\n\ts_nop 0\n\tbuffer_load_dwordx4 %1, %2, %3 offen sc1 lds" :: "s"(ldsaddr), "v"(voff), "s"(srd), "s"(soff) : "memory");
}
DI void st128s(u32x4 v, srd_t srd, unsigned voff, unsigned soff) {
  asm volatile("buffer_store_dwordx4 %0, %1, %2, %3 offen sc1\n\ts_nop 1" :: "v"(v), "v"(voff), "s"(srd), "s"(soff) : "memory");
}
DI u32x4 ld128s(srd_t srd, unsigned voff, unsigned soff) { return __builtin_amdgcn_raw_buffer_load_b128(srd, voff, soff, SC1); }

struct Ctx {
  const u16 *x, *wqkv, *wo, *w1, *b1, *w2, *b2;
  u16* out;
  char* ws;
  srd_t sws;
  unsigned lds0;
  unsigned long long nonce;
  int b, xcd, bi, w, lane;
};
DI void publish(const Ctx& c, int flag, bool stored) {
  if (stored) wait_vm<0>();
  bar();
  if (threadIdx.x == 0) __hip_atomic_store((unsigned long long*)(c.ws + (size_t)flag * 128), c.nonce, __ATOMIC_RELAXED, __HIP_MEMORY_SCOPE_AGENT);
}
DI void poll(const Ctx& c, int flag, bool active) {
  const unsigned long long* p = (const unsigned long long*)(c.ws + (size_t)flag * 128);
  const unsigned long long t0 = __builtin_amdgcn_s_memrealtime();
  for (;;) {
    const unsigned long long v = active ? __hip_atomic_load(p, __ATOMIC_RELAXED, __HIP_MEMORY_SCOPE_AGENT) : c.nonce;
    if (__all(v == c.nonce)) break;
    if (__builtin_amdgcn_s_memrealtime() - t0 > 2000000ull) break;
  }
}

constexpr int L_DUMP = 163840 - 256;
DI u32x2 entry_load(const Ctx& c) {
  u32x2 nv;
  asm volatile("buffer_load_dwordx2 %0, %1, %2, 0 offen sc1" : "=&v"(nv) : "v"((unsigned)(FL_N * 128)), "s"(c.sws) : "memory");
  return nv;
}
DI void entry_nonce(Ctx& c, u32x2 nv) {
  asm volatile("" : "+v"(nv));
  c.nonce = (((unsigned long long)rfl(nv[1]) << 32) | rfl(nv[0])) + 1ull;
  if (threadIdx.x == 0) __hip_atomic_store((unsigned long long*)(c.ws + (size_t)(FL_D + c.b) * 128), c.nonce, __ATOMIC_RELAXED, __HIP_MEMORY_SCOPE_AGENT);
}

constexpr int L_W1 = 4096;
DI void relayout_w1_fetch(const Ctx& c, int fc, int part) {
  const int l = c.w - 8, lane = c.lane;
  const srd_t sw = mk_srd(c.w1, D * F * 2);
#pragma unroll 1
  for (int pc = l; pc < 81; pc += 4) {
    const int ch = pc * 64 + lane, row = ch / 36, cc = ch % 36;
    dma16(c.lds0 + L_W1 + (unsigned)pc * 1024u, (unsigned)row * (F * 2) + (unsigned)cc * 16u, sw, (unsigned)part * 144u * (F * 2) + (unsigned)fc * 576u);
  }
}
DI void helper_stats(Ctx& c, int hb, int fc, int part) {
  const int w = c.w, lane = c.lane;
  const srd_t sx = mk_srd(c.x, S * D * 2), sw = mk_srd(c.wqkv, hb < 27 ? D * NQKV * 2 : 0);
  if (w >= 8) {
    const u32x2 nv = entry_load(c);
    wait_vm<0>();
    entry_nonce(c, nv);
    bar();
    publish(c, FL_S1 + hb, false);
    relayout_w1_fetch(c, fc, part);
    return;
  }
  const int tid = w * 64 + lane, ch = tid & 7, rg = tid >> 3;
  u32x4 a[4], b2[4], v9[9];
#pragma unroll
  for (int r = 0; r < 4; r++) {
    const unsigned so = (unsigned)(hb * 32 + w * 4 + r) * (D * 2);
    a[r] = __builtin_amdgcn_raw_buffer_load_b128(sx, (unsigned)lane * 16u, so, 0);
    b2[r] = __builtin_amdgcn_raw_buffer_load_b128(sx, lane < 8 ? 1024u + (unsigned)lane * 16u : 0x7ff00000u, so, 0);
  }
#pragma unroll
  for (int i = 0; i < 9; i++) v9[i] = __builtin_amdgcn_raw_buffer_load_b128(sw, (unsigned)(rg * 9 + i) * (NQKV * 2) + (unsigned)ch * 16u, (unsigned)hb * 128u, 0);
  const u32x2 nv = entry_load(c);
  float sm[4], sq[4];
#pragma unroll
  for (int r = 0; r < 4; r++) {
    sm[r] = 0; sq[r] = 0;
#pragma unroll
    for (int e = 0; e < 4; e++) { sm[r] = dot2(a[r][e], 0x3f803f80u, sm[r]); sq[r] = dot2(a[r][e], a[r][e], sq[r]); sm[r] = dot2(b2[r][e], 0x3f803f80u, sm[r]); sq[r] = dot2(b2[r][e], b2[r][e], sq[r]); }
  }
#pragma unroll
  for (int r = 0; r < 4; r++) {
    const float s1 = __builtin_bit_cast(float, __builtin_amdgcn_readlane(__builtin_bit_cast(int, wave_sum63(sm[r])), 63));
    const float s2 = __builtin_bit_cast(float, __builtin_amdgcn_readlane(__builtin_bit_cast(int, wave_sum63(sq[r])), 63));
    const float mean = s1 * (1.0f / D), var = fmaxf(s2 * (1.0f / D) - mean * mean, 0.f);
    sm[r] = mean; sq[r] = rsqrtf(var + LN_EPS);
  }
  {
    const int r = lane & 3;
    const u32x2 v = {__float_as_uint(r == 0 ? sm[0] : r == 1 ? sm[1] : r == 2 ? sm[2] : sm[3]), __float_as_uint(r == 0 ? sq[0] : r == 1 ? sq[1] : r == 2 ? sq[2] : sq[3])};
    __builtin_amdgcn_raw_buffer_store_b64(v, c.sws, lane < 4 ? (unsigned)lane * 8u : 0x7ff00000u, (unsigned)WS_ST1 + (unsigned)(hb * 32 + w * 4) * 8u, SC1);
  }
  float s8[8] = {0, 0, 0, 0, 0, 0, 0, 0};
#pragma unroll
  for (int i = 0; i < 9; i++)
#pragma unroll
    for (int e = 0; e < 4; e++) { s8[2 * e] += bflo(v9[i][e]); s8[2 * e + 1] += bfhi(v9[i][e]); }
#pragma unroll
  for (int e = 0; e < 8; e++) s8[e] = sum_x32(sum_x16(sum_x8(s8[e])));
  if (lane < 8) { lds_w128(c.lds0 + (unsigned)(w * 64 + lane * 8) * 4u, f32x4{s8[0], s8[1], s8[2], s8[3]}); lds_w128(c.lds0 + (unsigned)(w * 64 + lane * 8) * 4u + 16, f32x4{s8[4], s8[5], s8[6], s8[7]}); }
  wait_lgkm0(); bar();
  if (w == 0) {
    float t = 0;
#pragma unroll
    for (int k = 0; k < 8; k++) t += *(const LAS float*)(size_t)(c.lds0 + (unsigned)(k * 64 + lane) * 4u);
    __builtin_amdgcn_raw_buffer_store_b32(__float_as_uint(t), c.sws, hb < 27 ? (unsigned)lane * 4u : 0x7ff00000u, (unsigned)WS_CSQ + (unsigned)hb * 256u, SC1);
  }
  wait_lgkm0();
  wait_vm<0>();
  entry_nonce(c, nv);
  publish(c, FL_S1 + hb, true);
}

DI void relayout_w1(const Ctx& c, int fc, int part) {
  const int w = c.w, lane = c.lane; const unsigned lds0 = c.lds0 + L_W1;
  const int h = lane >> 5, blk = (lane >> 4) & 1, q = (lane >> 2) & 3, p = lane & 3;
  const unsigned ra = lds0 + (unsigned)(8 * h + q) * 576u + (unsigned)(16 * blk + 8 * (p & 1) + 4 * (p >> 1)) * 2u;
  for (int fr = w; fr < 81; fr += 12) {
    const int sl = fr / 9, tile = fr % 9, s = part * 9 + sl, kh = s / 18;
    const unsigned a = ra + (unsigned)sl * 16u * 576u + (unsigned)tile * 64u;
    const u32x4 v = cat4(lds_tr(a), lds_tr(a + 4 * 576));
    const int slot = tile == 8 ? 8 : (kh == 0 ? (tile ^ 1) : tile);
    const int s18 = s % 18;
    st128s(v, c.sws, (unsigned)lane * 16u, (unsigned)WS_W1P + ((unsigned)(fc * 9 + slot) * 36u + (unsigned)((s18 / 9) * 18 + kh * 9 + s18 % 9)) * 1024u);
  }
}
DI void relayout_w2(const Ctx& c, unsigned ldsw, int kappa, int ct) {
  const int lane = c.lane;
  const srd_t sw = mk_srd(c.w2, D * F * 2);
#pragma unroll
  for (int pc = 0; pc < 9; pc++) {
    const int ch = pc * 64 + lane, row = ch / 18, cc = ch % 18;
    dma16(ldsw + (unsigned)pc * 1024u, (unsigned)row * (D * 2) + (unsigned)cc * 16u, sw, (unsigned)kappa * 32u * (D * 2) + (unsigned)ct * 288u);
  }
  wait_vm<0>();
  const int g = lane >> 4, q = (lane >> 2) & 3, p = lane & 3;
  const unsigned ra = ldsw + (unsigned)(8 * g + q) * 288u;
  const int ks = kappa / 36, kst = (kappa % 36 + 36 - 9 * ct) % 36;
#pragma unroll
  for (int nt = 0; nt < 9; nt++) {
    const unsigned col = nt < 8 ? (unsigned)(32 * (nt >> 1) + 8 * p + 4 * (nt & 1)) * 2u : (unsigned)(128 + 4 * p) * 2u;
    const u32x4 v = cat4(lds_tr(ra + col), lds_tr(ra + col + 4 * 288));
    st128s(v, c.sws, (unsigned)lane * 16u, (unsigned)WS_W2P + ((unsigned)(ks * 4 + ct) * 324u + (unsigned)(((kst >> 2) * 2 + (kst & 1)) * 18 + ((kst >> 1) & 1) * 9 + nt)) * 1024u);
  }
  wait_lgkm0();
}

constexpr int Q_SLOT = 36864;
constexpr int Q_CT = 4 * Q_SLOT;
DI unsigned q_voff(int p, int lane) {
  if (p < 16) return (unsigned)(lane >> 3) * (D * 2) + (unsigned)(((lane & 7) ^ ((4 * (p & 1) + (lane >> 4)) & 7)) * 16);
  const int ch = 64 * (p - 16) + lane, row = ch / 20, cc = ch % 20;
  return (unsigned)row * (NQKV * 2) + (unsigned)cc * 16u;
}
DI void q_issue(const Ctx& c, srd_t sx, srd_t sw, int rb, int ct, int p, unsigned voff, int t, unsigned ring) {
  if (p < 16) dma16(c.lds0 + ring + (unsigned)p * 1024u, voff, sx, ((unsigned)rb * 128u + (unsigned)p * 8u) * (D * 2) + (unsigned)t * 128u);
  else dma16(c.lds0 + ring + (unsigned)p * 1024u, voff, sw, (unsigned)ct * 288u + (unsigned)t * 64u * (NQKV * 2));
}
DI void p1_loader(Ctx& c, int rb, int ct) {
  const int w = c.w, lane = c.lane, l = w - 8;
  const srd_t sx = mk_srd(c.x, S * D * 2), sw = mk_srd(c.wqkv, D * NQKV * 2);
  const u32x2 nv = entry_load(c);
#pragma unroll
  for (int t = 0; t < 2; t++)
#pragma unroll
    for (int i = 0; i < 3; i++) q_issue(c, sx, sw, rb, ct, w + 12 * i, q_voff(w + 12 * i, lane), t, (unsigned)t * Q_SLOT);
  unsigned vw[5];
#pragma unroll
  for (int i = 0; i < 5; i++) vw[i] = q_voff(16 + l + 4 * i, lane);
#define Q_W_ISSUE(t, ring) do { \
    _Pragma("unroll") for (int i = 0; i < 5; i++) dma16(c.lds0 + (ring) + (unsigned)(16 + l + 4 * i) * 1024u, vw[i], sw, (unsigned)ct * 288u + (unsigned)(t) * 64u * (NQKV * 2)); } while (0)
  Q_W_ISSUE(2, 2u * Q_SLOT);
  wait_vm<5>();
  entry_nonce(c, nv);
  bar();
  Q_W_ISSUE(3, 3u * Q_SLOT);
#pragma unroll 1
  for (int t = 1; t < 6; t++) {
    wait_vm<5>(); bar();
    Q_W_ISSUE(t + 3, (unsigned)((t + 3) & 3) * Q_SLOT);
  }
  wait_vm<5>(); bar();
  wait_vm<0>(); bar();
  if (l == 0) { const int g0 = (144 * ct) >> 6; poll(c, FL_S1 + (lane < 4 ? 4 * rb + lane : min(g0 + (lane & 3), 26)), lane < 8); }
  bar();
  bar();
  bar();
}
template <int NT, int T0>
DI void p1_body(Ctx& c, int rb, int ct) {
  const int w = c.w, lane = c.lane, rg = w & 3; const unsigned lds0 = c.lds0;
  constexpr bool XI = NT == 2;
  const srd_t sx = mk_srd(c.x, S * D * 2);
  const unsigned vx = q_voff(rg, lane);
#define Q_X_ISSUE1(t, ring, i) dma16(lds0 + (ring) + (unsigned)(rg + 4 * (i)) * 1024u, vx, sx, ((unsigned)rb * 128u + (unsigned)(rg + 4 * (i)) * 8u) * (D * 2) + (unsigned)(t) * 128u)
  {
    const srd_t sw = mk_srd(c.wqkv, D * NQKV * 2);
    const u32x2 nv = entry_load(c);
#pragma unroll
    for (int t = 0; t < 2; t++)
#pragma unroll
      for (int i = 0; i < 3; i++) q_issue(c, sx, sw, rb, ct, w + 12 * i, q_voff(w + 12 * i, lane), t, (unsigned)t * Q_SLOT);
    if (XI) { Q_X_ISSUE1(2, 2u * Q_SLOT, 0); Q_X_ISSUE1(2, 2u * Q_SLOT, 1); Q_X_ISSUE1(2, 2u * Q_SLOT, 2); Q_X_ISSUE1(2, 2u * Q_SLOT, 3); wait_vm<4>(); } else wait_vm<0>();
    entry_nonce(c, nv);
  }
  const int tok = lane & 31, h = lane >> 5, blk = (lane >> 4) & 1, q4 = (lane >> 2) & 3, pp = lane & 3;
  const unsigned xrow = lds0 + (unsigned)(rg * 32 + tok) * 128u;
  unsigned xj[4];
#pragma unroll
  for (int j = 0; j < 4; j++) xj[j] = xrow + (unsigned)(((2 * j + h) ^ ((tok >> 1) & 7)) * 16);
  const unsigned wj = lds0 + 16384u + (unsigned)(8 * h + q4) * 320u + (unsigned)(32 * T0 + 16 * blk + 8 * (pp & 1) + 4 * (pp >> 1)) * 2u;
  f32x16 acc[NT];
#pragma unroll
  for (int n = 0; n < NT; n++) acc[n] = f32x16{};
  bar();
  u32x4 wf[4][NT], xf[4];
#pragma unroll
  for (int j = 0; j < 2; j++) {
    xf[j] = lds_u128(xj[j]);
#pragma unroll
    for (int n = 0; n < NT; n++) wf[j][n] = cat4(lds_tr(wj + j * 5120 + n * 64), lds_tr(wj + j * 5120 + n * 64 + 4 * 320));
  }
  unsigned r0 = 0, r1 = Q_SLOT, r3 = 3 * Q_SLOT;
#define Q_PERIOD(t, SYNC, ISSUE, EXTRA) do { \
    if (XI && (SYNC) == 2) wait_vm<4>(); \
    if (XI && (SYNC) == 3) wait_vm<0>(); \
    if (SYNC) bar(); \
    EXTRA; \
    unsigned wa_ = wj + r0, wn_ = wj + r1, xa_[4]; \
    _Pragma("unroll") for (int j = 0; j < 4; j++) { xa_[j] = xj[(j + 2) & 3] + (j < 2 ? r0 : r1); asm volatile("" : "+v"(xa_[j])); } \
    asm volatile("" : "+v"(wa_), "+v"(wn_)); \
    _Pragma("unroll") for (int j = 0; j < 4; j++) { \
      const u32x4 xc = xf[j]; \
      xf[(j + 2) & 3] = lds_u128(xa_[j]); \
      _Pragma("unroll") for (int n = 0; n < NT; n++) { \
        const u32x4 cur = wf[j][n]; \
        const unsigned a_ = (j < 2 ? wa_ : wn_) + ((j + 2) & 3) * 5120 + n * 64; \
        wf[(j + 2) & 3][n] = cat4(lds_tr(a_), lds_tr(a_ + 4 * 320)); \
        acc[n] = mfma32(cur, xc, acc[n]); \
        if (XI && (ISSUE) && n == 0) Q_X_ISSUE1((t) + 3, r3, j); \
        SB(); \
      } \
    } \
    r0 = r1; r1 = r1 == 3u * Q_SLOT ? 0u : r1 + Q_SLOT; r3 = r3 == 3u * Q_SLOT ? 0u : r3 + Q_SLOT; \
  } while (0)
  Q_PERIOD(0, 0, true, (void)0);
#pragma unroll 1
  for (int t = 1; t < 6; t++) Q_PERIOD(t, 2, true, (void)0);
  const int tl = rg * 32 + tok, type = ct >> 2;
  u32x2 stv; u32x4 csv;
  Q_PERIOD(6, 2, false, (void)0);
  Q_PERIOD(7, 3, false, (void)0);
  Q_PERIOD(8, 1, false, (
    { asm volatile("buffer_load_dwordx2 %0, %1, %2, %3 offen sc1" : "=&v"(stv) : "v"((unsigned)tl * 8u), "s"(c.sws), "s"((unsigned)WS_ST1 + (unsigned)rb * 1024u) : "memory");
      asm volatile("buffer_load_dwordx4 %0, %1, %2, %3 offen sc1" : "=&v"(csv) : "v"((unsigned)min(lane, 39) * 16u), "s"(c.sws), "s"((unsigned)WS_CSQ + (unsigned)ct * 576u) : "memory"); }));
  asm volatile("s_waitcnt vmcnt(0)" : "+v"(stv), "+v"(csv) :: "memory");
  if (w == 0 && lane < 40) *(LAS u32x4*)(size_t)(lds0 + Q_CT + (unsigned)lane * 16u) = csv;
  wait_lgkm0(); bar();
  f32x2 st;
  st[0] = __uint_as_float(stv[1]) * (type == 0 ? 0.23570226039551584f * 1.4426950408889634f : 1.0f);
  st[1] = -__uint_as_float(stv[0]) * st[0];
#pragma unroll
  for (int n = 0; n < NT; n++)
#pragma unroll
    for (int hf = 0; hf < 2; hf++) {
      if (T0 + n == 4 && hf == 1) continue;
      const int c0 = 32 * (T0 + n) + 16 * hf;
      const f32x4 ca = lds_f128(lds0 + Q_CT + (unsigned)(c0 + 8 * h) * 4u), cb = lds_f128(lds0 + Q_CT + (unsigned)(c0 + 8 * h) * 4u + 16);
      u32x4 o;
#pragma unroll
      for (int e = 0; e < 4; e++) {
        const float c0v = e < 2 ? ca[2 * e] : cb[2 * e - 4], c1v = e < 2 ? ca[2 * e + 1] : cb[2 * e - 3];
        o[e] = pk2(__builtin_fmaf(acc[n][8 * hf + 2 * e], st[0], c0v * st[1]), __builtin_fmaf(acc[n][8 * hf + 2 * e + 1], st[0], c1v * st[1]));
      }
      *(LAS u32x4*)(size_t)(lds0 + (unsigned)tl * 304u + (unsigned)(c0 + 8 * h) * 2u) = o;
    }
  wait_lgkm0(); bar();
}
DI void p1_qkv(Ctx& c, int rb, int ct) {
  if (c.w >= 8) p1_loader(c, rb, ct); else if (c.w < 4) p1_body<3, 0>(c, rb, ct); else p1_body<2, 3>(c, rb, ct);
  {
    const int tid = c.w * 64 + c.lane, tg = tid >= 384, t = tid - 384 * tg, tk = t / 3, part = t - 3 * tk, type = ct >> 2;
    const unsigned src = c.lds0 + (unsigned)tk * 304u + (unsigned)part * 16u + (unsigned)tg * 144u;
    const unsigned keep = part < 2 ? 0xffffffffu : 0u, padv = part < 2 ? 0u : (type == 0 ? 0x3f800000u : 0x00003f80u);
    const unsigned dst = (unsigned)WS_QKV + ((unsigned)(type * 32 + (ct & 3) * 8) * S + (unsigned)rb * 128u) * 48u;
    u32x4 v[4];
#pragma unroll
    for (int hh = 0; hh < 4; hh++)
#pragma unroll
      for (int e = 0; e < 4; e++) v[hh][e] = *(const LAS unsigned*)(size_t)(src + hh * 36 + e * 4);
#pragma unroll
    for (int hh = 0; hh < 4; hh++) {
      v[hh][1] = (v[hh][1] & keep) | padv; v[hh][2] &= keep; v[hh][3] &= keep;
      st128s(v[hh], c.sws, (unsigned)t * 16u + (unsigned)(4 * tg + hh) * (S * 48), dst);
    }
  }
  wait_lgkm0();
  publish(c, FL_Q + rb * 12 + ct, true);
}

constexpr int L_K = 0, L_V = 24576, L_WO = 49216;
DI void p2_attn(const Ctx& c, int head, int qb) {
  const int w = c.w, lane = c.lane; const unsigned lds0 = c.lds0;
  const unsigned ldsK = lds0 + L_K, ldsV = lds0 + L_V, vl = lane * 16;
  const unsigned kbase = (unsigned)WS_QKV + (unsigned)((32 + head) * S + 256 * (qb - 1)) * 48u, vbase = (unsigned)WS_QKV + (unsigned)((64 + head) * S + 256 * (qb - 1)) * 48u;
  if (w >= 8) {
    bar();
#pragma unroll
    for (int j = 0; j < 6; j++) {
      const unsigned pc = (unsigned)((w - 8) * 6 + j) * 1024u;
      if (qb > 0 || pc >= 12288u) dma16s(ldsV + pc, vl, c.sws, vbase + pc);
    }
    bar();
    wait_vm<0>(); bar();
    publish(c, FL_A + qb * 32 + head, false);
    return;
  }
  const int qq = lane & 31, h = lane >> 5;
  const int q0 = 256 * qb + 32 * w;
  f32x16 cold, cdiag;
#pragma unroll
  for (int r = 0; r < 16; r++) {
    const int kk = 16 * (r >> 3) + 8 * h + (r & 7);
    cold[r] = kk > qq ? 0.f : -1e30f;
    cdiag[r] = kk <= qq ? 0.f : -1e30f;
  }
  const int key = swap23(lane & 31);
  unsigned ka0 = ldsK + (unsigned)(key + 32 * w) * 48 + 16 * h;
  unsigned ka1 = ldsK + (unsigned)(key + 32 * w) * 48 + 32;
  const int blk = (lane >> 4) & 1, q4 = (lane >> 2) & 3, pp = lane & 3;
  unsigned va = ldsV + (unsigned)(32 * w + 8 * h + q4) * 48 + 32 * blk + 8 * pp;
  asm volatile("" : "+v"(ka0), "+v"(ka1), "+v"(va), "+v"(cold), "+v"(cdiag));
  if (qb == 0) {
    const int tid = w * 64 + lane;
    if (tid < 256) {
      const u32x4 z = {0, 0, 0, 0};
      *(LAS u32x4*)(size_t)(ldsK + tid * 48) = z; *(LAS u32x4*)(size_t)(ldsK + tid * 48 + 16) = z; *(LAS u32x4*)(size_t)(ldsK + tid * 48 + 32) = u32x4{0, 0xf1493f80u, 0, 0};
      *(LAS u32x4*)(size_t)(ldsV + tid * 48) = z; *(LAS u32x4*)(size_t)(ldsV + tid * 48 + 16) = z; *(LAS u32x4*)(size_t)(ldsV + tid * 48 + 32) = z;
    }
  }
  if (w == 0) {
    const int hg = head >> 3, l = lane;
    const int ty = l < 2 ? 0 : (l < 6 ? 1 : 2), rbl = l < 2 ? 2 * qb + l : 2 * qb - 2 + ((l - 2) & 3);
    poll(c, FL_Q + rbl * 12 + ty * 4 + hg, l < 10 && rbl >= 0);
  }
  bar();
#pragma unroll
  for (int j = 0; j < 3; j++) {
    const unsigned pc = (unsigned)(w * 3 + j) * 1024u;
    if (qb > 0 || pc >= 12288u) dma16s(ldsK + pc, vl, c.sws, kbase + pc);
  }
  const unsigned qo = (unsigned)WS_QKV + (unsigned)(head * S + q0) * 48u;
  const u32x4 qf0 = ld128s(c.sws, (unsigned)qq * 48u + 16u * h, qo);
  u32x4 qf1 = ld128s(c.sws, (unsigned)qq * 48u + 32u, qo);
  if (h) qf1 = u32x4{0, 0, 0, 0};
  wait_vm<0>(); wait_lgkm0(); bar();
  const f32x16 zero = {};
  u32x4 k0[9], k1[9];
#pragma unroll
  for (int j = 0; j < 9; j++) { k0[j] = lds_u128(ka0 + j * 1536); k1[j] = lds_u128(ka1 + j * 1536); }
  float m = -1e30f;
  {
    f32x16 sA = mfma32(k1[0], qf1, mfma32(k0[0], qf0, cold)), sB;
#pragma unroll
    for (int j = 0; j < 9; j++) {
      f32x16& cur = (j & 1) ? sB : sA; f32x16& nxt = (j & 1) ? sA : sB;
      if (j + 1 < 9) nxt = mfma32(k1[j + 1], qf1, mfma32(k0[j + 1], qf0, j + 1 == 8 ? cdiag : zero));
      SB();
      m = j == 0 ? max16<11>(m, cur) : (j < 8 ? max16<1>(m, cur) : max16<3>(m, cur));
      SB();
    }
  }
  { const auto pr = __builtin_amdgcn_permlane32_swap(__float_as_uint(m), __float_as_uint(m), false, false); m = fmaxf(__uint_as_float(pr[0]), __uint_as_float(pr[1])); }
  if (h == 0) qf1[1] = (qf1[1] & 0xffff0000u) | (pk2(-m, 0.f) & 0xffffu);
  bar();
  f32x16 oacc = {};
  {
    f32x16 sA = mfma32(k1[0], qf1, mfma32(k0[0], qf0, cold)), sB;
    u32x4 v0 = cat4(lds_tr(va), lds_tr(va + 4 * 48)), v1 = cat4(lds_tr(va + 16 * 48), lds_tr(va + 20 * 48));
#pragma unroll
    for (int j = 0; j < 9; j++) {
      f32x16& cur = (j & 1) ? sB : sA; f32x16& nxt = (j & 1) ? sA : sB;
      if (j + 1 < 9) nxt = mfma32(k0[j + 1], qf0, j + 1 == 8 ? cdiag : zero);
      SB();
      u32x4 p0, p1;
      {
        float e0 = cur[0]; asm volatile("" : "+v"(e0));
        p0[0] = pk2(__builtin_amdgcn_exp2f(e0), __builtin_amdgcn_exp2f(cur[1]));
#pragma unroll
        for (int e = 1; e < 4; e++) p0[e] = pk2(__builtin_amdgcn_exp2f(cur[2 * e]), __builtin_amdgcn_exp2f(cur[2 * e + 1]));
        asm volatile("" : "+v"(p0));
      }
      SB();
      if (j + 1 < 9) nxt = mfma32(k1[j + 1], qf1, nxt);
      oacc = mfma32(v0, p0, oacc);
      if (j + 1 < 9) v0 = cat4(lds_tr(va + (j + 1) * 1536), lds_tr(va + (j + 1) * 1536 + 4 * 48));
      SB();
      {
        float e8 = cur[8]; asm volatile("" : "+v"(e8));
        p1[0] = pk2(__builtin_amdgcn_exp2f(e8), __builtin_amdgcn_exp2f(cur[9]));
#pragma unroll
        for (int e = 1; e < 4; e++) p1[e] = pk2(__builtin_amdgcn_exp2f(cur[8 + 2 * e]), __builtin_amdgcn_exp2f(cur[8 + 2 * e + 1]));
        asm volatile("" : "+v"(p1));
      }
      SB();
      oacc = mfma32(v1, p1, oacc);
      if (j + 1 < 9) v1 = cat4(lds_tr(va + (j + 1) * 1536 + 16 * 48), lds_tr(va + (j + 1) * 1536 + 20 * 48));
      SB();
    }
  }
  const float l = __uint_as_float(__builtin_amdgcn_permlane32_swap(__float_as_uint(oacc[10]), __float_as_uint(oacc[10]), false, false)[0]);
  const float inv = __builtin_amdgcn_rcpf(l);
  const u32x4 om = {pk2(oacc[0] * inv, oacc[1] * inv), pk2(oacc[2] * inv, oacc[3] * inv), pk2(oacc[4] * inv, oacc[5] * inv), pk2(oacc[6] * inv, oacc[7] * inv)};
  st128s(om, c.sws, (unsigned)lane * 16u, (unsigned)WS_OP + (unsigned)((q0 >> 5) * 32 + head) * 1024u);
  __builtin_amdgcn_raw_buffer_store_b32(pk2(oacc[8] * inv, oacc[9] * inv), c.sws, h == 0 ? (unsigned)qq * 4u : 0x7ff00000u, (unsigned)WS_OL + (unsigned)(head * S + q0) * 4u, SC1);
  publish(c, FL_A + qb * 32 + head, true);
}

DI void p3_prefetch(const Ctx& c, int ct) {
  const int l = c.w - 8, lane = c.lane;
  const srd_t sw = mk_srd(c.wo, D * D * 2);
  unsigned vw[3];
#pragma unroll
  for (int i = 0; i < 3; i++) { const int ch = 64 * i + lane; vw[i] = (unsigned)(ch / 12) * (D * 2) + (unsigned)(ch % 12) * 16u; }
#pragma unroll 1
  for (int gi = 0; gi < 9; gi++) {
    const int grp = l * 9 + gi;
#pragma unroll
    for (int i = 0; i < 3; i++) dma16(c.lds0 + L_WO + (unsigned)(grp * 3 + i) * 1024u, vw[i], sw, (unsigned)grp * 16u * (D * 2) + (unsigned)ct * 192u);
  }
}
template <int KQ>
DI void p3_body(const Ctx& c, int rb, int ct) {
  constexpr int kq = KQ;
  const int lane = c.lane, rg = c.w & 1; const unsigned lds0 = c.lds0;
  const int qt = rb * 2 + rg, tok = lane & 31, h = lane >> 5;
  const int row = qt * 32 + tok;
  const u32x4 xr0 = *(const u32x4*)(c.x + (size_t)row * D + ct * 96 + 16 * kq + 8 * h);
  const u32x4 xr1 = *(const u32x4*)(c.x + (size_t)row * D + ct * 96 + 16 * (kq < 2 ? kq + 4 : kq) + 8 * h);
  const int blk = (lane >> 4) & 1, q4 = (lane >> 2) & 3, pp = lane & 3;
  const unsigned colb = lds0 + L_WO + (unsigned)(16 * blk + 8 * (pp & 1) + 4 * (pp >> 1)) * 2u;
  unsigned ah = colb + (unsigned)(18 * 9 * kq + 4 * h + q4) * 192u;
  unsigned al = colb + (unsigned)(18 * (4 * h + (q4 >> 1)) + 16 + (q4 & 1)) * 192u;
  asm volatile("" : "+v"(ah), "+v"(al));
#define P3_W(s, n) ((kq < 3 || (s) < 5) ? cat4(lds_tr(ah + (s) * 3456 + (n) * 64), lds_tr(ah + (s) * 3456 + 1536 + (n) * 64)) \
                                        : cat4(lds_tr(al + ((s) - 5) * 27648 + (n) * 64), lds_tr(al + ((s) - 5) * 27648 + 6912 + (n) * 64)))
  bar();
  u32x4 of[9];
  {
    const unsigned so = (unsigned)WS_OP + (unsigned)(qt * 32 + kq * 9) * 1024u;
#pragma unroll
    for (int s = 0; s < (kq < 3 ? 9 : 5); s++) of[s] = ld128s(c.sws, (unsigned)lane * 16u + s * 1024u, so);
    if (kq == 3) {
#pragma unroll
      for (int G = 0; G < 4; G++)
#pragma unroll
        for (int i = 0; i < 4; i++)
          of[5 + G][i] = __builtin_amdgcn_raw_buffer_load_b32(c.sws, (unsigned)((4 * h + i) * S + tok) * 4u, (unsigned)WS_OL + (unsigned)(8 * G * S + qt * 32) * 4u, SC1);
    }
  }
  u32x4 wf[3][3];
#pragma unroll
  for (int s = 0; s < 2; s++)
#pragma unroll
    for (int n = 0; n < 3; n++) wf[s][n] = P3_W(s, n);
  SB();
  f32x16 acc[3] = {};
#pragma unroll
  for (int s = 0; s < 9; s++) {
#pragma unroll
    for (int n = 0; n < 3; n++) {
      if (s + 2 < 9) wf[(s + 2) % 3][n] = P3_W(s + 2, n);
      acc[n] = mfma32(wf[s % 3][n], of[s], acc[n]);
    }
    SB();
  }
  wait_lgkm0(); bar();
  const unsigned xb = lds0 + L_WO + (unsigned)rg * 49152u + (unsigned)lane * 16u;
#pragma unroll
  for (int f = 0; f < 6; f++) {
    if ((f & 3) == kq) continue;
    const int r8 = 8 * (f & 1);
    lds_w128(xb + (unsigned)(f * 4 + kq) * 2048u, f32x4{acc[f >> 1][r8], acc[f >> 1][r8 + 1], acc[f >> 1][r8 + 2], acc[f >> 1][r8 + 3]});
    lds_w128(xb + (unsigned)(f * 4 + kq) * 2048u + 1024u, f32x4{acc[f >> 1][r8 + 4], acc[f >> 1][r8 + 5], acc[f >> 1][r8 + 6], acc[f >> 1][r8 + 7]});
  }
  wait_lgkm0(); bar();
  float ssum = 0, ssq = 0;
#pragma unroll
  for (int i = 0; i < (kq < 2 ? 2 : 1); i++) {
    const int f = kq + 4 * i, r8 = 8 * (f & 1);
    f32x4 a = {acc[f >> 1][r8], acc[f >> 1][r8 + 1], acc[f >> 1][r8 + 2], acc[f >> 1][r8 + 3]}, cc = {acc[f >> 1][r8 + 4], acc[f >> 1][r8 + 5], acc[f >> 1][r8 + 6], acc[f >> 1][r8 + 7]};
#pragma unroll
    for (int k = 0; k < 4; k++)
      if (k != kq) { a += lds_f128(xb + (unsigned)(f * 4 + k) * 2048u); cc += lds_f128(xb + (unsigned)(f * 4 + k) * 2048u + 1024u); }
    const u32x4 xr = i ? xr1 : xr0;
    u32x4 o;
    o[0] = pk2(bflo(xr[0]) + a[0], bfhi(xr[0]) + a[1]); o[1] = pk2(bflo(xr[1]) + a[2], bfhi(xr[1]) + a[3]);
    o[2] = pk2(bflo(xr[2]) + cc[0], bfhi(xr[2]) + cc[1]); o[3] = pk2(bflo(xr[3]) + cc[2], bfhi(xr[3]) + cc[3]);
    st128s(o, c.sws, (unsigned)lane * 16u, (unsigned)WS_HP + (unsigned)(qt * 36 + ct * 6 + f) * 1024u);
#pragma unroll
    for (int e = 0; e < 4; e++) { ssum = dot2(o[e], 0x3f803f80u, ssum); ssq = dot2(o[e], o[e], ssq); }
  }
  { const auto a = __builtin_amdgcn_permlane32_swap(__float_as_uint(ssum), __float_as_uint(ssum), false, false); ssum = __uint_as_float(a[0]) + __uint_as_float(a[1]);
    const auto b2 = __builtin_amdgcn_permlane32_swap(__float_as_uint(ssq), __float_as_uint(ssq), false, false); ssq = __uint_as_float(b2[0]) + __uint_as_float(b2[1]); }
  const unsigned stb = lds0 + L_WO + 98304u;
  if (h == 0) *(LAS f32x2*)(size_t)(stb + (unsigned)(rg * 32 + tok) * 32u + kq * 8u) = f32x2{ssum, ssq};
  wait_lgkm0(); bar();
  if (c.w == 0) {
    const f32x4 a = lds_f128(stb + lane * 32u), cc = lds_f128(stb + lane * 32u + 16);
    const u32x2 v = {__float_as_uint((a[0] + a[2]) + (cc[0] + cc[2])), __float_as_uint((a[1] + a[3]) + (cc[1] + cc[3]))};
    __builtin_amdgcn_raw_buffer_store_b64(v, c.sws, (unsigned)lane * 8u, (unsigned)WS_ST2 + (unsigned)(rb * 6 + ct) * 512u, SC1);
  }
}
DI void p3_wo(const Ctx& c, int rb, int ct) {
  const int kq = c.w >> 1;
  if (kq == 0) p3_body<0>(c, rb, ct); else if (kq == 1) p3_body<1>(c, rb, ct); else if (kq == 2) p3_body<2>(c, rb, ct); else p3_body<3>(c, rb, ct);
  publish(c, FL_H + rb * 6 + ct, true);
}

constexpr float GC0 = -2.0f * 0.7978845608028654f * 1.4426950408889634f, GC1 = GC0 * 0.044715f;
DI f32x2 gelu2(f32x2 x) {
  const f32x2 t = x * (GC1 * (x * x) + GC0);
  f32x2 e; e[0] = __builtin_amdgcn_exp2f(t[0]); e[1] = __builtin_amdgcn_exp2f(t[1]);
  const f32x2 d = e + 1.0f;
  f32x2 r; r[0] = __builtin_amdgcn_rcpf(d[0]); r[1] = __builtin_amdgcn_rcpf(d[1]);
  return x * r;
}
struct G2 { float x0, x1, t0, t1; };
DI void g2_s1(G2& g, float a0, float a1, float p0, float p1) {
  asm volatile("v_add_f32_e32 %0, %4, %6\n\tv_add_f32_e32 %1, %5, %7\n\tv_mul_f32_e32 %2, %0, %0\n\tv_mul_f32_e32 %3, %1, %1"
               : "=&v"(g.x0), "=&v"(g.x1), "=&v"(g.t0), "=&v"(g.t1) : "v"(a0), "v"(a1), "v"(p0), "v"(p1));
}
DI void g2_s2(G2& g, float vc1) {
  asm volatile("v_fmaak_f32 %0, %0, %4, 0xc0135761\n\tv_fmaak_f32 %1, %1, %4, 0xc0135761\n\tv_mul_f32_e32 %0, %2, %0\n\tv_mul_f32_e32 %1, %3, %1"
               : "+v"(g.t0), "+v"(g.t1) : "v"(g.x0), "v"(g.x1), "v"(vc1));
}
DI void g2_s3(G2& g) {
  asm volatile("v_exp_f32_e32 %0, %0\n\tv_exp_f32_e32 %1, %1\n\tv_add_f32_e32 %0, 1.0, %0\n\tv_add_f32_e32 %1, 1.0, %1" : "+v"(g.t0), "+v"(g.t1));
}
DI unsigned g2_s4(G2& g) {
  unsigned o;
  asm volatile("v_rcp_f32_e32 %1, %1\n\tv_rcp_f32_e32 %2, %2\n\tv_mul_f32_e32 %1, %3, %1\n\tv_mul_f32_e32 %2, %4, %2\n\tv_cvt_pk_bf16_f32 %0, %1, %2"
               : "=v"(o), "+v"(g.t0), "+v"(g.t1) : "v"(g.x0), "v"(g.x1));
  return o;
}

constexpr int F_SLOT = 36864, H_SLOT = 18432, F1_XCH = 6 * H_SLOT, F1_UV = F1_XCH + 32768, F_LDS = F1_UV + 1152;
DI unsigned f_stream(int fc, int g) {
  return g < 18 ? (unsigned)WS_W1P + (unsigned)(fc * 18 + g) * H_SLOT : (unsigned)WS_W2P + (unsigned)(fc * 18 + g - 18) * H_SLOT;
}
DI void f_issue(const Ctx& c, int fc, int g) {
  const int l = c.w - 8;
  const unsigned o = (unsigned)(l < 2 ? 5 * l : 2 + 4 * l) * 1024u, vl = c.lane * 16, gb = f_stream(fc, g) + o, lb = c.lds0 + (unsigned)(g % 6) * H_SLOT + o;
#pragma unroll
  for (int i = 0; i < 4; i++) dma16s(lb + i * 1024u, vl, c.sws, gb + i * 1024u);
  if (l < 2) dma16s(lb + 4096u, vl, c.sws, gb + 4096u);
}
template <int N> DI void f_wait(int l) { if (l < 2) wait_vm<5 * N>(); else wait_vm<4 * N>(); }
constexpr int F_FIN = 4 * H_SLOT;
DI void p6_finish(const Ctx& c, int rb, int fc) {
  const int w = c.w, lane = c.lane, ks = fc >> 2, ct = fc & 3, tk = lane & 15, g = lane >> 4;
  const bool pair = w < 8;
  const int T = w & 3, rt = pair ? w >> 2 : w - 8;
  const int col = ct * 144 + (pair ? 32 * T + 8 * g : 128 + 4 * g), row = rb * 128 + ks * 32 + rt * 16 + tk;
  u32x4 hv = {}, bv = {};
  const unsigned ho = (unsigned)(((col >> 3) & 1) * 32 + rt * 16 + tk) * 16u + (unsigned)(col >> 4) * 1024u + (unsigned)(col & 7) * 2u, hb = (unsigned)WS_HP + (unsigned)(rb * 4 + ks) * 36u * 1024u;
  if (w < 10) {
    if (pair) { hv = ld128s(c.sws, ho, hb); bv = *(const u32x4*)(c.b2 + col); }
    else { const u32x2 a = __builtin_amdgcn_raw_buffer_load_b64(c.sws, ho, hb, SC1), b2 = *(const u32x2*)(c.b2 + col); hv[0] = a[0]; hv[1] = a[1]; bv[0] = b2[0]; bv[1] = b2[1]; }
  }
  if (w == 10) poll(c, FL_P + (rb * 4 + ct) * 4 + (lane & 3), lane < 4);
  bar();
  if (w >= 10) return;
  const unsigned xg = (unsigned)WS_XCH + (unsigned)((rb * 4 + ct) * 16 + ks * 4) * 9216u + (unsigned)rt * 4608u;
  const unsigned fin = c.lds0 + F_FIN + (unsigned)rt * 9216u + (unsigned)lane * 16u;
  if (pair) {
    u32x4 pv[3];
#pragma unroll
    for (int i = 0; i < 3; i++) pv[i] = ld128s(c.sws, (unsigned)T * 1024u + (unsigned)lane * 16u, xg + (unsigned)(i + (i >= ks)) * 9216u);
    f32x4 a = lds_f128(fin + (unsigned)T * 2048u), b2 = lds_f128(fin + (unsigned)T * 2048u + 1024u);
#pragma unroll
    for (int i = 0; i < 3; i++) { a += f32x4{bflo(pv[i][0]), bfhi(pv[i][0]), bflo(pv[i][1]), bfhi(pv[i][1])}; b2 += f32x4{bflo(pv[i][2]), bfhi(pv[i][2]), bflo(pv[i][3]), bfhi(pv[i][3])}; }
    u32x4 r;
    r[0] = pk2(bflo(hv[0]) + (a[0] + bflo(bv[0])), bfhi(hv[0]) + (a[1] + bfhi(bv[0])));
    r[1] = pk2(bflo(hv[1]) + (a[2] + bflo(bv[1])), bfhi(hv[1]) + (a[3] + bfhi(bv[1])));
    r[2] = pk2(bflo(hv[2]) + (b2[0] + bflo(bv[2])), bfhi(hv[2]) + (b2[1] + bfhi(bv[2])));
    r[3] = pk2(bflo(hv[3]) + (b2[2] + bflo(bv[3])), bfhi(hv[3]) + (b2[3] + bfhi(bv[3])));
    *(u32x4*)(c.out + (size_t)row * D + col) = r;
  } else {
    u32x2 pv[3];
#pragma unroll
    for (int i = 0; i < 3; i++) pv[i] = __builtin_amdgcn_raw_buffer_load_b64(c.sws, 4096u + (unsigned)lane * 8u, xg + (unsigned)(i + (i >= ks)) * 9216u, SC1);
    f32x4 a = lds_f128(fin + 8192u);
#pragma unroll
    for (int i = 0; i < 3; i++) a += f32x4{bflo(pv[i][0]), bfhi(pv[i][0]), bflo(pv[i][1]), bfhi(pv[i][1])};
    u32x2 r;
    r[0] = pk2(bflo(hv[0]) + (a[0] + bflo(bv[0])), bfhi(hv[0]) + (a[1] + bfhi(bv[0])));
    r[1] = pk2(bflo(hv[1]) + (a[2] + bflo(bv[1])), bfhi(hv[1]) + (a[3] + bfhi(bv[1])));
    *(u32x2*)(c.out + (size_t)row * D + col) = r;
  }
}
DI void p45_loader(const Ctx& c, int rb, int fc) {
  const int l = c.w - 8, lane = c.lane, ks = fc >> 2, ct = fc & 3;
  f_issue(c, fc, 2); f_issue(c, fc, 3); f_issue(c, fc, 4);
  if (l == 0) poll(c, FL_H + (2 * rb + lane / 6) * 6 + lane % 6, lane < 12);
  bar();
  u32x2 fd[4] = {};
#pragma unroll 1
  for (int g = 0; g < 36; g++) {
    if (g == 18) { bar(); bar(); bar(); }
    if (g == 8 && l == 0) poll(c, FL_W2 + (ks * 4 + ct) * 4 + (lane & 3), lane < 4);
    if (g == 20 && l == 0) poll(c, FL_H1 + rb * 16 + ks * 4 + (lane & 3), lane < 4);
    if (g < 32) f_wait<3>(l); else if (g == 32) f_wait<2>(l); else if (g == 33) f_wait<1>(l); else wait_vm<0>();
    bar();
    if (g == 8 || g == 10) {
      const unsigned ln = (unsigned)((l * 2 + ((g >> 1) & 1)) * 64 + lane);
      touch128(c.lds0 + L_DUMP, ln < 324u ? ln * 128u : 0x7ff00000u, c.sws, f_stream(fc, 18) + (unsigned)(c.bi & 7) * 41472u);
    }
    if (g == 6 && l == 3 && c.b == 0) {
#pragma unroll
      for (int i = 0; i < 4; i++) asm volatile("buffer_load_dwordx2 %0, %1, %2, 0 offen sc1" : "=&v"(fd[i]) : "v"((unsigned)(FL_D + i * 64 + lane) * 128u), "s"(c.sws) : "memory");
    }
    if (g == 12 && l == 3 && c.b == 0) {
      asm volatile("" : "+v"(fd[0]), "+v"(fd[1]), "+v"(fd[2]), "+v"(fd[3]));
      const unsigned n0 = (unsigned)c.nonce, n1 = (unsigned)(c.nonce >> 32);
      if (!__all(fd[0][0] == n0 && fd[0][1] == n1 && fd[1][0] == n0 && fd[1][1] == n1 && fd[2][0] == n0 && fd[2][1] == n1 && fd[3][0] == n0 && fd[3][1] == n1)) {
#pragma unroll 1
        for (int i = 0; i < 4; i++) poll(c, FL_D + i * 64 + lane, true);
      }
      if (lane == 0) __hip_atomic_store((unsigned long long*)(c.ws + FL_N * 128), c.nonce, __ATOMIC_RELAXED, __HIP_MEMORY_SCOPE_AGENT);
    }
    if (g + 5 < 36) f_issue(c, fc, g + 5);
  }
  bar(); bar();
  bar();
  p6_finish(c, rb, fc);
}
template <int KH>
DI void p45_ffn(const Ctx& c, int rb, int fc) {
  constexpr int kh = KH;
  const int w = c.w, lane = c.lane, rg = w & 3; const unsigned lds0 = c.lds0;
  const unsigned vl = lane * 16;
  const int ks = fc >> 2, ct = fc & 3;
  const int tok = lane & 31, h = lane >> 5;
  if (threadIdx.x < 288) *(LAS float*)(size_t)(lds0 + F1_UV + threadIdx.x * 4) = __uint_as_float((unsigned)c.b1[fc * 288 + threadIdx.x] << 16);
  wait_lgkm0(); bar();
  u32x2 stv[6];
#pragma unroll
  for (int e = 0; e < 6; e++)
    asm volatile("buffer_load_dwordx2 %0, %1, %2, %3 offen offset:%4 sc1" : "=&v"(stv[e]) : "v"((unsigned)tok * 8u), "s"(c.sws),
                 "s"((unsigned)WS_ST2 + (unsigned)((2 * rb + (rg >> 1)) * 6) * 512u + (unsigned)(rg & 1) * 256u), "i"(e * 512) : "memory");
  u32x4 xf[18];
  {
    const unsigned so = (unsigned)WS_HP + ((unsigned)(rb * 4 + rg) * 36u + (unsigned)kh * 18u) * 1024u;
#pragma unroll
    for (int s = 0; s < 18; s++)
      asm volatile("buffer_load_dwordx4 %0, %1, %2, %3 offen offset:%4 sc1" : "=&v"(xf[s]) : "v"(vl), "s"(c.sws), "s"(so + (s >> 2) * 4096u), "i"((s & 3) * 1024) : "memory");
  }
  float ra, rc;
  {
    asm volatile("s_waitcnt vmcnt(18)" : "+v"(stv[0]), "+v"(stv[1]), "+v"(stv[2]), "+v"(stv[3]), "+v"(stv[4]), "+v"(stv[5]) :: "memory");
    float s = 0, q = 0;
#pragma unroll
    for (int e = 0; e < 6; e++) { s += __uint_as_float(stv[e][0]); q += __uint_as_float(stv[e][1]); }
    const float mean = s * (1.0f / D), var = fmaxf(q * (1.0f / D) - mean * mean, 0.f), rstd = rsqrtf(var + LN_EPS);
    ra = rstd; rc = -mean * rstd;
  }
  const unsigned rd = lds0 + (unsigned)kh * 9216u + vl;
  const unsigned hvo = (unsigned)(tok >> 4) * 1024u + (unsigned)h * 256u + (unsigned)(tok & 15) * 16u;
  const unsigned hbase = (unsigned)WS_H1P + ((unsigned)(rb * 4 + rg) * 144u + (unsigned)fc * 9u) * 2048u;
  const unsigned bia = lds0 + F1_UV + (unsigned)h * 32u;
  {
#pragma unroll
    for (int s = 0; s < 18; s++) {
      asm volatile("s_waitcnt vmcnt(%1)" : "+v"(xf[s]) : "i"(17 - s) : "memory");
#pragma unroll
      for (int e = 0; e < 4; e++) {
        const f32x2 v = f32x2{bflo(xf[s][e]), bfhi(xf[s][e])} * ra + rc;
        xf[s][e] = pk2(v[0], v[1]);
      }
      asm volatile("" : "+v"(xf[s]));
      SB();
    }
  }
  constexpr int PF = 3;
  u32x4 wf[PF];
  const f32x16 zero = {};
#define F1_BIAS(ACC, ba_) do { const f32x4 b0_ = lds_f128(ba_), b1_ = lds_f128(ba_ + 16), b2_ = lds_f128(ba_ + 64), b3_ = lds_f128(ba_ + 80); \
    ACC = f32x16{b0_[0], b0_[1], b0_[2], b0_[3], b1_[0], b1_[1], b1_[2], b1_[3], b2_[0], b2_[1], b2_[2], b2_[3], b3_[0], b3_[1], b3_[2], b3_[3]}; } while (0)
  f32x16 accG, accP, accQ;
  F1_BIAS(accG, bia + (unsigned)(1 - kh) * 128u);
  float vc1 = GC1; asm volatile("" : "+v"(vc1));
  unsigned r0 = 0;
#define F_NEXT(r) ((r) == 5u * H_SLOT ? 0u : (r) + H_SLOT)
  const unsigned xwr = lds0 + F1_XCH + (unsigned)(kh ^ 1) * 16384u + (unsigned)rg * 4096u + vl;
  const unsigned xrd = lds0 + F1_XCH + (unsigned)kh * 16384u + (unsigned)rg * 4096u + vl;
  f32x4 pp[4] = {};
#define F1_STAGE(q, G, SRC, R0, O) do { const int p_ = (q) >> 2, st_ = (q) & 3, r_ = (R0) + 2 * p_; \
      if (st_ == 0) g2_s1(G, SRC[r_], SRC[r_ + 1], pp[r_ >> 2][r_ & 3], pp[r_ >> 2][(r_ & 3) + 1]); \
      if (st_ == 1) g2_s2(G, vc1); if (st_ == 2) g2_s3(G); if (st_ == 3) O[p_] = g2_s4(G); } while (0)
#define F_HALF_BEGIN(s) do { if ((s) % 9 == 0) { bar(); sb = rd + r0; r0 = F_NEXT(r0); sn = rd + r0; asm volatile("" : "+v"(sb), "+v"(sn)); } } while (0)
#define F_FRAG(s) lds_u128((s) % 9 + PF < 9 ? sb + ((s) % 9 + PF) * 1024 : sn + ((s) % 9 + PF - 9) * 1024)
#define F1_PERIOD(t, ACC, PREV, GELU, HALF) do { \
    unsigned sb, sn; \
    u32x4 o; G2 g; \
    const int gt_ = ((t) - 2 - (HALF)) + kh;      \
    _Pragma("unroll") for (int s = 0; s < 18; s++) { \
      F_HALF_BEGIN(s); \
      if (s == 0 && (t) == 0) { _Pragma("unroll") for (int i = 0; i < PF; i++) wf[i] = lds_u128(sb + i * 1024); } \
      if (s == 0 && (GELU) && (HALF) == 0) { _Pragma("unroll") for (int e = 0; e < 4; e++) pp[e] = lds_f128(xrd + e * 1024); SB(); } \
      const u32x4 cur = wf[s % PF]; \
      wf[s % PF] = F_FRAG(s); \
      ACC = mfma32(cur, xf[s], (s == 0 && (HALF) == 1) ? zero : ACC); \
      if ((HALF) == 1 && s % 4 == 1 && s / 4 < 4) { const int e = s / 4; lds_w128(xwr + e * 1024, f32x4{accG[4 * e], accG[4 * e + 1], accG[4 * e + 2], accG[4 * e + 3]}); } \
      if (GELU) { \
        if (s >= 2 && s <= 17) F1_STAGE(s - 2, g, PREV, 8 * (HALF), o); \
        if (s == 17) st128s(o, c.sws, hvo + (HALF) * 512u, hbase + (unsigned)gt_ * 2048u); \
      } \
      if ((HALF) == 1 && s == 15) {     \
        const int nt_ = (t) + 1 == 8 ? 8 : (t) + 1 + (1 - kh); \
        if ((t) + 1 == 8 && kh == 0) accG = zero; \
        else { unsigned ba_ = bia + (unsigned)nt_ * 128u; asm volatile("" : "+v"(ba_)); F1_BIAS(accG, ba_); } \
      } \
      SB(); \
    } \
    asm volatile("" :: "v"(ACC)); \
  } while (0)
  const unsigned hoff0 = (unsigned)WS_H1P + ((unsigned)(rb * 4 + rg) * 144u + (unsigned)ks * 36u) * 2048u;
  u32x4 hA[4], hB[4], hC[4];
#define HLOAD1(dst, t, i) do { const int p_ = 4 * (t) + 2 * kh + ((i) >> 1) + 9 * ct; const unsigned so_ = hoff0 + (unsigned)(p_ >= 36 ? p_ - 36 : p_) * 2048u; \
    if (((i) & 1) == 0) asm volatile("buffer_load_dwordx4 %0, %1, %2, %3 offen sc1" : "=&v"(dst[i]) : "v"(vl), "s"(c.sws), "s"(so_) : "memory"); \
    else asm volatile("buffer_load_dwordx4 %0, %1, %2, %3 offen offset:1024 sc1" : "=&v"(dst[i]) : "v"(vl), "s"(c.sws), "s"(so_) : "memory"); } while (0)
#define HWAIT(N, dst) asm volatile("s_waitcnt vmcnt(" #N ")" : "+v"(dst[0]), "+v"(dst[1]), "+v"(dst[2]), "+v"(dst[3]) :: "memory")
  {
    F1_PERIOD(0, accG, accQ, false, 0);
    F1_PERIOD(1, accP, accQ, false, 1);
    F1_PERIOD(2, accG, accP, true, 0);
    F1_PERIOD(3, accQ, accP, true, 1);
    F1_PERIOD(4, accG, accQ, true, 0);
    F1_PERIOD(5, accP, accQ, true, 1);
    F1_PERIOD(6, accG, accP, true, 0);
    F1_PERIOD(7, accQ, accP, true, 1);
    {
      wait_vm<2>();
      unsigned sb, sn;
      u32x4 o, o2; G2 g, g2;
      const int gt_ = 6 + kh;
#pragma unroll
      for (int s = 0; s < 18; s++) {
        F_HALF_BEGIN(s);
        if (s == 0) {
#pragma unroll
          for (int e = 0; e < 4; e++) pp[e] = lds_f128(xrd + e * 1024);
          SB();
        }
        const u32x4 cur = wf[s % PF];
        wf[s % PF] = F_FRAG(s);
        accG = mfma32(cur, xf[s], accG);
        if (s == 10 + KH) HLOAD1(hA, 0, 0); if (s == 12 + KH) HLOAD1(hA, 0, 1); if (s == 14 + KH) HLOAD1(hA, 0, 2); if (s == 16 + KH) HLOAD1(hA, 0, 3);
        if (s >= 2 && s <= 17) { F1_STAGE(s - 2, g, accQ, 0, o); F1_STAGE(s - 2, g2, accQ, 8, o2); }
        if (s == 17) { st128s(o, c.sws, hvo, hbase + (unsigned)gt_ * 2048u); st128s(o2, c.sws, hvo + 512u, hbase + (unsigned)gt_ * 2048u); }
        SB();
      }
      bar();
      lds_w128(xwr, kh ? f32x4{accG[0], accG[1], accG[2], accG[3]} : f32x4{accG[8], accG[9], accG[10], accG[11]});
      lds_w128(xwr + 1024, kh ? f32x4{accG[4], accG[5], accG[6], accG[7]} : f32x4{accG[12], accG[13], accG[14], accG[15]});
      wait_lgkm0(); bar();
      const f32x4 q0 = lds_f128(xrd), q1 = lds_f128(xrd + 1024);
      const f32x4 k0 = (kh ? f32x4{accG[8], accG[9], accG[10], accG[11]} : f32x4{accG[0], accG[1], accG[2], accG[3]}) + q0;
      const f32x4 k1 = (kh ? f32x4{accG[12], accG[13], accG[14], accG[15]} : f32x4{accG[4], accG[5], accG[6], accG[7]}) + q1;
      const f32x2 z0 = gelu2(f32x2{k0[0], k0[1]}), z1 = gelu2(f32x2{k0[2], k0[3]}), z2 = gelu2(f32x2{k1[0], k1[1]}), z3 = gelu2(f32x2{k1[2], k1[3]});
      o = u32x4{pk2(z0[0], z0[1]), pk2(z1[0], z1[1]), pk2(z2[0], z2[1]), pk2(z3[0], z3[1])};
      st128s(o, c.sws, hvo + (unsigned)kh * 512u, hbase + 8u * 2048u);
    }
  }
  publish(c, FL_H1 + rb * 16 + fc, true);
  HLOAD1(hB, 1, 0); HLOAD1(hB, 1, 1); HLOAD1(hB, 1, 2); HLOAD1(hB, 1, 3);
  f32x4 acc[2][9];
#pragma unroll
  for (int a = 0; a < 2; a++)
#pragma unroll
    for (int n = 0; n < 9; n++) acc[a][n] = f32x4{0, 0, 0, 0};
#define F2_PERIOD(t, HC, HN, HM, VMW, MODE) do { \
    HWAIT(VMW, HC); \
    unsigned sb, sn; \
    _Pragma("unroll") for (int s = 0; s < 18; s++) { \
      F_HALF_BEGIN(s); \
      const u32x4 cur = wf[s % PF]; \
      wf[s % PF] = F_FRAG(s); \
      const int k = s / 9, nt = s % 9; \
      acc[0][nt] = mfma16(cur, HC[2 * k], acc[0][nt]); \
      acc[1][nt] = mfma16(cur, HC[2 * k + 1], acc[1][nt]); \
      if ((MODE) == 3) { if (s == KH) HLOAD1(HM, (t) + 1, 0); if (s == 2 + KH) HLOAD1(HM, (t) + 1, 1); if (s == 4 + KH) HLOAD1(HM, (t) + 1, 2); if (s == 6 + KH) HLOAD1(HM, (t) + 1, 3); } \
      if ((MODE) == 0 || (MODE) == 3) { if (s == 9 + KH) HLOAD1(HN, (t) + 2, 0); if (s == 11 + KH) HLOAD1(HN, (t) + 2, 1); if (s == 13 + KH) HLOAD1(HN, (t) + 2, 2); if (s == 15 + KH) HLOAD1(HN, (t) + 2, 3); } \
      SB(); \
    } \
  } while (0)
  F2_PERIOD(0, hA, hC, hC, 4, 1);
  F2_PERIOD(1, hB, hA, hC, 0, 3);
  F2_PERIOD(2, hC, hB, hB, 4, 0);
  F2_PERIOD(3, hA, hC, hC, 4, 0);
  F2_PERIOD(4, hB, hA, hA, 4, 0);
  F2_PERIOD(5, hC, hB, hB, 4, 0);
  F2_PERIOD(6, hA, hC, hC, 4, 0);
  F2_PERIOD(7, hB, hA, hA, 4, 1);
  F2_PERIOD(8, hC, hB, hB, 0, 1);
  wait_lgkm0(); bar();
  const unsigned xb = lds0 + (unsigned)rg * 18432u + vl;
#pragma unroll
  for (int n = 0; n < 9; n++) lds_w128(xb + (unsigned)(kh ^ 1) * 9216u + n * 1024, kh ? acc[0][n] : acc[1][n]);
  wait_lgkm0(); bar();
  f32x4 o[9];
#pragma unroll
  for (int n = 0; n < 9; n++) o[n] = lds_f128(xb + kh * 9216u + n * 1024) + (kh ? acc[1][n] : acc[0][n]);
  const unsigned xg = (unsigned)WS_XCH + (unsigned)((rb * 4 + ct) * 16) * 9216u + (unsigned)kh * 4608u;
  if (rg != ks) {
    const unsigned d = xg + (unsigned)(rg * 4 + ks) * 9216u;
#pragma unroll
    for (int T = 0; T < 4; T++)
      st128s(u32x4{pk2(o[2 * T][0], o[2 * T][1]), pk2(o[2 * T][2], o[2 * T][3]), pk2(o[2 * T + 1][0], o[2 * T + 1][1]), pk2(o[2 * T + 1][2], o[2 * T + 1][3])}, c.sws, vl + T * 1024u, d);
    __builtin_amdgcn_raw_buffer_store_b64(u32x2{pk2(o[8][0], o[8][1]), pk2(o[8][2], o[8][3])}, c.sws, 4096u + lane * 8u, d, SC1);
  } else {
#pragma unroll
    for (int n = 0; n < 9; n++) lds_w128(lds0 + F_FIN + (unsigned)kh * 9216u + n * 1024u + vl, o[n]);
  }
  wait_lgkm0();
  publish(c, FL_P + (rb * 4 + ct) * 4 + ks, true);
  p6_finish(c, rb, fc);
}

constexpr int M_LDS = 163840;
static_assert(L_WO + 110592 <= L_DUMP && 4 * Q_SLOT + 640 <= L_DUMP && F_LDS <= L_DUMP, "LDS map");
__global__ KATTR void fused_block(const u16* __restrict__ x, const u16* __restrict__ wqkv, const u16* __restrict__ wo, const u16* __restrict__ w1,
                                  const u16* __restrict__ b1, const u16* __restrict__ w2, const u16* __restrict__ b2, u16* __restrict__ out, char* __restrict__ ws) {
  extern __shared__ __attribute__((aligned(16))) char smem[];
  Ctx c;
  c.x = x; c.wqkv = wqkv; c.wo = wo; c.w1 = w1; c.b1 = b1; c.w2 = w2; c.b2 = b2; c.out = out; c.ws = ws;
  c.sws = mk_srd(ws, WS_BYTES);
  c.lds0 = (unsigned)(size_t)smem;
  c.b = blockIdx.x; c.xcd = c.b & 7; c.bi = c.b >> 3;
  c.w = rfl(threadIdx.x >> 6); c.lane = threadIdx.x & 63;
  const int xcd = c.xcd, bi = c.bi, w = c.w, lane = c.lane;
  const int rb4 = (xcd >> 2) * 8 + (bi & 7), fc4 = (xcd & 3) * 4 + (bi >> 3);
  if (bi < 24) {
    p1_qkv(c, (xcd >> 1) * 4 + (bi & 3), (xcd & 1) * 6 + (bi >> 2));
  } else {
    const int j = bi - 24, fc = (xcd & 3) * 4 + (j >> 1), part = (xcd >> 2) * 2 + (j & 1);
    helper_stats(c, xcd * 8 + j, fc, part);
    if (w >= 8) wait_vm<0>();
    bar();
    relayout_w1(c, fc, part);
    publish(c, FL_W1 + fc * 4 + part, true);
    if (w >= 8) {
      const srd_t sw2 = mk_srd(w2, D * F * 2);
      const int ks = xcd & 3, ct2 = j >> 1, row0 = (ks * 36 + part * 9) * 32;
#pragma unroll
      for (int i = 0; i < 4; i++) {
        const int l = ((w - 8) * 4 + i) * 64 + lane, row = l / 3;
        touch128(c.lds0 + L_DUMP, l < 864 ? (unsigned)row * (D * 2) + (unsigned)(l - 3 * row) * 128u : 0x7ff00000u, sw2, (unsigned)row0 * (D * 2) + (((unsigned)ct2 * 288u) & ~127u));
      }
    }
  }
  if (bi < 24 && w >= 8) p3_prefetch(c, bi >> 2);
  p2_attn(c, xcd * 4 + (bi & 3), bi >> 2);
  if (w >= 8) {
    if (w == 8 && bi < 24) poll(c, FL_A + xcd * 32 + (lane & 31), lane < 32);
    if (w == 9) poll(c, FL_W1 + fc4 * 4 + (lane & 3), lane < 4);
    if (bi < 24) wait_vm<0>();
    bar();
    f_issue(c, fc4, 0); f_issue(c, fc4, 1);
    {
#pragma unroll
      for (int i = 0; i < 2; i++) { const unsigned l = (unsigned)(((w - 8) * 2 + i) * 64 + lane); touch128(c.lds0 + L_DUMP, l < 288u ? l * 128u : 0x7ff00000u, c.sws, (unsigned)WS_W1P + (unsigned)(fc4 * 9 + 1) * F_SLOT + (unsigned)(bi & 7) * 36864u); }
    }
  }
  if (bi < 24) {
    if (w >= 8) {
      bar(); bar(); bar();
      publish(c, FL_H + (xcd * 4 + (bi & 3)) * 6 + (bi >> 2), false);
    } else {
      p3_wo(c, xcd * 4 + (bi & 3), bi >> 2);
    }
  } else {
    if (w < 8) bar();
    const int j = bi - 24, ks = xcd & 3, ct = j >> 1, part = (xcd >> 2) * 2 + (j & 1);
    if (w < 9) relayout_w2(c, c.lds0 + L_WO + (unsigned)w * 9216u, ks * 36 + part * 9 + w, ct);
    publish(c, FL_W2 + (ks * 4 + ct) * 4 + part, w < 9);
  }
  if (w >= 8) p45_loader(c, rb4, fc4); else if (w < 4) p45_ffn<0>(c, rb4, fc4); else p45_ffn<1>(c, rb4, fc4);
}

extern "C" void kernel_launch(void* const* d_in, const int* in_sizes, int n_in, void* d_out, int out_size, void* d_ws, size_t ws_size,
                              hipStream_t stream) {
  (void)hipFuncSetAttribute((const void*)fused_block, hipFuncAttributeMaxDynamicSharedMemorySize, M_LDS);
  fused_block<<<256, 768, M_LDS, stream>>>((const u16*)d_in[0], (const u16*)d_in[1], (const u16*)d_in[2], (const u16*)d_in[4], (const u16*)d_in[5],
                                           (const u16*)d_in[6], (const u16*)d_in[7], (u16*)d_out, (char*)d_ws);
}
```

```cpp
#include <hip/hip_runtime.h>
#include <stdint.h>

typedef unsigned short u16;
typedef __bf16 bf16x8 __attribute__((ext_vector_type(8)));
typedef __bf16 bf16x4 __attribute__((ext_vector_type(4)));
typedef __bf16 bf16x2 __attribute__((ext_vector_type(2)));
typedef float f32x2 __attribute__((ext_vector_type(2)));
typedef float f32x4 __attribute__((ext_vector_type(4)));
typedef float f32x16 __attribute__((ext_vector_type(16)));
typedef unsigned u32x2 __attribute__((ext_vector_type(2)));
typedef unsigned u32x4 __attribute__((ext_vector_type(4)));
#define LAS __attribute__((address_space(3)))
#define DI __device__ __forceinline__
#define SB() __builtin_amdgcn_sched_barrier(0)
#define KATTR __launch_bounds__(768, 3) __attribute__((amdgpu_waves_per_eu(3, 3)))

constexpr int S = 2048, D = 576, H = 32, DH = 18, F = 4608, NQKV = 1728;
constexpr float LN_EPS = 1e-5f;


constexpr size_t WS_FLG = 0;
constexpr int FL_N = 0, FL_D = 1, FL_Q = FL_D + 256, FL_W1 = FL_Q + 192, FL_A = FL_W1 + 64, FL_W2 = FL_A + 256, FL_H = FL_W2 + 64, FL_H1 = FL_H + 192,
              FL_P = FL_H1 + 256, FL_S1 = FL_P + 256, FL_END = FL_S1 + 64;
constexpr size_t WS_QKV = 262144;
static_assert(FL_END * 128 <= WS_QKV, "flag area");
constexpr size_t WS_QKV_SZ = (size_t)3 * H * S * 24 * 2;
constexpr size_t WS_OP = WS_QKV + WS_QKV_SZ;
constexpr size_t WS_OL = WS_OP + (size_t)64 * 32 * 1024;
constexpr size_t WS_HP = WS_OL + (size_t)H * S * 4;
constexpr size_t WS_ST2 = WS_HP + (size_t)S * D * 2;
constexpr size_t WS_W1P = WS_ST2 + (size_t)S * 48;
constexpr size_t WS_W2P = WS_W1P + (size_t)D * F * 2;
constexpr size_t WS_H1P = WS_W2P + (size_t)D * F * 2;
constexpr size_t WS_XCH = WS_H1P + (size_t)S * F * 2;
constexpr size_t WS_ST1 = WS_XCH + (size_t)16 * 4 * 4 * 4 * 2 * 4608;
constexpr size_t WS_CSQ = WS_ST1 + (size_t)S * 8;
constexpr size_t WS_END = WS_CSQ + (size_t)(NQKV + 64) * 4;
constexpr unsigned WS_BYTES = (unsigned)WS_END;


DI unsigned rfl(unsigned x) { return __builtin_amdgcn_readfirstlane(x); }
DI float bflo(unsigned w) { return __uint_as_float(w << 16); }
DI float bfhi(unsigned w) { return __uint_as_float(w & 0xffff0000u); }
DI unsigned pk2(float lo, float hi) { f32x2 v = {lo, hi}; bf16x2 b = __builtin_convertvector(v, bf16x2); return __builtin_bit_cast(unsigned, b); }
DI float dot2(unsigned a, unsigned b, float c) { return __builtin_amdgcn_fdot2_f32_bf16(__builtin_bit_cast(bf16x2, a), __builtin_bit_cast(bf16x2, b), c, false); }

typedef __amdgpu_buffer_rsrc_t srd_t;
DI srd_t mk_srd(const void* p, unsigned bytes) { return __builtin_amdgcn_make_buffer_rsrc((void*)p, (short)0, (int)bytes, 0x00020000); }
DI void dma16(unsigned ldsaddr, unsigned voff, srd_t srd, unsigned soff) {
  asm volatile("s_mov_b32 m0, %0\n\ts_nop 0\n\tbuffer_load_dwordx4 %1, %2, %3 offen lds" :: "s"(ldsaddr), "v"(voff), "s"(srd), "s"(soff) : "memory");
}
DI void touch128(unsigned ldsdump, unsigned voff, srd_t srd, unsigned soff) {
  asm volatile("s_mov_b32 m0, %0\n\ts_nop 0\n\tbuffer_load_dword %1, %2, %3 offen lds" :: "s"(ldsdump), "v"(voff), "s"(srd), "s"(soff) : "memory");
}
template <int N> DI void wait_vm() { asm volatile("s_waitcnt vmcnt(%0)" :: "i"(N) : "memory"); }
DI void wait_lgkm0() { asm volatile("s_waitcnt lgkmcnt(0)" ::: "memory"); }
DI void bar() { asm volatile("" ::: "memory"); __builtin_amdgcn_s_barrier(); asm volatile("" ::: "memory"); }
DI u32x4 lds_u128(unsigned addr) { return *(const LAS u32x4*)(size_t)addr; }
DI f32x4 lds_f128(unsigned addr) { return *(const LAS f32x4*)(size_t)addr; }
DI void lds_w128(unsigned addr, f32x4 v) { *(LAS f32x4*)(size_t)addr = v; }
DI bf16x8 asbf(u32x4 v) { return __builtin_bit_cast(bf16x8, v); }
DI u32x2 lds_tr(unsigned addr) { return __builtin_bit_cast(u32x2, __builtin_amdgcn_ds_read_tr16_b64_v4bf16((LAS bf16x4*)(size_t)addr)); }
DI u32x4 cat4(u32x2 a, u32x2 b) { return u32x4{a[0], a[1], b[0], b[1]}; }
DI f32x4 mfma16(u32x4 a, u32x4 b, f32x4 c) { return __builtin_amdgcn_mfma_f32_16x16x32_bf16(asbf(a), asbf(b), c, 0, 0, 0); }
DI f32x16 mfma32(u32x4 a, u32x4 b, f32x16 c) { return __builtin_amdgcn_mfma_f32_32x32x16_bf16(asbf(a), asbf(b), c, 0, 0, 0); }
template <int NOP> DI float max16(float m, const f32x16& t) {
  float u;
  asm volatile("s_nop %18\n\tv_max3_f32 %1, %2, %3, %4\n\tv_max3_f32 %0, %0, %5, %6\n\tv_max3_f32 %1, %1, %7, %8\n\tv_max3_f32 %0, %0, %9, %10\n\t"
               "v_max3_f32 %1, %1, %11, %12\n\tv_max3_f32 %0, %0, %13, %14\n\tv_max3_f32 %1, %1, %15, %16\n\tv_max3_f32 %0, %0, %1, %17"
               : "+v"(m), "=&v"(u)
               : "v"(t[0]), "v"(t[1]), "v"(t[2]), "v"(t[3]), "v"(t[4]), "v"(t[5]), "v"(t[6]), "v"(t[7]), "v"(t[8]), "v"(t[9]), "v"(t[10]), "v"(t[11]), "v"(t[12]),
                 "v"(t[13]), "v"(t[14]), "v"(t[15]), "i"(NOP));
  return m;
}
template <int CTRL, int RM> DI float dpp_add(float v) { return v + __builtin_bit_cast(float, __builtin_amdgcn_update_dpp(0, __builtin_bit_cast(int, v), CTRL, RM, 0xf, true)); }
DI float wave_sum63(float v) { v = dpp_add<0x111, 0xf>(v); v = dpp_add<0x112, 0xf>(v); v = dpp_add<0x114, 0xf>(v); v = dpp_add<0x118, 0xf>(v); v = dpp_add<0x142, 0xa>(v); return dpp_add<0x143, 0xc>(v); }
DI float sum_x32(float v) { const auto a = __builtin_amdgcn_permlane32_swap(__float_as_uint(v), __float_as_uint(v), false, false); return __uint_as_float(a[0]) + __uint_as_float(a[1]); }
DI float sum_x16(float v) { const auto a = __builtin_amdgcn_permlane16_swap(__float_as_uint(v), __float_as_uint(v), false, false); return __uint_as_float(a[0]) + __uint_as_float(a[1]); }
DI float sum_x8(float v) { return dpp_add<0x128, 0xf>(v); }
DI int swap23(int i) { return (i & 19) | ((i & 4) << 1) | ((i & 8) >> 1); }


constexpr int SC1 = 16;
DI void dma16s(unsigned ldsaddr, unsigned voff, srd_t srd, unsigned soff) {
  asm volatile("s_mov_b32 m0, %0\n\ts_nop 0\n\tbuffer_load_dwordx4 %1, %2, %3 offen sc1 lds" :: "s"(ldsaddr), "v"(voff), "s"(srd), "s"(soff) : "memory");
}
DI void st128s(u32x4 v, srd_t srd, unsigned voff, unsigned soff) {
  asm volatile("buffer_store_dwordx4 %0, %1, %2, %3 offen sc1\n\ts_nop 1" :: "v"(v), "v"(voff), "s"(srd), "s"(soff) : "memory");
}
DI u32x4 ld128s(srd_t srd, unsigned voff, unsigned soff) { return __builtin_amdgcn_raw_buffer_load_b128(srd, voff, soff, SC1); }

struct Ctx {
  const u16 *x, *wqkv, *wo, *w1, *b1, *w2, *b2;
  u16* out;
  char* ws;
  srd_t sws;
  unsigned lds0;
  unsigned long long nonce;
  int b, xcd, bi, w, lane;
};
DI void publish(const Ctx& c, int flag, bool stored) {
  if (stored) wait_vm<0>();
  bar();
  if (threadIdx.x == 0) __hip_atomic_store((unsigned long long*)(c.ws + (size_t)flag * 128), c.nonce, __ATOMIC_RELAXED, __HIP_MEMORY_SCOPE_AGENT);
}
DI void poll(const Ctx& c, int flag, bool active) {
  const unsigned long long* p = (const unsigned long long*)(c.ws + (size_t)flag * 128);
  const unsigned long long t0 = __builtin_amdgcn_s_memrealtime();
  for (;;) {
    const unsigned long long v = active ? __hip_atomic_load(p, __ATOMIC_RELAXED, __HIP_MEMORY_SCOPE_AGENT) : c.nonce;
    if (__all(v == c.nonce)) break;
    if (__builtin_amdgcn_s_memrealtime() - t0 > 2000000ull) break;
  }
}

constexpr int L_DUMP = 163840 - 256;
DI u32x2 entry_load(const Ctx& c) {
  u32x2 nv;
  asm volatile("buffer_load_dwordx2 %0, %1, %2, 0 offen sc1" : "=&v"(nv) : "v"((unsigned)(FL_N * 128)), "s"(c.sws) : "memory");
  return nv;
}
DI void entry_nonce(Ctx& c, u32x2 nv) {
  asm volatile("" : "+v"(nv));
  c.nonce = (((unsigned long long)rfl(nv[1]) << 32) | rfl(nv[0])) + 1ull;
  if (threadIdx.x == 0) __hip_atomic_store((unsigned long long*)(c.ws + (size_t)(FL_D + c.b) * 128), c.nonce, __ATOMIC_RELAXED, __HIP_MEMORY_SCOPE_AGENT);
}

constexpr int L_W1 = 4096;
DI void relayout_w1_fetch(const Ctx& c, int fc, int part) {
  const int l = c.w - 8, lane = c.lane;
  const srd_t sw = mk_srd(c.w1, D * F * 2);
#pragma unroll 1
  for (int pc = l; pc < 81; pc += 4) {
    const int ch = pc * 64 + lane, row = ch / 36, cc = ch % 36;
    dma16(c.lds0 + L_W1 + (unsigned)pc * 1024u, (unsigned)row * (F * 2) + (unsigned)cc * 16u, sw, (unsigned)part * 144u * (F * 2) + (unsigned)fc * 576u);
  }
}
DI void helper_stats(Ctx& c, int hb, int fc, int part) {
  const int w = c.w, lane = c.lane;
  const srd_t sx = mk_srd(c.x, S * D * 2), sw = mk_srd(c.wqkv, hb < 27 ? D * NQKV * 2 : 0);
  if (w >= 8) {
    const u32x2 nv = entry_load(c);
    wait_vm<0>();
    entry_nonce(c, nv);
    bar();
    publish(c, FL_S1 + hb, false);
    relayout_w1_fetch(c, fc, part);
    return;
  }
  const int tid = w * 64 + lane, ch = tid & 7, rg = tid >> 3;
  u32x4 a[4], b2[4], v9[9];
#pragma unroll
  for (int r = 0; r < 4; r++) {
    const unsigned so = (unsigned)(hb * 32 + w * 4 + r) * (D * 2);
    a[r] = __builtin_amdgcn_raw_buffer_load_b128(sx, (unsigned)lane * 16u, so, 0);
    b2[r] = __builtin_amdgcn_raw_buffer_load_b128(sx, lane < 8 ? 1024u + (unsigned)lane * 16u : 0x7ff00000u, so, 0);
  }
#pragma unroll
  for (int i = 0; i < 9; i++) v9[i] = __builtin_amdgcn_raw_buffer_load_b128(sw, (unsigned)(rg * 9 + i) * (NQKV * 2) + (unsigned)ch * 16u, (unsigned)hb * 128u, 0);
  const u32x2 nv = entry_load(c);
  float sm[4], sq[4];
#pragma unroll
  for (int r = 0; r < 4; r++) {
    sm[r] = 0; sq[r] = 0;
#pragma unroll
    for (int e = 0; e < 4; e++) { sm[r] = dot2(a[r][e], 0x3f803f80u, sm[r]); sq[r] = dot2(a[r][e], a[r][e], sq[r]); sm[r] = dot2(b2[r][e], 0x3f803f80u, sm[r]); sq[r] = dot2(b2[r][e], b2[r][e], sq[r]); }
  }
#pragma unroll
  for (int r = 0; r < 4; r++) {
    const float s1 = __builtin_bit_cast(float, __builtin_amdgcn_readlane(__builtin_bit_cast(int, wave_sum63(sm[r])), 63));
    const float s2 = __builtin_bit_cast(float, __builtin_amdgcn_readlane(__builtin_bit_cast(int, wave_sum63(sq[r])), 63));
    const float mean = s1 * (1.0f / D), var = fmaxf(s2 * (1.0f / D) - mean * mean, 0.f);
    sm[r] = mean; sq[r] = rsqrtf(var + LN_EPS);
  }
  {
    const int r = lane & 3;
    const u32x2 v = {__float_as_uint(r == 0 ? sm[0] : r == 1 ? sm[1] : r == 2 ? sm[2] : sm[3]), __float_as_uint(r == 0 ? sq[0] : r == 1 ? sq[1] : r == 2 ? sq[2] : sq[3])};
    __builtin_amdgcn_raw_buffer_store_b64(v, c.sws, lane < 4 ? (unsigned)lane * 8u : 0x7ff00000u, (unsigned)WS_ST1 + (unsigned)(hb * 32 + w * 4) * 8u, SC1);
  }
  float s8[8] = {0, 0, 0, 0, 0, 0, 0, 0};
#pragma unroll
  for (int i = 0; i < 9; i++)
#pragma unroll
    for (int e = 0; e < 4; e++) { s8[2 * e] += bflo(v9[i][e]); s8[2 * e + 1] += bfhi(v9[i][e]); }
#pragma unroll
  for (int e = 0; e < 8; e++) s8[e] = sum_x32(sum_x16(sum_x8(s8[e])));
  if (lane < 8) { lds_w128(c.lds0 + (unsigned)(w * 64 + lane * 8) * 4u, f32x4{s8[0], s8[1], s8[2], s8[3]}); lds_w128(c.lds0 + (unsigned)(w * 64 + lane * 8) * 4u + 16, f32x4{s8[4], s8[5], s8[6], s8[7]}); }
  wait_lgkm0(); bar();
  if (w == 0) {
    float t = 0;
#pragma unroll
    for (int k = 0; k < 8; k++) t += *(const LAS float*)(size_t)(c.lds0 + (unsigned)(k * 64 + lane) * 4u);
    __builtin_amdgcn_raw_buffer_store_b32(__float_as_uint(t), c.sws, hb < 27 ? (unsigned)lane * 4u : 0x7ff00000u, (unsigned)WS_CSQ + (unsigned)hb * 256u, SC1);
  }
  wait_lgkm0();
  wait_vm<0>();
  entry_nonce(c, nv);
  publish(c, FL_S1 + hb, true);
}

DI void relayout_w1(const Ctx& c, int fc, int part) {
  const int w = c.w, lane = c.lane; const unsigned lds0 = c.lds0 + L_W1;
  const int h = lane >> 5, blk = (lane >> 4) & 1, q = (lane >> 2) & 3, p = lane & 3;
  const unsigned ra = lds0 + (unsigned)(8 * h + q) * 576u + (unsigned)(16 * blk + 8 * (p & 1) + 4 * (p >> 1)) * 2u;
  for (int fr = w; fr < 81; fr += 12) {
    const int sl = fr / 9, tile = fr % 9, s = part * 9 + sl, kh = s / 18;
    const unsigned a = ra + (unsigned)sl * 16u * 576u + (unsigned)tile * 64u;
    const u32x4 v = cat4(lds_tr(a), lds_tr(a + 4 * 576));
    const int slot = tile == 8 ? 8 : (kh == 0 ? (tile ^ 1) : tile);
    const int s18 = s % 18;
    st128s(v, c.sws, (unsigned)lane * 16u, (unsigned)WS_W1P + ((unsigned)(fc * 9 + slot) * 36u + (unsigned)((s18 / 9) * 18 + kh * 9 + s18 % 9)) * 1024u);
  }
}
DI void relayout_w2(const Ctx& c, unsigned ldsw, int kappa, int ct) {
  const int lane = c.lane;
  const srd_t sw = mk_srd(c.w2, D * F * 2);
#pragma unroll
  for (int pc = 0; pc < 9; pc++) {
    const int ch = pc * 64 + lane, row = ch / 18, cc = ch % 18;
    dma16(ldsw + (unsigned)pc * 1024u, (unsigned)row * (D * 2) + (unsigned)cc * 16u, sw, (unsigned)kappa * 32u * (D * 2) + (unsigned)ct * 288u);
  }
  wait_vm<0>();
  const int g = lane >> 4, q = (lane >> 2) & 3, p = lane & 3;
  const unsigned ra = ldsw + (unsigned)(8 * g + q) * 288u;
  const int ks = kappa / 36, kst = (kappa % 36 + 36 - 9 * ct) % 36;
#pragma unroll
  for (int nt = 0; nt < 9; nt++) {
    const unsigned col = nt < 8 ? (unsigned)(32 * (nt >> 1) + 8 * p + 4 * (nt & 1)) * 2u : (unsigned)(128 + 4 * p) * 2u;
    const u32x4 v = cat4(lds_tr(ra + col), lds_tr(ra + col + 4 * 288));
    st128s(v, c.sws, (unsigned)lane * 16u, (unsigned)WS_W2P + ((unsigned)(ks * 4 + ct) * 324u + (unsigned)(((kst >> 2) * 2 + (kst & 1)) * 18 + ((kst >> 1) & 1) * 9 + nt)) * 1024u);
  }
  wait_lgkm0();
}

constexpr int Q_SLOT = 36864;
constexpr int Q_CT = 4 * Q_SLOT;
DI unsigned q_voff(int p, int lane) {
  if (p < 16) return (unsigned)(lane >> 3) * (D * 2) + (unsigned)(((lane & 7) ^ ((4 * (p & 1) + (lane >> 4)) & 7)) * 16);
  const int ch = 64 * (p - 16) + lane, row = ch / 20, cc = ch % 20;
  return (unsigned)row * (NQKV * 2) + (unsigned)cc * 16u;
}
DI void q_issue(const Ctx& c, srd_t sx, srd_t sw, int rb, int ct, int p, unsigned voff, int t, unsigned ring) {
  if (p < 16) dma16(c.lds0 + ring + (unsigned)p * 1024u, voff, sx, ((unsigned)rb * 128u + (unsigned)p * 8u) * (D * 2) + (unsigned)t * 128u);
  else dma16(c.lds0 + ring + (unsigned)p * 1024u, voff, sw, (unsigned)ct * 288u + (unsigned)t * 64u * (NQKV * 2));
}
DI void p1_loader(Ctx& c, int rb, int ct) {
  const int w = c.w, lane = c.lane, l = w - 8;
  const srd_t sx = mk_srd(c.x, S * D * 2), sw = mk_srd(c.wqkv, D * NQKV * 2);
  const u32x2 nv = entry_load(c);
#pragma unroll
  for (int t = 0; t < 2; t++)
#pragma unroll
    for (int i = 0; i < 3; i++) q_issue(c, sx, sw, rb, ct, w + 12 * i, q_voff(w + 12 * i, lane), t, (unsigned)t * Q_SLOT);
  unsigned vw[5];
#pragma unroll
  for (int i = 0; i < 5; i++) vw[i] = q_voff(16 + l + 4 * i, lane);
#define Q_W_ISSUE(t, ring) do { \
    _Pragma("unroll") for (int i = 0; i < 5; i++) dma16(c.lds0 + (ring) + (unsigned)(16 + l + 4 * i) * 1024u, vw[i], sw, (unsigned)ct * 288u + (unsigned)(t) * 64u * (NQKV * 2)); } while (0)
  Q_W_ISSUE(2, 2u * Q_SLOT);
  wait_vm<8>();
  entry_nonce(c, nv);
  bar();
  wait_vm<5>(); bar();
  Q_W_ISSUE(3, 3u * Q_SLOT);
#pragma unroll 1
  for (int t = 1; t < 6; t++) {
    wait_vm<5>(); bar();
    Q_W_ISSUE(t + 3, (unsigned)((t + 3) & 3) * Q_SLOT);
  }
  wait_vm<5>(); bar();
  wait_vm<0>(); bar();
  if (l == 0) { const int g0 = (144 * ct) >> 6; poll(c, FL_S1 + (lane < 4 ? 4 * rb + lane : min(g0 + (lane & 3), 26)), lane < 8); }
  bar();
  bar();
  bar();
}
template <int NT, int T0>
DI void p1_body(Ctx& c, int rb, int ct) {
  const int w = c.w, lane = c.lane, rg = w & 3; const unsigned lds0 = c.lds0;
  constexpr bool XI = NT == 2;
  const srd_t sx = mk_srd(c.x, S * D * 2);
  const unsigned vx = q_voff(rg, lane);
#define Q_X_ISSUE1(t, ring, i) dma16(lds0 + (ring) + (unsigned)(rg + 4 * (i)) * 1024u, vx, sx, ((unsigned)rb * 128u + (unsigned)(rg + 4 * (i)) * 8u) * (D * 2) + (unsigned)(t) * 128u)
  {
    const srd_t sw = mk_srd(c.wqkv, D * NQKV * 2);
    const u32x2 nv = entry_load(c);
#pragma unroll
    for (int t = 0; t < 2; t++)
#pragma unroll
      for (int i = 0; i < 3; i++) q_issue(c, sx, sw, rb, ct, w + 12 * i, q_voff(w + 12 * i, lane), t, (unsigned)t * Q_SLOT);
    if (XI) { Q_X_ISSUE1(2, 2u * Q_SLOT, 0); Q_X_ISSUE1(2, 2u * Q_SLOT, 1); Q_X_ISSUE1(2, 2u * Q_SLOT, 2); Q_X_ISSUE1(2, 2u * Q_SLOT, 3); wait_vm<7>(); } else wait_vm<3>();
    entry_nonce(c, nv);
  }
  const int tok = lane & 31, h = lane >> 5, blk = (lane >> 4) & 1, q4 = (lane >> 2) & 3, pp = lane & 3;
  const unsigned xrow = lds0 + (unsigned)(rg * 32 + tok) * 128u;
  unsigned xj[4];
#pragma unroll
  for (int j = 0; j < 4; j++) xj[j] = xrow + (unsigned)(((2 * j + h) ^ ((tok >> 1) & 7)) * 16);
  const unsigned wj = lds0 + 16384u + (unsigned)(8 * h + q4) * 320u + (unsigned)(32 * T0 + 16 * blk + 8 * (pp & 1) + 4 * (pp >> 1)) * 2u;
  f32x16 acc[NT];
#pragma unroll
  for (int n = 0; n < NT; n++) acc[n] = f32x16{};
  bar();
  u32x4 wf[4][NT], xf[4];
#pragma unroll
  for (int j = 0; j < 2; j++) {
    xf[j] = lds_u128(xj[j]);
#pragma unroll
    for (int n = 0; n < NT; n++) wf[j][n] = cat4(lds_tr(wj + j * 5120 + n * 64), lds_tr(wj + j * 5120 + n * 64 + 4 * 320));
  }
  unsigned r0 = 0, r1 = Q_SLOT, r3 = 3 * Q_SLOT;
#define Q_PERIOD(t, SYNC, ISSUE, EXTRA) do { \
    if (XI && (SYNC) == 2) wait_vm<4>(); \
    if (XI && (SYNC) == 3) wait_vm<0>(); \
    if (SYNC) bar(); \
    EXTRA; \
    unsigned wa_ = wj + r0, wn_ = wj + r1, xa_[4]; \
    _Pragma("unroll") for (int j = 0; j < 4; j++) { xa_[j] = xj[(j + 2) & 3] + (j < 2 ? r0 : r1); asm volatile("" : "+v"(xa_[j])); } \
    asm volatile("" : "+v"(wa_), "+v"(wn_)); \
    _Pragma("unroll") for (int j = 0; j < 4; j++) { \
      if ((SYNC) == 0 && j == 2) { if (XI) wait_vm<6>(); else wait_vm<0>(); bar(); }        \
      const u32x4 xc = xf[j]; \
      xf[(j + 2) & 3] = lds_u128(xa_[j]); \
      _Pragma("unroll") for (int n = 0; n < NT; n++) { \
        const u32x4 cur = wf[j][n]; \
        const unsigned a_ = (j < 2 ? wa_ : wn_) + ((j + 2) & 3) * 5120 + n * 64; \
        wf[(j + 2) & 3][n] = cat4(lds_tr(a_), lds_tr(a_ + 4 * 320)); \
        acc[n] = mfma32(cur, xc, acc[n]); \
        if (XI && (ISSUE) && n == 0) Q_X_ISSUE1((t) + 3, r3, j); \
        SB(); \
      } \
    } \
    r0 = r1; r1 = r1 == 3u * Q_SLOT ? 0u : r1 + Q_SLOT; r3 = r3 == 3u * Q_SLOT ? 0u : r3 + Q_SLOT; \
  } while (0)
  Q_PERIOD(0, 0, true, (void)0);
#pragma unroll 1
  for (int t = 1; t < 6; t++) Q_PERIOD(t, 2, true, (void)0);
  const int tl = rg * 32 + tok, type = ct >> 2;
  u32x2 stv; u32x4 csv;
  Q_PERIOD(6, 2, false, (void)0);
  Q_PERIOD(7, 3, false, (void)0);
  Q_PERIOD(8, 1, false, (
    { asm volatile("buffer_load_dwordx2 %0, %1, %2, %3 offen sc1" : "=&v"(stv) : "v"((unsigned)tl * 8u), "s"(c.sws), "s"((unsigned)WS_ST1 + (unsigned)rb * 1024u) : "memory");
      asm volatile("buffer_load_dwordx4 %0, %1, %2, %3 offen sc1" : "=&v"(csv) : "v"((unsigned)min(lane, 39) * 16u), "s"(c.sws), "s"((unsigned)WS_CSQ + (unsigned)ct * 576u) : "memory"); }));
  asm volatile("s_waitcnt vmcnt(0)" : "+v"(stv), "+v"(csv) :: "memory");
  if (w == 0 && lane < 40) *(LAS u32x4*)(size_t)(lds0 + Q_CT + (unsigned)lane * 16u) = csv;
  wait_lgkm0(); bar();
  f32x2 st;
  st[0] = __uint_as_float(stv[1]) * (type == 0 ? 0.23570226039551584f * 1.4426950408889634f : 1.0f);
  st[1] = -__uint_as_float(stv[0]) * st[0];
#pragma unroll
  for (int n = 0; n < NT; n++)
#pragma unroll
    for (int hf = 0; hf < 2; hf++) {
      if (T0 + n == 4 && hf == 1) continue;
      const int c0 = 32 * (T0 + n) + 16 * hf;
      const f32x4 ca = lds_f128(lds0 + Q_CT + (unsigned)(c0 + 8 * h) * 4u), cb = lds_f128(lds0 + Q_CT + (unsigned)(c0 + 8 * h) * 4u + 16);
      u32x4 o;
#pragma unroll
      for (int e = 0; e < 4; e++) {
        const float c0v = e < 2 ? ca[2 * e] : cb[2 * e - 4], c1v = e < 2 ? ca[2 * e + 1] : cb[2 * e - 3];
        o[e] = pk2(__builtin_fmaf(acc[n][8 * hf + 2 * e], st[0], c0v * st[1]), __builtin_fmaf(acc[n][8 * hf + 2 * e + 1], st[0], c1v * st[1]));
      }
      *(LAS u32x4*)(size_t)(lds0 + (unsigned)tl * 304u + (unsigned)(c0 + 8 * h) * 2u) = o;
    }
  wait_lgkm0(); bar();
}
DI void p1_qkv(Ctx& c, int rb, int ct) {
  if (c.w >= 8) p1_loader(c, rb, ct); else if (c.w < 4) p1_body<3, 0>(c, rb, ct); else p1_body<2, 3>(c, rb, ct);
  {
    const int tid = c.w * 64 + c.lane, tg = tid >= 384, t = tid - 384 * tg, tk = t / 3, part = t - 3 * tk, type = ct >> 2;
    const unsigned src = c.lds0 + (unsigned)tk * 304u + (unsigned)part * 16u + (unsigned)tg * 144u;
    const unsigned keep = part < 2 ? 0xffffffffu : 0u, padv = part < 2 ? 0u : (type == 0 ? 0x3f800000u : 0x00003f80u);
    const unsigned dst = (unsigned)WS_QKV + ((unsigned)(type * 32 + (ct & 3) * 8) * S + (unsigned)rb * 128u) * 48u;
    u32x4 v[4];
#pragma unroll
    for (int hh = 0; hh < 4; hh++)
#pragma unroll
      for (int e = 0; e < 4; e++) v[hh][e] = *(const LAS unsigned*)(size_t)(src + hh * 36 + e * 4);
#pragma unroll
    for (int hh = 0; hh < 4; hh++) {
      v[hh][1] = (v[hh][1] & keep) | padv; v[hh][2] &= keep; v[hh][3] &= keep;
      st128s(v[hh], c.sws, (unsigned)t * 16u + (unsigned)(4 * tg + hh) * (S * 48), dst);
    }
  }
  wait_lgkm0();
  publish(c, FL_Q + rb * 12 + ct, true);
}

constexpr int L_K = 0, L_V = 24576, L_WO = 49216;
DI void p2_attn(const Ctx& c, int head, int qb) {
  const int w = c.w, lane = c.lane; const unsigned lds0 = c.lds0;
  const unsigned ldsK = lds0 + L_K, ldsV = lds0 + L_V, vl = lane * 16;
  const unsigned kbase = (unsigned)WS_QKV + (unsigned)((32 + head) * S + 256 * (qb - 1)) * 48u;
  const int qq = lane & 31, h = lane >> 5;
  const int q0 = 256 * qb + 32 * w;
  f32x16 cold, cdiag;
#pragma unroll
  for (int r = 0; r < 16; r++) {
    const int kk = 16 * (r >> 3) + 8 * h + (r & 7);
    cold[r] = kk > qq ? 0.f : -1e30f;
    cdiag[r] = kk <= qq ? 0.f : -1e30f;
  }
  const int key = swap23(lane & 31);
  unsigned ka0 = ldsK + (unsigned)(key + 32 * w) * 48 + 16 * h;
  unsigned ka1 = ldsK + (unsigned)(key + 32 * w) * 48 + 32;
  const int blk = (lane >> 4) & 1, q4 = (lane >> 2) & 3, pp = lane & 3;
  unsigned va = ldsV + (unsigned)(32 * w + 8 * h + q4) * 48 + 32 * blk + 8 * pp;
  asm volatile("" : "+v"(ka0), "+v"(ka1), "+v"(va), "+v"(cold), "+v"(cdiag));
  if (qb == 0) {
    const int tid = w * 64 + lane;
    if (tid < 256) {
      const u32x4 z = {0, 0, 0, 0};
      *(LAS u32x4*)(size_t)(ldsK + tid * 48) = z; *(LAS u32x4*)(size_t)(ldsK + tid * 48 + 16) = z; *(LAS u32x4*)(size_t)(ldsK + tid * 48 + 32) = u32x4{0, 0xf1493f80u, 0, 0};
      *(LAS u32x4*)(size_t)(ldsV + tid * 48) = z; *(LAS u32x4*)(size_t)(ldsV + tid * 48 + 16) = z; *(LAS u32x4*)(size_t)(ldsV + tid * 48 + 32) = z;
    }
  }
  if (w == 0) {
    const int hg = head >> 3, l = lane;
    const int ty = l < 2 ? 0 : (l < 6 ? 1 : 2), rbl = l < 2 ? 2 * qb + l : 2 * qb - 2 + ((l - 2) & 3);
    poll(c, FL_Q + rbl * 12 + ty * 4 + hg, l < 10 && rbl >= 0);
  }
  bar();
#pragma unroll
  for (int j = 0; j < 3; j++) {
    const unsigned pc = (unsigned)(w * 3 + j) * 1024u;
    if (qb > 0 || pc >= 12288u) dma16s(ldsK + pc, vl, c.sws, kbase + pc);
  }
  const unsigned qo = (unsigned)WS_QKV + (unsigned)(head * S + q0) * 48u;
  const u32x4 qf0 = ld128s(c.sws, (unsigned)qq * 48u + 16u * h, qo);
  u32x4 qf1 = ld128s(c.sws, (unsigned)qq * 48u + 32u, qo);
  if (h) qf1 = u32x4{0, 0, 0, 0};
  wait_vm<0>(); wait_lgkm0(); bar();
  const f32x16 zero = {};
  u32x4 k0[9], k1[9];
#pragma unroll
  for (int j = 0; j < 9; j++) { k0[j] = lds_u128(ka0 + j * 1536); k1[j] = lds_u128(ka1 + j * 1536); }
  float m = -1e30f;
  {
    f32x16 sA = mfma32(k1[0], qf1, mfma32(k0[0], qf0, cold)), sB;
#pragma unroll
    for (int j = 0; j < 9; j++) {
      f32x16& cur = (j & 1) ? sB : sA; f32x16& nxt = (j & 1) ? sA : sB;
      if (j + 1 < 9) nxt = mfma32(k1[j + 1], qf1, mfma32(k0[j + 1], qf0, j + 1 == 8 ? cdiag : zero));
      SB();
      m = j == 0 ? max16<11>(m, cur) : (j < 8 ? max16<1>(m, cur) : max16<3>(m, cur));
      SB();
    }
  }
  { const auto pr = __builtin_amdgcn_permlane32_swap(__float_as_uint(m), __float_as_uint(m), false, false); m = fmaxf(__uint_as_float(pr[0]), __uint_as_float(pr[1])); }
  if (h == 0) qf1[1] = (qf1[1] & 0xffff0000u) | (pk2(-m, 0.f) & 0xffffu);
  bar();
  f32x16 oacc = {};
  {
    f32x16 sA = mfma32(k1[0], qf1, mfma32(k0[0], qf0, cold)), sB;
    u32x4 v0 = cat4(lds_tr(va), lds_tr(va + 4 * 48)), v1 = cat4(lds_tr(va + 16 * 48), lds_tr(va + 20 * 48));
#pragma unroll
    for (int j = 0; j < 9; j++) {
      f32x16& cur = (j & 1) ? sB : sA; f32x16& nxt = (j & 1) ? sA : sB;
      if (j + 1 < 9) nxt = mfma32(k0[j + 1], qf0, j + 1 == 8 ? cdiag : zero);
      SB();
      u32x4 p0, p1;
      {
        float e0 = cur[0]; asm volatile("" : "+v"(e0));
        p0[0] = pk2(__builtin_amdgcn_exp2f(e0), __builtin_amdgcn_exp2f(cur[1]));
#pragma unroll
        for (int e = 1; e < 4; e++) p0[e] = pk2(__builtin_amdgcn_exp2f(cur[2 * e]), __builtin_amdgcn_exp2f(cur[2 * e + 1]));
        asm volatile("" : "+v"(p0));
      }
      SB();
      if (j + 1 < 9) nxt = mfma32(k1[j + 1], qf1, nxt);
      oacc = mfma32(v0, p0, oacc);
      if (j + 1 < 9) v0 = cat4(lds_tr(va + (j + 1) * 1536), lds_tr(va + (j + 1) * 1536 + 4 * 48));
      SB();
      {
        float e8 = cur[8]; asm volatile("" : "+v"(e8));
        p1[0] = pk2(__builtin_amdgcn_exp2f(e8), __builtin_amdgcn_exp2f(cur[9]));
#pragma unroll
        for (int e = 1; e < 4; e++) p1[e] = pk2(__builtin_amdgcn_exp2f(cur[8 + 2 * e]), __builtin_amdgcn_exp2f(cur[8 + 2 * e + 1]));
        asm volatile("" : "+v"(p1));
      }
      SB();
      oacc = mfma32(v1, p1, oacc);
      if (j + 1 < 9) v1 = cat4(lds_tr(va + (j + 1) * 1536 + 16 * 48), lds_tr(va + (j + 1) * 1536 + 20 * 48));
      SB();
    }
  }
  const float l = __uint_as_float(__builtin_amdgcn_permlane32_swap(__float_as_uint(oacc[10]), __float_as_uint(oacc[10]), false, false)[0]);
  const float inv = __builtin_amdgcn_rcpf(l);
  const u32x4 om = {pk2(oacc[0] * inv, oacc[1] * inv), pk2(oacc[2] * inv, oacc[3] * inv), pk2(oacc[4] * inv, oacc[5] * inv), pk2(oacc[6] * inv, oacc[7] * inv)};
  st128s(om, c.sws, (unsigned)lane * 16u, (unsigned)WS_OP + (unsigned)((q0 >> 5) * 32 + head) * 1024u);
  __builtin_amdgcn_raw_buffer_store_b32(pk2(oacc[8] * inv, oacc[9] * inv), c.sws, h == 0 ? (unsigned)qq * 4u : 0x7ff00000u, (unsigned)WS_OL + (unsigned)(head * S + q0) * 4u, SC1);
  publish(c, FL_A + qb * 32 + head, true);
}

DI void p3_prefetch(const Ctx& c, int ct) {
  const int l = c.w - 8, lane = c.lane;
  const srd_t sw = mk_srd(c.wo, D * D * 2);
  unsigned vw[3];
#pragma unroll
  for (int i = 0; i < 3; i++) { const int ch = 64 * i + lane; vw[i] = (unsigned)(ch / 12) * (D * 2) + (unsigned)(ch % 12) * 16u; }
#pragma unroll 1
  for (int gi = 0; gi < 9; gi++) {
    const int grp = l * 9 + gi;
#pragma unroll
    for (int i = 0; i < 3; i++) dma16(c.lds0 + L_WO + (unsigned)(grp * 3 + i) * 1024u, vw[i], sw, (unsigned)grp * 16u * (D * 2) + (unsigned)ct * 192u);
  }
}
DI void p2_loader(const Ctx& c, int head, int qb) {
  const int xcd = c.xcd, bi = c.bi, l = c.w - 8;
  const unsigned vbase = (unsigned)WS_QKV + (unsigned)((64 + head) * S + 256 * (qb - 1)) * 48u;
  bar();
#pragma unroll
  for (int j = 0; j < 6; j++) {
    const unsigned pc = (unsigned)(l * 6 + j) * 1024u;
    if (qb > 0 || pc >= 12288u) dma16s(c.lds0 + L_V + pc, c.lane * 16u, c.sws, vbase + pc);
  }
  bar();
  wait_vm<0>(); bar();
  if (bi < 24) {
    p3_prefetch(c, bi >> 2);
  } else {
    const srd_t sw2 = mk_srd(c.w2, D * F * 2);
    const int j = bi - 24, ks = xcd & 3, ct2 = j >> 1, part = (xcd >> 2) * 2 + (j & 1), row0 = (ks * 36 + part * 9) * 32;
#pragma unroll
    for (int i = 0; i < 4; i++) {
      const int ln = (l * 4 + i) * 64 + c.lane, row = ln / 3;
      touch128(c.lds0 + L_DUMP, ln < 864 ? (unsigned)row * (D * 2) + (unsigned)(ln - 3 * row) * 128u : 0x7ff00000u, sw2, (unsigned)row0 * (D * 2) + (((unsigned)ct2 * 288u) & ~127u));
    }
  }
  publish(c, FL_A + qb * 32 + head, false);
}
template <int KQ>
DI void p3_body(const Ctx& c, int rb, int ct) {
  constexpr int kq = KQ;
  const int lane = c.lane, rg = c.w & 1; const unsigned lds0 = c.lds0;
  const int qt = rb * 2 + rg, tok = lane & 31, h = lane >> 5;
  const int row = qt * 32 + tok;
  const u32x4 xr0 = *(const u32x4*)(c.x + (size_t)row * D + ct * 96 + 16 * kq + 8 * h);
  const u32x4 xr1 = *(const u32x4*)(c.x + (size_t)row * D + ct * 96 + 16 * (kq < 2 ? kq + 4 : kq) + 8 * h);
  unsigned hso = rfl((unsigned)WS_HP + (unsigned)(qt * 36 + ct * 6) * 1024u);
  asm volatile("" : "+s"(hso));
  const int blk = (lane >> 4) & 1, q4 = (lane >> 2) & 3, pp = lane & 3;
  const unsigned colb = lds0 + L_WO + (unsigned)(16 * blk + 8 * (pp & 1) + 4 * (pp >> 1)) * 2u;
  unsigned ah = colb + (unsigned)(18 * 9 * kq + 4 * h + q4) * 192u;
  unsigned al = colb + (unsigned)(18 * (4 * h + (q4 >> 1)) + 16 + (q4 & 1)) * 192u;
  asm volatile("" : "+v"(ah), "+v"(al));
#define P3_W(s, n) ((kq < 3 || (s) < 5) ? cat4(lds_tr(ah + (s) * 3456 + (n) * 64), lds_tr(ah + (s) * 3456 + 1536 + (n) * 64)) \
                                        : cat4(lds_tr(al + ((s) - 5) * 27648 + (n) * 64), lds_tr(al + ((s) - 5) * 27648 + 6912 + (n) * 64)))
  bar();
  u32x4 of[9];
  {
    const unsigned so = (unsigned)WS_OP + (unsigned)(qt * 32 + kq * 9) * 1024u;
#pragma unroll
    for (int s = 0; s < (kq < 3 ? 9 : 5); s++) of[s] = ld128s(c.sws, (unsigned)lane * 16u + s * 1024u, so);
    if (kq == 3) {
#pragma unroll
      for (int G = 0; G < 4; G++)
#pragma unroll
        for (int i = 0; i < 4; i++)
          of[5 + G][i] = __builtin_amdgcn_raw_buffer_load_b32(c.sws, (unsigned)((4 * h + i) * S + tok) * 4u, (unsigned)WS_OL + (unsigned)(8 * G * S + qt * 32) * 4u, SC1);
    }
  }
  u32x4 wf[3][3];
#pragma unroll
  for (int s = 0; s < 2; s++)
#pragma unroll
    for (int n = 0; n < 3; n++) wf[s][n] = P3_W(s, n);
  SB();
  f32x16 acc[3] = {};
#pragma unroll
  for (int s = 0; s < 9; s++) {
#pragma unroll
    for (int n = 0; n < 3; n++) {
      if (s + 2 < 9) wf[(s + 2) % 3][n] = P3_W(s + 2, n);
      acc[n] = mfma32(wf[s % 3][n], of[s], acc[n]);
    }
    SB();
  }
  wait_lgkm0(); bar();
  const unsigned xb = lds0 + L_WO + (unsigned)rg * 49152u + (unsigned)lane * 16u;
#pragma unroll
  for (int f = 0; f < 6; f++) {
    if ((f & 3) == kq) continue;
    const int r8 = 8 * (f & 1);
    lds_w128(xb + (unsigned)(f * 4 + kq) * 2048u, f32x4{acc[f >> 1][r8], acc[f >> 1][r8 + 1], acc[f >> 1][r8 + 2], acc[f >> 1][r8 + 3]});
    lds_w128(xb + (unsigned)(f * 4 + kq) * 2048u + 1024u, f32x4{acc[f >> 1][r8 + 4], acc[f >> 1][r8 + 5], acc[f >> 1][r8 + 6], acc[f >> 1][r8 + 7]});
  }
  wait_lgkm0(); bar();
  float ssum = 0, ssq = 0;
#pragma unroll
  for (int i = 0; i < (kq < 2 ? 2 : 1); i++) {
    const int f = kq + 4 * i, r8 = 8 * (f & 1);
    f32x4 a = {acc[f >> 1][r8], acc[f >> 1][r8 + 1], acc[f >> 1][r8 + 2], acc[f >> 1][r8 + 3]}, cc = {acc[f >> 1][r8 + 4], acc[f >> 1][r8 + 5], acc[f >> 1][r8 + 6], acc[f >> 1][r8 + 7]};
#pragma unroll
    for (int k = 0; k < 4; k++)
      if (k != kq) { a += lds_f128(xb + (unsigned)(f * 4 + k) * 2048u); cc += lds_f128(xb + (unsigned)(f * 4 + k) * 2048u + 1024u); }
    const u32x4 xr = i ? xr1 : xr0;
    u32x4 o;
    o[0] = pk2(bflo(xr[0]) + a[0], bfhi(xr[0]) + a[1]); o[1] = pk2(bflo(xr[1]) + a[2], bfhi(xr[1]) + a[3]);
    o[2] = pk2(bflo(xr[2]) + cc[0], bfhi(xr[2]) + cc[1]); o[3] = pk2(bflo(xr[3]) + cc[2], bfhi(xr[3]) + cc[3]);
    st128s(o, c.sws, (unsigned)lane * 16u, hso + (unsigned)f * 1024u);
#pragma unroll
    for (int e = 0; e < 4; e++) { ssum = dot2(o[e], 0x3f803f80u, ssum); ssq = dot2(o[e], o[e], ssq); }
  }
  { const auto a = __builtin_amdgcn_permlane32_swap(__float_as_uint(ssum), __float_as_uint(ssum), false, false); ssum = __uint_as_float(a[0]) + __uint_as_float(a[1]);
    const auto b2 = __builtin_amdgcn_permlane32_swap(__float_as_uint(ssq), __float_as_uint(ssq), false, false); ssq = __uint_as_float(b2[0]) + __uint_as_float(b2[1]); }
  const unsigned stb = lds0 + L_WO + 98304u;
  if (h == 0) *(LAS f32x2*)(size_t)(stb + (unsigned)(rg * 32 + tok) * 32u + kq * 8u) = f32x2{ssum, ssq};
  wait_lgkm0(); bar();
  if (c.w == 0) {
    const f32x4 a = lds_f128(stb + lane * 32u), cc = lds_f128(stb + lane * 32u + 16);
    const u32x2 v = {__float_as_uint((a[0] + a[2]) + (cc[0] + cc[2])), __float_as_uint((a[1] + a[3]) + (cc[1] + cc[3]))};
    __builtin_amdgcn_raw_buffer_store_b64(v, c.sws, (unsigned)lane * 8u, (unsigned)WS_ST2 + (unsigned)(rb * 6 + ct) * 512u, SC1);
  }
}
DI void p3_wo(const Ctx& c, int rb, int ct) {
  const int kq = c.w >> 1;
  if (kq == 0) p3_body<0>(c, rb, ct); else if (kq == 1) p3_body<1>(c, rb, ct); else if (kq == 2) p3_body<2>(c, rb, ct); else p3_body<3>(c, rb, ct);
  publish(c, FL_H + rb * 6 + ct, true);
}

constexpr float GC0 = -2.0f * 0.7978845608028654f * 1.4426950408889634f, GC1 = GC0 * 0.044715f;
DI f32x2 gelu2(f32x2 x) {
  const f32x2 t = x * (GC1 * (x * x) + GC0);
  f32x2 e; e[0] = __builtin_amdgcn_exp2f(t[0]); e[1] = __builtin_amdgcn_exp2f(t[1]);
  const f32x2 d = e + 1.0f;
  f32x2 r; r[0] = __builtin_amdgcn_rcpf(d[0]); r[1] = __builtin_amdgcn_rcpf(d[1]);
  return x * r;
}
struct G2 { float x0, x1, t0, t1; };
DI void g2_s1(G2& g, float a0, float a1, float p0, float p1) {
  asm volatile("v_add_f32_e32 %0, %4, %6\n\tv_add_f32_e32 %1, %5, %7\n\tv_mul_f32_e32 %2, %0, %0\n\tv_mul_f32_e32 %3, %1, %1"
               : "=&v"(g.x0), "=&v"(g.x1), "=&v"(g.t0), "=&v"(g.t1) : "v"(a0), "v"(a1), "v"(p0), "v"(p1));
}
DI void g2_s2(G2& g, float vc1) {
  asm volatile("v_fmaak_f32 %0, %0, %4, 0xc0135761\n\tv_fmaak_f32 %1, %1, %4, 0xc0135761\n\tv_mul_f32_e32 %0, %2, %0\n\tv_mul_f32_e32 %1, %3, %1"
               : "+v"(g.t0), "+v"(g.t1) : "v"(g.x0), "v"(g.x1), "v"(vc1));
}
DI void g2_s3(G2& g) {
  asm volatile("v_exp_f32_e32 %0, %0\n\tv_exp_f32_e32 %1, %1\n\tv_add_f32_e32 %0, 1.0, %0\n\tv_add_f32_e32 %1, 1.0, %1" : "+v"(g.t0), "+v"(g.t1));
}
DI unsigned g2_s4(G2& g) {
  unsigned o;
  asm volatile("v_rcp_f32_e32 %1, %1\n\tv_rcp_f32_e32 %2, %2\n\tv_mul_f32_e32 %1, %3, %1\n\tv_mul_f32_e32 %2, %4, %2\n\tv_cvt_pk_bf16_f32 %0, %1, %2"
               : "=v"(o), "+v"(g.t0), "+v"(g.t1) : "v"(g.x0), "v"(g.x1));
  return o;
}

constexpr int F_SLOT = 36864, H_SLOT = 18432, F1_XCH = 6 * H_SLOT, F1_UV = F1_XCH + 32768, F_LDS = F1_UV + 1152;
DI unsigned f_stream(int fc, int g) {
  return g < 18 ? (unsigned)WS_W1P + (unsigned)(fc * 18 + g) * H_SLOT : (unsigned)WS_W2P + (unsigned)(fc * 18 + g - 18) * H_SLOT;
}
DI void f_issue(const Ctx& c, int fc, int g) {
  const int l = c.w - 8;
  const unsigned o = (unsigned)(l < 2 ? 5 * l : 2 + 4 * l) * 1024u, vl = c.lane * 16, gb = f_stream(fc, g) + o, lb = c.lds0 + (unsigned)(g % 6) * H_SLOT + o;
#pragma unroll
  for (int i = 0; i < 4; i++) dma16s(lb + i * 1024u, vl, c.sws, gb + i * 1024u);
  if (l < 2) dma16s(lb + 4096u, vl, c.sws, gb + 4096u);
}
template <int N> DI void f_wait(int l) { if (l < 2) wait_vm<5 * N>(); else wait_vm<4 * N>(); }
constexpr int F_FIN = 4 * H_SLOT;
DI void p6_finish(const Ctx& c, int rb, int fc) {
  const int w = c.w, lane = c.lane, ks = fc >> 2, ct = fc & 3, tk = lane & 15, g = lane >> 4;
  const bool pair = w < 8;
  const int T = w & 3, rt = pair ? w >> 2 : w - 8;
  const int col = ct * 144 + (pair ? 32 * T + 8 * g : 128 + 4 * g), row = rb * 128 + ks * 32 + rt * 16 + tk;
  u32x4 hv = {}, bv = {};
  const unsigned ho = (unsigned)(((col >> 3) & 1) * 32 + rt * 16 + tk) * 16u + (unsigned)(col >> 4) * 1024u + (unsigned)(col & 7) * 2u, hb = (unsigned)WS_HP + (unsigned)(rb * 4 + ks) * 36u * 1024u;
  if (w < 10) {
    if (pair) { hv = ld128s(c.sws, ho, hb); bv = *(const u32x4*)(c.b2 + col); }
    else { const u32x2 a = __builtin_amdgcn_raw_buffer_load_b64(c.sws, ho, hb, SC1), b2 = *(const u32x2*)(c.b2 + col); hv[0] = a[0]; hv[1] = a[1]; bv[0] = b2[0]; bv[1] = b2[1]; }
  }
  if (w == 10) poll(c, FL_P + (rb * 4 + ct) * 4 + (lane & 3), lane < 4);
  bar();
  if (w >= 10) return;
  const unsigned xg = (unsigned)WS_XCH + (unsigned)((rb * 4 + ct) * 16 + ks * 4) * 9216u + (unsigned)rt * 4608u;
  const unsigned fin = c.lds0 + F_FIN + (unsigned)rt * 9216u + (unsigned)lane * 16u;
  if (pair) {
    u32x4 pv[3];
#pragma unroll
    for (int i = 0; i < 3; i++) pv[i] = ld128s(c.sws, (unsigned)T * 1024u + (unsigned)lane * 16u, xg + (unsigned)(i + (i >= ks)) * 9216u);
    f32x4 a = lds_f128(fin + (unsigned)T * 2048u), b2 = lds_f128(fin + (unsigned)T * 2048u + 1024u);
#pragma unroll
    for (int i = 0; i < 3; i++) { a += f32x4{bflo(pv[i][0]), bfhi(pv[i][0]), bflo(pv[i][1]), bfhi(pv[i][1])}; b2 += f32x4{bflo(pv[i][2]), bfhi(pv[i][2]), bflo(pv[i][3]), bfhi(pv[i][3])}; }
    u32x4 r;
    r[0] = pk2(bflo(hv[0]) + (a[0] + bflo(bv[0])), bfhi(hv[0]) + (a[1] + bfhi(bv[0])));
    r[1] = pk2(bflo(hv[1]) + (a[2] + bflo(bv[1])), bfhi(hv[1]) + (a[3] + bfhi(bv[1])));
    r[2] = pk2(bflo(hv[2]) + (b2[0] + bflo(bv[2])), bfhi(hv[2]) + (b2[1] + bfhi(bv[2])));
    r[3] = pk2(bflo(hv[3]) + (b2[2] + bflo(bv[3])), bfhi(hv[3]) + (b2[3] + bfhi(bv[3])));
    *(u32x4*)(c.out + (size_t)row * D + col) = r;
  } else {
    u32x2 pv[3];
#pragma unroll
    for (int i = 0; i < 3; i++) pv[i] = __builtin_amdgcn_raw_buffer_load_b64(c.sws, 4096u + (unsigned)lane * 8u, xg + (unsigned)(i + (i >= ks)) * 9216u, SC1);
    f32x4 a = lds_f128(fin + 8192u);
#pragma unroll
    for (int i = 0; i < 3; i++) a += f32x4{bflo(pv[i][0]), bfhi(pv[i][0]), bflo(pv[i][1]), bfhi(pv[i][1])};
    u32x2 r;
    r[0] = pk2(bflo(hv[0]) + (a[0] + bflo(bv[0])), bfhi(hv[0]) + (a[1] + bfhi(bv[0])));
    r[1] = pk2(bflo(hv[1]) + (a[2] + bflo(bv[1])), bfhi(hv[1]) + (a[3] + bfhi(bv[1])));
    *(u32x2*)(c.out + (size_t)row * D + col) = r;
  }
}
DI void p45_loader(const Ctx& c, int rb, int fc) {
  const int l = c.w - 8, lane = c.lane, ks = fc >> 2, ct = fc & 3;
  f_issue(c, fc, 2); f_issue(c, fc, 3); f_issue(c, fc, 4);
  bar();
  u32x2 fd[4] = {};
#pragma unroll 1
  for (int g = 0; g < 36; g++) {
    if (g == 18) { bar(); bar(); bar(); }
    if (g == 8 && l == 0) poll(c, FL_W2 + (ks * 4 + ct) * 4 + (lane & 3), lane < 4);
    if (g == 20 && l == 0) poll(c, FL_H1 + rb * 16 + ks * 4 + (lane & 3), lane < 4);
    if (g < 32) f_wait<3>(l); else if (g == 32) f_wait<2>(l); else if (g == 33) f_wait<1>(l); else wait_vm<0>();
    bar();
    if (g == 8 || g == 10) {
      const unsigned ln = (unsigned)((l * 2 + ((g >> 1) & 1)) * 64 + lane);
      touch128(c.lds0 + L_DUMP, ln < 324u ? ln * 128u : 0x7ff00000u, c.sws, f_stream(fc, 18) + (unsigned)(c.bi & 7) * 41472u);
    }
    if (g == 6 && l == 3 && c.b == 0) {
#pragma unroll
      for (int i = 0; i < 4; i++) asm volatile("buffer_load_dwordx2 %0, %1, %2, 0 offen sc1" : "=&v"(fd[i]) : "v"((unsigned)(FL_D + i * 64 + lane) * 128u), "s"(c.sws) : "memory");
    }
    if (g == 12 && l == 3 && c.b == 0) {
      asm volatile("" : "+v"(fd[0]), "+v"(fd[1]), "+v"(fd[2]), "+v"(fd[3]));
      const unsigned n0 = (unsigned)c.nonce, n1 = (unsigned)(c.nonce >> 32);
      if (!__all(fd[0][0] == n0 && fd[0][1] == n1 && fd[1][0] == n0 && fd[1][1] == n1 && fd[2][0] == n0 && fd[2][1] == n1 && fd[3][0] == n0 && fd[3][1] == n1)) {
#pragma unroll 1
        for (int i = 0; i < 4; i++) poll(c, FL_D + i * 64 + lane, true);
      }
      if (lane == 0) __hip_atomic_store((unsigned long long*)(c.ws + FL_N * 128), c.nonce, __ATOMIC_RELAXED, __HIP_MEMORY_SCOPE_AGENT);
    }
    if (g + 5 < 36) f_issue(c, fc, g + 5);
  }
  bar(); bar();
  bar();
  p6_finish(c, rb, fc);
}
template <int KH>
DI void p45_ffn(const Ctx& c, int rb, int fc) {
  constexpr int kh = KH;
  const int w = c.w, lane = c.lane, rg = w & 3; const unsigned lds0 = c.lds0;
  const unsigned vl = lane * 16;
  const int ks = fc >> 2, ct = fc & 3;
  const int tok = lane & 31, h = lane >> 5;
  if (threadIdx.x < 288) *(LAS float*)(size_t)(lds0 + F1_UV + threadIdx.x * 4) = __uint_as_float((unsigned)c.b1[fc * 288 + threadIdx.x] << 16);
  if (w == 0) poll(c, FL_H + (2 * rb + lane / 6) * 6 + lane % 6, lane < 12);
  wait_lgkm0(); bar();
  u32x2 stv[6];
#pragma unroll
  for (int e = 0; e < 6; e++)
    asm volatile("buffer_load_dwordx2 %0, %1, %2, %3 offen offset:%4 sc1" : "=&v"(stv[e]) : "v"((unsigned)tok * 8u), "s"(c.sws),
                 "s"((unsigned)WS_ST2 + (unsigned)((2 * rb + (rg >> 1)) * 6) * 512u + (unsigned)(rg & 1) * 256u), "i"(e * 512) : "memory");
  u32x4 xf[18];
  {
    const unsigned so = (unsigned)WS_HP + ((unsigned)(rb * 4 + rg) * 36u + (unsigned)kh * 18u) * 1024u;
#pragma unroll
    for (int s = 0; s < 18; s++)
      asm volatile("buffer_load_dwordx4 %0, %1, %2, %3 offen offset:%4 sc1" : "=&v"(xf[s]) : "v"(vl), "s"(c.sws), "s"(so + (s >> 2) * 4096u), "i"((s & 3) * 1024) : "memory");
  }
  float ra, rc;
  {
    asm volatile("s_waitcnt vmcnt(18)" : "+v"(stv[0]), "+v"(stv[1]), "+v"(stv[2]), "+v"(stv[3]), "+v"(stv[4]), "+v"(stv[5]) :: "memory");
    float s = 0, q = 0;
#pragma unroll
    for (int e = 0; e < 6; e++) { s += __uint_as_float(stv[e][0]); q += __uint_as_float(stv[e][1]); }
    const float mean = s * (1.0f / D), var = fmaxf(q * (1.0f / D) - mean * mean, 0.f), rstd = rsqrtf(var + LN_EPS);
    ra = rstd; rc = -mean * rstd;
  }
  const unsigned rd = lds0 + (unsigned)kh * 9216u + vl;
  const unsigned hvo = (unsigned)(tok >> 4) * 1024u + (unsigned)h * 256u + (unsigned)(tok & 15) * 16u;
  const unsigned hbase = (unsigned)WS_H1P + ((unsigned)(rb * 4 + rg) * 144u + (unsigned)fc * 9u) * 2048u;
  const unsigned bia = lds0 + F1_UV + (unsigned)h * 32u;
  {
#pragma unroll
    for (int s = 0; s < 18; s++) {
      asm volatile("s_waitcnt vmcnt(%1)" : "+v"(xf[s]) : "i"(17 - s) : "memory");
#pragma unroll
      for (int e = 0; e < 4; e++) {
        const f32x2 v = f32x2{bflo(xf[s][e]), bfhi(xf[s][e])} * ra + rc;
        xf[s][e] = pk2(v[0], v[1]);
      }
      asm volatile("" : "+v"(xf[s]));
      SB();
    }
  }
  constexpr int PF = 3;
  u32x4 wf[PF];
  const f32x16 zero = {};
#define F1_BIAS(ACC, ba_) do { const f32x4 b0_ = lds_f128(ba_), b1_ = lds_f128(ba_ + 16), b2_ = lds_f128(ba_ + 64), b3_ = lds_f128(ba_ + 80); \
    ACC = f32x16{b0_[0], b0_[1], b0_[2], b0_[3], b1_[0], b1_[1], b1_[2], b1_[3], b2_[0], b2_[1], b2_[2], b2_[3], b3_[0], b3_[1], b3_[2], b3_[3]}; } while (0)
  f32x16 accG, accP, accQ;
  F1_BIAS(accG, bia + (unsigned)(1 - kh) * 128u);
  float vc1 = GC1; asm volatile("" : "+v"(vc1));
  unsigned r0 = 0;
#define F_NEXT(r) ((r) == 5u * H_SLOT ? 0u : (r) + H_SLOT)
  const unsigned xwr = lds0 + F1_XCH + (unsigned)(kh ^ 1) * 16384u + (unsigned)rg * 4096u + vl;
  const unsigned xrd = lds0 + F1_XCH + (unsigned)kh * 16384u + (unsigned)rg * 4096u + vl;
  f32x4 pp[4] = {};
#define F1_STAGE(q, G, SRC, R0, O) do { const int p_ = (q) >> 2, st_ = (q) & 3, r_ = (R0) + 2 * p_; \
      if (st_ == 0) g2_s1(G, SRC[r_], SRC[r_ + 1], pp[r_ >> 2][r_ & 3], pp[r_ >> 2][(r_ & 3) + 1]); \
      if (st_ == 1) g2_s2(G, vc1); if (st_ == 2) g2_s3(G); if (st_ == 3) O[p_] = g2_s4(G); } while (0)
#define F_HALF_BEGIN(s) do { if ((s) % 9 == 0) { bar(); sb = rd + r0; r0 = F_NEXT(r0); sn = rd + r0; asm volatile("" : "+v"(sb), "+v"(sn)); } } while (0)
#define F_FRAG(s) lds_u128((s) % 9 + PF < 9 ? sb + ((s) % 9 + PF) * 1024 : sn + ((s) % 9 + PF - 9) * 1024)
#define F1_PERIOD(t, ACC, PREV, GELU, HALF) do { \
    unsigned sb, sn; \
    u32x4 o; G2 g; \
    const int gt_ = ((t) - 2 - (HALF)) + kh;      \
    _Pragma("unroll") for (int s = 0; s < 18; s++) { \
      F_HALF_BEGIN(s); \
      if (s == 0 && (t) == 0) { _Pragma("unroll") for (int i = 0; i < PF; i++) wf[i] = lds_u128(sb + i * 1024); } \
      if (s == 0 && (GELU) && (HALF) == 0) { _Pragma("unroll") for (int e = 0; e < 4; e++) pp[e] = lds_f128(xrd + e * 1024); SB(); } \
      const u32x4 cur = wf[s % PF]; \
      wf[s % PF] = F_FRAG(s); \
      ACC = mfma32(cur, xf[s], (s == 0 && (HALF) == 1) ? zero : ACC); \
      if ((HALF) == 1 && s % 4 == 1 && s / 4 < 4) { const int e = s / 4; lds_w128(xwr + e * 1024, f32x4{accG[4 * e], accG[4 * e + 1], accG[4 * e + 2], accG[4 * e + 3]}); } \
      if (GELU) { \
        if (s >= 2 && s <= 17) F1_STAGE(s - 2, g, PREV, 8 * (HALF), o); \
        if (s == 17) st128s(o, c.sws, hvo + (HALF) * 512u, hbase + (unsigned)gt_ * 2048u); \
      } \
      if ((HALF) == 1 && s == 15) {     \
        const int nt_ = (t) + 1 == 8 ? 8 : (t) + 1 + (1 - kh); \
        if ((t) + 1 == 8 && kh == 0) accG = zero; \
        else { unsigned ba_ = bia + (unsigned)nt_ * 128u; asm volatile("" : "+v"(ba_)); F1_BIAS(accG, ba_); } \
      } \
      SB(); \
    } \
    asm volatile("" :: "v"(ACC)); \
  } while (0)
  const unsigned hoff0 = (unsigned)WS_H1P + ((unsigned)(rb * 4 + rg) * 144u + (unsigned)ks * 36u) * 2048u;
  u32x4 hA[4], hB[4], hC[4];
#define HLOAD1(dst, t, i) do { const int p_ = 4 * (t) + 2 * kh + ((i) >> 1) + 9 * ct; const unsigned so_ = hoff0 + (unsigned)(p_ >= 36 ? p_ - 36 : p_) * 2048u; \
    if (((i) & 1) == 0) asm volatile("buffer_load_dwordx4 %0, %1, %2, %3 offen sc1" : "=&v"(dst[i]) : "v"(vl), "s"(c.sws), "s"(so_) : "memory"); \
    else asm volatile("buffer_load_dwordx4 %0, %1, %2, %3 offen offset:1024 sc1" : "=&v"(dst[i]) : "v"(vl), "s"(c.sws), "s"(so_) : "memory"); } while (0)
#define HWAIT(N, dst) asm volatile("s_waitcnt vmcnt(" #N ")" : "+v"(dst[0]), "+v"(dst[1]), "+v"(dst[2]), "+v"(dst[3]) :: "memory")
  {
    F1_PERIOD(0, accG, accQ, false, 0);
    F1_PERIOD(1, accP, accQ, false, 1);
    F1_PERIOD(2, accG, accP, true, 0);
    F1_PERIOD(3, accQ, accP, true, 1);
    F1_PERIOD(4, accG, accQ, true, 0);
    F1_PERIOD(5, accP, accQ, true, 1);
    F1_PERIOD(6, accG, accP, true, 0);
    F1_PERIOD(7, accQ, accP, true, 1);
    {
      wait_vm<2>();
      unsigned sb, sn;
      u32x4 o, o2; G2 g, g2;
      const int gt_ = 6 + kh;
#pragma unroll
      for (int s = 0; s < 18; s++) {
        F_HALF_BEGIN(s);
        if (s == 0) {
#pragma unroll
          for (int e = 0; e < 4; e++) pp[e] = lds_f128(xrd + e * 1024);
          SB();
        }
        const u32x4 cur = wf[s % PF];
        wf[s % PF] = F_FRAG(s);
        accG = mfma32(cur, xf[s], accG);
        if (s == 10 + KH) HLOAD1(hA, 0, 0); if (s == 12 + KH) HLOAD1(hA, 0, 1); if (s == 14 + KH) HLOAD1(hA, 0, 2); if (s == 16 + KH) HLOAD1(hA, 0, 3);
        if (s >= 2 && s <= 17) { F1_STAGE(s - 2, g, accQ, 0, o); F1_STAGE(s - 2, g2, accQ, 8, o2); }
        if (s == 17) { st128s(o, c.sws, hvo, hbase + (unsigned)gt_ * 2048u); st128s(o2, c.sws, hvo + 512u, hbase + (unsigned)gt_ * 2048u); }
        SB();
      }
      bar();
      lds_w128(xwr, kh ? f32x4{accG[0], accG[1], accG[2], accG[3]} : f32x4{accG[8], accG[9], accG[10], accG[11]});
      lds_w128(xwr + 1024, kh ? f32x4{accG[4], accG[5], accG[6], accG[7]} : f32x4{accG[12], accG[13], accG[14], accG[15]});
      wait_lgkm0(); bar();
      const f32x4 q0 = lds_f128(xrd), q1 = lds_f128(xrd + 1024);
      const f32x4 k0 = (kh ? f32x4{accG[8], accG[9], accG[10], accG[11]} : f32x4{accG[0], accG[1], accG[2], accG[3]}) + q0;
      const f32x4 k1 = (kh ? f32x4{accG[12], accG[13], accG[14], accG[15]} : f32x4{accG[4], accG[5], accG[6], accG[7]}) + q1;
      const f32x2 z0 = gelu2(f32x2{k0[0], k0[1]}), z1 = gelu2(f32x2{k0[2], k0[3]}), z2 = gelu2(f32x2{k1[0], k1[1]}), z3 = gelu2(f32x2{k1[2], k1[3]});
      o = u32x4{pk2(z0[0], z0[1]), pk2(z1[0], z1[1]), pk2(z2[0], z2[1]), pk2(z3[0], z3[1])};
      st128s(o, c.sws, hvo + (unsigned)kh * 512u, hbase + 8u * 2048u);
    }
  }
  publish(c, FL_H1 + rb * 16 + fc, true);
  HLOAD1(hB, 1, 0); HLOAD1(hB, 1, 1); HLOAD1(hB, 1, 2); HLOAD1(hB, 1, 3);
  f32x4 acc[2][9];
#pragma unroll
  for (int a = 0; a < 2; a++)
#pragma unroll
    for (int n = 0; n < 9; n++) acc[a][n] = f32x4{0, 0, 0, 0};
#define F2_PERIOD(t, HC, HN, HM, VMW, MODE) do { \
    HWAIT(VMW, HC); \
    unsigned sb, sn; \
    _Pragma("unroll") for (int s = 0; s < 18; s++) { \
      F_HALF_BEGIN(s); \
      const u32x4 cur = wf[s % PF]; \
      wf[s % PF] = F_FRAG(s); \
      const int k = s / 9, nt = s % 9; \
      acc[0][nt] = mfma16(cur, HC[2 * k], acc[0][nt]); \
      acc[1][nt] = mfma16(cur, HC[2 * k + 1], acc[1][nt]); \
      if ((MODE) == 3) { if (s == KH) HLOAD1(HM, (t) + 1, 0); if (s == 2 + KH) HLOAD1(HM, (t) + 1, 1); if (s == 4 + KH) HLOAD1(HM, (t) + 1, 2); if (s == 6 + KH) HLOAD1(HM, (t) + 1, 3); } \
      if ((MODE) == 0 || (MODE) == 3) { if (s == 9 + KH) HLOAD1(HN, (t) + 2, 0); if (s == 11 + KH) HLOAD1(HN, (t) + 2, 1); if (s == 13 + KH) HLOAD1(HN, (t) + 2, 2); if (s == 15 + KH) HLOAD1(HN, (t) + 2, 3); } \
      SB(); \
    } \
  } while (0)
  F2_PERIOD(0, hA, hC, hC, 4, 1);
  F2_PERIOD(1, hB, hA, hC, 0, 3);
  F2_PERIOD(2, hC, hB, hB, 4, 0);
  F2_PERIOD(3, hA, hC, hC, 4, 0);
  F2_PERIOD(4, hB, hA, hA, 4, 0);
  F2_PERIOD(5, hC, hB, hB, 4, 0);
  F2_PERIOD(6, hA, hC, hC, 4, 0);
  F2_PERIOD(7, hB, hA, hA, 4, 1);
  F2_PERIOD(8, hC, hB, hB, 0, 1);
  wait_lgkm0(); bar();
  const unsigned xb = lds0 + (unsigned)rg * 18432u + vl;
#pragma unroll
  for (int n = 0; n < 9; n++) lds_w128(xb + (unsigned)(kh ^ 1) * 9216u + n * 1024, kh ? acc[0][n] : acc[1][n]);
  wait_lgkm0(); bar();
  f32x4 o[9];
#pragma unroll
  for (int n = 0; n < 9; n++) o[n] = lds_f128(xb + kh * 9216u + n * 1024) + (kh ? acc[1][n] : acc[0][n]);
  const unsigned xg = (unsigned)WS_XCH + (unsigned)((rb * 4 + ct) * 16) * 9216u + (unsigned)kh * 4608u;
  if (rg != ks) {
    const unsigned d = xg + (unsigned)(rg * 4 + ks) * 9216u;
#pragma unroll
    for (int T = 0; T < 4; T++)
      st128s(u32x4{pk2(o[2 * T][0], o[2 * T][1]), pk2(o[2 * T][2], o[2 * T][3]), pk2(o[2 * T + 1][0], o[2 * T + 1][1]), pk2(o[2 * T + 1][2], o[2 * T + 1][3])}, c.sws, vl + T * 1024u, d);
    __builtin_amdgcn_raw_buffer_store_b64(u32x2{pk2(o[8][0], o[8][1]), pk2(o[8][2], o[8][3])}, c.sws, 4096u + lane * 8u, d, SC1);
  } else {
#pragma unroll
    for (int n = 0; n < 9; n++) lds_w128(lds0 + F_FIN + (unsigned)kh * 9216u + n * 1024u + vl, o[n]);
  }
  wait_lgkm0();
  publish(c, FL_P + (rb * 4 + ct) * 4 + ks, true);
  p6_finish(c, rb, fc);
}

constexpr int M_LDS = 163840;
static_assert(L_WO + 110592 <= L_DUMP && 4 * Q_SLOT + 640 <= L_DUMP && F_LDS <= L_DUMP, "LDS map");
__global__ KATTR void fused_block(const u16* __restrict__ x, const u16* __restrict__ wqkv, const u16* __restrict__ wo, const u16* __restrict__ w1,
                                  const u16* __restrict__ b1, const u16* __restrict__ w2, const u16* __restrict__ b2, u16* __restrict__ out, char* __restrict__ ws) {
  extern __shared__ __attribute__((aligned(16))) char smem[];
  Ctx c;
  c.x = x; c.wqkv = wqkv; c.wo = wo; c.w1 = w1; c.b1 = b1; c.w2 = w2; c.b2 = b2; c.out = out; c.ws = ws;
  c.sws = mk_srd(ws, WS_BYTES);
  c.lds0 = (unsigned)(size_t)smem;
  c.b = blockIdx.x; c.xcd = c.b & 7; c.bi = c.b >> 3;
  c.w = rfl(threadIdx.x >> 6); c.lane = threadIdx.x & 63;
  const int xcd = c.xcd, bi = c.bi, w = c.w, lane = c.lane;
  const int rb4 = (xcd >> 2) * 8 + (bi & 7), fc4 = (xcd & 3) * 4 + (bi >> 3);
  if (bi < 24) {
    p1_qkv(c, (xcd >> 1) * 4 + (bi & 3), (xcd & 1) * 6 + (bi >> 2));
  } else {
    const int j = bi - 24, fc = (xcd & 3) * 4 + (j >> 1), part = (xcd >> 2) * 2 + (j & 1);
    helper_stats(c, xcd * 8 + j, fc, part);
    if (w >= 8) wait_vm<0>();
    bar();
    relayout_w1(c, fc, part);
    publish(c, FL_W1 + fc * 4 + part, true);
  }
  if (w >= 8) p2_loader(c, xcd * 4 + (bi & 3), bi >> 2); else p2_attn(c, xcd * 4 + (bi & 3), bi >> 2);
  if (w >= 8) {
    if (w == 8 && bi < 24) poll(c, FL_A + xcd * 32 + (lane & 31), lane < 32);
    if (w == 9) poll(c, FL_W1 + fc4 * 4 + (lane & 3), lane < 4);
    if (bi < 24) wait_vm<0>();
    bar();
    f_issue(c, fc4, 0); f_issue(c, fc4, 1);
    {
#pragma unroll
      for (int i = 0; i < 2; i++) { const unsigned l = (unsigned)(((w - 8) * 2 + i) * 64 + lane); touch128(c.lds0 + L_DUMP, l < 288u ? l * 128u : 0x7ff00000u, c.sws, (unsigned)WS_W1P + (unsigned)(fc4 * 9 + 1) * F_SLOT + (unsigned)(bi & 7) * 36864u); }
    }
  }
  if (bi < 24) {
    if (w >= 8) {
      bar(); bar(); bar();
      publish(c, FL_H + (xcd * 4 + (bi & 3)) * 6 + (bi >> 2), false);
    } else {
      p3_wo(c, xcd * 4 + (bi & 3), bi >> 2);
    }
  } else {
    if (w < 8) bar();
    const int j = bi - 24, ks = xcd & 3, ct = j >> 1, part = (xcd >> 2) * 2 + (j & 1);
    if (w < 9) relayout_w2(c, c.lds0 + L_WO + (unsigned)w * 9216u, ks * 36 + part * 9 + w, ct);
    publish(c, FL_W2 + (ks * 4 + ct) * 4 + part, w < 9);
  }
  if (w >= 8) p45_loader(c, rb4, fc4); else if (w < 4) p45_ffn<0>(c, rb4, fc4); else p45_ffn<1>(c, rb4, fc4);
}

extern "C" void kernel_launch(void* const* d_in, const int* in_sizes, int n_in, void* d_out, int out_size, void* d_ws, size_t ws_size,
                              hipStream_t stream) {
  (void)hipFuncSetAttribute((const void*)fused_block, hipFuncAttributeMaxDynamicSharedMemorySize, M_LDS);
  fused_block<<<256, 768, M_LDS, stream>>>((const u16*)d_in[0], (const u16*)d_in[1], (const u16*)d_in[2], (const u16*)d_in[4], (const u16*)d_in[5],
                                           (const u16*)d_in[6], (const u16*)d_in[7], (u16*)d_out, (char*)d_ws);
}
```

```cpp
#include <hip/hip_runtime.h>
#include <stdint.h>

typedef unsigned short u16;
typedef __bf16 bf16x8 __attribute__((ext_vector_type(8)));
typedef __bf16 bf16x4 __attribute__((ext_vector_type(4)));
typedef __bf16 bf16x2 __attribute__((ext_vector_type(2)));
typedef float f32x2 __attribute__((ext_vector_type(2)));
typedef float f32x4 __attribute__((ext_vector_type(4)));
typedef float f32x16 __attribute__((ext_vector_type(16)));
typedef unsigned u32x2 __attribute__((ext_vector_type(2)));
typedef unsigned u32x4 __attribute__((ext_vector_type(4)));
#define LAS __attribute__((address_space(3)))
#define DI __device__ __forceinline__
#define SB() __builtin_amdgcn_sched_barrier(0)
#define KATTR __launch_bounds__(768, 3) __attribute__((amdgpu_waves_per_eu(3, 3)))

constexpr int S = 2048, D = 576, H = 32, DH = 18, F = 4608, NQKV = 1728;
constexpr float LN_EPS = 1e-5f;


constexpr size_t WS_FLG = 0;
constexpr int FL_N = 0, FL_D = 1, FL_Q = FL_D + 256, FL_W1 = FL_Q + 192, FL_A = FL_W1 + 64, FL_W2 = FL_A + 256, FL_H = FL_W2 + 64, FL_H1 = FL_H + 192,
              FL_P = FL_H1 + 256, FL_S1 = FL_P + 256, FL_END = FL_S1 + 64;
constexpr size_t WS_QKV = 262144;
static_assert(FL_END * 128 <= WS_QKV, "flag area");
constexpr size_t WS_QKV_SZ = (size_t)3 * H * S * 24 * 2;
constexpr size_t WS_OP = WS_QKV + WS_QKV_SZ;
constexpr size_t WS_OL = WS_OP + (size_t)64 * 32 * 1024;
constexpr size_t WS_HP = WS_OL + (size_t)H * S * 4;
constexpr size_t WS_ST2 = WS_HP + (size_t)S * D * 2;
constexpr size_t WS_W1P = WS_ST2 + (size_t)S * 48;
constexpr size_t WS_W2P = WS_W1P + (size_t)D * F * 2;
constexpr size_t WS_H1P = WS_W2P + (size_t)D * F * 2;
constexpr size_t WS_XCH = WS_H1P + (size_t)S * F * 2;
constexpr size_t WS_ST1 = WS_XCH + (size_t)16 * 4 * 4 * 4 * 2 * 4608;
constexpr size_t WS_CSQ = WS_ST1 + (size_t)S * 8;
constexpr size_t WS_END = WS_CSQ + (size_t)(NQKV + 64) * 4;
constexpr unsigned WS_BYTES = (unsigned)WS_END;


DI unsigned rfl(unsigned x) { return __builtin_amdgcn_readfirstlane(x); }
DI float bflo(unsigned w) { return __uint_as_float(w << 16); }
DI float bfhi(unsigned w) { return __uint_as_float(w & 0xffff0000u); }
DI unsigned pk2(float lo, float hi) { f32x2 v = {lo, hi}; bf16x2 b = __builtin_convertvector(v, bf16x2); return __builtin_bit_cast(unsigned, b); }
DI float dot2(unsigned a, unsigned b, float c) { return __builtin_amdgcn_fdot2_f32_bf16(__builtin_bit_cast(bf16x2, a), __builtin_bit_cast(bf16x2, b), c, false); }

typedef __amdgpu_buffer_rsrc_t srd_t;
DI srd_t mk_srd(const void* p, unsigned bytes) { return __builtin_amdgcn_make_buffer_rsrc((void*)p, (short)0, (int)bytes, 0x00020000); }
DI void dma16(unsigned ldsaddr, unsigned voff, srd_t srd, unsigned soff) {
  asm volatile("s_mov_b32 m0, %0\n\ts_nop 0\n\tbuffer_load_dwordx4 %1, %2, %3 offen lds" :: "s"(ldsaddr), "v"(voff), "s"(srd), "s"(soff) : "memory");
}
DI void touch128(unsigned ldsdump, unsigned voff, srd_t srd, unsigned soff) {
  asm volatile("s_mov_b32 m0, %0\n\ts_nop 0\n\tbuffer_load_dword %1, %2, %3 offen lds" :: "s"(ldsdump), "v"(voff), "s"(srd), "s"(soff) : "memory");
}
template <int N> DI void wait_vm() { asm volatile("s_waitcnt vmcnt(%0)" :: "i"(N) : "memory"); }
DI void wait_lgkm0() { asm volatile("s_waitcnt lgkmcnt(0)" ::: "memory"); }
DI void bar() { asm volatile("" ::: "memory"); __builtin_amdgcn_s_barrier(); asm volatile("" ::: "memory"); }
DI u32x4 lds_u128(unsigned addr) { return *(const LAS u32x4*)(size_t)addr; }
DI f32x4 lds_f128(unsigned addr) { return *(const LAS f32x4*)(size_t)addr; }
DI void lds_w128(unsigned addr, f32x4 v) { *(LAS f32x4*)(size_t)addr = v; }
DI bf16x8 asbf(u32x4 v) { return __builtin_bit_cast(bf16x8, v); }
DI u32x2 lds_tr(unsigned addr) { return __builtin_bit_cast(u32x2, __builtin_amdgcn_ds_read_tr16_b64_v4bf16((LAS bf16x4*)(size_t)addr)); }
DI u32x4 cat4(u32x2 a, u32x2 b) { return u32x4{a[0], a[1], b[0], b[1]}; }
DI f32x4 mfma16(u32x4 a, u32x4 b, f32x4 c) { return __builtin_amdgcn_mfma_f32_16x16x32_bf16(asbf(a), asbf(b), c, 0, 0, 0); }
DI f32x16 mfma32(u32x4 a, u32x4 b, f32x16 c) { return __builtin_amdgcn_mfma_f32_32x32x16_bf16(asbf(a), asbf(b), c, 0, 0, 0); }
template <int NOP> DI float max16(float m, const f32x16& t) {
  float u;
  asm volatile("s_nop %18\n\tv_max3_f32 %1, %2, %3, %4\n\tv_max3_f32 %0, %0, %5, %6\n\tv_max3_f32 %1, %1, %7, %8\n\tv_max3_f32 %0, %0, %9, %10\n\t"
               "v_max3_f32 %1, %1, %11, %12\n\tv_max3_f32 %0, %0, %13, %14\n\tv_max3_f32 %1, %1, %15, %16\n\tv_max3_f32 %0, %0, %1, %17"
               : "+v"(m), "=&v"(u)
               : "v"(t[0]), "v"(t[1]), "v"(t[2]), "v"(t[3]), "v"(t[4]), "v"(t[5]), "v"(t[6]), "v"(t[7]), "v"(t[8]), "v"(t[9]), "v"(t[10]), "v"(t[11]), "v"(t[12]),
                 "v"(t[13]), "v"(t[14]), "v"(t[15]), "i"(NOP));
  return m;
}
template <int CTRL, int RM> DI float dpp_add(float v) { return v + __builtin_bit_cast(float, __builtin_amdgcn_update_dpp(0, __builtin_bit_cast(int, v), CTRL, RM, 0xf, true)); }
DI float wave_sum63(float v) { v = dpp_add<0x111, 0xf>(v); v = dpp_add<0x112, 0xf>(v); v = dpp_add<0x114, 0xf>(v); v = dpp_add<0x118, 0xf>(v); v = dpp_add<0x142, 0xa>(v); return dpp_add<0x143, 0xc>(v); }
DI float sum_x32(float v) { const auto a = __builtin_amdgcn_permlane32_swap(__float_as_uint(v), __float_as_uint(v), false, false); return __uint_as_float(a[0]) + __uint_as_float(a[1]); }
DI float sum_x16(float v) { const auto a = __builtin_amdgcn_permlane16_swap(__float_as_uint(v), __float_as_uint(v), false, false); return __uint_as_float(a[0]) + __uint_as_float(a[1]); }
DI float sum_x8(float v) { return dpp_add<0x128, 0xf>(v); }
DI int swap23(int i) { return (i & 19) | ((i & 4) << 1) | ((i & 8) >> 1); }


constexpr int SC1 = 16;
DI void dma16s(unsigned ldsaddr, unsigned voff, srd_t srd, unsigned soff) {
  asm volatile("s_mov_b32 m0, %0\n\ts_nop 0\n\tbuffer_load_dwordx4 %1, %2, %3 offen sc1 lds" :: "s"(ldsaddr), "v"(voff), "s"(srd), "s"(soff) : "memory");
}
DI void st128s(u32x4 v, srd_t srd, unsigned voff, unsigned soff) {
  asm volatile("buffer_store_dwordx4 %0, %1, %2, %3 offen sc1\n\ts_nop 1" :: "v"(v), "v"(voff), "s"(srd), "s"(soff) : "memory");
}
DI u32x4 ld128s(srd_t srd, unsigned voff, unsigned soff) { return __builtin_amdgcn_raw_buffer_load_b128(srd, voff, soff, SC1); }

struct Ctx {
  const u16 *x, *wqkv, *wo, *w1, *b1, *w2, *b2;
  u16* out;
  char* ws;
  srd_t sws;
  unsigned lds0;
  unsigned long long nonce;
  int b, xcd, bi, w, lane;
};
DI void publish(const Ctx& c, int flag, bool stored) {
  if (stored) wait_vm<0>();
  bar();
  if (threadIdx.x == 0) __hip_atomic_store((unsigned long long*)(c.ws + (size_t)flag * 128), c.nonce, __ATOMIC_RELAXED, __HIP_MEMORY_SCOPE_AGENT);
}
DI void poll(const Ctx& c, int flag, bool active) {
  const unsigned long long* p = (const unsigned long long*)(c.ws + (size_t)flag * 128);
  const unsigned long long t0 = __builtin_amdgcn_s_memrealtime();
  for (;;) {
    const unsigned long long v = active ? __hip_atomic_load(p, __ATOMIC_RELAXED, __HIP_MEMORY_SCOPE_AGENT) : c.nonce;
    if (__all(v == c.nonce)) break;
    if (__builtin_amdgcn_s_memrealtime() - t0 > 2000000ull) break;
  }
}

constexpr int L_DUMP = 163840 - 256;
DI u32x2 entry_load(const Ctx& c) {
  u32x2 nv;
  asm volatile("buffer_load_dwordx2 %0, %1, %2, 0 offen sc1" : "=&v"(nv) : "v"((unsigned)(FL_N * 128)), "s"(c.sws) : "memory");
  return nv;
}
DI void entry_nonce(Ctx& c, u32x2 nv) {
  asm volatile("" : "+v"(nv));
  c.nonce = (((unsigned long long)rfl(nv[1]) << 32) | rfl(nv[0])) + 1ull;
  if (threadIdx.x == 0) __hip_atomic_store((unsigned long long*)(c.ws + (size_t)(FL_D + c.b) * 128), c.nonce, __ATOMIC_RELAXED, __HIP_MEMORY_SCOPE_AGENT);
}

constexpr int L_W1 = 4096;
DI void relayout_w1_fetch(const Ctx& c, int fc, int part) {
  const int l = c.w - 8, lane = c.lane;
  const srd_t sw = mk_srd(c.w1, D * F * 2);
#pragma unroll 1
  for (int pc = l; pc < 81; pc += 4) {
    const int ch = pc * 64 + lane, row = ch / 36, cc = ch % 36;
    dma16(c.lds0 + L_W1 + (unsigned)pc * 1024u, (unsigned)row * (F * 2) + (unsigned)cc * 16u, sw, (unsigned)part * 144u * (F * 2) + (unsigned)fc * 576u);
  }
}
DI void helper_stats(Ctx& c, int hb, int fc, int part) {
  const int w = c.w, lane = c.lane;
  const srd_t sx = mk_srd(c.x, S * D * 2), sw = mk_srd(c.wqkv, hb < 27 ? D * NQKV * 2 : 0);
  if (w >= 8) {
    const u32x2 nv = entry_load(c);
    wait_vm<0>();
    entry_nonce(c, nv);
    bar();
    publish(c, FL_S1 + hb, false);
    relayout_w1_fetch(c, fc, part);
    return;
  }
  const int tid = w * 64 + lane, ch = tid & 7, rg = tid >> 3;
  u32x4 a[4], b2[4], v9[9];
#pragma unroll
  for (int r = 0; r < 4; r++) {
    const unsigned so = (unsigned)(hb * 32 + w * 4 + r) * (D * 2);
    a[r] = __builtin_amdgcn_raw_buffer_load_b128(sx, (unsigned)lane * 16u, so, 0);
    b2[r] = __builtin_amdgcn_raw_buffer_load_b128(sx, lane < 8 ? 1024u + (unsigned)lane * 16u : 0x7ff00000u, so, 0);
  }
#pragma unroll
  for (int i = 0; i < 9; i++) v9[i] = __builtin_amdgcn_raw_buffer_load_b128(sw, (unsigned)(rg * 9 + i) * (NQKV * 2) + (unsigned)ch * 16u, (unsigned)hb * 128u, 0);
  const u32x2 nv = entry_load(c);
  float sm[4], sq[4];
#pragma unroll
  for (int r = 0; r < 4; r++) {
    sm[r] = 0; sq[r] = 0;
#pragma unroll
    for (int e = 0; e < 4; e++) { sm[r] = dot2(a[r][e], 0x3f803f80u, sm[r]); sq[r] = dot2(a[r][e], a[r][e], sq[r]); sm[r] = dot2(b2[r][e], 0x3f803f80u, sm[r]); sq[r] = dot2(b2[r][e], b2[r][e], sq[r]); }
  }
#pragma unroll
  for (int r = 0; r < 4; r++) {
    const float s1 = __builtin_bit_cast(float, __builtin_amdgcn_readlane(__builtin_bit_cast(int, wave_sum63(sm[r])), 63));
    const float s2 = __builtin_bit_cast(float, __builtin_amdgcn_readlane(__builtin_bit_cast(int, wave_sum63(sq[r])), 63));
    const float mean = s1 * (1.0f / D), var = fmaxf(s2 * (1.0f / D) - mean * mean, 0.f);
    sm[r] = mean; sq[r] = rsqrtf(var + LN_EPS);
  }
  {
    const int r = lane & 3;
    const u32x2 v = {__float_as_uint(r == 0 ? sm[0] : r == 1 ? sm[1] : r == 2 ? sm[2] : sm[3]), __float_as_uint(r == 0 ? sq[0] : r == 1 ? sq[1] : r == 2 ? sq[2] : sq[3])};
    __builtin_amdgcn_raw_buffer_store_b64(v, c.sws, lane < 4 ? (unsigned)lane * 8u : 0x7ff00000u, (unsigned)WS_ST1 + (unsigned)(hb * 32 + w * 4) * 8u, SC1);
  }
  float s8[8] = {0, 0, 0, 0, 0, 0, 0, 0};
#pragma unroll
  for (int i = 0; i < 9; i++)
#pragma unroll
    for (int e = 0; e < 4; e++) { s8[2 * e] += bflo(v9[i][e]); s8[2 * e + 1] += bfhi(v9[i][e]); }
#pragma unroll
  for (int e = 0; e < 8; e++) s8[e] = sum_x32(sum_x16(sum_x8(s8[e])));
  if (lane < 8) { lds_w128(c.lds0 + (unsigned)(w * 64 + lane * 8) * 4u, f32x4{s8[0], s8[1], s8[2], s8[3]}); lds_w128(c.lds0 + (unsigned)(w * 64 + lane * 8) * 4u + 16, f32x4{s8[4], s8[5], s8[6], s8[7]}); }
  wait_lgkm0(); bar();
  if (w == 0) {
    float t = 0;
#pragma unroll
    for (int k = 0; k < 8; k++) t += *(const LAS float*)(size_t)(c.lds0 + (unsigned)(k * 64 + lane) * 4u);
    __builtin_amdgcn_raw_buffer_store_b32(__float_as_uint(t), c.sws, hb < 27 ? (unsigned)lane * 4u : 0x7ff00000u, (unsigned)WS_CSQ + (unsigned)hb * 256u, SC1);
  }
  wait_lgkm0();
  wait_vm<0>();
  entry_nonce(c, nv);
  publish(c, FL_S1 + hb, true);
}

DI void relayout_w1(const Ctx& c, int fc, int part) {
  const int w = c.w, lane = c.lane; const unsigned lds0 = c.lds0 + L_W1;
  const int h = lane >> 5, blk = (lane >> 4) & 1, q = (lane >> 2) & 3, p = lane & 3;
  const unsigned ra = lds0 + (unsigned)(8 * h + q) * 576u + (unsigned)(16 * blk + 8 * (p & 1) + 4 * (p >> 1)) * 2u;
  for (int fr = w; fr < 81; fr += 12) {
    const int sl = fr / 9, tile = fr % 9, s = part * 9 + sl, kh = s / 18;
    const unsigned a = ra + (unsigned)sl * 16u * 576u + (unsigned)tile * 64u;
    const u32x4 v = cat4(lds_tr(a), lds_tr(a + 4 * 576));
    const int slot = tile == 8 ? 8 : (kh == 0 ? (tile ^ 1) : tile);
    const int s18 = s % 18;
    st128s(v, c.sws, (unsigned)lane * 16u, (unsigned)WS_W1P + ((unsigned)(fc * 9 + slot) * 36u + (unsigned)((s18 / 9) * 18 + kh * 9 + s18 % 9)) * 1024u);
  }
}
DI void relayout_w2(const Ctx& c, unsigned ldsw, int kappa, int ct) {
  const int lane = c.lane;
  const srd_t sw = mk_srd(c.w2, D * F * 2);
#pragma unroll
  for (int pc = 0; pc < 9; pc++) {
    const int ch = pc * 64 + lane, row = ch / 18, cc = ch % 18;
    dma16(ldsw + (unsigned)pc * 1024u, (unsigned)row * (D * 2) + (unsigned)cc * 16u, sw, (unsigned)kappa * 32u * (D * 2) + (unsigned)ct * 288u);
  }
  wait_vm<0>();
  const int g = lane >> 4, q = (lane >> 2) & 3, p = lane & 3;
  const unsigned ra = ldsw + (unsigned)(8 * g + q) * 288u;
  const int ks = kappa / 36, kst = (kappa % 36 + 36 - 9 * ct) % 36;
#pragma unroll
  for (int nt = 0; nt < 9; nt++) {
    const unsigned col = nt < 8 ? (unsigned)(32 * (nt >> 1) + 8 * p + 4 * (nt & 1)) * 2u : (unsigned)(128 + 4 * p) * 2u;
    const u32x4 v = cat4(lds_tr(ra + col), lds_tr(ra + col + 4 * 288));
    st128s(v, c.sws, (unsigned)lane * 16u, (unsigned)WS_W2P + ((unsigned)(ks * 4 + ct) * 324u + (unsigned)(((kst >> 2) * 2 + (kst & 1)) * 18 + ((kst >> 1) & 1) * 9 + nt)) * 1024u);
  }
  wait_lgkm0();
}

constexpr int Q_SLOT = 36864;
constexpr int Q_CT = 4 * Q_SLOT;
DI unsigned q_voff(int p, int lane) {
  if (p < 16) return (unsigned)(lane >> 3) * (D * 2) + (unsigned)(((lane & 7) ^ ((4 * (p & 1) + (lane >> 4)) & 7)) * 16);
  const int ch = 64 * (p - 16) + lane, row = ch / 20, cc = ch % 20;
  return (unsigned)row * (NQKV * 2) + (unsigned)cc * 16u;
}
DI void q_issue(const Ctx& c, srd_t sx, srd_t sw, int rb, int ct, int p, unsigned voff, int t, unsigned ring) {
  if (p < 16) dma16(c.lds0 + ring + (unsigned)p * 1024u, voff, sx, ((unsigned)rb * 128u + (unsigned)p * 8u) * (D * 2) + (unsigned)t * 128u);
  else dma16(c.lds0 + ring + (unsigned)p * 1024u, voff, sw, (unsigned)ct * 288u + (unsigned)t * 64u * (NQKV * 2));
}
DI void p1_loader(Ctx& c, int rb, int ct) {
  const int w = c.w, lane = c.lane, l = w - 8;
  const srd_t sx = mk_srd(c.x, S * D * 2), sw = mk_srd(c.wqkv, D * NQKV * 2);
  const u32x2 nv = entry_load(c);
#pragma unroll
  for (int t = 0; t < 2; t++)
#pragma unroll
    for (int i = 0; i < 3; i++) q_issue(c, sx, sw, rb, ct, w + 12 * i, q_voff(w + 12 * i, lane), t, (unsigned)t * Q_SLOT);
  unsigned vw[5];
#pragma unroll
  for (int i = 0; i < 5; i++) vw[i] = q_voff(16 + l + 4 * i, lane);
#define Q_W_ISSUE(t, ring) do { \
    _Pragma("unroll") for (int i = 0; i < 5; i++) dma16(c.lds0 + (ring) + (unsigned)(16 + l + 4 * i) * 1024u, vw[i], sw, (unsigned)ct * 288u + (unsigned)(t) * 64u * (NQKV * 2)); } while (0)
  Q_W_ISSUE(2, 2u * Q_SLOT);
  wait_vm<8>();
  entry_nonce(c, nv);
  bar();
  wait_vm<5>(); bar();
  Q_W_ISSUE(3, 3u * Q_SLOT);
#pragma unroll 1
  for (int t = 1; t < 6; t++) {
    wait_vm<5>(); bar();
    Q_W_ISSUE(t + 3, (unsigned)((t + 3) & 3) * Q_SLOT);
  }
  wait_vm<5>(); bar();
  wait_vm<0>(); bar();
  if (l == 0) { const int g0 = (144 * ct) >> 6; poll(c, FL_S1 + (lane < 4 ? 4 * rb + lane : min(g0 + (lane & 3), 26)), lane < 8); }
  bar();
  bar();
  bar();
}
template <int NT, int T0>
DI void p1_body(Ctx& c, int rb, int ct) {
  const int w = c.w, lane = c.lane, rg = w & 3; const unsigned lds0 = c.lds0;
  constexpr bool XI = NT == 2;
  const srd_t sx = mk_srd(c.x, S * D * 2);
  const unsigned vx = q_voff(rg, lane);
#define Q_X_ISSUE1(t, ring, i) dma16(lds0 + (ring) + (unsigned)(rg + 4 * (i)) * 1024u, vx, sx, ((unsigned)rb * 128u + (unsigned)(rg + 4 * (i)) * 8u) * (D * 2) + (unsigned)(t) * 128u)
  {
    const srd_t sw = mk_srd(c.wqkv, D * NQKV * 2);
    const u32x2 nv = entry_load(c);
#pragma unroll
    for (int t = 0; t < 2; t++)
#pragma unroll
      for (int i = 0; i < 3; i++) q_issue(c, sx, sw, rb, ct, w + 12 * i, q_voff(w + 12 * i, lane), t, (unsigned)t * Q_SLOT);
    if (XI) { Q_X_ISSUE1(2, 2u * Q_SLOT, 0); Q_X_ISSUE1(2, 2u * Q_SLOT, 1); Q_X_ISSUE1(2, 2u * Q_SLOT, 2); Q_X_ISSUE1(2, 2u * Q_SLOT, 3); wait_vm<7>(); } else wait_vm<3>();
    entry_nonce(c, nv);
  }
  const int tok = lane & 31, h = lane >> 5, blk = (lane >> 4) & 1, q4 = (lane >> 2) & 3, pp = lane & 3;
  const unsigned xrow = lds0 + (unsigned)(rg * 32 + tok) * 128u;
  unsigned xj[4];
#pragma unroll
  for (int j = 0; j < 4; j++) xj[j] = xrow + (unsigned)(((2 * j + h) ^ ((tok >> 1) & 7)) * 16);
  const unsigned wj = lds0 + 16384u + (unsigned)(8 * h + q4) * 320u + (unsigned)(32 * T0 + 16 * blk + 8 * (pp & 1) + 4 * (pp >> 1)) * 2u;
  f32x16 acc[NT];
#pragma unroll
  for (int n = 0; n < NT; n++) acc[n] = f32x16{};
  bar();
  u32x4 wf[4][NT], xf[4];
#pragma unroll
  for (int j = 0; j < 2; j++) {
    xf[j] = lds_u128(xj[j]);
#pragma unroll
    for (int n = 0; n < NT; n++) wf[j][n] = cat4(lds_tr(wj + j * 5120 + n * 64), lds_tr(wj + j * 5120 + n * 64 + 4 * 320));
  }
  unsigned r0 = 0, r1 = Q_SLOT, r3 = 3 * Q_SLOT;
#define Q_PERIOD(t, SYNC, ISSUE, EXTRA) do { \
    if (XI && (SYNC) == 2) wait_vm<4>(); \
    if (XI && (SYNC) == 3) wait_vm<0>(); \
    if (SYNC) bar(); \
    EXTRA; \
    unsigned wa_ = wj + r0, wn_ = wj + r1, xa_[4]; \
    _Pragma("unroll") for (int j = 0; j < 4; j++) { xa_[j] = xj[(j + 2) & 3] + (j < 2 ? r0 : r1); asm volatile("" : "+v"(xa_[j])); } \
    asm volatile("" : "+v"(wa_), "+v"(wn_)); \
    _Pragma("unroll") for (int j = 0; j < 4; j++) { \
      if ((SYNC) == 0 && j == 2) { if (XI) wait_vm<6>(); else wait_vm<0>(); bar(); }        \
      const u32x4 xc = xf[j]; \
      xf[(j + 2) & 3] = lds_u128(xa_[j]); \
      _Pragma("unroll") for (int n = 0; n < NT; n++) { \
        const u32x4 cur = wf[j][n]; \
        const unsigned a_ = (j < 2 ? wa_ : wn_) + ((j + 2) & 3) * 5120 + n * 64; \
        wf[(j + 2) & 3][n] = cat4(lds_tr(a_), lds_tr(a_ + 4 * 320)); \
        acc[n] = mfma32(cur, xc, acc[n]); \
        if (XI && (ISSUE) && n == 0) Q_X_ISSUE1((t) + 3, r3, j); \
        SB(); \
      } \
    } \
    r0 = r1; r1 = r1 == 3u * Q_SLOT ? 0u : r1 + Q_SLOT; r3 = r3 == 3u * Q_SLOT ? 0u : r3 + Q_SLOT; \
  } while (0)
  Q_PERIOD(0, 0, true, (void)0);
#pragma unroll 1
  for (int t = 1; t < 6; t++) Q_PERIOD(t, 2, true, (void)0);
  const int tl = rg * 32 + tok, type = ct >> 2;
  u32x2 stv; u32x4 csv;
  Q_PERIOD(6, 2, false, (void)0);
  Q_PERIOD(7, 3, false, (void)0);
  Q_PERIOD(8, 1, false, (
    { asm volatile("buffer_load_dwordx2 %0, %1, %2, %3 offen sc1" : "=&v"(stv) : "v"((unsigned)tl * 8u), "s"(c.sws), "s"((unsigned)WS_ST1 + (unsigned)rb * 1024u) : "memory");
      asm volatile("buffer_load_dwordx4 %0, %1, %2, %3 offen sc1" : "=&v"(csv) : "v"((unsigned)min(lane, 39) * 16u), "s"(c.sws), "s"((unsigned)WS_CSQ + (unsigned)ct * 576u) : "memory"); }));
  asm volatile("s_waitcnt vmcnt(0)" : "+v"(stv), "+v"(csv) :: "memory");
  if (w == 0 && lane < 40) *(LAS u32x4*)(size_t)(lds0 + Q_CT + (unsigned)lane * 16u) = csv;
  wait_lgkm0(); bar();
  f32x2 st;
  st[0] = __uint_as_float(stv[1]) * (type == 0 ? 0.23570226039551584f * 1.4426950408889634f : 1.0f);
  st[1] = -__uint_as_float(stv[0]) * st[0];
#pragma unroll
  for (int n = 0; n < NT; n++)
#pragma unroll
    for (int hf = 0; hf < 2; hf++) {
      if (T0 + n == 4 && hf == 1) continue;
      const int c0 = 32 * (T0 + n) + 16 * hf;
      const f32x4 ca = lds_f128(lds0 + Q_CT + (unsigned)(c0 + 8 * h) * 4u), cb = lds_f128(lds0 + Q_CT + (unsigned)(c0 + 8 * h) * 4u + 16);
      u32x4 o;
#pragma unroll
      for (int e = 0; e < 4; e++) {
        const float c0v = e < 2 ? ca[2 * e] : cb[2 * e - 4], c1v = e < 2 ? ca[2 * e + 1] : cb[2 * e - 3];
        o[e] = pk2(__builtin_fmaf(acc[n][8 * hf + 2 * e], st[0], c0v * st[1]), __builtin_fmaf(acc[n][8 * hf + 2 * e + 1], st[0], c1v * st[1]));
      }
      *(LAS u32x4*)(size_t)(lds0 + (unsigned)tl * 304u + (unsigned)(c0 + 8 * h) * 2u) = o;
    }
  wait_lgkm0(); bar();
}
DI void p1_qkv(Ctx& c, int rb, int ct) {
  if (c.w >= 8) p1_loader(c, rb, ct); else if (c.w < 4) p1_body<3, 0>(c, rb, ct); else p1_body<2, 3>(c, rb, ct);
  {
    const int tid = c.w * 64 + c.lane, tg = tid >= 384, t = tid - 384 * tg, tk = t / 3, part = t - 3 * tk, type = ct >> 2;
    const unsigned src = c.lds0 + (unsigned)tk * 304u + (unsigned)part * 16u + (unsigned)tg * 144u;
    const unsigned keep = part < 2 ? 0xffffffffu : 0u, pad1 = part < 2 ? 0u : (type == 0 ? 0x3f800000u : 0x00003f80u), pad2 = part < 2 || type == 0 ? 0u : 0x3f803f80u;
    const unsigned dst = (unsigned)WS_QKV + ((unsigned)(type * 32 + (ct & 3) * 8) * S + (unsigned)rb * 128u) * 48u;
    u32x4 v[4];
#pragma unroll
    for (int hh = 0; hh < 4; hh++)
#pragma unroll
      for (int e = 0; e < 4; e++) v[hh][e] = *(const LAS unsigned*)(size_t)(src + hh * 36 + e * 4);
#pragma unroll
    for (int hh = 0; hh < 4; hh++) {
      v[hh][1] = (v[hh][1] & keep) | pad1; v[hh][2] = (v[hh][2] & keep) | pad2; v[hh][3] &= keep;
      st128s(v[hh], c.sws, (unsigned)t * 16u + (unsigned)(4 * tg + hh) * (S * 48), dst);
    }
  }
  wait_lgkm0();
  publish(c, FL_Q + rb * 12 + ct, true);
}

constexpr int L_K = 0, L_V = 24576, L_WO = 49216;
DI void p2_attn(const Ctx& c, int head, int qb) {
  const int w = c.w, lane = c.lane; const unsigned lds0 = c.lds0;
  const unsigned ldsK = lds0 + L_K, ldsV = lds0 + L_V, vl = lane * 16;
  const unsigned kbase = (unsigned)WS_QKV + (unsigned)((32 + head) * S + 256 * (qb - 1)) * 48u;
  const int qq = lane & 31, h = lane >> 5;
  const int q0 = 256 * qb + 32 * w;
  f32x16 cold, cdiag;
#pragma unroll
  for (int r = 0; r < 16; r++) {
    const int kk = 16 * (r >> 3) + 8 * h + (r & 7);
    cold[r] = kk > qq ? 0.f : -1e30f;
    cdiag[r] = kk <= qq ? 0.f : -1e30f;
  }
  const int key = swap23(lane & 31);
  unsigned ka0 = ldsK + (unsigned)(key + 32 * w) * 48 + 16 * h;
  unsigned ka1 = ldsK + (unsigned)(key + 32 * w) * 48 + 32;
  const int blk = (lane >> 4) & 1, q4 = (lane >> 2) & 3, pp = lane & 3;
  unsigned va = ldsV + (unsigned)(32 * w + 8 * h + q4) * 48 + 32 * blk + 8 * pp;
  asm volatile("" : "+v"(ka0), "+v"(ka1), "+v"(va), "+v"(cold), "+v"(cdiag));
  if (qb == 0) {
    const int tid = w * 64 + lane;
    if (tid < 256) {
      const u32x4 z = {0, 0, 0, 0};
      *(LAS u32x4*)(size_t)(ldsK + tid * 48) = z; *(LAS u32x4*)(size_t)(ldsK + tid * 48 + 16) = z; *(LAS u32x4*)(size_t)(ldsK + tid * 48 + 32) = u32x4{0, 0xf1493f80u, 0, 0};
      *(LAS u32x4*)(size_t)(ldsV + tid * 48) = z; *(LAS u32x4*)(size_t)(ldsV + tid * 48 + 16) = z; *(LAS u32x4*)(size_t)(ldsV + tid * 48 + 32) = z;
    }
  }
  if (w == 0) {
    const int hg = head >> 3, l = lane;
    const int ty = l < 2 ? 0 : (l < 6 ? 1 : 2), rbl = l < 2 ? 2 * qb + l : 2 * qb - 2 + ((l - 2) & 3);
    poll(c, FL_Q + rbl * 12 + ty * 4 + hg, l < 10 && rbl >= 0);
  }
  bar();
#pragma unroll
  for (int j = 0; j < 3; j++) {
    const unsigned pc = (unsigned)(w * 3 + j) * 1024u;
    if (qb > 0 || pc >= 12288u) dma16s(ldsK + pc, vl, c.sws, kbase + pc);
  }
  const unsigned qo = (unsigned)WS_QKV + (unsigned)(head * S + q0) * 48u;
  const u32x4 qf0 = ld128s(c.sws, (unsigned)qq * 48u + 16u * h, qo);
  u32x4 qf1 = ld128s(c.sws, (unsigned)qq * 48u + 32u, qo);
  if (h) qf1 = u32x4{0, 0, 0, 0};
  wait_vm<0>(); wait_lgkm0(); bar();
  const f32x16 zero = {};
  u32x4 k0[9], k1[9];
#pragma unroll
  for (int j = 0; j < 9; j++) { k0[j] = lds_u128(ka0 + j * 1536); k1[j] = lds_u128(ka1 + j * 1536); }
  float m = -1e30f;
  {
    f32x16 sA = mfma32(k1[0], qf1, mfma32(k0[0], qf0, cold)), sB;
#pragma unroll
    for (int j = 0; j < 9; j++) {
      f32x16& cur = (j & 1) ? sB : sA; f32x16& nxt = (j & 1) ? sA : sB;
      if (j + 1 < 9) nxt = mfma32(k1[j + 1], qf1, mfma32(k0[j + 1], qf0, j + 1 == 8 ? cdiag : zero));
      SB();
      m = j == 0 ? max16<11>(m, cur) : (j < 8 ? max16<1>(m, cur) : max16<3>(m, cur));
      SB();
    }
  }
  { const auto pr = __builtin_amdgcn_permlane32_swap(__float_as_uint(m), __float_as_uint(m), false, false); m = fmaxf(__uint_as_float(pr[0]), __uint_as_float(pr[1])); }
  if (h == 0) {
    const unsigned t0 = pk2(-m, 0.f) & 0xffffu; const float r1 = -m - bflo(t0);
    const unsigned t1 = pk2(r1, 0.f) & 0xffffu; const float r2 = r1 - bflo(t1);
    qf1[1] = (qf1[1] & 0xffff0000u) | t0;
    qf1[2] = pk2(bflo(t1), r2);
  }
  bar();
  f32x16 oacc = {};
  {
    f32x16 sA = mfma32(k1[0], qf1, mfma32(k0[0], qf0, cold)), sB;
    u32x4 v0 = cat4(lds_tr(va), lds_tr(va + 4 * 48)), v1 = cat4(lds_tr(va + 16 * 48), lds_tr(va + 20 * 48));
#pragma unroll
    for (int j = 0; j < 9; j++) {
      f32x16& cur = (j & 1) ? sB : sA; f32x16& nxt = (j & 1) ? sA : sB;
      if (j + 1 < 9) nxt = mfma32(k0[j + 1], qf0, j + 1 == 8 ? cdiag : zero);
      SB();
      u32x4 p0, p1;
      {
        float e0 = cur[0]; asm volatile("" : "+v"(e0));
        p0[0] = pk2(__builtin_amdgcn_exp2f(e0), __builtin_amdgcn_exp2f(cur[1]));
#pragma unroll
        for (int e = 1; e < 4; e++) p0[e] = pk2(__builtin_amdgcn_exp2f(cur[2 * e]), __builtin_amdgcn_exp2f(cur[2 * e + 1]));
        asm volatile("" : "+v"(p0));
      }
      SB();
      if (j + 1 < 9) nxt = mfma32(k1[j + 1], qf1, nxt);
      oacc = mfma32(v0, p0, oacc);
      if (j + 1 < 9) v0 = cat4(lds_tr(va + (j + 1) * 1536), lds_tr(va + (j + 1) * 1536 + 4 * 48));
      SB();
      {
        float e8 = cur[8]; asm volatile("" : "+v"(e8));
        p1[0] = pk2(__builtin_amdgcn_exp2f(e8), __builtin_amdgcn_exp2f(cur[9]));
#pragma unroll
        for (int e = 1; e < 4; e++) p1[e] = pk2(__builtin_amdgcn_exp2f(cur[8 + 2 * e]), __builtin_amdgcn_exp2f(cur[8 + 2 * e + 1]));
        asm volatile("" : "+v"(p1));
      }
      SB();
      oacc = mfma32(v1, p1, oacc);
      if (j + 1 < 9) v1 = cat4(lds_tr(va + (j + 1) * 1536 + 16 * 48), lds_tr(va + (j + 1) * 1536 + 20 * 48));
      SB();
    }
  }
  const float l = __uint_as_float(__builtin_amdgcn_permlane32_swap(__float_as_uint(oacc[10]), __float_as_uint(oacc[10]), false, false)[0]);
  const float inv = __builtin_amdgcn_rcpf(l);
  const u32x4 om = {pk2(oacc[0] * inv, oacc[1] * inv), pk2(oacc[2] * inv, oacc[3] * inv), pk2(oacc[4] * inv, oacc[5] * inv), pk2(oacc[6] * inv, oacc[7] * inv)};
  st128s(om, c.sws, (unsigned)lane * 16u, (unsigned)WS_OP + (unsigned)((q0 >> 5) * 32 + head) * 1024u);
  __builtin_amdgcn_raw_buffer_store_b32(pk2(oacc[8] * inv, oacc[9] * inv), c.sws, h == 0 ? (unsigned)qq * 4u : 0x7ff00000u, (unsigned)WS_OL + (unsigned)(head * S + q0) * 4u, SC1);
  publish(c, FL_A + qb * 32 + head, true);
}

DI void p3_prefetch(const Ctx& c, int ct) {
  const int l = c.w - 8, lane = c.lane;
  const srd_t sw = mk_srd(c.wo, D * D * 2);
  unsigned vw[3];
#pragma unroll
  for (int i = 0; i < 3; i++) { const int ch = 64 * i + lane; vw[i] = (unsigned)(ch / 12) * (D * 2) + (unsigned)(ch % 12) * 16u; }
#pragma unroll 1
  for (int gi = 0; gi < 9; gi++) {
    const int grp = l * 9 + gi;
#pragma unroll
    for (int i = 0; i < 3; i++) dma16(c.lds0 + L_WO + (unsigned)(grp * 3 + i) * 1024u, vw[i], sw, (unsigned)grp * 16u * (D * 2) + (unsigned)ct * 192u);
  }
}
DI void p2_loader(const Ctx& c, int head, int qb) {
  const int xcd = c.xcd, bi = c.bi, l = c.w - 8;
  const unsigned vbase = (unsigned)WS_QKV + (unsigned)((64 + head) * S + 256 * (qb - 1)) * 48u;
  bar();
#pragma unroll
  for (int j = 0; j < 6; j++) {
    const unsigned pc = (unsigned)(l * 6 + j) * 1024u;
    if (qb > 0 || pc >= 12288u) dma16s(c.lds0 + L_V + pc, c.lane * 16u, c.sws, vbase + pc);
  }
  bar();
  wait_vm<0>(); bar();
  if (bi < 24) {
    p3_prefetch(c, bi >> 2);
  } else {
    const srd_t sw2 = mk_srd(c.w2, D * F * 2);
    const int j = bi - 24, ks = xcd & 3, ct2 = j >> 1, part = (xcd >> 2) * 2 + (j & 1), row0 = (ks * 36 + part * 9) * 32;
#pragma unroll
    for (int i = 0; i < 4; i++) {
      const int ln = (l * 4 + i) * 64 + c.lane, row = ln / 3;
      touch128(c.lds0 + L_DUMP, ln < 864 ? (unsigned)row * (D * 2) + (unsigned)(ln - 3 * row) * 128u : 0x7ff00000u, sw2, (unsigned)row0 * (D * 2) + (((unsigned)ct2 * 288u) & ~127u));
    }
  }
  publish(c, FL_A + qb * 32 + head, false);
}
template <int KQ>
DI void p3_body(const Ctx& c, int rb, int ct) {
  constexpr int kq = KQ;
  const int lane = c.lane, rg = c.w & 1; const unsigned lds0 = c.lds0;
  const int qt = rb * 2 + rg, tok = lane & 31, h = lane >> 5;
  const int row = qt * 32 + tok;
  const u32x4 xr0 = *(const u32x4*)(c.x + (size_t)row * D + ct * 96 + 16 * kq + 8 * h);
  const u32x4 xr1 = *(const u32x4*)(c.x + (size_t)row * D + ct * 96 + 16 * (kq < 2 ? kq + 4 : kq) + 8 * h);
  unsigned hso = rfl((unsigned)WS_HP + (unsigned)(qt * 36 + ct * 6) * 1024u);
  asm volatile("" : "+s"(hso));
  const int blk = (lane >> 4) & 1, q4 = (lane >> 2) & 3, pp = lane & 3;
  const unsigned colb = lds0 + L_WO + (unsigned)(16 * blk + 8 * (pp & 1) + 4 * (pp >> 1)) * 2u;
  unsigned ah = colb + (unsigned)(18 * 9 * kq + 4 * h + q4) * 192u;
  unsigned al = colb + (unsigned)(18 * (4 * h + (q4 >> 1)) + 16 + (q4 & 1)) * 192u;
  asm volatile("" : "+v"(ah), "+v"(al));
#define P3_W(s, n) ((kq < 3 || (s) < 5) ? cat4(lds_tr(ah + (s) * 3456 + (n) * 64), lds_tr(ah + (s) * 3456 + 1536 + (n) * 64)) \
                                        : cat4(lds_tr(al + ((s) - 5) * 27648 + (n) * 64), lds_tr(al + ((s) - 5) * 27648 + 6912 + (n) * 64)))
  bar();
  u32x4 of[9];
  {
    const unsigned so = (unsigned)WS_OP + (unsigned)(qt * 32 + kq * 9) * 1024u;
#pragma unroll
    for (int s = 0; s < (kq < 3 ? 9 : 5); s++) of[s] = ld128s(c.sws, (unsigned)lane * 16u + s * 1024u, so);
    if (kq == 3) {
#pragma unroll
      for (int G = 0; G < 4; G++)
#pragma unroll
        for (int i = 0; i < 4; i++)
          of[5 + G][i] = __builtin_amdgcn_raw_buffer_load_b32(c.sws, (unsigned)((4 * h + i) * S + tok) * 4u, (unsigned)WS_OL + (unsigned)(8 * G * S + qt * 32) * 4u, SC1);
    }
  }
  u32x4 wf[3][3];
#pragma unroll
  for (int s = 0; s < 2; s++)
#pragma unroll
    for (int n = 0; n < 3; n++) wf[s][n] = P3_W(s, n);
  SB();
  f32x16 acc[3] = {};
#pragma unroll
  for (int s = 0; s < 9; s++) {
#pragma unroll
    for (int n = 0; n < 3; n++) {
      if (s + 2 < 9) wf[(s + 2) % 3][n] = P3_W(s + 2, n);
      acc[n] = mfma32(wf[s % 3][n], of[s], acc[n]);
    }
    SB();
  }
  wait_lgkm0(); bar();
  const unsigned xb = lds0 + L_WO + (unsigned)rg * 49152u + (unsigned)lane * 16u;
#pragma unroll
  for (int f = 0; f < 6; f++) {
    if ((f & 3) == kq) continue;
    const int r8 = 8 * (f & 1);
    lds_w128(xb + (unsigned)(f * 4 + kq) * 2048u, f32x4{acc[f >> 1][r8], acc[f >> 1][r8 + 1], acc[f >> 1][r8 + 2], acc[f >> 1][r8 + 3]});
    lds_w128(xb + (unsigned)(f * 4 + kq) * 2048u + 1024u, f32x4{acc[f >> 1][r8 + 4], acc[f >> 1][r8 + 5], acc[f >> 1][r8 + 6], acc[f >> 1][r8 + 7]});
  }
  wait_lgkm0(); bar();
  float ssum = 0, ssq = 0;
#pragma unroll
  for (int i = 0; i < (kq < 2 ? 2 : 1); i++) {
    const int f = kq + 4 * i, r8 = 8 * (f & 1);
    f32x4 a = {acc[f >> 1][r8], acc[f >> 1][r8 + 1], acc[f >> 1][r8 + 2], acc[f >> 1][r8 + 3]}, cc = {acc[f >> 1][r8 + 4], acc[f >> 1][r8 + 5], acc[f >> 1][r8 + 6], acc[f >> 1][r8 + 7]};
#pragma unroll
    for (int k = 0; k < 4; k++)
      if (k != kq) { a += lds_f128(xb + (unsigned)(f * 4 + k) * 2048u); cc += lds_f128(xb + (unsigned)(f * 4 + k) * 2048u + 1024u); }
    const u32x4 xr = i ? xr1 : xr0;
    u32x4 o;
    o[0] = pk2(bflo(xr[0]) + a[0], bfhi(xr[0]) + a[1]); o[1] = pk2(bflo(xr[1]) + a[2], bfhi(xr[1]) + a[3]);
    o[2] = pk2(bflo(xr[2]) + cc[0], bfhi(xr[2]) + cc[1]); o[3] = pk2(bflo(xr[3]) + cc[2], bfhi(xr[3]) + cc[3]);
    st128s(o, c.sws, (unsigned)lane * 16u, hso + (unsigned)f * 1024u);
#pragma unroll
    for (int e = 0; e < 4; e++) { ssum = dot2(o[e], 0x3f803f80u, ssum); ssq = dot2(o[e], o[e], ssq); }
  }
  { const auto a = __builtin_amdgcn_permlane32_swap(__float_as_uint(ssum), __float_as_uint(ssum), false, false); ssum = __uint_as_float(a[0]) + __uint_as_float(a[1]);
    const auto b2 = __builtin_amdgcn_permlane32_swap(__float_as_uint(ssq), __float_as_uint(ssq), false, false); ssq = __uint_as_float(b2[0]) + __uint_as_float(b2[1]); }
  const unsigned stb = lds0 + L_WO + 98304u;
  if (h == 0) *(LAS f32x2*)(size_t)(stb + (unsigned)(rg * 32 + tok) * 32u + kq * 8u) = f32x2{ssum, ssq};
  wait_lgkm0(); bar();
  if (c.w == 0) {
    const f32x4 a = lds_f128(stb + lane * 32u), cc = lds_f128(stb + lane * 32u + 16);
    const u32x2 v = {__float_as_uint((a[0] + a[2]) + (cc[0] + cc[2])), __float_as_uint((a[1] + a[3]) + (cc[1] + cc[3]))};
    __builtin_amdgcn_raw_buffer_store_b64(v, c.sws, (unsigned)lane * 8u, (unsigned)WS_ST2 + (unsigned)(rb * 6 + ct) * 512u, SC1);
  }
}
DI void p3_wo(const Ctx& c, int rb, int ct) {
  const int kq = c.w >> 1;
  if (kq == 0) p3_body<0>(c, rb, ct); else if (kq == 1) p3_body<1>(c, rb, ct); else if (kq == 2) p3_body<2>(c, rb, ct); else p3_body<3>(c, rb, ct);
  publish(c, FL_H + rb * 6 + ct, true);
}

constexpr float GC0 = -2.0f * 0.7978845608028654f * 1.4426950408889634f, GC1 = GC0 * 0.044715f;
DI f32x2 gelu2(f32x2 x) {
  const f32x2 t = x * (GC1 * (x * x) + GC0);
  f32x2 e; e[0] = __builtin_amdgcn_exp2f(t[0]); e[1] = __builtin_amdgcn_exp2f(t[1]);
  const f32x2 d = e + 1.0f;
  f32x2 r; r[0] = __builtin_amdgcn_rcpf(d[0]); r[1] = __builtin_amdgcn_rcpf(d[1]);
  return x * r;
}
struct G2 { float x0, x1, t0, t1; };
DI void g2_s1(G2& g, float a0, float a1, float p0, float p1) {
  asm volatile("v_add_f32_e32 %0, %4, %6\n\tv_add_f32_e32 %1, %5, %7\n\tv_mul_f32_e32 %2, %0, %0\n\tv_mul_f32_e32 %3, %1, %1"
               : "=&v"(g.x0), "=&v"(g.x1), "=&v"(g.t0), "=&v"(g.t1) : "v"(a0), "v"(a1), "v"(p0), "v"(p1));
}
DI void g2_s2(G2& g, float vc1) {
  asm volatile("v_fmaak_f32 %0, %0, %4, 0xc0135761\n\tv_fmaak_f32 %1, %1, %4, 0xc0135761\n\tv_mul_f32_e32 %0, %2, %0\n\tv_mul_f32_e32 %1, %3, %1"
               : "+v"(g.t0), "+v"(g.t1) : "v"(g.x0), "v"(g.x1), "v"(vc1));
}
DI void g2_s3(G2& g) {
  asm volatile("v_exp_f32_e32 %0, %0\n\tv_exp_f32_e32 %1, %1\n\tv_add_f32_e32 %0, 1.0, %0\n\tv_add_f32_e32 %1, 1.0, %1" : "+v"(g.t0), "+v"(g.t1));
}
DI unsigned g2_s4(G2& g) {
  unsigned o;
  asm volatile("v_rcp_f32_e32 %1, %1\n\tv_rcp_f32_e32 %2, %2\n\tv_mul_f32_e32 %1, %3, %1\n\tv_mul_f32_e32 %2, %4, %2\n\tv_cvt_pk_bf16_f32 %0, %1, %2"
               : "=v"(o), "+v"(g.t0), "+v"(g.t1) : "v"(g.x0), "v"(g.x1));
  return o;
}

constexpr int F_SLOT = 36864, H_SLOT = 18432, F1_XCH = 6 * H_SLOT, F1_UV = F1_XCH + 32768, F_LDS = F1_UV + 1152;
DI unsigned f_stream(int fc, int g) {
  return g < 18 ? (unsigned)WS_W1P + (unsigned)(fc * 18 + g) * H_SLOT : (unsigned)WS_W2P + (unsigned)(fc * 18 + g - 18) * H_SLOT;
}
DI void f_issue(const Ctx& c, int fc, int g) {
  const int l = c.w - 8;
  const unsigned o = (unsigned)(l < 2 ? 5 * l : 2 + 4 * l) * 1024u, vl = c.lane * 16, gb = f_stream(fc, g) + o, lb = c.lds0 + (unsigned)(g % 6) * H_SLOT + o;
#pragma unroll
  for (int i = 0; i < 4; i++) dma16s(lb + i * 1024u, vl, c.sws, gb + i * 1024u);
  if (l < 2) dma16s(lb + 4096u, vl, c.sws, gb + 4096u);
}
template <int N> DI void f_wait(int l) { if (l < 2) wait_vm<5 * N>(); else wait_vm<4 * N>(); }
constexpr int F_FIN = 4 * H_SLOT;
DI void p6_finish(const Ctx& c, int rb, int fc) {
  const int w = c.w, lane = c.lane, ks = fc >> 2, ct = fc & 3, tk = lane & 15, g = lane >> 4;
  const bool pair = w < 8;
  const int T = w & 3, rt = pair ? w >> 2 : w - 8;
  const int col = ct * 144 + (pair ? 32 * T + 8 * g : 128 + 4 * g), row = rb * 128 + ks * 32 + rt * 16 + tk;
  u32x4 hv = {}, bv = {};
  const unsigned ho = (unsigned)(((col >> 3) & 1) * 32 + rt * 16 + tk) * 16u + (unsigned)(col >> 4) * 1024u + (unsigned)(col & 7) * 2u, hb = (unsigned)WS_HP + (unsigned)(rb * 4 + ks) * 36u * 1024u;
  if (w < 10) {
    if (pair) { hv = ld128s(c.sws, ho, hb); bv = *(const u32x4*)(c.b2 + col); }
    else { const u32x2 a = __builtin_amdgcn_raw_buffer_load_b64(c.sws, ho, hb, SC1), b2 = *(const u32x2*)(c.b2 + col); hv[0] = a[0]; hv[1] = a[1]; bv[0] = b2[0]; bv[1] = b2[1]; }
  }
  if (w == 10) poll(c, FL_P + (rb * 4 + ct) * 4 + (lane & 3), lane < 4);
  bar();
  if (w >= 10) return;
  const unsigned xg = (unsigned)WS_XCH + (unsigned)((rb * 4 + ct) * 16 + ks * 4) * 9216u + (unsigned)rt * 4608u;
  const unsigned fin = c.lds0 + F_FIN + (unsigned)rt * 9216u + (unsigned)lane * 16u;
  if (pair) {
    u32x4 pv[3];
#pragma unroll
    for (int i = 0; i < 3; i++) pv[i] = ld128s(c.sws, (unsigned)T * 1024u + (unsigned)lane * 16u, xg + (unsigned)(i + (i >= ks)) * 9216u);
    f32x4 a = lds_f128(fin + (unsigned)T * 2048u), b2 = lds_f128(fin + (unsigned)T * 2048u + 1024u);
#pragma unroll
    for (int i = 0; i < 3; i++) { a += f32x4{bflo(pv[i][0]), bfhi(pv[i][0]), bflo(pv[i][1]), bfhi(pv[i][1])}; b2 += f32x4{bflo(pv[i][2]), bfhi(pv[i][2]), bflo(pv[i][3]), bfhi(pv[i][3])}; }
    u32x4 r;
    r[0] = pk2(bflo(hv[0]) + (a[0] + bflo(bv[0])), bfhi(hv[0]) + (a[1] + bfhi(bv[0])));
    r[1] = pk2(bflo(hv[1]) + (a[2] + bflo(bv[1])), bfhi(hv[1]) + (a[3] + bfhi(bv[1])));
    r[2] = pk2(bflo(hv[2]) + (b2[0] + bflo(bv[2])), bfhi(hv[2]) + (b2[1] + bfhi(bv[2])));
    r[3] = pk2(bflo(hv[3]) + (b2[2] + bflo(bv[3])), bfhi(hv[3]) + (b2[3] + bfhi(bv[3])));
    *(u32x4*)(c.out + (size_t)row * D + col) = r;
  } else {
    u32x2 pv[3];
#pragma unroll
    for (int i = 0; i < 3; i++) pv[i] = __builtin_amdgcn_raw_buffer_load_b64(c.sws, 4096u + (unsigned)lane * 8u, xg + (unsigned)(i + (i >= ks)) * 9216u, SC1);
    f32x4 a = lds_f128(fin + 8192u);
#pragma unroll
    for (int i = 0; i < 3; i++) a += f32x4{bflo(pv[i][0]), bfhi(pv[i][0]), bflo(pv[i][1]), bfhi(pv[i][1])};
    u32x2 r;
    r[0] = pk2(bflo(hv[0]) + (a[0] + bflo(bv[0])), bfhi(hv[0]) + (a[1] + bfhi(bv[0])));
    r[1] = pk2(bflo(hv[1]) + (a[2] + bflo(bv[1])), bfhi(hv[1]) + (a[3] + bfhi(bv[1])));
    *(u32x2*)(c.out + (size_t)row * D + col) = r;
  }
}
DI void p45_loader(const Ctx& c, int rb, int fc) {
  const int l = c.w - 8, lane = c.lane, ks = fc >> 2, ct = fc & 3;
  f_issue(c, fc, 2); f_issue(c, fc, 3); f_issue(c, fc, 4);
  bar();
  u32x2 fd[4] = {};
#pragma unroll 1
  for (int g = 0; g < 36; g++) {
    if (g == 18) { bar(); bar(); bar(); }
    if (g == 8 && l == 0) poll(c, FL_W2 + (ks * 4 + ct) * 4 + (lane & 3), lane < 4);
    if (g == 20 && l == 0) poll(c, FL_H1 + rb * 16 + ks * 4 + (lane & 3), lane < 4);
    if (g < 32) f_wait<3>(l); else if (g == 32) f_wait<2>(l); else if (g == 33) f_wait<1>(l); else wait_vm<0>();
    bar();
    if (g == 8 || g == 10) {
      const unsigned ln = (unsigned)((l * 2 + ((g >> 1) & 1)) * 64 + lane);
      touch128(c.lds0 + L_DUMP, ln < 324u ? ln * 128u : 0x7ff00000u, c.sws, f_stream(fc, 18) + (unsigned)(c.bi & 7) * 41472u);
    }
    if (g == 6 && l == 3 && c.b == 0) {
#pragma unroll
      for (int i = 0; i < 4; i++) asm volatile("buffer_load_dwordx2 %0, %1, %2, 0 offen sc1" : "=&v"(fd[i]) : "v"((unsigned)(FL_D + i * 64 + lane) * 128u), "s"(c.sws) : "memory");
    }
    if (g == 12 && l == 3 && c.b == 0) {
      asm volatile("" : "+v"(fd[0]), "+v"(fd[1]), "+v"(fd[2]), "+v"(fd[3]));
      const unsigned n0 = (unsigned)c.nonce, n1 = (unsigned)(c.nonce >> 32);
      if (!__all(fd[0][0] == n0 && fd[0][1] == n1 && fd[1][0] == n0 && fd[1][1] == n1 && fd[2][0] == n0 && fd[2][1] == n1 && fd[3][0] == n0 && fd[3][1] == n1)) {
#pragma unroll 1
        for (int i = 0; i < 4; i++) poll(c, FL_D + i * 64 + lane, true);
      }
      if (lane == 0) __hip_atomic_store((unsigned long long*)(c.ws + FL_N * 128), c.nonce, __ATOMIC_RELAXED, __HIP_MEMORY_SCOPE_AGENT);
    }
    if (g + 5 < 36) f_issue(c, fc, g + 5);
  }
  bar(); bar();
  bar();
  p6_finish(c, rb, fc);
}
template <int KH>
DI void p45_ffn(const Ctx& c, int rb, int fc) {
  constexpr int kh = KH;
  const int w = c.w, lane = c.lane, rg = w & 3; const unsigned lds0 = c.lds0;
  const unsigned vl = lane * 16;
  const int ks = fc >> 2, ct = fc & 3;
  const int tok = lane & 31, h = lane >> 5;
  if (threadIdx.x < 288) *(LAS float*)(size_t)(lds0 + F1_UV + threadIdx.x * 4) = __uint_as_float((unsigned)c.b1[fc * 288 + threadIdx.x] << 16);
  if (w == 0) poll(c, FL_H + (2 * rb + lane / 6) * 6 + lane % 6, lane < 12);
  wait_lgkm0(); bar();
  u32x2 stv[6];
#pragma unroll
  for (int e = 0; e < 6; e++)
    asm volatile("buffer_load_dwordx2 %0, %1, %2, %3 offen offset:%4 sc1" : "=&v"(stv[e]) : "v"((unsigned)tok * 8u), "s"(c.sws),
                 "s"((unsigned)WS_ST2 + (unsigned)((2 * rb + (rg >> 1)) * 6) * 512u + (unsigned)(rg & 1) * 256u), "i"(e * 512) : "memory");
  u32x4 xf[18];
  {
    const unsigned so = (unsigned)WS_HP + ((unsigned)(rb * 4 + rg) * 36u + (unsigned)kh * 18u) * 1024u;
#pragma unroll
    for (int s = 0; s < 18; s++)
      asm volatile("buffer_load_dwordx4 %0, %1, %2, %3 offen offset:%4 sc1" : "=&v"(xf[s]) : "v"(vl), "s"(c.sws), "s"(so + (s >> 2) * 4096u), "i"((s & 3) * 1024) : "memory");
  }
  float ra, rc;
  {
    asm volatile("s_waitcnt vmcnt(18)" : "+v"(stv[0]), "+v"(stv[1]), "+v"(stv[2]), "+v"(stv[3]), "+v"(stv[4]), "+v"(stv[5]) :: "memory");
    float s = 0, q = 0;
#pragma unroll
    for (int e = 0; e < 6; e++) { s += __uint_as_float(stv[e][0]); q += __uint_as_float(stv[e][1]); }
    const float mean = s * (1.0f / D), var = fmaxf(q * (1.0f / D) - mean * mean, 0.f), rstd = rsqrtf(var + LN_EPS);
    ra = rstd; rc = -mean * rstd;
  }
  const unsigned rd = lds0 + (unsigned)kh * 9216u + vl;
  const unsigned hvo = (unsigned)(tok >> 4) * 1024u + (unsigned)h * 256u + (unsigned)(tok & 15) * 16u;
  const unsigned hbase = (unsigned)WS_H1P + ((unsigned)(rb * 4 + rg) * 144u + (unsigned)fc * 9u) * 2048u;
  const unsigned bia = lds0 + F1_UV + (unsigned)h * 32u;
  {
#pragma unroll
    for (int s = 0; s < 18; s++) {
      asm volatile("s_waitcnt vmcnt(%1)" : "+v"(xf[s]) : "i"(17 - s) : "memory");
#pragma unroll
      for (int e = 0; e < 4; e++) {
        const f32x2 v = f32x2{bflo(xf[s][e]), bfhi(xf[s][e])} * ra + rc;
        xf[s][e] = pk2(v[0], v[1]);
      }
      asm volatile("" : "+v"(xf[s]));
      SB();
    }
  }
  constexpr int PF = 3;
  u32x4 wf[PF];
  const f32x16 zero = {};
#define F1_BIAS(ACC, ba_) do { const f32x4 b0_ = lds_f128(ba_), b1_ = lds_f128(ba_ + 16), b2_ = lds_f128(ba_ + 64), b3_ = lds_f128(ba_ + 80); \
    ACC = f32x16{b0_[0], b0_[1], b0_[2], b0_[3], b1_[0], b1_[1], b1_[2], b1_[3], b2_[0], b2_[1], b2_[2], b2_[3], b3_[0], b3_[1], b3_[2], b3_[3]}; } while (0)
  f32x16 accG, accP, accQ;
  F1_BIAS(accG, bia + (unsigned)(1 - kh) * 128u);
  float vc1 = GC1; asm volatile("" : "+v"(vc1));
  unsigned r0 = 0;
#define F_NEXT(r) ((r) == 5u * H_SLOT ? 0u : (r) + H_SLOT)
  const unsigned xwr = lds0 + F1_XCH + (unsigned)(kh ^ 1) * 16384u + (unsigned)rg * 4096u + vl;
  const unsigned xrd = lds0 + F1_XCH + (unsigned)kh * 16384u + (unsigned)rg * 4096u + vl;
  f32x4 pp[4] = {};
#define F1_STAGE(q, G, SRC, R0, O) do { const int p_ = (q) >> 2, st_ = (q) & 3, r_ = (R0) + 2 * p_; \
      if (st_ == 0) g2_s1(G, SRC[r_], SRC[r_ + 1], pp[r_ >> 2][r_ & 3], pp[r_ >> 2][(r_ & 3) + 1]); \
      if (st_ == 1) g2_s2(G, vc1); if (st_ == 2) g2_s3(G); if (st_ == 3) O[p_] = g2_s4(G); } while (0)
#define F_HALF_BEGIN(s) do { if ((s) % 9 == 0) { bar(); sb = rd + r0; r0 = F_NEXT(r0); sn = rd + r0; asm volatile("" : "+v"(sb), "+v"(sn)); } } while (0)
#define F_FRAG(s) lds_u128((s) % 9 + PF < 9 ? sb + ((s) % 9 + PF) * 1024 : sn + ((s) % 9 + PF - 9) * 1024)
#define F1_PERIOD(t, ACC, PREV, GELU, HALF) do { \
    unsigned sb, sn; \
    u32x4 o; G2 g; \
    const int gt_ = ((t) - 2 - (HALF)) + kh;      \
    _Pragma("unroll") for (int s = 0; s < 18; s++) { \
      F_HALF_BEGIN(s); \
      if (s == 0 && (t) == 0) { _Pragma("unroll") for (int i = 0; i < PF; i++) wf[i] = lds_u128(sb + i * 1024); } \
      if (s == 0 && (GELU) && (HALF) == 0) { _Pragma("unroll") for (int e = 0; e < 4; e++) pp[e] = lds_f128(xrd + e * 1024); SB(); } \
      const u32x4 cur = wf[s % PF]; \
      wf[s % PF] = F_FRAG(s); \
      ACC = mfma32(cur, xf[s], (s == 0 && (HALF) == 1) ? zero : ACC); \
      if ((HALF) == 1 && s % 4 == 1 && s / 4 < 4) { const int e = s / 4; lds_w128(xwr + e * 1024, f32x4{accG[4 * e], accG[4 * e + 1], accG[4 * e + 2], accG[4 * e + 3]}); } \
      if (GELU) { \
        if (s >= 2 && s <= 17) F1_STAGE(s - 2, g, PREV, 8 * (HALF), o); \
        if (s == 17) st128s(o, c.sws, hvo + (HALF) * 512u, hbase + (unsigned)gt_ * 2048u); \
      } \
      if ((HALF) == 1 && s == 15) {     \
        const int nt_ = (t) + 1 == 8 ? 8 : (t) + 1 + (1 - kh); \
        if ((t) + 1 == 8 && kh == 0) accG = zero; \
        else { unsigned ba_ = bia + (unsigned)nt_ * 128u; asm volatile("" : "+v"(ba_)); F1_BIAS(accG, ba_); } \
      } \
      SB(); \
    } \
    asm volatile("" :: "v"(ACC)); \
  } while (0)
  const unsigned hoff0 = (unsigned)WS_H1P + ((unsigned)(rb * 4 + rg) * 144u + (unsigned)ks * 36u) * 2048u;
  u32x4 hA[4], hB[4], hC[4];
#define HLOAD1(dst, t, i) do { const int p_ = 4 * (t) + 2 * kh + ((i) >> 1) + 9 * ct; const unsigned so_ = hoff0 + (unsigned)(p_ >= 36 ? p_ - 36 : p_) * 2048u; \
    if (((i) & 1) == 0) asm volatile("buffer_load_dwordx4 %0, %1, %2, %3 offen sc1" : "=&v"(dst[i]) : "v"(vl), "s"(c.sws), "s"(so_) : "memory"); \
    else asm volatile("buffer_load_dwordx4 %0, %1, %2, %3 offen offset:1024 sc1" : "=&v"(dst[i]) : "v"(vl), "s"(c.sws), "s"(so_) : "memory"); } while (0)
#define HWAIT(N, dst) asm volatile("s_waitcnt vmcnt(" #N ")" : "+v"(dst[0]), "+v"(dst[1]), "+v"(dst[2]), "+v"(dst[3]) :: "memory")
  {
    F1_PERIOD(0, accG, accQ, false, 0);
    F1_PERIOD(1, accP, accQ, false, 1);
    F1_PERIOD(2, accG, accP, true, 0);
    F1_PERIOD(3, accQ, accP, true, 1);
    F1_PERIOD(4, accG, accQ, true, 0);
    F1_PERIOD(5, accP, accQ, true, 1);
    F1_PERIOD(6, accG, accP, true, 0);
    F1_PERIOD(7, accQ, accP, true, 1);
    {
      wait_vm<2>();
      unsigned sb, sn;
      u32x4 o, o2; G2 g, g2;
      const int gt_ = 6 + kh;
#pragma unroll
      for (int s = 0; s < 18; s++) {
        F_HALF_BEGIN(s);
        if (s == 0) {
#pragma unroll
          for (int e = 0; e < 4; e++) pp[e] = lds_f128(xrd + e * 1024);
          SB();
        }
        const u32x4 cur = wf[s % PF];
        wf[s % PF] = F_FRAG(s);
        accG = mfma32(cur, xf[s], accG);
        if (s == 10 + KH) HLOAD1(hA, 0, 0); if (s == 12 + KH) HLOAD1(hA, 0, 1); if (s == 14 + KH) HLOAD1(hA, 0, 2); if (s == 16 + KH) HLOAD1(hA, 0, 3);
        if (s >= 2 && s <= 17) { F1_STAGE(s - 2, g, accQ, 0, o); F1_STAGE(s - 2, g2, accQ, 8, o2); }
        if (s == 17) { st128s(o, c.sws, hvo, hbase + (unsigned)gt_ * 2048u); st128s(o2, c.sws, hvo + 512u, hbase + (unsigned)gt_ * 2048u); }
        SB();
      }
      bar();
      lds_w128(xwr, kh ? f32x4{accG[0], accG[1], accG[2], accG[3]} : f32x4{accG[8], accG[9], accG[10], accG[11]});
      lds_w128(xwr + 1024, kh ? f32x4{accG[4], accG[5], accG[6], accG[7]} : f32x4{accG[12], accG[13], accG[14], accG[15]});
      wait_lgkm0(); bar();
      const f32x4 q0 = lds_f128(xrd), q1 = lds_f128(xrd + 1024);
      const f32x4 k0 = (kh ? f32x4{accG[8], accG[9], accG[10], accG[11]} : f32x4{accG[0], accG[1], accG[2], accG[3]}) + q0;
      const f32x4 k1 = (kh ? f32x4{accG[12], accG[13], accG[14], accG[15]} : f32x4{accG[4], accG[5], accG[6], accG[7]}) + q1;
      const f32x2 z0 = gelu2(f32x2{k0[0], k0[1]}), z1 = gelu2(f32x2{k0[2], k0[3]}), z2 = gelu2(f32x2{k1[0], k1[1]}), z3 = gelu2(f32x2{k1[2], k1[3]});
      o = u32x4{pk2(z0[0], z0[1]), pk2(z1[0], z1[1]), pk2(z2[0], z2[1]), pk2(z3[0], z3[1])};
      st128s(o, c.sws, hvo + (unsigned)kh * 512u, hbase + 8u * 2048u);
    }
  }
  publish(c, FL_H1 + rb * 16 + fc, true);
  HLOAD1(hB, 1, 0); HLOAD1(hB, 1, 1); HLOAD1(hB, 1, 2); HLOAD1(hB, 1, 3);
  f32x4 acc[2][9];
#pragma unroll
  for (int a = 0; a < 2; a++)
#pragma unroll
    for (int n = 0; n < 9; n++) acc[a][n] = f32x4{0, 0, 0, 0};
#define F2_PERIOD(t, HC, HN, HM, VMW, MODE) do { \
    HWAIT(VMW, HC); \
    unsigned sb, sn; \
    _Pragma("unroll") for (int s = 0; s < 18; s++) { \
      F_HALF_BEGIN(s); \
      const u32x4 cur = wf[s % PF]; \
      wf[s % PF] = F_FRAG(s); \
      const int k = s / 9, nt = s % 9; \
      acc[0][nt] = mfma16(cur, HC[2 * k], acc[0][nt]); \
      acc[1][nt] = mfma16(cur, HC[2 * k + 1], acc[1][nt]); \
      if ((MODE) == 3) { if (s == KH) HLOAD1(HM, (t) + 1, 0); if (s == 2 + KH) HLOAD1(HM, (t) + 1, 1); if (s == 4 + KH) HLOAD1(HM, (t) + 1, 2); if (s == 6 + KH) HLOAD1(HM, (t) + 1, 3); } \
      if ((MODE) == 0 || (MODE) == 3) { if (s == 9 + KH) HLOAD1(HN, (t) + 2, 0); if (s == 11 + KH) HLOAD1(HN, (t) + 2, 1); if (s == 13 + KH) HLOAD1(HN, (t) + 2, 2); if (s == 15 + KH) HLOAD1(HN, (t) + 2, 3); } \
      SB(); \
    } \
  } while (0)
  F2_PERIOD(0, hA, hC, hC, 4, 1);
  F2_PERIOD(1, hB, hA, hC, 0, 3);
  F2_PERIOD(2, hC, hB, hB, 4, 0);
  F2_PERIOD(3, hA, hC, hC, 4, 0);
  F2_PERIOD(4, hB, hA, hA, 4, 0);
  F2_PERIOD(5, hC, hB, hB, 4, 0);
  F2_PERIOD(6, hA, hC, hC, 4, 0);
  F2_PERIOD(7, hB, hA, hA, 4, 1);
  F2_PERIOD(8, hC, hB, hB, 0, 1);
  wait_lgkm0(); bar();
  const unsigned xb = lds0 + (unsigned)rg * 18432u + vl;
#pragma unroll
  for (int n = 0; n < 9; n++) lds_w128(xb + (unsigned)(kh ^ 1) * 9216u + n * 1024, kh ? acc[0][n] : acc[1][n]);
  wait_lgkm0(); bar();
  f32x4 o[9];
#pragma unroll
  for (int n = 0; n < 9; n++) o[n] = lds_f128(xb + kh * 9216u + n * 1024) + (kh ? acc[1][n] : acc[0][n]);
  const unsigned xg = (unsigned)WS_XCH + (unsigned)((rb * 4 + ct) * 16) * 9216u + (unsigned)kh * 4608u;
  if (rg != ks) {
    const unsigned d = xg + (unsigned)(rg * 4 + ks) * 9216u;
#pragma unroll
    for (int T = 0; T < 4; T++)
      st128s(u32x4{pk2(o[2 * T][0], o[2 * T][1]), pk2(o[2 * T][2], o[2 * T][3]), pk2(o[2 * T + 1][0], o[2 * T + 1][1]), pk2(o[2 * T + 1][2], o[2 * T + 1][3])}, c.sws, vl + T * 1024u, d);
    __builtin_amdgcn_raw_buffer_store_b64(u32x2{pk2(o[8][0], o[8][1]), pk2(o[8][2], o[8][3])}, c.sws, 4096u + lane * 8u, d, SC1);
  } else {
#pragma unroll
    for (int n = 0; n < 9; n++) lds_w128(lds0 + F_FIN + (unsigned)kh * 9216u + n * 1024u + vl, o[n]);
  }
  wait_lgkm0();
  publish(c, FL_P + (rb * 4 + ct) * 4 + ks, true);
  p6_finish(c, rb, fc);
}

constexpr int M_LDS = 163840;
static_assert(L_WO + 110592 <= L_DUMP && 4 * Q_SLOT + 640 <= L_DUMP && F_LDS <= L_DUMP, "LDS map");
__global__ KATTR void fused_block(const u16* __restrict__ x, const u16* __restrict__ wqkv, const u16* __restrict__ wo, const u16* __restrict__ w1,
                                  const u16* __restrict__ b1, const u16* __restrict__ w2, const u16* __restrict__ b2, u16* __restrict__ out, char* __restrict__ ws) {
  extern __shared__ __attribute__((aligned(16))) char smem[];
  Ctx c;
  c.x = x; c.wqkv = wqkv; c.wo = wo; c.w1 = w1; c.b1 = b1; c.w2 = w2; c.b2 = b2; c.out = out; c.ws = ws;
  c.sws = mk_srd(ws, WS_BYTES);
  c.lds0 = (unsigned)(size_t)smem;
  c.b = blockIdx.x; c.xcd = c.b & 7; c.bi = c.b >> 3;
  c.w = rfl(threadIdx.x >> 6); c.lane = threadIdx.x & 63;
  const int xcd = c.xcd, bi = c.bi, w = c.w, lane = c.lane;
  const int rb4 = (xcd >> 2) * 8 + (bi & 7), fc4 = (xcd & 3) * 4 + (bi >> 3);
  if (bi < 24) {
    p1_qkv(c, (xcd >> 1) * 4 + (bi & 3), (xcd & 1) * 6 + (bi >> 2));
  } else {
    const int j = bi - 24, fc = (xcd & 3) * 4 + (j >> 1), part = (xcd >> 2) * 2 + (j & 1);
    helper_stats(c, xcd * 8 + j, fc, part);
    if (w >= 8) wait_vm<0>();
    bar();
    relayout_w1(c, fc, part);
    publish(c, FL_W1 + fc * 4 + part, true);
  }
  if (w >= 8) p2_loader(c, xcd * 4 + (bi & 3), bi >> 2); else p2_attn(c, xcd * 4 + (bi & 3), bi >> 2);
  if (w >= 8) {
    if (w == 8 && bi < 24) poll(c, FL_A + xcd * 32 + (lane & 31), lane < 32);
    if (w == 9) poll(c, FL_W1 + fc4 * 4 + (lane & 3), lane < 4);
    if (bi < 24) wait_vm<0>();
    bar();
    f_issue(c, fc4, 0); f_issue(c, fc4, 1);
    {
#pragma unroll
      for (int i = 0; i < 2; i++) { const unsigned l = (unsigned)(((w - 8) * 2 + i) * 64 + lane); touch128(c.lds0 + L_DUMP, l < 288u ? l * 128u : 0x7ff00000u, c.sws, (unsigned)WS_W1P + (unsigned)(fc4 * 9 + 1) * F_SLOT + (unsigned)(bi & 7) * 36864u); }
    }
  }
  if (bi < 24) {
    if (w >= 8) {
      bar(); bar(); bar();
      publish(c, FL_H + (xcd * 4 + (bi & 3)) * 6 + (bi >> 2), false);
    } else {
      p3_wo(c, xcd * 4 + (bi & 3), bi >> 2);
    }
  } else {
    if (w < 8) bar();
    const int j = bi - 24, ks = xcd & 3, ct = j >> 1, part = (xcd >> 2) * 2 + (j & 1);
    if (w < 9) relayout_w2(c, c.lds0 + L_WO + (unsigned)w * 9216u, ks * 36 + part * 9 + w, ct);
    publish(c, FL_W2 + (ks * 4 + ct) * 4 + part, w < 9);
  }
  if (w >= 8) p45_loader(c, rb4, fc4); else if (w < 4) p45_ffn<0>(c, rb4, fc4); else p45_ffn<1>(c, rb4, fc4);
}

extern "C" void kernel_launch(void* const* d_in, const int* in_sizes, int n_in, void* d_out, int out_size, void* d_ws, size_t ws_size,
                              hipStream_t stream) {
  (void)hipFuncSetAttribute((const void*)fused_block, hipFuncAttributeMaxDynamicSharedMemorySize, M_LDS);
  fused_block<<<256, 768, M_LDS, stream>>>((const u16*)d_in[0], (const u16*)d_in[1], (const u16*)d_in[2], (const u16*)d_in[4], (const u16*)d_in[5],
                                           (const u16*)d_in[6], (const u16*)d_in[7], (u16*)d_out, (char*)d_ws);
}
```

```cpp
#include <hip/hip_runtime.h>
#include <stdint.h>

typedef unsigned short u16;
typedef __bf16 bf16x8 __attribute__((ext_vector_type(8)));
typedef __bf16 bf16x4 __attribute__((ext_vector_type(4)));
typedef __bf16 bf16x2 __attribute__((ext_vector_type(2)));
typedef float f32x2 __attribute__((ext_vector_type(2)));
typedef float f32x4 __attribute__((ext_vector_type(4)));
typedef float f32x16 __attribute__((ext_vector_type(16)));
typedef unsigned u32x2 __attribute__((ext_vector_type(2)));
typedef unsigned u32x4 __attribute__((ext_vector_type(4)));
#define LAS __attribute__((address_space(3)))
#define DI __device__ __forceinline__
#define SB() __builtin_amdgcn_sched_barrier(0)
#define KATTR __launch_bounds__(768, 3) __attribute__((amdgpu_waves_per_eu(3, 3)))

constexpr int S = 2048, D = 576, H = 32, F = 4608, NQKV = 1728;
constexpr float LN_EPS = 1e-5f;


constexpr int FL_N = 0, FL_D = 1, FL_Q = FL_D + 256, FL_W1 = FL_Q + 192, FL_A = FL_W1 + 64, FL_W2 = FL_A + 256, FL_H = FL_W2 + 64, FL_H1 = FL_H + 192,
              FL_P = FL_H1 + 256, FL_S1 = FL_P + 256, FL_END = FL_S1 + 64;
constexpr size_t WS_QKV = 262144;
static_assert(FL_END * 128 <= WS_QKV, "flag area");
constexpr size_t WS_QKV_SZ = (size_t)3 * H * S * 24 * 2;
constexpr size_t WS_OP = WS_QKV + WS_QKV_SZ;
constexpr size_t WS_OL = WS_OP + (size_t)64 * 32 * 1024;
constexpr size_t WS_HP = WS_OL + (size_t)H * S * 4;
constexpr size_t WS_ST2 = WS_HP + (size_t)S * D * 2;
constexpr size_t WS_W1P = WS_ST2 + (size_t)S * 48;
constexpr size_t WS_W2P = WS_W1P + (size_t)D * F * 2;
constexpr size_t WS_H1P = WS_W2P + (size_t)D * F * 2;
constexpr size_t WS_XCH = WS_H1P + (size_t)S * F * 2;
constexpr size_t WS_ST1 = WS_XCH + (size_t)16 * 4 * 4 * 4 * 2 * 4608;
constexpr size_t WS_CSQ = WS_ST1 + (size_t)S * 8;
constexpr size_t WS_END = WS_CSQ + (size_t)(NQKV + 64) * 4;
constexpr unsigned WS_BYTES = (unsigned)WS_END;


DI unsigned rfl(unsigned x) { return __builtin_amdgcn_readfirstlane(x); }
DI float bflo(unsigned w) { return __uint_as_float(w << 16); }
DI float bfhi(unsigned w) { return __uint_as_float(w & 0xffff0000u); }
DI unsigned pk2(float lo, float hi) { f32x2 v = {lo, hi}; bf16x2 b = __builtin_convertvector(v, bf16x2); return __builtin_bit_cast(unsigned, b); }
DI float dot2(unsigned a, unsigned b, float c) { return __builtin_amdgcn_fdot2_f32_bf16(__builtin_bit_cast(bf16x2, a), __builtin_bit_cast(bf16x2, b), c, false); }

typedef __amdgpu_buffer_rsrc_t srd_t;
DI srd_t mk_srd(const void* p, unsigned bytes) { return __builtin_amdgcn_make_buffer_rsrc((void*)p, (short)0, (int)bytes, 0x00020000); }
DI void dma16(unsigned ldsaddr, unsigned voff, srd_t srd, unsigned soff) {
  asm volatile("s_mov_b32 m0, %0\n\ts_nop 0\n\tbuffer_load_dwordx4 %1, %2, %3 offen lds" :: "s"(ldsaddr), "v"(voff), "s"(srd), "s"(soff) : "memory");
}
DI void touch128(unsigned ldsdump, unsigned voff, srd_t srd, unsigned soff) {
  asm volatile("s_mov_b32 m0, %0\n\ts_nop 0\n\tbuffer_load_dword %1, %2, %3 offen lds" :: "s"(ldsdump), "v"(voff), "s"(srd), "s"(soff) : "memory");
}
template <int N> DI void wait_vm() { asm volatile("s_waitcnt vmcnt(%0)" :: "i"(N) : "memory"); }
DI void wait_lgkm0() { asm volatile("s_waitcnt lgkmcnt(0)" ::: "memory"); }
DI void bar() { asm volatile("" ::: "memory"); __builtin_amdgcn_s_barrier(); asm volatile("" ::: "memory"); }
DI u32x4 lds_u128(unsigned addr) { return *(const LAS u32x4*)(size_t)addr; }
DI f32x4 lds_f128(unsigned addr) { return *(const LAS f32x4*)(size_t)addr; }
DI void lds_w128(unsigned addr, f32x4 v) { *(LAS f32x4*)(size_t)addr = v; }
DI bf16x8 asbf(u32x4 v) { return __builtin_bit_cast(bf16x8, v); }
DI u32x2 lds_tr(unsigned addr) { return __builtin_bit_cast(u32x2, __builtin_amdgcn_ds_read_tr16_b64_v4bf16((LAS bf16x4*)(size_t)addr)); }
DI u32x4 cat4(u32x2 a, u32x2 b) { return u32x4{a[0], a[1], b[0], b[1]}; }
DI f32x4 mfma16(u32x4 a, u32x4 b, f32x4 c) { return __builtin_amdgcn_mfma_f32_16x16x32_bf16(asbf(a), asbf(b), c, 0, 0, 0); }
DI f32x16 mfma32(u32x4 a, u32x4 b, f32x16 c) { return __builtin_amdgcn_mfma_f32_32x32x16_bf16(asbf(a), asbf(b), c, 0, 0, 0); }
template <int NOP> DI float max16(float m, const f32x16& t) {
  float u;
  asm volatile("s_nop %18\n\tv_max3_f32 %1, %2, %3, %4\n\tv_max3_f32 %0, %0, %5, %6\n\tv_max3_f32 %1, %1, %7, %8\n\tv_max3_f32 %0, %0, %9, %10\n\t"
               "v_max3_f32 %1, %1, %11, %12\n\tv_max3_f32 %0, %0, %13, %14\n\tv_max3_f32 %1, %1, %15, %16\n\tv_max3_f32 %0, %0, %1, %17"
               : "+v"(m), "=&v"(u)
               : "v"(t[0]), "v"(t[1]), "v"(t[2]), "v"(t[3]), "v"(t[4]), "v"(t[5]), "v"(t[6]), "v"(t[7]), "v"(t[8]), "v"(t[9]), "v"(t[10]), "v"(t[11]), "v"(t[12]),
                 "v"(t[13]), "v"(t[14]), "v"(t[15]), "i"(NOP));
  return m;
}
template <int CTRL, int RM> DI float dpp_add(float v) { return v + __builtin_bit_cast(float, __builtin_amdgcn_update_dpp(0, __builtin_bit_cast(int, v), CTRL, RM, 0xf, true)); }
DI float wave_sum63(float v) { v = dpp_add<0x111, 0xf>(v); v = dpp_add<0x112, 0xf>(v); v = dpp_add<0x114, 0xf>(v); v = dpp_add<0x118, 0xf>(v); v = dpp_add<0x142, 0xa>(v); return dpp_add<0x143, 0xc>(v); }
DI float sum_x32(float v) { const auto a = __builtin_amdgcn_permlane32_swap(__float_as_uint(v), __float_as_uint(v), false, false); return __uint_as_float(a[0]) + __uint_as_float(a[1]); }
DI float sum_x16(float v) { const auto a = __builtin_amdgcn_permlane16_swap(__float_as_uint(v), __float_as_uint(v), false, false); return __uint_as_float(a[0]) + __uint_as_float(a[1]); }
DI float sum_x8(float v) { return dpp_add<0x128, 0xf>(v); }
DI int swap23(int i) { return (i & 19) | ((i & 4) << 1) | ((i & 8) >> 1); }


constexpr int SC1 = 16;
DI void dma16s(unsigned ldsaddr, unsigned voff, srd_t srd, unsigned soff) {
  asm volatile("s_mov_b32 m0, %0\n\ts_nop 0\n\tbuffer_load_dwordx4 %1, %2, %3 offen sc1 lds" :: "s"(ldsaddr), "v"(voff), "s"(srd), "s"(soff) : "memory");
}
DI void st128s(u32x4 v, srd_t srd, unsigned voff, unsigned soff) {
  asm volatile("buffer_store_dwordx4 %0, %1, %2, %3 offen sc1\n\ts_nop 1" :: "v"(v), "v"(voff), "s"(srd), "s"(soff) : "memory");
}
DI u32x4 ld128s(srd_t srd, unsigned voff, unsigned soff) { return __builtin_amdgcn_raw_buffer_load_b128(srd, voff, soff, SC1); }

struct Ctx {
  const u16 *x, *wqkv, *wo, *w1, *b1, *w2, *b2;
  u16* out;
  char* ws;
  srd_t sws;
  unsigned lds0;
  unsigned long long nonce;
  int b, xcd, bi, w, lane;
};
DI void publish(const Ctx& c, int flag, bool stored) {
  if (stored) wait_vm<0>();
  bar();
  if (threadIdx.x == 0) __hip_atomic_store((unsigned long long*)(c.ws + (size_t)flag * 128), c.nonce, __ATOMIC_RELAXED, __HIP_MEMORY_SCOPE_AGENT);
}
DI void poll(const Ctx& c, int flag, bool active) {
  const unsigned long long* p = (const unsigned long long*)(c.ws + (size_t)flag * 128);
  const unsigned long long t0 = __builtin_amdgcn_s_memrealtime();
  for (;;) {
    const unsigned long long v = active ? __hip_atomic_load(p, __ATOMIC_RELAXED, __HIP_MEMORY_SCOPE_AGENT) : c.nonce;
    if (__all(v == c.nonce)) break;
    if (__builtin_amdgcn_s_memrealtime() - t0 > 2000000ull) break;
  }
}

constexpr int L_DUMP = 163840 - 256;
DI u32x2 entry_load(const Ctx& c) {
  u32x2 nv;
  asm volatile("buffer_load_dwordx2 %0, %1, %2, 0 offen sc1" : "=&v"(nv) : "v"((unsigned)(FL_N * 128)), "s"(c.sws) : "memory");
  return nv;
}
DI void entry_nonce(Ctx& c, u32x2 nv) {
  asm volatile("" : "+v"(nv));
  c.nonce = (((unsigned long long)rfl(nv[1]) << 32) | rfl(nv[0])) + 1ull;
  if (threadIdx.x == 0) __hip_atomic_store((unsigned long long*)(c.ws + (size_t)(FL_D + c.b) * 128), c.nonce, __ATOMIC_RELAXED, __HIP_MEMORY_SCOPE_AGENT);
}

constexpr int L_W1 = 4096;
DI void relayout_w1_fetch(const Ctx& c, int fc, int part) {
  const int l = c.w - 8, lane = c.lane;
  const srd_t sw = mk_srd(c.w1, D * F * 2);
#pragma unroll 1
  for (int pc = l; pc < 81; pc += 4) {
    const int ch = pc * 64 + lane, row = ch / 36, cc = ch % 36;
    dma16(c.lds0 + L_W1 + (unsigned)pc * 1024u, (unsigned)row * (F * 2) + (unsigned)cc * 16u, sw, (unsigned)part * 144u * (F * 2) + (unsigned)fc * 576u);
  }
}
DI void helper_stats(Ctx& c, int hb, int fc, int part) {
  const int w = c.w, lane = c.lane;
  const srd_t sx = mk_srd(c.x, S * D * 2), sw = mk_srd(c.wqkv, hb < 27 ? D * NQKV * 2 : 0);
  if (w >= 8) {
    const u32x2 nv = entry_load(c);
    wait_vm<0>();
    entry_nonce(c, nv);
    bar();
    publish(c, FL_S1 + hb, false);
    relayout_w1_fetch(c, fc, part);
    return;
  }
  const int tid = w * 64 + lane, ch = tid & 7, rg = tid >> 3;
  u32x4 a[4], b2[4], v9[9];
#pragma unroll
  for (int r = 0; r < 4; r++) {
    const unsigned so = (unsigned)(hb * 32 + w * 4 + r) * (D * 2);
    a[r] = __builtin_amdgcn_raw_buffer_load_b128(sx, (unsigned)lane * 16u, so, 0);
    b2[r] = __builtin_amdgcn_raw_buffer_load_b128(sx, lane < 8 ? 1024u + (unsigned)lane * 16u : 0x7ff00000u, so, 0);
  }
#pragma unroll
  for (int i = 0; i < 9; i++) v9[i] = __builtin_amdgcn_raw_buffer_load_b128(sw, (unsigned)(rg * 9 + i) * (NQKV * 2) + (unsigned)ch * 16u, (unsigned)hb * 128u, 0);
  const u32x2 nv = entry_load(c);
  float sm[4], sq[4];
#pragma unroll
  for (int r = 0; r < 4; r++) {
    sm[r] = 0; sq[r] = 0;
#pragma unroll
    for (int e = 0; e < 4; e++) { sm[r] = dot2(a[r][e], 0x3f803f80u, sm[r]); sq[r] = dot2(a[r][e], a[r][e], sq[r]); sm[r] = dot2(b2[r][e], 0x3f803f80u, sm[r]); sq[r] = dot2(b2[r][e], b2[r][e], sq[r]); }
  }
#pragma unroll
  for (int r = 0; r < 4; r++) {
    const float s1 = __builtin_bit_cast(float, __builtin_amdgcn_readlane(__builtin_bit_cast(int, wave_sum63(sm[r])), 63));
    const float s2 = __builtin_bit_cast(float, __builtin_amdgcn_readlane(__builtin_bit_cast(int, wave_sum63(sq[r])), 63));
    const float mean = s1 * (1.0f / D), var = fmaxf(s2 * (1.0f / D) - mean * mean, 0.f);
    sm[r] = mean; sq[r] = rsqrtf(var + LN_EPS);
  }
  {
    const int r = lane & 3;
    const u32x2 v = {__float_as_uint(r == 0 ? sm[0] : r == 1 ? sm[1] : r == 2 ? sm[2] : sm[3]), __float_as_uint(r == 0 ? sq[0] : r == 1 ? sq[1] : r == 2 ? sq[2] : sq[3])};
    __builtin_amdgcn_raw_buffer_store_b64(v, c.sws, lane < 4 ? (unsigned)lane * 8u : 0x7ff00000u, (unsigned)WS_ST1 + (unsigned)(hb * 32 + w * 4) * 8u, SC1);
  }
  float s8[8] = {0, 0, 0, 0, 0, 0, 0, 0};
#pragma unroll
  for (int i = 0; i < 9; i++)
#pragma unroll
    for (int e = 0; e < 4; e++) { s8[2 * e] += bflo(v9[i][e]); s8[2 * e + 1] += bfhi(v9[i][e]); }
#pragma unroll
  for (int e = 0; e < 8; e++) s8[e] = sum_x32(sum_x16(sum_x8(s8[e])));
  if (lane < 8) { lds_w128(c.lds0 + (unsigned)(w * 64 + lane * 8) * 4u, f32x4{s8[0], s8[1], s8[2], s8[3]}); lds_w128(c.lds0 + (unsigned)(w * 64 + lane * 8) * 4u + 16, f32x4{s8[4], s8[5], s8[6], s8[7]}); }
  wait_lgkm0(); bar();
  if (w == 0) {
    float t = 0;
#pragma unroll
    for (int k = 0; k < 8; k++) t += *(const LAS float*)(size_t)(c.lds0 + (unsigned)(k * 64 + lane) * 4u);
    __builtin_amdgcn_raw_buffer_store_b32(__float_as_uint(t), c.sws, hb < 27 ? (unsigned)lane * 4u : 0x7ff00000u, (unsigned)WS_CSQ + (unsigned)hb * 256u, SC1);
  }
  wait_lgkm0();
  wait_vm<0>();
  entry_nonce(c, nv);
  publish(c, FL_S1 + hb, true);
}

DI void relayout_w1(const Ctx& c, int fc, int part) {
  const int w = c.w, lane = c.lane; const unsigned lds0 = c.lds0 + L_W1;
  const int h = lane >> 5, blk = (lane >> 4) & 1, q = (lane >> 2) & 3, p = lane & 3;
  const unsigned ra = lds0 + (unsigned)(8 * h + q) * 576u + (unsigned)(16 * blk + 8 * (p & 1) + 4 * (p >> 1)) * 2u;
  for (int fr = w; fr < 81; fr += 12) {
    const int sl = fr / 9, tile = fr % 9, s = part * 9 + sl, kh = s / 18;
    const unsigned a = ra + (unsigned)sl * 16u * 576u + (unsigned)tile * 64u;
    const u32x4 v = cat4(lds_tr(a), lds_tr(a + 4 * 576));
    const int slot = tile == 8 ? 8 : (kh == 0 ? (tile ^ 1) : tile);
    const int s18 = s % 18;
    st128s(v, c.sws, (unsigned)lane * 16u, (unsigned)WS_W1P + ((unsigned)(fc * 9 + slot) * 36u + (unsigned)((s18 / 9) * 18 + kh * 9 + s18 % 9)) * 1024u);
  }
}
DI void relayout_w2(const Ctx& c, unsigned ldsw, int kappa, int ct) {
  const int lane = c.lane;
  const srd_t sw = mk_srd(c.w2, D * F * 2);
#pragma unroll
  for (int pc = 0; pc < 9; pc++) {
    const int ch = pc * 64 + lane, row = ch / 18, cc = ch % 18;
    dma16(ldsw + (unsigned)pc * 1024u, (unsigned)row * (D * 2) + (unsigned)cc * 16u, sw, (unsigned)kappa * 32u * (D * 2) + (unsigned)ct * 288u);
  }
  wait_vm<0>();
  const int g = lane >> 4, q = (lane >> 2) & 3, p = lane & 3;
  const unsigned ra = ldsw + (unsigned)(8 * g + q) * 288u;
  const int ks = kappa / 36, kst = (kappa % 36 + 36 - 9 * ct) % 36;
#pragma unroll
  for (int nt = 0; nt < 9; nt++) {
    const unsigned col = nt < 8 ? (unsigned)(32 * (nt >> 1) + 8 * p + 4 * (nt & 1)) * 2u : (unsigned)(128 + 4 * p) * 2u;
    const u32x4 v = cat4(lds_tr(ra + col), lds_tr(ra + col + 4 * 288));
    st128s(v, c.sws, (unsigned)lane * 16u, (unsigned)WS_W2P + ((unsigned)(ks * 4 + ct) * 324u + (unsigned)(((kst >> 2) * 2 + (kst & 1)) * 18 + ((kst >> 1) & 1) * 9 + nt)) * 1024u);
  }
  wait_lgkm0();
}

constexpr int Q_SLOT = 36864;
constexpr int Q_CT = 4 * Q_SLOT;
constexpr int Q_STG = Q_SLOT;
DI unsigned q_voff(int p, int lane) {
  if (p < 16) return (unsigned)(lane >> 3) * (D * 2) + (unsigned)(((lane & 7) ^ ((4 * (p & 1) + (lane >> 4)) & 7)) * 16);
  const int ch = 64 * (p - 16) + lane, row = ch / 20, cc = ch % 20;
  return (unsigned)row * (NQKV * 2) + (unsigned)cc * 16u;
}
DI void q_issue(const Ctx& c, srd_t sx, srd_t sw, int rb, int ct, int p, unsigned voff, int t, unsigned ring) {
  if (p < 16) dma16(c.lds0 + ring + (unsigned)p * 1024u, voff, sx, ((unsigned)rb * 128u + (unsigned)p * 8u) * (D * 2) + (unsigned)t * 128u);
  else dma16(c.lds0 + ring + (unsigned)p * 1024u, voff, sw, (unsigned)ct * 288u + (unsigned)t * 64u * (NQKV * 2));
}
DI void p1_loader(Ctx& c, int rb, int ct) {
  const int w = c.w, lane = c.lane, l = w - 8;
  const srd_t sx = mk_srd(c.x, S * D * 2), sw = mk_srd(c.wqkv, D * NQKV * 2);
  const u32x2 nv = entry_load(c);
#pragma unroll
  for (int t = 0; t < 2; t++)
#pragma unroll
    for (int i = 0; i < 3; i++) q_issue(c, sx, sw, rb, ct, w + 12 * i, q_voff(w + 12 * i, lane), t, (unsigned)t * Q_SLOT);
  unsigned vw[5];
#pragma unroll
  for (int i = 0; i < 5; i++) vw[i] = q_voff(16 + l + 4 * i, lane);
#define Q_W_ISSUE(t, ring) do { \
    _Pragma("unroll") for (int i = 0; i < 5; i++) dma16(c.lds0 + (ring) + (unsigned)(16 + l + 4 * i) * 1024u, vw[i], sw, (unsigned)ct * 288u + (unsigned)(t) * 64u * (NQKV * 2)); } while (0)
  Q_W_ISSUE(2, 2u * Q_SLOT);
  wait_vm<8>();
  entry_nonce(c, nv);
  bar();
  wait_vm<5>(); bar();
  Q_W_ISSUE(3, 3u * Q_SLOT);
#pragma unroll 1
  for (int t = 1; t < 6; t++) {
    wait_vm<5>(); bar();
    Q_W_ISSUE(t + 3, (unsigned)((t + 3) & 3) * Q_SLOT);
  }
  wait_vm<5>(); bar();
  if (l == 0) { const int g0 = (144 * ct) >> 6; poll(c, FL_S1 + (lane < 4 ? 4 * rb + lane : min(g0 + (lane & 3), 26)), lane < 8); }
  wait_vm<0>(); bar();
  bar();
  bar();
}
template <int NT, int T0>
DI void p1_body(Ctx& c, int rb, int ct) {
  const int w = c.w, lane = c.lane, rg = w & 3; const unsigned lds0 = c.lds0;
  constexpr bool XI = NT == 2;
  const srd_t sx = mk_srd(c.x, S * D * 2);
  const unsigned vx = q_voff(rg, lane);
#define Q_X_ISSUE1(t, ring, i) dma16(lds0 + (ring) + (unsigned)(rg + 4 * (i)) * 1024u, vx, sx, ((unsigned)rb * 128u + (unsigned)(rg + 4 * (i)) * 8u) * (D * 2) + (unsigned)(t) * 128u)
  {
    const srd_t sw = mk_srd(c.wqkv, D * NQKV * 2);
    const u32x2 nv = entry_load(c);
#pragma unroll
    for (int t = 0; t < 2; t++)
#pragma unroll
      for (int i = 0; i < 3; i++) q_issue(c, sx, sw, rb, ct, w + 12 * i, q_voff(w + 12 * i, lane), t, (unsigned)t * Q_SLOT);
    if (XI) { Q_X_ISSUE1(2, 2u * Q_SLOT, 0); Q_X_ISSUE1(2, 2u * Q_SLOT, 1); Q_X_ISSUE1(2, 2u * Q_SLOT, 2); Q_X_ISSUE1(2, 2u * Q_SLOT, 3); wait_vm<7>(); } else wait_vm<3>();
    entry_nonce(c, nv);
  }
  const int tok = lane & 31, h = lane >> 5, blk = (lane >> 4) & 1, q4 = (lane >> 2) & 3, pp = lane & 3;
  const unsigned xrow = lds0 + (unsigned)(rg * 32 + tok) * 128u;
  unsigned xj[4];
#pragma unroll
  for (int j = 0; j < 4; j++) xj[j] = xrow + (unsigned)(((2 * j + h) ^ ((tok >> 1) & 7)) * 16);
  const unsigned wj = lds0 + 16384u + (unsigned)(8 * h + q4) * 320u + (unsigned)(32 * T0 + 16 * blk + 8 * (pp & 1) + 4 * (pp >> 1)) * 2u;
  f32x16 acc[NT];
#pragma unroll
  for (int n = 0; n < NT; n++) acc[n] = f32x16{};
  bar();
  u32x4 wf[4][NT], xf[4];
#pragma unroll
  for (int j = 0; j < 2; j++) {
    xf[j] = lds_u128(xj[j]);
#pragma unroll
    for (int n = 0; n < NT; n++) wf[j][n] = cat4(lds_tr(wj + j * 5120 + n * 64), lds_tr(wj + j * 5120 + n * 64 + 4 * 320));
  }
  unsigned r0 = 0, r1 = Q_SLOT, r3 = 3 * Q_SLOT;
#define Q_PERIOD(t, SYNC, ISSUE, EXTRA) do { \
    if (XI && (SYNC) == 2) wait_vm<4>(); \
    if (XI && (SYNC) == 3) wait_vm<0>(); \
    if (SYNC) bar(); \
    EXTRA; \
    unsigned wa_ = wj + r0, wn_ = wj + r1, xa_[4]; \
    _Pragma("unroll") for (int j = 0; j < 4; j++) { xa_[j] = xj[(j + 2) & 3] + (j < 2 ? r0 : r1); asm volatile("" : "+v"(xa_[j])); } \
    asm volatile("" : "+v"(wa_), "+v"(wn_)); \
    _Pragma("unroll") for (int j = 0; j < 4; j++) { \
      if ((SYNC) == 0 && j == 2) { if (XI) wait_vm<6>(); else wait_vm<0>(); bar(); }        \
      const u32x4 xc = xf[j]; \
      xf[(j + 2) & 3] = lds_u128(xa_[j]); \
      _Pragma("unroll") for (int n = 0; n < NT; n++) { \
        const u32x4 cur = wf[j][n]; \
        const unsigned a_ = (j < 2 ? wa_ : wn_) + ((j + 2) & 3) * 5120 + n * 64; \
        wf[(j + 2) & 3][n] = cat4(lds_tr(a_), lds_tr(a_ + 4 * 320)); \
        acc[n] = mfma32(cur, xc, acc[n]); \
        if (XI && (ISSUE) && n == 0) Q_X_ISSUE1((t) + 3, r3, j); \
        SB(); \
      } \
    } \
    r0 = r1; r1 = r1 == 3u * Q_SLOT ? 0u : r1 + Q_SLOT; r3 = r3 == 3u * Q_SLOT ? 0u : r3 + Q_SLOT; \
  } while (0)
  Q_PERIOD(0, 0, true, (void)0);
#pragma unroll 1
  for (int t = 1; t < 6; t++) Q_PERIOD(t, 2, true, (void)0);
  const int tl = rg * 32 + tok, type = ct >> 2;
  u32x2 stv; u32x4 csv;
  Q_PERIOD(6, 2, false, (void)0);
  Q_PERIOD(7, 3, false, (
    { asm volatile("buffer_load_dwordx2 %0, %1, %2, %3 offen sc1" : "=&v"(stv) : "v"((unsigned)tl * 8u), "s"(c.sws), "s"((unsigned)WS_ST1 + (unsigned)rb * 1024u) : "memory");
      asm volatile("buffer_load_dwordx4 %0, %1, %2, %3 offen sc1" : "=&v"(csv) : "v"((unsigned)min(lane, 39) * 16u), "s"(c.sws), "s"((unsigned)WS_CSQ + (unsigned)ct * 576u) : "memory"); }));
  asm volatile("s_waitcnt vmcnt(0)" : "+v"(stv), "+v"(csv) :: "memory");
  if (w == 0 && lane < 40) *(LAS u32x4*)(size_t)(lds0 + Q_CT + (unsigned)lane * 16u) = csv;
  wait_lgkm0();
  Q_PERIOD(8, 1, false, (void)0);
  f32x2 st;
  st[0] = __uint_as_float(stv[1]) * (type == 0 ? 0.23570226039551584f * 1.4426950408889634f : 1.0f);
  st[1] = -__uint_as_float(stv[0]) * st[0];
#pragma unroll
  for (int n = 0; n < NT; n++)
#pragma unroll
    for (int hf = 0; hf < 2; hf++) {
      if (T0 + n == 4 && hf == 1) continue;
      const int c0 = 32 * (T0 + n) + 16 * hf;
      const f32x4 ca = lds_f128(lds0 + Q_CT + (unsigned)(c0 + 8 * h) * 4u), cb = lds_f128(lds0 + Q_CT + (unsigned)(c0 + 8 * h) * 4u + 16);
      u32x4 o;
#pragma unroll
      for (int e = 0; e < 4; e++) {
        const float c0v = e < 2 ? ca[2 * e] : cb[2 * e - 4], c1v = e < 2 ? ca[2 * e + 1] : cb[2 * e - 3];
        o[e] = pk2(__builtin_fmaf(acc[n][8 * hf + 2 * e], st[0], c0v * st[1]), __builtin_fmaf(acc[n][8 * hf + 2 * e + 1], st[0], c1v * st[1]));
      }
      *(LAS u32x4*)(size_t)(lds0 + Q_STG + (unsigned)tl * 304u + (unsigned)(c0 + 8 * h) * 2u) = o;
    }
  wait_lgkm0(); bar();
}
DI void p1_qkv(Ctx& c, int rb, int ct) {
  if (c.w >= 8) p1_loader(c, rb, ct); else if (c.w < 4) p1_body<3, 0>(c, rb, ct); else p1_body<2, 3>(c, rb, ct);
  {
    const int tid = c.w * 64 + c.lane, tg = tid >= 384, t = tid - 384 * tg, tk = t / 3, part = t - 3 * tk, type = ct >> 2;
    const unsigned src = c.lds0 + Q_STG + (unsigned)tk * 304u + (unsigned)part * 16u + (unsigned)tg * 144u;
    const unsigned keep = part < 2 ? 0xffffffffu : 0u, pad1 = part < 2 ? 0u : (type == 0 ? 0x3f800000u : 0x00003f80u), pad2 = part < 2 || type == 0 ? 0u : 0x3f803f80u;
    const unsigned dst = (unsigned)WS_QKV + ((unsigned)(type * 32 + (ct & 3) * 8) * S + (unsigned)rb * 128u) * 48u;
    u32x4 v[4];
#pragma unroll
    for (int hh = 0; hh < 4; hh++)
#pragma unroll
      for (int e = 0; e < 4; e++) v[hh][e] = *(const LAS unsigned*)(size_t)(src + hh * 36 + e * 4);
#pragma unroll
    for (int hh = 0; hh < 4; hh++) {
      v[hh][1] = (v[hh][1] & keep) | pad1; v[hh][2] = (v[hh][2] & keep) | pad2; v[hh][3] &= keep;
      st128s(v[hh], c.sws, (unsigned)t * 16u + (unsigned)(4 * tg + hh) * (S * 48), dst);
    }
  }
  wait_lgkm0();
  publish(c, FL_Q + rb * 12 + ct, true);
}

constexpr int L_K = 0, L_V = 24576, L_WO = 49216;
DI void p2_attn(const Ctx& c, int head, int qb) {
  const int w = c.w, lane = c.lane; const unsigned lds0 = c.lds0;
  const unsigned ldsK = lds0 + L_K, ldsV = lds0 + L_V, vl = lane * 16;
  const unsigned kbase = (unsigned)WS_QKV + (unsigned)((32 + head) * S + 256 * (qb - 1)) * 48u;
  const int qq = lane & 31, h = lane >> 5;
  const int q0 = 256 * qb + 32 * w;
  f32x16 cold, cdiag;
#pragma unroll
  for (int r = 0; r < 16; r++) {
    const int kk = 16 * (r >> 3) + 8 * h + (r & 7);
    cold[r] = kk > qq ? 0.f : -1e30f;
    cdiag[r] = kk <= qq ? 0.f : -1e30f;
  }
  const int key = swap23(lane & 31);
  unsigned ka0 = ldsK + (unsigned)(key + 32 * w) * 48 + 16 * h;
  unsigned ka1 = ldsK + (unsigned)(key + 32 * w) * 48 + 32;
  const int blk = (lane >> 4) & 1, q4 = (lane >> 2) & 3, pp = lane & 3;
  unsigned va = ldsV + (unsigned)(32 * w + 8 * h + q4) * 48 + 32 * blk + 8 * pp;
  asm volatile("" : "+v"(ka0), "+v"(ka1), "+v"(va), "+v"(cold), "+v"(cdiag));
  if (qb == 0) {
    const int tid = w * 64 + lane;
    if (tid < 256) {
      const u32x4 z = {0, 0, 0, 0};
      *(LAS u32x4*)(size_t)(ldsK + tid * 48) = z; *(LAS u32x4*)(size_t)(ldsK + tid * 48 + 16) = z; *(LAS u32x4*)(size_t)(ldsK + tid * 48 + 32) = u32x4{0, 0xf1493f80u, 0, 0};
      *(LAS u32x4*)(size_t)(ldsV + tid * 48) = z; *(LAS u32x4*)(size_t)(ldsV + tid * 48 + 16) = z; *(LAS u32x4*)(size_t)(ldsV + tid * 48 + 32) = z;
    }
  }
  if (w == 0) {
    const int hg = head >> 3, l = lane;
    const int ty = l < 2 ? 0 : (l < 6 ? 1 : 2), rbl = l < 2 ? 2 * qb + l : 2 * qb - 2 + ((l - 2) & 3);
    poll(c, FL_Q + rbl * 12 + ty * 4 + hg, l < 10 && rbl >= 0);
  }
  bar();
#pragma unroll
  for (int j = 0; j < 3; j++) {
    const unsigned pc = (unsigned)(w * 3 + j) * 1024u;
    if (qb > 0 || pc >= 12288u) dma16s(ldsK + pc, vl, c.sws, kbase + pc);
  }
  const unsigned qo = (unsigned)WS_QKV + (unsigned)(head * S + q0) * 48u;
  const u32x4 qf0 = ld128s(c.sws, (unsigned)qq * 48u + 16u * h, qo);
  u32x4 qf1 = ld128s(c.sws, (unsigned)qq * 48u + 32u, qo);
  if (h) qf1 = u32x4{0, 0, 0, 0};
  wait_vm<0>(); wait_lgkm0(); bar();
  const f32x16 zero = {};
  u32x4 k0[9], k1[9];
#pragma unroll
  for (int j = 0; j < 9; j++) { k0[j] = lds_u128(ka0 + j * 1536); k1[j] = lds_u128(ka1 + j * 1536); }
  float m = -1e30f;
  {
    f32x16 sA = mfma32(k1[0], qf1, mfma32(k0[0], qf0, cold)), sB;
#pragma unroll
    for (int j = 0; j < 9; j++) {
      f32x16& cur = (j & 1) ? sB : sA; f32x16& nxt = (j & 1) ? sA : sB;
      if (j + 1 < 9) nxt = mfma32(k1[j + 1], qf1, mfma32(k0[j + 1], qf0, j + 1 == 8 ? cdiag : zero));
      SB();
      m = j == 0 ? max16<11>(m, cur) : (j < 8 ? max16<1>(m, cur) : max16<3>(m, cur));
      SB();
    }
  }
  { const auto pr = __builtin_amdgcn_permlane32_swap(__float_as_uint(m), __float_as_uint(m), false, false); m = fmaxf(__uint_as_float(pr[0]), __uint_as_float(pr[1])); }
  if (h == 0) {
    const unsigned t0 = pk2(-m, 0.f) & 0xffffu; const float r1 = -m - bflo(t0);
    const unsigned t1 = pk2(r1, 0.f) & 0xffffu; const float r2 = r1 - bflo(t1);
    qf1[1] = (qf1[1] & 0xffff0000u) | t0;
    qf1[2] = pk2(bflo(t1), r2);
  }
  bar();
  f32x16 oacc = {};
  {
    f32x16 sA = mfma32(k1[0], qf1, mfma32(k0[0], qf0, cold)), sB;
    u32x4 v0 = cat4(lds_tr(va), lds_tr(va + 4 * 48)), v1 = cat4(lds_tr(va + 16 * 48), lds_tr(va + 20 * 48));
#pragma unroll
    for (int j = 0; j < 9; j++) {
      f32x16& cur = (j & 1) ? sB : sA; f32x16& nxt = (j & 1) ? sA : sB;
      if (j + 1 < 9) nxt = mfma32(k0[j + 1], qf0, j + 1 == 8 ? cdiag : zero);
      SB();
      u32x4 p0, p1;
      {
        float e0 = cur[0]; asm volatile("" : "+v"(e0));
        p0[0] = pk2(__builtin_amdgcn_exp2f(e0), __builtin_amdgcn_exp2f(cur[1]));
#pragma unroll
        for (int e = 1; e < 4; e++) p0[e] = pk2(__builtin_amdgcn_exp2f(cur[2 * e]), __builtin_amdgcn_exp2f(cur[2 * e + 1]));
        asm volatile("" : "+v"(p0));
      }
      SB();
      if (j + 1 < 9) nxt = mfma32(k1[j + 1], qf1, nxt);
      oacc = mfma32(v0, p0, oacc);
      if (j + 1 < 9) v0 = cat4(lds_tr(va + (j + 1) * 1536), lds_tr(va + (j + 1) * 1536 + 4 * 48));
      SB();
      {
        float e8 = cur[8]; asm volatile("" : "+v"(e8));
        p1[0] = pk2(__builtin_amdgcn_exp2f(e8), __builtin_amdgcn_exp2f(cur[9]));
#pragma unroll
        for (int e = 1; e < 4; e++) p1[e] = pk2(__builtin_amdgcn_exp2f(cur[8 + 2 * e]), __builtin_amdgcn_exp2f(cur[8 + 2 * e + 1]));
        asm volatile("" : "+v"(p1));
      }
      SB();
      oacc = mfma32(v1, p1, oacc);
      if (j + 1 < 9) v1 = cat4(lds_tr(va + (j + 1) * 1536 + 16 * 48), lds_tr(va + (j + 1) * 1536 + 20 * 48));
      SB();
    }
  }
  const float l = __uint_as_float(__builtin_amdgcn_permlane32_swap(__float_as_uint(oacc[10]), __float_as_uint(oacc[10]), false, false)[0]);
  const float inv = __builtin_amdgcn_rcpf(l);
  const u32x4 om = {pk2(oacc[0] * inv, oacc[1] * inv), pk2(oacc[2] * inv, oacc[3] * inv), pk2(oacc[4] * inv, oacc[5] * inv), pk2(oacc[6] * inv, oacc[7] * inv)};
  st128s(om, c.sws, (unsigned)lane * 16u, (unsigned)WS_OP + (unsigned)((q0 >> 5) * 32 + head) * 1024u);
  __builtin_amdgcn_raw_buffer_store_b32(pk2(oacc[8] * inv, oacc[9] * inv), c.sws, h == 0 ? (unsigned)qq * 4u : 0x7ff00000u, (unsigned)WS_OL + (unsigned)(head * S + q0) * 4u, SC1);
  publish(c, FL_A + qb * 32 + head, true);
}

DI void p3_prefetch(const Ctx& c, int ct) {
  const int l = c.w - 8, lane = c.lane;
  const srd_t sw = mk_srd(c.wo, D * D * 2);
  unsigned vw[3];
#pragma unroll
  for (int i = 0; i < 3; i++) { const int ch = 64 * i + lane; vw[i] = (unsigned)(ch / 12) * (D * 2) + (unsigned)(ch % 12) * 16u; }
#pragma unroll 1
  for (int gi = 0; gi < 9; gi++) {
    const int grp = l * 9 + gi;
#pragma unroll
    for (int i = 0; i < 3; i++) dma16(c.lds0 + L_WO + (unsigned)(grp * 3 + i) * 1024u, vw[i], sw, (unsigned)grp * 16u * (D * 2) + (unsigned)ct * 192u);
  }
}
DI void p2_loader(const Ctx& c, int head, int qb) {
  const int xcd = c.xcd, bi = c.bi, l = c.w - 8;
  const unsigned vbase = (unsigned)WS_QKV + (unsigned)((64 + head) * S + 256 * (qb - 1)) * 48u;
  bar();
#pragma unroll
  for (int j = 0; j < 6; j++) {
    const unsigned pc = (unsigned)(l * 6 + j) * 1024u;
    if (qb > 0 || pc >= 12288u) dma16s(c.lds0 + L_V + pc, c.lane * 16u, c.sws, vbase + pc);
  }
  bar();
  wait_vm<0>(); bar();
  if (bi < 24) {
    p3_prefetch(c, bi >> 2);
  } else {
    const srd_t sw2 = mk_srd(c.w2, D * F * 2);
    const int j = bi - 24, ks = xcd & 3, ct2 = j >> 1, part = (xcd >> 2) * 2 + (j & 1), row0 = (ks * 36 + part * 9) * 32;
#pragma unroll
    for (int i = 0; i < 4; i++) {
      const int ln = (l * 4 + i) * 64 + c.lane, row = ln / 3;
      touch128(c.lds0 + L_DUMP, ln < 864 ? (unsigned)row * (D * 2) + (unsigned)(ln - 3 * row) * 128u : 0x7ff00000u, sw2, (unsigned)row0 * (D * 2) + (((unsigned)ct2 * 288u) & ~127u));
    }
  }
  publish(c, FL_A + qb * 32 + head, false);
}
template <int KQ>
DI void p3_body(const Ctx& c, int rb, int ct) {
  constexpr int kq = KQ;
  const int lane = c.lane, rg = c.w & 1; const unsigned lds0 = c.lds0;
  const int qt = rb * 2 + rg, tok = lane & 31, h = lane >> 5;
  const int row = qt * 32 + tok;
  const u32x4 xr0 = *(const u32x4*)(c.x + (size_t)row * D + ct * 96 + 16 * kq + 8 * h);
  const u32x4 xr1 = *(const u32x4*)(c.x + (size_t)row * D + ct * 96 + 16 * (kq < 2 ? kq + 4 : kq) + 8 * h);
  unsigned hso = rfl((unsigned)WS_HP + (unsigned)(qt * 36 + ct * 6) * 1024u);
  asm volatile("" : "+s"(hso));
  const int blk = (lane >> 4) & 1, q4 = (lane >> 2) & 3, pp = lane & 3;
  const unsigned colb = lds0 + L_WO + (unsigned)(16 * blk + 8 * (pp & 1) + 4 * (pp >> 1)) * 2u;
  unsigned ah = colb + (unsigned)(18 * 9 * kq + 4 * h + q4) * 192u;
  unsigned al = colb + (unsigned)(18 * (4 * h + (q4 >> 1)) + 16 + (q4 & 1)) * 192u;
  asm volatile("" : "+v"(ah), "+v"(al));
#define P3_W(s, n) ((kq < 3 || (s) < 5) ? cat4(lds_tr(ah + (s) * 3456 + (n) * 64), lds_tr(ah + (s) * 3456 + 1536 + (n) * 64)) \
                                        : cat4(lds_tr(al + ((s) - 5) * 27648 + (n) * 64), lds_tr(al + ((s) - 5) * 27648 + 6912 + (n) * 64)))
  bar();
  u32x4 of[9];
  {
    const unsigned so = (unsigned)WS_OP + (unsigned)(qt * 32 + kq * 9) * 1024u;
#pragma unroll
    for (int s = 0; s < (kq < 3 ? 9 : 5); s++) of[s] = ld128s(c.sws, (unsigned)lane * 16u + s * 1024u, so);
    if (kq == 3) {
#pragma unroll
      for (int G = 0; G < 4; G++)
#pragma unroll
        for (int i = 0; i < 4; i++)
          of[5 + G][i] = __builtin_amdgcn_raw_buffer_load_b32(c.sws, (unsigned)((4 * h + i) * S + tok) * 4u, (unsigned)WS_OL + (unsigned)(8 * G * S + qt * 32) * 4u, SC1);
    }
  }
  u32x4 wf[3][3];
#pragma unroll
  for (int s = 0; s < 2; s++)
#pragma unroll
    for (int n = 0; n < 3; n++) wf[s][n] = P3_W(s, n);
  SB();
  f32x16 acc[3] = {};
#pragma unroll
  for (int s = 0; s < 9; s++) {
#pragma unroll
    for (int n = 0; n < 3; n++) {
      if (s + 2 < 9) wf[(s + 2) % 3][n] = P3_W(s + 2, n);
      acc[n] = mfma32(wf[s % 3][n], of[s], acc[n]);
    }
    SB();
  }
  wait_lgkm0(); bar();
  const unsigned xb = lds0 + L_WO + (unsigned)rg * 49152u + (unsigned)lane * 16u;
#pragma unroll
  for (int f = 0; f < 6; f++) {
    if ((f & 3) == kq) continue;
    const int r8 = 8 * (f & 1);
    lds_w128(xb + (unsigned)(f * 4 + kq) * 2048u, f32x4{acc[f >> 1][r8], acc[f >> 1][r8 + 1], acc[f >> 1][r8 + 2], acc[f >> 1][r8 + 3]});
    lds_w128(xb + (unsigned)(f * 4 + kq) * 2048u + 1024u, f32x4{acc[f >> 1][r8 + 4], acc[f >> 1][r8 + 5], acc[f >> 1][r8 + 6], acc[f >> 1][r8 + 7]});
  }
  wait_lgkm0(); bar();
  float ssum = 0, ssq = 0;
#pragma unroll
  for (int i = 0; i < (kq < 2 ? 2 : 1); i++) {
    const int f = kq + 4 * i, r8 = 8 * (f & 1);
    f32x4 a = {acc[f >> 1][r8], acc[f >> 1][r8 + 1], acc[f >> 1][r8 + 2], acc[f >> 1][r8 + 3]}, cc = {acc[f >> 1][r8 + 4], acc[f >> 1][r8 + 5], acc[f >> 1][r8 + 6], acc[f >> 1][r8 + 7]};
#pragma unroll
    for (int k = 0; k < 4; k++)
      if (k != kq) { a += lds_f128(xb + (unsigned)(f * 4 + k) * 2048u); cc += lds_f128(xb + (unsigned)(f * 4 + k) * 2048u + 1024u); }
    const u32x4 xr = i ? xr1 : xr0;
    u32x4 o;
    o[0] = pk2(bflo(xr[0]) + a[0], bfhi(xr[0]) + a[1]); o[1] = pk2(bflo(xr[1]) + a[2], bfhi(xr[1]) + a[3]);
    o[2] = pk2(bflo(xr[2]) + cc[0], bfhi(xr[2]) + cc[1]); o[3] = pk2(bflo(xr[3]) + cc[2], bfhi(xr[3]) + cc[3]);
    st128s(o, c.sws, (unsigned)lane * 16u, hso + (unsigned)f * 1024u);
#pragma unroll
    for (int e = 0; e < 4; e++) { ssum = dot2(o[e], 0x3f803f80u, ssum); ssq = dot2(o[e], o[e], ssq); }
  }
  { const auto a = __builtin_amdgcn_permlane32_swap(__float_as_uint(ssum), __float_as_uint(ssum), false, false); ssum = __uint_as_float(a[0]) + __uint_as_float(a[1]);
    const auto b2 = __builtin_amdgcn_permlane32_swap(__float_as_uint(ssq), __float_as_uint(ssq), false, false); ssq = __uint_as_float(b2[0]) + __uint_as_float(b2[1]); }
  const unsigned stb = lds0 + L_WO + 98304u;
  if (h == 0) *(LAS f32x2*)(size_t)(stb + (unsigned)(rg * 32 + tok) * 32u + kq * 8u) = f32x2{ssum, ssq};
  wait_lgkm0(); bar();
  if (c.w == 0) {
    const f32x4 a = lds_f128(stb + lane * 32u), cc = lds_f128(stb + lane * 32u + 16);
    const u32x2 v = {__float_as_uint((a[0] + a[2]) + (cc[0] + cc[2])), __float_as_uint((a[1] + a[3]) + (cc[1] + cc[3]))};
    __builtin_amdgcn_raw_buffer_store_b64(v, c.sws, (unsigned)lane * 8u, (unsigned)WS_ST2 + (unsigned)(rb * 6 + ct) * 512u, SC1);
  }
}
DI void p3_wo(const Ctx& c, int rb, int ct) {
  const int kq = c.w >> 1;
  if (kq == 0) p3_body<0>(c, rb, ct); else if (kq == 1) p3_body<1>(c, rb, ct); else if (kq == 2) p3_body<2>(c, rb, ct); else p3_body<3>(c, rb, ct);
  publish(c, FL_H + rb * 6 + ct, true);
}

constexpr float GC0 = -2.0f * 0.7978845608028654f * 1.4426950408889634f, GC1 = GC0 * 0.044715f;
DI f32x2 gelu2(f32x2 x) {
  const f32x2 t = x * (GC1 * (x * x) + GC0);
  f32x2 e; e[0] = __builtin_amdgcn_exp2f(t[0]); e[1] = __builtin_amdgcn_exp2f(t[1]);
  const f32x2 d = e + 1.0f;
  f32x2 r; r[0] = __builtin_amdgcn_rcpf(d[0]); r[1] = __builtin_amdgcn_rcpf(d[1]);
  return x * r;
}
struct G2 { float x0, x1, t0, t1; };
DI void g2_s1(G2& g, float a0, float a1, float p0, float p1) {
  asm volatile("v_add_f32_e32 %0, %4, %6\n\tv_add_f32_e32 %1, %5, %7\n\tv_mul_f32_e32 %2, %0, %0\n\tv_mul_f32_e32 %3, %1, %1"
               : "=&v"(g.x0), "=&v"(g.x1), "=&v"(g.t0), "=&v"(g.t1) : "v"(a0), "v"(a1), "v"(p0), "v"(p1));
}
DI void g2_s2(G2& g, float vc1) {
  asm volatile("v_fmaak_f32 %0, %0, %4, 0xc0135761\n\tv_fmaak_f32 %1, %1, %4, 0xc0135761\n\tv_mul_f32_e32 %0, %2, %0\n\tv_mul_f32_e32 %1, %3, %1"
               : "+v"(g.t0), "+v"(g.t1) : "v"(g.x0), "v"(g.x1), "v"(vc1));
}
DI void g2_s3(G2& g) {
  asm volatile("v_exp_f32_e32 %0, %0\n\tv_exp_f32_e32 %1, %1\n\tv_add_f32_e32 %0, 1.0, %0\n\tv_add_f32_e32 %1, 1.0, %1" : "+v"(g.t0), "+v"(g.t1));
}
DI unsigned g2_s4(G2& g) {
  unsigned o;
  asm volatile("v_rcp_f32_e32 %1, %1\n\tv_rcp_f32_e32 %2, %2\n\tv_mul_f32_e32 %1, %3, %1\n\tv_mul_f32_e32 %2, %4, %2\n\tv_cvt_pk_bf16_f32 %0, %1, %2"
               : "=v"(o), "+v"(g.t0), "+v"(g.t1) : "v"(g.x0), "v"(g.x1));
  return o;
}

constexpr int F_SLOT = 36864, H_SLOT = 18432, F1_XCH = 6 * H_SLOT, F1_UV = F1_XCH + 32768, F_LDS = F1_UV + 1152;
DI unsigned f_stream(int fc, int g) {
  return g < 18 ? (unsigned)WS_W1P + (unsigned)(fc * 18 + g) * H_SLOT : (unsigned)WS_W2P + (unsigned)(fc * 18 + g - 18) * H_SLOT;
}
DI void f_issue(const Ctx& c, int fc, int g) {
  const int l = c.w - 8;
  const unsigned o = (unsigned)(l < 2 ? 5 * l : 2 + 4 * l) * 1024u, vl = c.lane * 16, gb = f_stream(fc, g) + o, lb = c.lds0 + (unsigned)(g % 6) * H_SLOT + o;
#pragma unroll
  for (int i = 0; i < 4; i++) dma16s(lb + i * 1024u, vl, c.sws, gb + i * 1024u);
  if (l < 2) dma16s(lb + 4096u, vl, c.sws, gb + 4096u);
}
template <int N> DI void f_wait(int l) { if (l < 2) wait_vm<5 * N>(); else wait_vm<4 * N>(); }
constexpr int F_FIN = 4 * H_SLOT;
DI void p6_finish(const Ctx& c, int rb, int fc) {
  const int w = c.w, lane = c.lane, ks = fc >> 2, ct = fc & 3, tk = lane & 15, g = lane >> 4;
  const bool pair = w < 8;
  const int T = w & 3, rt = pair ? w >> 2 : w - 8;
  const int col = ct * 144 + (pair ? 32 * T + 8 * g : 128 + 4 * g), row = rb * 128 + ks * 32 + rt * 16 + tk;
  u32x4 hv = {}, bv = {};
  const unsigned ho = (unsigned)(((col >> 3) & 1) * 32 + rt * 16 + tk) * 16u + (unsigned)(col >> 4) * 1024u + (unsigned)(col & 7) * 2u, hb = (unsigned)WS_HP + (unsigned)(rb * 4 + ks) * 36u * 1024u;
  if (w < 10) {
    if (pair) { hv = ld128s(c.sws, ho, hb); bv = *(const u32x4*)(c.b2 + col); }
    else { const u32x2 a = __builtin_amdgcn_raw_buffer_load_b64(c.sws, ho, hb, SC1), b2 = *(const u32x2*)(c.b2 + col); hv[0] = a[0]; hv[1] = a[1]; bv[0] = b2[0]; bv[1] = b2[1]; }
  }
  if (w == 10) poll(c, FL_P + (rb * 4 + ct) * 4 + (lane & 3), lane < 4);
  bar();
  if (w >= 10) return;
  const unsigned xg = (unsigned)WS_XCH + (unsigned)((rb * 4 + ct) * 16 + ks * 4) * 9216u + (unsigned)rt * 4608u;
  const unsigned fin = c.lds0 + F_FIN + (unsigned)rt * 9216u + (unsigned)lane * 16u;
  if (pair) {
    u32x4 pv[3];
#pragma unroll
    for (int i = 0; i < 3; i++) pv[i] = ld128s(c.sws, (unsigned)T * 1024u + (unsigned)lane * 16u, xg + (unsigned)(i + (i >= ks)) * 9216u);
    f32x4 a = lds_f128(fin + (unsigned)T * 2048u), b2 = lds_f128(fin + (unsigned)T * 2048u + 1024u);
#pragma unroll
    for (int i = 0; i < 3; i++) { a += f32x4{bflo(pv[i][0]), bfhi(pv[i][0]), bflo(pv[i][1]), bfhi(pv[i][1])}; b2 += f32x4{bflo(pv[i][2]), bfhi(pv[i][2]), bflo(pv[i][3]), bfhi(pv[i][3])}; }
    u32x4 r;
    r[0] = pk2(bflo(hv[0]) + (a[0] + bflo(bv[0])), bfhi(hv[0]) + (a[1] + bfhi(bv[0])));
    r[1] = pk2(bflo(hv[1]) + (a[2] + bflo(bv[1])), bfhi(hv[1]) + (a[3] + bfhi(bv[1])));
    r[2] = pk2(bflo(hv[2]) + (b2[0] + bflo(bv[2])), bfhi(hv[2]) + (b2[1] + bfhi(bv[2])));
    r[3] = pk2(bflo(hv[3]) + (b2[2] + bflo(bv[3])), bfhi(hv[3]) + (b2[3] + bfhi(bv[3])));
    *(u32x4*)(c.out + (size_t)row * D + col) = r;
  } else {
    u32x2 pv[3];
#pragma unroll
    for (int i = 0; i < 3; i++) pv[i] = __builtin_amdgcn_raw_buffer_load_b64(c.sws, 4096u + (unsigned)lane * 8u, xg + (unsigned)(i + (i >= ks)) * 9216u, SC1);
    f32x4 a = lds_f128(fin + 8192u);
#pragma unroll
    for (int i = 0; i < 3; i++) a += f32x4{bflo(pv[i][0]), bfhi(pv[i][0]), bflo(pv[i][1]), bfhi(pv[i][1])};
    u32x2 r;
    r[0] = pk2(bflo(hv[0]) + (a[0] + bflo(bv[0])), bfhi(hv[0]) + (a[1] + bfhi(bv[0])));
    r[1] = pk2(bflo(hv[1]) + (a[2] + bflo(bv[1])), bfhi(hv[1]) + (a[3] + bfhi(bv[1])));
    *(u32x2*)(c.out + (size_t)row * D + col) = r;
  }
}
DI void p45_loader(const Ctx& c, int rb, int fc) {
  const int l = c.w - 8, lane = c.lane, ks = fc >> 2, ct = fc & 3;
  f_issue(c, fc, 2); f_issue(c, fc, 3); f_issue(c, fc, 4);
  bar();
  u32x2 fd[4] = {};
#pragma unroll 1
  for (int g = 0; g < 36; g++) {
    if (g == 18) { bar(); bar(); }
    if (g == 8 && l == 0) poll(c, FL_W2 + (ks * 4 + ct) * 4 + (lane & 3), lane < 4);
    if (g == 20 && l == 0) poll(c, FL_H1 + rb * 16 + ks * 4 + (lane & 3), lane < 4);
    if (g < 32) f_wait<3>(l); else if (g == 32) f_wait<2>(l); else if (g == 33) f_wait<1>(l); else wait_vm<0>();
    bar();
    if (g == 8 || g == 10) {
      const unsigned ln = (unsigned)((l * 2 + ((g >> 1) & 1)) * 64 + lane);
      touch128(c.lds0 + L_DUMP, ln < 324u ? ln * 128u : 0x7ff00000u, c.sws, f_stream(fc, 18) + (unsigned)(c.bi & 7) * 41472u);
    }
    if (g == 6 && l == 3 && c.b == 0) {
#pragma unroll
      for (int i = 0; i < 4; i++) asm volatile("buffer_load_dwordx2 %0, %1, %2, 0 offen sc1" : "=&v"(fd[i]) : "v"((unsigned)(FL_D + i * 64 + lane) * 128u), "s"(c.sws) : "memory");
    }
    if (g == 12 && l == 3 && c.b == 0) {
      asm volatile("" : "+v"(fd[0]), "+v"(fd[1]), "+v"(fd[2]), "+v"(fd[3]));
      const unsigned n0 = (unsigned)c.nonce, n1 = (unsigned)(c.nonce >> 32);
      if (!__all(fd[0][0] == n0 && fd[0][1] == n1 && fd[1][0] == n0 && fd[1][1] == n1 && fd[2][0] == n0 && fd[2][1] == n1 && fd[3][0] == n0 && fd[3][1] == n1)) {
#pragma unroll 1
        for (int i = 0; i < 4; i++) poll(c, FL_D + i * 64 + lane, true);
      }
      if (lane == 0) __hip_atomic_store((unsigned long long*)(c.ws + FL_N * 128), c.nonce, __ATOMIC_RELAXED, __HIP_MEMORY_SCOPE_AGENT);
    }
    if (g + 5 < 36) f_issue(c, fc, g + 5);
  }
  bar();
  bar();
  p6_finish(c, rb, fc);
}
template <int KH>
DI void p45_ffn(const Ctx& c, int rb, int fc) {
  constexpr int kh = KH;
  const int w = c.w, lane = c.lane, rg = w & 3; const unsigned lds0 = c.lds0;
  const unsigned vl = lane * 16;
  const int ks = fc >> 2, ct = fc & 3;
  const int tok = lane & 31, h = lane >> 5;
  if (threadIdx.x < 288) *(LAS float*)(size_t)(lds0 + F1_UV + threadIdx.x * 4) = __uint_as_float((unsigned)c.b1[fc * 288 + threadIdx.x] << 16);
  if (w == 0) poll(c, FL_H + (2 * rb + lane / 6) * 6 + lane % 6, lane < 12);
  wait_lgkm0(); bar();
  u32x2 stv[6];
#pragma unroll
  for (int e = 0; e < 6; e++)
    asm volatile("buffer_load_dwordx2 %0, %1, %2, %3 offen offset:%4 sc1" : "=&v"(stv[e]) : "v"((unsigned)tok * 8u), "s"(c.sws),
                 "s"((unsigned)WS_ST2 + (unsigned)((2 * rb + (rg >> 1)) * 6) * 512u + (unsigned)(rg & 1) * 256u), "i"(e * 512) : "memory");
  u32x4 xf[18];
  {
    const unsigned so = (unsigned)WS_HP + ((unsigned)(rb * 4 + rg) * 36u + (unsigned)kh * 18u) * 1024u;
#pragma unroll
    for (int s = 0; s < 18; s++)
      asm volatile("buffer_load_dwordx4 %0, %1, %2, %3 offen offset:%4 sc1" : "=&v"(xf[s]) : "v"(vl), "s"(c.sws), "s"(so + (s >> 2) * 4096u), "i"((s & 3) * 1024) : "memory");
  }
  float ra, rc;
  {
    asm volatile("s_waitcnt vmcnt(18)" : "+v"(stv[0]), "+v"(stv[1]), "+v"(stv[2]), "+v"(stv[3]), "+v"(stv[4]), "+v"(stv[5]) :: "memory");
    float s = 0, q = 0;
#pragma unroll
    for (int e = 0; e < 6; e++) { s += __uint_as_float(stv[e][0]); q += __uint_as_float(stv[e][1]); }
    const float mean = s * (1.0f / D), var = fmaxf(q * (1.0f / D) - mean * mean, 0.f), rstd = rsqrtf(var + LN_EPS);
    ra = rstd; rc = -mean * rstd;
  }
  const unsigned rd = lds0 + (unsigned)kh * 9216u + vl;
  const unsigned hvo = (unsigned)(tok >> 4) * 1024u + (unsigned)h * 256u + (unsigned)(tok & 15) * 16u;
  const unsigned hbase = (unsigned)WS_H1P + ((unsigned)(rb * 4 + rg) * 144u + (unsigned)fc * 9u) * 2048u;
  const unsigned bia = lds0 + F1_UV + (unsigned)h * 32u;
  {
#pragma unroll
    for (int s = 0; s < 18; s++) {
      asm volatile("s_waitcnt vmcnt(%1)" : "+v"(xf[s]) : "i"(17 - s) : "memory");
#pragma unroll
      for (int e = 0; e < 4; e++) {
        const f32x2 v = f32x2{bflo(xf[s][e]), bfhi(xf[s][e])} * ra + rc;
        xf[s][e] = pk2(v[0], v[1]);
      }
      asm volatile("" : "+v"(xf[s]));
      SB();
    }
  }
  constexpr int PF = 3;
  u32x4 wf[PF];
  const f32x16 zero = {};
#define F1_BIAS(ACC, ba_) do { const f32x4 b0_ = lds_f128(ba_), b1_ = lds_f128(ba_ + 16), b2_ = lds_f128(ba_ + 64), b3_ = lds_f128(ba_ + 80); \
    ACC = f32x16{b0_[0], b0_[1], b0_[2], b0_[3], b1_[0], b1_[1], b1_[2], b1_[3], b2_[0], b2_[1], b2_[2], b2_[3], b3_[0], b3_[1], b3_[2], b3_[3]}; } while (0)
  f32x16 accG, accP, accQ;
  F1_BIAS(accG, bia + (unsigned)(1 - kh) * 128u);
  float vc1 = GC1; asm volatile("" : "+v"(vc1));
  unsigned r0 = 0;
#define F_NEXT(r) ((r) == 5u * H_SLOT ? 0u : (r) + H_SLOT)
  const unsigned xwr = lds0 + F1_XCH + (unsigned)(kh ^ 1) * 16384u + (unsigned)rg * 4096u + vl;
  const unsigned xrd = lds0 + F1_XCH + (unsigned)kh * 16384u + (unsigned)rg * 4096u + vl;
  f32x4 pp[4] = {};
#define F1_STAGE(q, G, SRC, R0, O) do { const int p_ = (q) >> 2, st_ = (q) & 3, r_ = (R0) + 2 * p_; \
      if (st_ == 0) g2_s1(G, SRC[r_], SRC[r_ + 1], pp[r_ >> 2][r_ & 3], pp[r_ >> 2][(r_ & 3) + 1]); \
      if (st_ == 1) g2_s2(G, vc1); if (st_ == 2) g2_s3(G); if (st_ == 3) O[p_] = g2_s4(G); } while (0)
#define F_HALF_BEGIN(s) do { if ((s) % 9 == 0) { bar(); sb = rd + r0; r0 = F_NEXT(r0); sn = rd + r0; asm volatile("" : "+v"(sb), "+v"(sn)); } } while (0)
#define F_FRAG(s) lds_u128((s) % 9 + PF < 9 ? sb + ((s) % 9 + PF) * 1024 : sn + ((s) % 9 + PF - 9) * 1024)
#define F1_PERIOD(t, ACC, PREV, GELU, HALF) do { \
    unsigned sb, sn; \
    u32x4 o; G2 g; \
    const int gt_ = ((t) - 2 - (HALF)) + kh;      \
    _Pragma("unroll") for (int s = 0; s < 18; s++) { \
      F_HALF_BEGIN(s); \
      if (s == 0 && (t) == 0) { _Pragma("unroll") for (int i = 0; i < PF; i++) wf[i] = lds_u128(sb + i * 1024); } \
      if (s == 0 && (GELU) && (HALF) == 0) { _Pragma("unroll") for (int e = 0; e < 4; e++) pp[e] = lds_f128(xrd + e * 1024); SB(); } \
      const u32x4 cur = wf[s % PF]; \
      wf[s % PF] = F_FRAG(s); \
      ACC = mfma32(cur, xf[s], (s == 0 && (HALF) == 1) ? zero : ACC); \
      if ((HALF) == 1 && s % 4 == 1 && s / 4 < 4) { const int e = s / 4; lds_w128(xwr + e * 1024, f32x4{accG[4 * e], accG[4 * e + 1], accG[4 * e + 2], accG[4 * e + 3]}); } \
      if (GELU) { \
        if (s >= 2 && s <= 17) F1_STAGE(s - 2, g, PREV, 8 * (HALF), o); \
        if (s == 17) st128s(o, c.sws, hvo + (HALF) * 512u, hbase + (unsigned)gt_ * 2048u); \
      } \
      if ((HALF) == 1 && s == 15) {     \
        const int nt_ = (t) + 1 == 8 ? 8 : (t) + 1 + (1 - kh); \
        if ((t) + 1 == 8 && kh == 0) accG = zero; \
        else { unsigned ba_ = bia + (unsigned)nt_ * 128u; asm volatile("" : "+v"(ba_)); F1_BIAS(accG, ba_); } \
      } \
      SB(); \
    } \
    asm volatile("" :: "v"(ACC)); \
  } while (0)
  const unsigned hoff0 = (unsigned)WS_H1P + ((unsigned)(rb * 4 + rg) * 144u + (unsigned)ks * 36u) * 2048u;
  u32x4 hA[4], hB[4], hC[4];
#define HLOAD1(dst, t, i) do { const int p_ = 4 * (t) + 2 * kh + ((i) >> 1) + 9 * ct; const unsigned so_ = hoff0 + (unsigned)(p_ >= 36 ? p_ - 36 : p_) * 2048u; \
    if (((i) & 1) == 0) asm volatile("buffer_load_dwordx4 %0, %1, %2, %3 offen sc1" : "=&v"(dst[i]) : "v"(vl), "s"(c.sws), "s"(so_) : "memory"); \
    else asm volatile("buffer_load_dwordx4 %0, %1, %2, %3 offen offset:1024 sc1" : "=&v"(dst[i]) : "v"(vl), "s"(c.sws), "s"(so_) : "memory"); } while (0)
#define HWAIT(N, dst) asm volatile("s_waitcnt vmcnt(" #N ")" : "+v"(dst[0]), "+v"(dst[1]), "+v"(dst[2]), "+v"(dst[3]) :: "memory")
  {
    F1_PERIOD(0, accG, accQ, false, 0);
    F1_PERIOD(1, accP, accQ, false, 1);
    F1_PERIOD(2, accG, accP, true, 0);
    F1_PERIOD(3, accQ, accP, true, 1);
    F1_PERIOD(4, accG, accQ, true, 0);
    F1_PERIOD(5, accP, accQ, true, 1);
    F1_PERIOD(6, accG, accP, true, 0);
    F1_PERIOD(7, accQ, accP, true, 1);
    {
      wait_vm<2>();
      unsigned sb, sn;
      u32x4 o, o2; G2 g, g2;
      const int gt_ = 6 + kh;
#pragma unroll
      for (int s = 0; s < 18; s++) {
        F_HALF_BEGIN(s);
        if (s == 0) {
#pragma unroll
          for (int e = 0; e < 4; e++) pp[e] = lds_f128(xrd + e * 1024);
          SB();
        }
        const u32x4 cur = wf[s % PF];
        wf[s % PF] = F_FRAG(s);
        accG = mfma32(cur, xf[s], accG);
        if (s == 10 + KH) HLOAD1(hA, 0, 0); if (s == 12 + KH) HLOAD1(hA, 0, 1); if (s == 14 + KH) HLOAD1(hA, 0, 2); if (s == 16 + KH) HLOAD1(hA, 0, 3);
        if (s >= 2 && s <= 17) { F1_STAGE(s - 2, g, accQ, 0, o); F1_STAGE(s - 2, g2, accQ, 8, o2); }
        if (s == 17) { st128s(o, c.sws, hvo, hbase + (unsigned)gt_ * 2048u); st128s(o2, c.sws, hvo + 512u, hbase + (unsigned)gt_ * 2048u); }
        SB();
      }
      lds_w128(xwr, kh ? f32x4{accG[0], accG[1], accG[2], accG[3]} : f32x4{accG[8], accG[9], accG[10], accG[11]});
      lds_w128(xwr + 1024, kh ? f32x4{accG[4], accG[5], accG[6], accG[7]} : f32x4{accG[12], accG[13], accG[14], accG[15]});
      wait_lgkm0(); bar();
      const f32x4 q0 = lds_f128(xrd), q1 = lds_f128(xrd + 1024);
      const f32x4 k0 = (kh ? f32x4{accG[8], accG[9], accG[10], accG[11]} : f32x4{accG[0], accG[1], accG[2], accG[3]}) + q0;
      const f32x4 k1 = (kh ? f32x4{accG[12], accG[13], accG[14], accG[15]} : f32x4{accG[4], accG[5], accG[6], accG[7]}) + q1;
      const f32x2 z0 = gelu2(f32x2{k0[0], k0[1]}), z1 = gelu2(f32x2{k0[2], k0[3]}), z2 = gelu2(f32x2{k1[0], k1[1]}), z3 = gelu2(f32x2{k1[2], k1[3]});
      o = u32x4{pk2(z0[0], z0[1]), pk2(z1[0], z1[1]), pk2(z2[0], z2[1]), pk2(z3[0], z3[1])};
      st128s(o, c.sws, hvo + (unsigned)kh * 512u, hbase + 8u * 2048u);
    }
  }
  publish(c, FL_H1 + rb * 16 + fc, true);
  HLOAD1(hB, 1, 0); HLOAD1(hB, 1, 1); HLOAD1(hB, 1, 2); HLOAD1(hB, 1, 3);
  f32x4 acc[2][9];
#pragma unroll
  for (int a = 0; a < 2; a++)
#pragma unroll
    for (int n = 0; n < 9; n++) acc[a][n] = f32x4{0, 0, 0, 0};
#define F2_PERIOD(t, HC, HN, HM, VMW, MODE) do { \
    HWAIT(VMW, HC); \
    unsigned sb, sn; \
    _Pragma("unroll") for (int s = 0; s < 18; s++) { \
      F_HALF_BEGIN(s); \
      const u32x4 cur = wf[s % PF]; \
      wf[s % PF] = F_FRAG(s); \
      const int k = s / 9, nt = s % 9; \
      acc[0][nt] = mfma16(cur, HC[2 * k], acc[0][nt]); \
      acc[1][nt] = mfma16(cur, HC[2 * k + 1], acc[1][nt]); \
      if ((MODE) == 3) { if (s == KH) HLOAD1(HM, (t) + 1, 0); if (s == 2 + KH) HLOAD1(HM, (t) + 1, 1); if (s == 4 + KH) HLOAD1(HM, (t) + 1, 2); if (s == 6 + KH) HLOAD1(HM, (t) + 1, 3); } \
      if ((MODE) == 0 || (MODE) == 3) { if (s == 9 + KH) HLOAD1(HN, (t) + 2, 0); if (s == 11 + KH) HLOAD1(HN, (t) + 2, 1); if (s == 13 + KH) HLOAD1(HN, (t) + 2, 2); if (s == 15 + KH) HLOAD1(HN, (t) + 2, 3); } \
      SB(); \
    } \
  } while (0)
  F2_PERIOD(0, hA, hC, hC, 4, 1);
  F2_PERIOD(1, hB, hA, hC, 0, 3);
  F2_PERIOD(2, hC, hB, hB, 4, 0);
  F2_PERIOD(3, hA, hC, hC, 4, 0);
  F2_PERIOD(4, hB, hA, hA, 4, 0);
  F2_PERIOD(5, hC, hB, hB, 4, 0);
  F2_PERIOD(6, hA, hC, hC, 4, 0);
  F2_PERIOD(7, hB, hA, hA, 4, 1);
  F2_PERIOD(8, hC, hB, hB, 0, 1);
  const unsigned xb = lds0 + (unsigned)rg * 18432u + vl;
#pragma unroll
  for (int n = 0; n < 9; n++) lds_w128(xb + (unsigned)(kh ^ 1) * 9216u + n * 1024, kh ? acc[0][n] : acc[1][n]);
  wait_lgkm0(); bar();
  f32x4 o[9];
#pragma unroll
  for (int n = 0; n < 9; n++) o[n] = lds_f128(xb + kh * 9216u + n * 1024) + (kh ? acc[1][n] : acc[0][n]);
  const unsigned xg = (unsigned)WS_XCH + (unsigned)((rb * 4 + ct) * 16) * 9216u + (unsigned)kh * 4608u;
  if (rg != ks) {
    const unsigned d = xg + (unsigned)(rg * 4 + ks) * 9216u;
#pragma unroll
    for (int T = 0; T < 4; T++)
      st128s(u32x4{pk2(o[2 * T][0], o[2 * T][1]), pk2(o[2 * T][2], o[2 * T][3]), pk2(o[2 * T + 1][0], o[2 * T + 1][1]), pk2(o[2 * T + 1][2], o[2 * T + 1][3])}, c.sws, vl + T * 1024u, d);
    __builtin_amdgcn_raw_buffer_store_b64(u32x2{pk2(o[8][0], o[8][1]), pk2(o[8][2], o[8][3])}, c.sws, 4096u + lane * 8u, d, SC1);
  } else {
#pragma unroll
    for (int n = 0; n < 9; n++) lds_w128(lds0 + F_FIN + (unsigned)kh * 9216u + n * 1024u + vl, o[n]);
  }
  wait_lgkm0();
  publish(c, FL_P + (rb * 4 + ct) * 4 + ks, true);
  p6_finish(c, rb, fc);
}

constexpr int M_LDS = 163840;
static_assert(L_WO + 110592 <= L_DUMP && 4 * Q_SLOT + 640 <= L_DUMP && F_LDS <= L_DUMP, "LDS map");
__global__ KATTR void fused_block(const u16* __restrict__ x, const u16* __restrict__ wqkv, const u16* __restrict__ wo, const u16* __restrict__ w1,
                                  const u16* __restrict__ b1, const u16* __restrict__ w2, const u16* __restrict__ b2, u16* __restrict__ out, char* __restrict__ ws) {
  extern __shared__ __attribute__((aligned(16))) char smem[];
  Ctx c;
  c.x = x; c.wqkv = wqkv; c.wo = wo; c.w1 = w1; c.b1 = b1; c.w2 = w2; c.b2 = b2; c.out = out; c.ws = ws;
  c.sws = mk_srd(ws, WS_BYTES);
  c.lds0 = (unsigned)(size_t)smem;
  c.b = blockIdx.x; c.xcd = c.b & 7; c.bi = c.b >> 3;
  c.w = rfl(threadIdx.x >> 6); c.lane = threadIdx.x & 63;
  const int xcd = c.xcd, bi = c.bi, w = c.w, lane = c.lane;
  const int rb4 = (xcd >> 2) * 8 + (bi & 7), fc4 = (xcd & 3) * 4 + (bi >> 3);
  if (bi < 24) {
    p1_qkv(c, (xcd >> 1) * 4 + (bi & 3), (xcd & 1) * 6 + (bi >> 2));
  } else {
    const int j = bi - 24, fc = (xcd & 3) * 4 + (j >> 1), part = (xcd >> 2) * 2 + (j & 1);
    helper_stats(c, xcd * 8 + j, fc, part);
    if (w >= 8) wait_vm<0>();
    bar();
    relayout_w1(c, fc, part);
    publish(c, FL_W1 + fc * 4 + part, true);
  }
  if (w >= 8) p2_loader(c, xcd * 4 + (bi & 3), bi >> 2); else p2_attn(c, xcd * 4 + (bi & 3), bi >> 2);
  if (w >= 8) {
    if (w == 8 && bi < 24) poll(c, FL_A + xcd * 32 + (lane & 31), lane < 32);
    if (w == 9) poll(c, FL_W1 + fc4 * 4 + (lane & 3), lane < 4);
    if (bi < 24) wait_vm<0>();
    bar();
    f_issue(c, fc4, 0); f_issue(c, fc4, 1);
    {
#pragma unroll
      for (int i = 0; i < 2; i++) { const unsigned l = (unsigned)(((w - 8) * 2 + i) * 64 + lane); touch128(c.lds0 + L_DUMP, l < 288u ? l * 128u : 0x7ff00000u, c.sws, (unsigned)WS_W1P + (unsigned)(fc4 * 9 + 1) * F_SLOT + (unsigned)(bi & 7) * 36864u); }
    }
  }
  if (bi < 24) {
    if (w >= 8) {
      bar(); bar(); bar();
      publish(c, FL_H + (xcd * 4 + (bi & 3)) * 6 + (bi >> 2), false);
    } else {
      p3_wo(c, xcd * 4 + (bi & 3), bi >> 2);
    }
  } else {
    if (w < 8) bar();
    const int j = bi - 24, ks = xcd & 3, ct = j >> 1, part = (xcd >> 2) * 2 + (j & 1);
    if (w < 9) relayout_w2(c, c.lds0 + L_WO + (unsigned)w * 9216u, ks * 36 + part * 9 + w, ct);
    publish(c, FL_W2 + (ks * 4 + ct) * 4 + part, w < 9);
  }
  if (w >= 8) p45_loader(c, rb4, fc4); else if (w < 4) p45_ffn<0>(c, rb4, fc4); else p45_ffn<1>(c, rb4, fc4);
}

extern "C" void kernel_launch(void* const* d_in, const int* in_sizes, int n_in, void* d_out, int out_size, void* d_ws, size_t ws_size,
                              hipStream_t stream) {
  (void)hipFuncSetAttribute((const void*)fused_block, hipFuncAttributeMaxDynamicSharedMemorySize, M_LDS);
  fused_block<<<256, 768, M_LDS, stream>>>((const u16*)d_in[0], (const u16*)d_in[1], (const u16*)d_in[2], (const u16*)d_in[4], (const u16*)d_in[5],
                                           (const u16*)d_in[6], (const u16*)d_in[7], (u16*)d_out, (char*)d_ws);
}
```
